```python
import torch

def _softmax(x, axis=-1):
    return torch.softmax(x, dim=axis)
def _gelu(x):
    return torch.nn.functional.gelu(x, approximate="tanh")
def _silu(x):
    return torch.nn.functional.silu(x)
def _sigmoid(x):
    return torch.sigmoid(x)
def _layernorm(x, axis=-1, eps=1e-5):
    m = x.mean(dim=axis, keepdim=True)
    v = x.var(dim=axis, keepdim=True, unbiased=False)
    return (x - m) / torch.sqrt(v + eps)
def _scatter_add(tgt, idx, src, axis=0):
    return tgt.clone().index_add_(axis, idx, src)
def _segment_sum(src, seg, n):
    out = torch.zeros((n,) + src.shape[1:], dtype=src.dtype)
    return out.index_add_(0, seg, src)
def _pad(x, pw):
    flat = tuple(v for pair in reversed(pw) for v in pair)
    return torch.nn.functional.pad(x, flat)

kernel_name = 'standalone__paged_block_table_kv_radixattention__sampling'

def setup_inputs():

    _w = (lambda a: (lambda t: t.to(torch.bfloat16) if t.is_floating_point() else t)(torch.as_tensor(a)))
    B, H, Hkv, Dh = 16, 32, 16, 128
    Lk = 4096
    bs = 16
    nb = max(2, Lk // bs)
    P = max(1, nb // 2)
    n_phys = P + B * (nb - P)
    shared = torch.arange(P, dtype=torch.int64)
    priv = P + torch.arange(B * (nb - P), dtype=torch.int64).reshape(B, nb - P)
    bt = torch.cat([torch.tile(shared[None, :], (B, 1)), priv], dim=1)
    ctx = nb * bs - (torch.arange(1, B + 1, dtype=torch.int64) * 2654435761) % max(1, (nb * bs - P * bs) // 2)
    return {
        "q": _w((lambda *s: torch.randn(*s, dtype=torch.bfloat16) * 0.8)(B, H, Dh)),
        "k_cache": _w((lambda *s: torch.randn(*s, dtype=torch.bfloat16) * 0.8)(n_phys, bs, Hkv, Dh)),
        "v_cache": _w((lambda *s: torch.randn(*s, dtype=torch.bfloat16) * 0.05)(n_phys, bs, Hkv, Dh)),
        "block_table": _w(bt.to(torch.int64)),
        "ctx_lens": _w(torch.as_tensor(ctx).to(torch.int64)),
    }

def reference(q, k_cache, v_cache, block_table, ctx_lens):
    B, H, Dh = q.shape
    n_phys, bs, Hkv, _ = k_cache.shape
    nb = block_table.shape[1]
    Lk = nb * bs
    rep = H // Hkv
    k = k_cache[block_table].reshape(B, Lk, Hkv, Dh)
    v = v_cache[block_table].reshape(B, Lk, Hkv, Dh)
    k = (lambda x, n, axis: torch.repeat_interleave(x, n, dim=axis))(k, rep, axis=2)
    v = (lambda x, n, axis: torch.repeat_interleave(x, n, dim=axis))(v, rep, axis=2)
    scores = torch.einsum("bhd,bkhd->bhk", q, k) * (Dh ** -0.5)
    pos = torch.arange(Lk)
    valid = pos[None, :] < ctx_lens[:, None]
    scores = torch.where(valid[:, None, :], scores, -1e9)
    p = _softmax(scores, axis=-1)
    return torch.einsum("bhk,bkhd->bhd", p, v)
```

```cpp
#include <hip/hip_runtime.h>
#include <hip/hip_bf16.h>
#include <cstdint>
#include <cstdio>

constexpr int D = 128, PAGE = 16, WAVES = 4, NTHR = WAVES * 64;
constexpr float SCALE = 0.088388347648318440f;
constexpr int B = 16, HQ = 32, HKV = 16, CTX = 4096, G = 2;
constexpr int GE = 2, GEP = 4;
constexpr int NBPS = CTX / PAGE;
#define NSPLIT 2
constexpr int PPS = NBPS / NSPLIT, PPW = PPS / WAVES;
constexpr int TOK_STRIDE = HKV * D;
constexpr size_t WS_PML = 0;
constexpr size_t WS_PO = 65536;
constexpr int LDS_P = 0, LDS_MRG = WAVES * 64, MRG_REC = 4 + D, LDS_FLOATS = LDS_MRG + WAVES * GEP * MRG_REC + 4;

using bf16 = __hip_bfloat16;
using bf16x2 = __attribute__((ext_vector_type(2))) __bf16;
using u32x4  = __attribute__((ext_vector_type(4))) unsigned;
using f32x4  = __attribute__((ext_vector_type(4))) float;
__device__ __forceinline__ float dot2(unsigned k, unsigned q, float acc) {
  return __builtin_amdgcn_fdot2_f32_bf16(__builtin_bit_cast(bf16x2, k), __builtin_bit_cast(bf16x2, q), acc, false);
}
template <int CTRL> __device__ __forceinline__ float dpp(float x) {
  return __builtin_bit_cast(float, __builtin_amdgcn_mov_dpp(__builtin_bit_cast(int, x), CTRL, 0xf, 0xf, true));
}
constexpr int XOR1 = 0xB1, XOR2 = 0x4E, XOR7 = 0x141, XOR8 = 0x128, ROR4 = 0x124;
__device__ __forceinline__ float xrow16_max(float x) {
  auto s = __builtin_amdgcn_permlane16_swap(__float_as_uint(x), __float_as_uint(x), false, false);
  x = fmaxf(__uint_as_float(s[0]), __uint_as_float(s[1]));
  auto t = __builtin_amdgcn_permlane32_swap(__float_as_uint(x), __float_as_uint(x), false, false);
  return fmaxf(__uint_as_float(t[0]), __uint_as_float(t[1]));
}
__device__ __forceinline__ float xrow16_sum(float x) {
  auto s = __builtin_amdgcn_permlane16_swap(__float_as_uint(x), __float_as_uint(x), false, false);
  x = __uint_as_float(s[0]) + __uint_as_float(s[1]);
  auto t = __builtin_amdgcn_permlane32_swap(__float_as_uint(x), __float_as_uint(x), false, false);
  return __uint_as_float(t[0]) + __uint_as_float(t[1]);
}
__device__ __forceinline__ u32x4 ldnt(const bf16* p) { return __builtin_nontemporal_load(reinterpret_cast<const u32x4*>(p)); }

__global__ void __launch_bounds__(NTHR) attn_decode_split(const bf16* q, const bf16* k, const bf16* v, const int* bt, const int* lens, unsigned char* ws) {
  __shared__ __attribute__((aligned(16))) float lds[LDS_FLOATS];
  const int s = blockIdx.x, kvh = blockIdx.y, b = blockIdx.z;
  const int tid = threadIdx.x, lane = tid & 63, wave = __builtin_amdgcn_readfirstlane(tid >> 6), r = lane >> 4, c = lane & 15;
  constexpr float C2 = SCALE * 1.4426950408889634f;
  const int len = lens[b];
  const int npages = (len + PAGE - 1) / PAGE;
  const int pps = (npages + NSPLIT - 1) / NSPLIT;
  const int p0 = s * pps, p1 = min(p0 + pps, npages);
  const int nwp = (p1 > p0 + wave) ? min(PPW, (p1 - p0 - wave + WAVES - 1) / WAVES) : 0;
  int btv = 0;
  if (lane < PPW) { const int pg = min(p0 + wave + 4 * lane, NBPS - 1); btv = bt[(size_t)b * NBPS + pg]; }
  u32x4 qpk[GE];
#pragma unroll
  for (int g = 0; g < GE; ++g) qpk[g] = *reinterpret_cast<const u32x4*>(q + ((size_t)b * HQ + kvh * GE + g) * D + 8 * c);
  const size_t loff = (size_t)r * TOK_STRIDE + 8 * c;
#define PAGE_BASE(phys) ((size_t)(phys) * (PAGE * HKV * D) + (size_t)kvh * D)
#define ISSUE(i_) do { const int phys_ = __builtin_amdgcn_readlane(btv, (i_)); const bf16* kb_ = k + PAGE_BASE(phys_) + loff; const bf16* vb_ = v + PAGE_BASE(phys_) + loff; \
    _Pragma("unroll") for (int t_ = 0; t_ < 4; ++t_) { Kn[t_] = ldnt(kb_ + (size_t)t_ * 4 * TOK_STRIDE); Vn[t_] = ldnt(vb_ + (size_t)t_ * 4 * TOK_STRIDE); } } while (0)
  u32x4 Kn[4], Vn[4];
  float m = -INFINITY, l = 0.f, acc[GE * 8];
#pragma unroll
  for (int kk = 0; kk < GE * 8; ++kk) acc[kk] = 0.f;
  if (nwp > 0) ISSUE(0);
  for (int i = 0; i < nwp; ++i) {
    u32x4 Kc[4], Vc[4];
#pragma unroll
    for (int t = 0; t < 4; ++t) { Kc[t] = Kn[t]; Vc[t] = Vn[t]; }
    const int pg = p0 + wave + 4 * i, rem = len - pg * PAGE;
    if (i + 1 < nwp) ISSUE(i + 1);
    float x[4 * GEP];
#pragma unroll
    for (int t = 0; t < 4; ++t)
#pragma unroll
      for (int g = 0; g < GEP; ++g) { float d = 0.f;
        if (g < GE) {
#pragma unroll
          for (int kk = 0; kk < 4; ++kk) d = dot2(Kc[t][kk], qpk[g][kk], d); }
        x[t * GEP + g] = d; }
#define TR_STEP(HALF, CTRL, BIT) _Pragma("unroll") for (int h = 0; h < (HALF); ++h) { \
      const float lo_ = x[h], hi_ = x[h + (HALF)]; const float t1_ = lo_ + dpp<CTRL>(lo_), t2_ = hi_ + dpp<CTRL>(hi_); \
      x[h] = (c & (BIT)) ? t2_ : t1_; }
    TR_STEP(8, XOR8, 8) TR_STEP(4, XOR7, 4) TR_STEP(2, XOR2, 2) TR_STEP(1, XOR1, 1)
#undef TR_STEP
    const bool tail = rem < PAGE;
    const bool live = !tail || (r + 4 * (c >> 2)) < rem;
    const float sc = live ? x[0] * C2 : -INFINITY;
    float pm = sc; pm = fmaxf(pm, dpp<ROR4>(pm)); pm = fmaxf(pm, dpp<XOR8>(pm)); pm = xrow16_max(pm);
    const float mn = fmaxf(m, pm), msub = (mn == -INFINITY) ? 0.f : mn;
    const float alpha = __builtin_amdgcn_exp2f(m - msub), p = __builtin_amdgcn_exp2f(sc - msub);
    l = fmaf(l, alpha, p); m = mn;
#pragma unroll
    for (int q4 = 0; q4 < GE; ++q4) {
      const float al = q4 == 0 ? dpp<0x00>(alpha) : dpp<0x55>(alpha);
#pragma unroll
      for (int kk = 0; kk < 8; ++kk) acc[q4 * 8 + kk] *= al; }
    float* prow = lds + LDS_P + (wave * 4 + r) * 16;
    prow[c] = p;
    asm volatile("s_waitcnt lgkmcnt(0)" ::: "memory");
    f32x4 pr[GEP];
#pragma unroll
    for (int u = 0; u < GEP; ++u) pr[u] = *reinterpret_cast<const f32x4*>(prow + 4 * u);
    if (tail) {
#pragma unroll
      for (int t = 0; t < 4; ++t) { const bool lv = (r + 4 * t) < rem;
#pragma unroll
        for (int kk = 0; kk < 4; ++kk) Vc[t][kk] = lv ? Vc[t][kk] : 0u; } }
#pragma unroll
    for (int t = 0; t < 4; ++t) {
      float vf[8];
#pragma unroll
      for (int kk = 0; kk < 4; ++kk) { vf[2 * kk] = __builtin_bit_cast(float, Vc[t][kk] << 16); vf[2 * kk + 1] = __builtin_bit_cast(float, Vc[t][kk] & 0xffff0000u); }
#pragma unroll
      for (int g = 0; g < GE; ++g) { const float pg_ = pr[t][g];
#pragma unroll
        for (int kk = 0; kk < 8; ++kk) acc[g * 8 + kk] = fmaf(pg_, vf[kk], acc[g * 8 + kk]); } }
  }
#pragma unroll
  for (int kk = 0; kk < GE * 8; ++kk) acc[kk] = xrow16_sum(acc[kk]);
  { float t = l; t += dpp<ROR4>(t); t += dpp<XOR8>(t); l = xrow16_sum(t); }
  float* mrg = lds + LDS_MRG;
  if (r == 0) {
#pragma unroll
    for (int g = 0; g < GE; ++g) {
      float* rec = mrg + (wave * GEP + g) * MRG_REC;
#pragma unroll
      for (int kk = 0; kk < 8; kk += 4) *reinterpret_cast<f32x4*>(rec + 4 + 8 * c + kk) = f32x4{acc[g * 8 + kk], acc[g * 8 + kk + 1], acc[g * 8 + kk + 2], acc[g * 8 + kk + 3]};
      if (c == g) { rec[0] = m; rec[1] = l; } } }
  __syncthreads();
  float* pml = reinterpret_cast<float*>(ws + WS_PML);
  float* po = reinterpret_cast<float*>(ws + WS_PO);
  {
    const int g = wave, d0 = 2 * lane;
    if (g < GE) {
    const size_t row = (size_t)b * HQ + kvh * GE + g;
    float M = -INFINITY;
#pragma unroll
    for (int w = 0; w < WAVES; ++w) M = fmaxf(M, mrg[(w * GEP + g) * MRG_REC]);
    float L = 0.f, o0 = 0.f, o1 = 0.f;
#pragma unroll
    for (int w = 0; w < WAVES; ++w) { const float* rec = mrg + (w * GEP + g) * MRG_REC; const float lw = rec[1];
      const float wt = (lw > 0.f) ? __builtin_amdgcn_exp2f(rec[0] - M) : 0.f;
      L = fmaf(wt, lw, L); o0 = fmaf(wt, rec[4 + d0], o0); o1 = fmaf(wt, rec[4 + d0 + 1], o1); }
      const size_t slot = row * NSPLIT + s;
      *reinterpret_cast<float2*>(po + slot * D + d0) = make_float2(o0, o1);
      if (lane == 0) *reinterpret_cast<float2*>(pml + 2 * slot) = make_float2(M, L);
    }
  }
}

__global__ void __launch_bounds__(64) attn_decode_combine(const float* pml_, const float* po_, bf16* out) {
  const size_t row = blockIdx.x;
  const float* pml = pml_ + row * NSPLIT * 2; const float* po = po_ + row * NSPLIT * D;
  const int d0 = 2 * threadIdx.x;
  float M = -INFINITY;
  for (int s = 0; s < NSPLIT; ++s) M = fmaxf(M, pml[2 * s]);
  float L = 0.f, o0 = 0.f, o1 = 0.f;
  for (int s = 0; s < NSPLIT; ++s) {
    const float ls = pml[2 * s + 1], w = (ls > 0.f) ? __builtin_amdgcn_exp2f(pml[2 * s] - M) : 0.f;
    const float2 a = *reinterpret_cast<const float2*>(po + (size_t)s * D + d0);
    L = fmaf(w, ls, L); o0 = fmaf(w, a.x, o0); o1 = fmaf(w, a.y, o1);
  }
  const float inv = (L > 0.f) ? 1.f / L : 0.f;
  __hip_bfloat162 r2; r2.x = __float2bfloat16(o0 * inv); r2.y = __float2bfloat16(o1 * inv);
  *reinterpret_cast<__hip_bfloat162*>(out + row * D + d0) = r2;
}

extern "C" void kernel_launch(void* const* d_in, const int* in_sizes, int n_in, void* d_out, int out_size, void* d_ws, size_t ws_size, hipStream_t stream) {
  const bf16* q = (const bf16*)d_in[0]; const bf16* k = (const bf16*)d_in[1]; const bf16* v = (const bf16*)d_in[2];
  const int* bt = (const int*)d_in[3]; const int* lens = (const int*)d_in[4];
  hipLaunchKernelGGL(attn_decode_split, dim3(NSPLIT, HKV, B), dim3(NTHR), 0, stream, q, k, v, bt, lens, (unsigned char*)d_ws);
  hipLaunchKernelGGL(attn_decode_combine, dim3(B * HQ), dim3(64), 0, stream, (const float*)((char*)d_ws + WS_PML), (const float*)((char*)d_ws + WS_PO), (bf16*)d_out);
}
```

```cpp
#include <hip/hip_runtime.h>
#include <hip/hip_bf16.h>
#include <stdint.h>

namespace rattn {

constexpr int NSEQ = 16, NH = 32, NKV = 16, DH = 128, NBLK = 256, BS = 16;
constexpr int LK = NBLK * BS;
#define RING 3
#define FCOST 56
#define SPEC 1
#define SLEEP 12
#define PACE_LEAD 32
#define SHPUB 0
#define APACE_DELTA 117
#define PACE 6900
#define MAPMODE 1
constexpr int R = RING;
constexpr int STAGE = 32768;
constexpr int LDS_BT = R * STAGE;
constexpr int LDS_QB = LDS_BT + NSEQ * NBLK * 4;
constexpr int LDS_SC = LDS_QB + 4 * R * 512;
constexpr int LDS_MISC = LDS_SC + 4096;
constexpr int LDS_PROG = LDS_MISC + 64;
constexpr int LDS_TOTAL = LDS_PROG + 512;
static_assert(LDS_TOTAL <= 160 * 1024, "LDS");

constexpr unsigned WS_G = 0;
constexpr unsigned WS_PROG = 2048;
constexpr unsigned WS_PP = 4096;
constexpr unsigned PP_W = 2176;
constexpr unsigned WS_SP = 4u << 20;
constexpr unsigned SP_W = 16384 + 256 + 128;
constexpr unsigned WS_END = WS_SP + 4u * 64u * 4u * SP_W;
constexpr unsigned WS_STAMP = 64u << 20;

typedef unsigned u32x4 __attribute__((ext_vector_type(4)));
typedef float f32x4 __attribute__((ext_vector_type(4)));
typedef float f32x2 __attribute__((ext_vector_type(2)));
typedef __bf16 bf16x8 __attribute__((ext_vector_type(8)));
typedef __bf16 bf16x2 __attribute__((ext_vector_type(2)));
typedef short s16x4 __attribute__((ext_vector_type(4)));
typedef int i32x4 __attribute__((ext_vector_type(4)));
#define LAS __attribute__((address_space(3)))
#define GAS __attribute__((address_space(1)))

constexpr float C2 = 0.088388347648318440f * 1.4426950408889634f;

__device__ __forceinline__ int rfl(int x) { return __builtin_amdgcn_readfirstlane(x); }
__device__ __forceinline__ int rl(int x, int l) { return __builtin_amdgcn_readlane(x, l); }
__device__ __forceinline__ float rlf(float x, int l) { return __builtin_bit_cast(float, __builtin_amdgcn_readlane(__builtin_bit_cast(int, x), l)); }

__device__ __forceinline__ void dma16(const void* sbase, unsigned voff, unsigned dst) {
  unsigned keep;
  asm volatile("s_mov_b32 %0, m0\n\ts_mov_b32 m0, %3\n\ts_nop 0\n\tglobal_load_lds_dwordx4 %1, %2 nt\n\ts_mov_b32 m0, %0"
               : "=&s"(keep) : "v"(voff), "s"(sbase), "s"(dst) : "memory");
}
__device__ __forceinline__ void dma16_keep(const void* sbase, unsigned voff, unsigned dst) {
  unsigned keep;
  asm volatile("s_mov_b32 %0, m0\n\ts_mov_b32 m0, %3\n\ts_nop 0\n\tglobal_load_lds_dwordx4 %1, %2\n\ts_mov_b32 m0, %0"
               : "=&s"(keep) : "v"(voff), "s"(sbase), "s"(dst) : "memory");
}
__device__ __forceinline__ void dma_stage(const void* sK, const void* sV, unsigned dstK, unsigned dstV,
                                          unsigned k0, unsigned k1, unsigned k2, unsigned k3,
                                          unsigned v0, unsigned v1, unsigned v2, unsigned v3) {
  unsigned keep;
  asm volatile("s_mov_b32 %0, m0\n\ts_mov_b32 m0, %3\n\ts_nop 0\n\t"
               "global_load_lds_dwordx4 %5, %1 nt\n\t"
               "global_load_lds_dwordx4 %6, %1 offset:1024 nt\n\t"
               "global_load_lds_dwordx4 %7, %1 offset:2048 nt\n\t"
               "global_load_lds_dwordx4 %8, %1 offset:3072 nt\n\t"
               "s_mov_b32 m0, %4\n\ts_nop 0\n\t"
               "global_load_lds_dwordx4 %9, %2 nt\n\t"
               "global_load_lds_dwordx4 %10, %2 offset:1024 nt\n\t"
               "global_load_lds_dwordx4 %11, %2 offset:2048 nt\n\t"
               "global_load_lds_dwordx4 %12, %2 offset:3072 nt\n\t"
               "s_mov_b32 m0, %0"
               : "=&s"(keep)
               : "s"(sK), "s"(sV), "s"(dstK), "s"(dstV), "v"(k0), "v"(k1), "v"(k2), "v"(k3), "v"(v0), "v"(v1), "v"(v2), "v"(v3)
               : "memory");
}
__device__ __forceinline__ void dma16_sc1(const void* sbase, unsigned voff, unsigned dst) {
  unsigned keep;
  asm volatile("s_mov_b32 %0, m0\n\ts_mov_b32 m0, %3\n\ts_nop 0\n\tglobal_load_lds_dwordx4 %1, %2 sc1\n\ts_mov_b32 m0, %0"
               : "=&s"(keep) : "v"(voff), "s"(sbase), "s"(dst) : "memory");
}
template <int CTRL> __device__ __forceinline__ int dppi(int x) { return __builtin_amdgcn_update_dpp(0, x, CTRL, 0xf, 0xf, true); }
template <int CTRL> __device__ __forceinline__ int dppi_keep(int old, int x) { return __builtin_amdgcn_update_dpp(old, x, CTRL, 0xf, 0xf, false); }

__device__ __forceinline__ float xg_max(float x) {
  auto s = __builtin_amdgcn_permlane16_swap(__float_as_uint(x), __float_as_uint(x), false, false);
  x = fmaxf(__uint_as_float(s[0]), __uint_as_float(s[1]));
  auto t = __builtin_amdgcn_permlane32_swap(__float_as_uint(x), __float_as_uint(x), false, false);
  return fmaxf(__uint_as_float(t[0]), __uint_as_float(t[1]));
}
__device__ __forceinline__ float xg_sum(float x) {
  auto s = __builtin_amdgcn_permlane16_swap(__float_as_uint(x), __float_as_uint(x), false, false);
  x = __uint_as_float(s[0]) + __uint_as_float(s[1]);
  auto t = __builtin_amdgcn_permlane32_swap(__float_as_uint(x), __float_as_uint(x), false, false);
  return __uint_as_float(t[0]) + __uint_as_float(t[1]);
}
__device__ __forceinline__ unsigned pk_bf16(float a, float b) {
  bf16x2 v = {(__bf16)a, (__bf16)b};
  return __builtin_bit_cast(unsigned, v);
}

struct One { static constexpr int value = 1; };
struct Two { static constexpr int value = 2; };

struct Params {
  const char* q; const char* k; const char* v; const int* bt; const int* lens; __hip_bfloat16* out; char* ws;
};

__global__ void __launch_bounds__(256, 2) radix_attn_kernel(Params p) {
  extern __shared__ __attribute__((aligned(16))) char smem[];
  LAS char* lds = (LAS char*)smem;
  const int tid = threadIdx.x, lane = tid & 63, w = rfl(tid >> 6);
  const int gidx = blockIdx.x;
  const int hg = (gidx >> 3) & 3, j = (gidx & 7) | ((gidx >> 5) << 3);
  const int h = hg * 4 + w;
  const int c16 = lane & 15, g4 = lane >> 4;
#define STAMP(i) do { } while (0)
  STAMP(0);
#define WSOFF 0
  const auto wsr = __builtin_amdgcn_make_buffer_rsrc(p.ws + (size_t)WSOFF, 0, WS_END, 0x00020000);

  const int lenraw = p.lens[c16];
  const unsigned gv = __builtin_amdgcn_raw_buffer_load_b32(wsr, WS_G + (h * 16 + c16) * 4, 0, 16);
  int pm;
  {
    const i32x4* bt4 = (const i32x4*)p.bt;
    const i32x4 r0 = bt4[lane];
    i32x4 rr[4];
#pragma unroll
    for (int i = 0; i < 4; ++i) rr[i] = bt4[(4 * w + i) * 64 + lane];
    bool n0 = false, n1 = false, n2 = false, n3 = false;
#pragma unroll
    for (int i = 0; i < 4; ++i) {
      n0 |= rr[i][0] != r0[0]; n1 |= rr[i][1] != r0[1]; n2 |= rr[i][2] != r0[2]; n3 |= rr[i][3] != r0[3];
      *(LAS i32x4*)(lds + LDS_BT + ((4 * w + i) * 256 + 4 * lane) * 4) = rr[i];
    }
    const int fc = n0 ? 0 : n1 ? 1 : n2 ? 2 : n3 ? 3 : 4;
    const unsigned long long mm = __ballot(fc < 4);
    int col = NBLK;
    if (mm) { const int fl = __builtin_ctzll(mm); col = 4 * fl + rl(fc, fl); }
    if (lane == 0) *(LAS int*)(lds + LDS_MISC + 4 * w) = col;
    asm volatile("s_waitcnt lgkmcnt(0)\n\ts_barrier" ::: "memory");
    const i32x4 mc = *(LAS i32x4*)(lds + LDS_MISC);
    pm = rfl(min(min(mc[0], mc[1]), min(mc[2], mc[3])));
  }
  asm volatile("" :: "v"(gv));
  const bool zq_l = lenraw <= 0;
  const int len_l = zq_l ? LK : min(lenraw, LK);
  int mn = len_l;
  mn = min(mn, dppi_keep<0x128>(mn, mn)); mn = min(mn, dppi_keep<0x124>(mn, mn));
  mn = min(mn, dppi_keep<0x122>(mn, mn)); mn = min(mn, dppi_keep<0x121>(mn, mn));
  const int P = min(pm, rfl(mn) >> 4);
  const int tsh = P * BS;
  const int vL = len_l - tsh;
  int inc = vL;
  inc += dppi<0x111>(inc); inc += dppi<0x112>(inc); inc += dppi<0x114>(inc); inc += dppi<0x118>(inc);
  const int lsum = rl(inc, 15);
  const int FX = min(FCOST, (tsh + lsum) >> 7);
  int n_sh = 0;
  if (tsh > 0) {
    const int n = lane + 1;
    n_sh = __builtin_ctzll(__ballot(n * (tsh + lsum + n * FX - 64 * FX) >= 64 * tsh)) + 1;
  }
  const int wsh = tsh + n_sh * FX;
  const int vS = wsh + inc - vL;
  const int N = wsh + lsum;
  const int x0 = (j * N) >> 6, x1 = ((j + 1) * N) >> 6;
  const unsigned zmask = (unsigned)(__ballot(zq_l) & 0xffffull);
  const unsigned umask = (unsigned)(__ballot(max(x0, vS) < min(x1, vS + vL)) & 0xffffull);
  const int sh_t0 = min(tsh, x0 - j * FX), sh_t1 = j < n_sh ? min(tsh, x1 - (j + 1) * FX) : 0;
  const bool has_sh = j < n_sh && sh_t1 > sh_t0;

  unsigned voffK[4], voffV[4];
#pragma unroll
  for (int i = 0; i < 4; ++i) {
    const int t = 4 * w + i;
    voffK[i] = g4 * 256 + ((c16 ^ t) << 4) + t * 4096 - i * 1024 + 4096;
    voffV[i] = g4 * 256 + ((c16 ^ (2 * (t & 7))) << 4) + t * 4096 - i * 1024 + 4096;
  }
  const unsigned kaddr0 = w * 256 + c16 * 1024 + ((g4 ^ c16) << 4);
  unsigned vaddr0;
  { const int tq = 4 * g4 + (c16 >> 2), pp = lane & 3; vaddr0 = 16384 + tq * 1024 + w * 256 + ((tq & 7) << 5) + ((pp >> 1) << 4) + ((pp & 1) << 3); }
  const char* kbase = p.k + hg * 1024 - 4096;
  const char* vbase = p.v + hg * 1024 - 4096;

  unsigned i_mask = umask; int i_row, i_t0, i_t1, i_page; bool i_more = true;
  int n_iss = 0, i_slot = 0;
  auto unit_range = [&](int b, int& t0, int& t1) {
    const int s = rl(vS, b), L = rl(vL, b);
    t0 = tsh + max(x0, s) - s; t1 = tsh + min(x1, s + L) - s;
  };
  auto q_dma_private = [&](int b, int slot) {
    if (lane < 32) dma16_keep(p.q + b * 8192 + h * 512, lane * 16, LDS_QB + (w * R + slot) * 512);
  };
  if (has_sh) {
    i_row = 0; i_t0 = sh_t0; i_t1 = sh_t1;
#pragma unroll
    for (int i = 0; i < 8; ++i)
      dma16_keep(p.q + i * 16384 + h * 512, (lane >> 5) * 8192 + (lane & 31) * 16, (R - 1) * STAGE + w * 8192 + i * 1024);
  } else if (i_mask) {
    i_row = __builtin_ctz(i_mask); i_mask &= i_mask - 1; unit_range(i_row, i_t0, i_t1);
    q_dma_private(i_row, 0);
  } else { i_more = false; i_row = 0; i_t0 = 0; i_t1 = 0; }
  i_page = i_t0 >> 4;
  int i_pg0 = i_page, vpg = *(LAS int*)(lds + LDS_BT + (i_row * NBLK + min(i_page + lane, NBLK - 1)) * 4);
  const unsigned long long t_ref = __builtin_amdgcn_s_memrealtime();
  int pace_tok = has_sh ? -100000 : 0;
  auto issue_stage = [&](bool steady) {
    const int phys = rl(vpg, i_page - i_pg0);
    const int lo = i_t0 - i_page * 16, hi = i_t1 - i_page * 16;
    {
      const int due = ((pace_tok - PACE_LEAD) * (int)(2048.0 * 256.0 * 1e8 * 64.0 / (PACE * 1e9))) >> 6;
      pace_tok += min(hi, 16) - max(lo, 0);
      while ((int)(__builtin_amdgcn_s_memrealtime() - t_ref) < due) __builtin_amdgcn_s_sleep(1);
    }
    const size_t pb = (size_t)(unsigned)phys * 65536;
    const unsigned dst = i_slot * STAGE + 4 * w * 1024;
    if (lo <= 0 && hi >= 16) {
      dma_stage(kbase + pb, vbase + pb, dst, dst + 16384, voffK[0], voffK[1], voffK[2], voffK[3], voffV[0], voffV[1], voffV[2], voffV[3]);
    } else {
      unsigned ok[4], ov[4];
#pragma unroll
      for (int i = 0; i < 4; ++i) {
        const int d = (min(max(4 * w + i, lo), hi - 1) - (4 * w + i)) * 4096;
        ok[i] = voffK[i] + d; ov[i] = voffV[i] + d;
      }
      dma_stage(kbase + pb, vbase + pb, dst, dst + 16384, ok[0], ok[1], ok[2], ok[3], ov[0], ov[1], ov[2], ov[3]);
    }
    ++n_iss; i_slot = (i_slot + 1 == R) ? 0 : i_slot + 1;
    ++i_page;
    if (i_page * 16 >= i_t1) {
      if (i_mask) {
        i_row = __builtin_ctz(i_mask); i_mask &= i_mask - 1; unit_range(i_row, i_t0, i_t1); i_page = i_t0 >> 4;
        q_dma_private(i_row, i_slot);
        i_pg0 = i_page; vpg = *(LAS int*)(lds + LDS_BT + (i_row * NBLK + min(i_page + lane, NBLK - 1)) * 4);
      } else i_more = false;
    } else if (i_page - i_pg0 == 64) {
      i_pg0 = i_page; vpg = *(LAS int*)(lds + LDS_BT + (i_row * NBLK + min(i_page + lane, NBLK - 1)) * 4);
    }
  };
#pragma unroll 1
  for (int s = 0; s < R - 1; ++s) if (i_more) issue_stage(false);

  const int r_lo = (lane * N) >> 6, r_hi = ((lane + 1) * N) >> 6;
  unsigned own = 0;
  {
    unsigned mrem = umask;
    while (mrem) {
      const int b = __builtin_ctz(mrem); mrem &= mrem - 1;
      const int s = rl(vS, b), L = rl(vL, b);
      unsigned long long pmk = __ballot(max(r_lo, s) < min(r_hi, s + L));
      const int cnt = __builtin_popcountll(pmk);
      int idx = w % cnt;
      while (idx--) pmk &= pmk - 1;
      if (__builtin_ctzll(pmk) == j) own |= 1u << b;
    }
    const unsigned zl = (unsigned)(__ballot(vL == 0) & 0xffffull);
    if ((j & 3) == 0 && ((zl >> (j >> 2)) & 1)) own |= 1u << (j >> 2);
  }
  const unsigned long long smask = __ballot(lane < n_sh && min(tsh, r_hi - (lane + 1) * FX) > min(tsh, r_lo - lane * FX));

  int n_comp = 0, c_slot = 0;
  unsigned c_mask = umask;
  float ro[2][2] = {{0.f, 0.f}, {0.f, 0.f}}, rm[2] = {-INFINITY, -INFINITY}, rlsum[2] = {0.f, 0.f};
  int last_b = -1;
  float ho[2][2] = {{0.f, 0.f}, {0.f, 0.f}}, hm[2] = {-INFINITY, -INFINITY}, hl[2] = {0.f, 0.f};
  int held_b = -1;
  const int pi_l = 2 * lane;
  int flag_wait = 0;
  auto store_flags = [&]() {
    if (lane < 16) {
      typedef unsigned u32x2 __attribute__((ext_vector_type(2)));
      const u32x2 f = {gv + 1u, gv + 1u};
      __builtin_amdgcn_raw_buffer_store_b64(f, wsr, WS_SP + ((hg * 64 + j) * 4 + w) * SP_W + 16640 + lane * 8, 0, 16);
    }
  };

  auto wait_stage = [&]() {
    const int young = n_iss - n_comp - 1;
    if (young >= R - 2) {
      if (R == 2) asm volatile("s_waitcnt vmcnt(0) lgkmcnt(0)\n\ts_barrier" ::: "memory");
      else if (R == 3) asm volatile("s_waitcnt vmcnt(8) lgkmcnt(0)\n\ts_barrier" ::: "memory");
      else asm volatile("s_waitcnt vmcnt(16) lgkmcnt(0)\n\ts_barrier" ::: "memory");
    } else if (R == 4 && young == 1) asm volatile("s_waitcnt vmcnt(8) lgkmcnt(0)\n\ts_barrier" ::: "memory");
    else asm volatile("s_waitcnt vmcnt(0) lgkmcnt(0)\n\ts_barrier" ::: "memory");
  };

  auto publish = [&](int b, const float (&o)[2][2], const float (&mm)[2], const float (&ll)[2]) {
    const unsigned tag = (unsigned)rl((int)gv, b) + 1u;
    const unsigned slot = WS_PP + ((hg * 80 + j + b + 1) * 4 + w) * PP_W;
    const u32x4 g0 = {__float_as_uint(o[0][0]), tag, __float_as_uint(o[0][1]), tag};
    const u32x4 g1 = {__float_as_uint(o[1][0]), tag, __float_as_uint(o[1][1]), tag};
    __builtin_amdgcn_raw_buffer_store_b128(g0, wsr, slot + lane * 16, 0, 16);
    __builtin_amdgcn_raw_buffer_store_b128(g1, wsr, slot + 1024 + lane * 16, 0, 16);
    if (lane < 2) {
      const u32x4 gm = {__float_as_uint(lane ? mm[1] : mm[0]), tag, __float_as_uint(lane ? ll[1] : ll[0]), tag};
      __builtin_amdgcn_raw_buffer_store_b128(gm, wsr, slot + 2048 + lane * 16, 0, 16);
    }
  };
  auto run_piece = [&](auto nq_tag, int t0, int t1, int b) {
    constexpr int NQ = decltype(nq_tag)::value;
    bf16x8 qf[NQ][4];
    f32x4 acc[NQ][8];
    float m[NQ], l[NQ];
#pragma unroll
    for (int qb = 0; qb < NQ; ++qb) {
      m[qb] = -INFINITY; l[qb] = 0.f;
#pragma unroll
      for (int k = 0; k < 8; ++k) acc[qb][k] = f32x4{0.f, 0.f, 0.f, 0.f};
    }
    if (NQ == 2) {
      if (n_iss >= 2) { if (R == 2) asm volatile("s_waitcnt vmcnt(8)" ::: "memory"); else if (R == 3) asm volatile("s_waitcnt vmcnt(16)" ::: "memory"); else asm volatile("s_waitcnt vmcnt(16)" ::: "memory"); }
      else asm volatile("s_waitcnt vmcnt(8)" ::: "memory");
#pragma unroll
      for (int qb = 0; qb < NQ; ++qb) {
        const bool z = (zmask >> (8 * qb + (c16 >> 1))) & 1;
#pragma unroll
        for (int s = 0; s < 4; ++s) {
          u32x4 x = *(LAS u32x4*)(lds + (R - 1) * STAGE + w * 8192 + (16 * qb + c16) * 256 + (4 * s + g4) * 16);
          if (z) x = u32x4{0u, 0u, 0u, 0u};
          qf[qb][s] = __builtin_bit_cast(bf16x8, x);
        }
      }
    }
    bool first = true;
    for (int page = t0 >> 4; page * 16 < t1; ++page) {
      wait_stage();
      if (NQ == 1 && flag_wait > 0 && --flag_wait == 0) store_flags();
      if (i_more) issue_stage(true);
      if (NQ == 1 && first) {
        const bool z = (c16 >= 2) || ((zmask >> b) & 1);
#pragma unroll
        for (int s = 0; s < 4; ++s) {
          u32x4 x = *(LAS u32x4*)(lds + LDS_QB + (w * R + c_slot) * 512 + (c16 & 1) * 256 + (4 * s + g4) * 16);
          if (z) x = u32x4{0u, 0u, 0u, 0u};
          qf[0][s] = __builtin_bit_cast(bf16x8, x);
        }
      }
      first = false;
      const unsigned sb = c_slot * STAGE;
      bf16x8 kf[4];
#pragma unroll
      for (int s = 0; s < 4; ++s) kf[s] = *(LAS bf16x8*)(lds + sb + (kaddr0 ^ (s << 6)));
      s16x4 vf[8];
#pragma unroll
      for (int k = 0; k < 8; ++k) vf[k] = __builtin_amdgcn_ds_read_tr16_b64_v4i16((LAS s16x4*)(lds + sb + (vaddr0 ^ (k << 5))));
      f32x4 S[NQ];
#pragma unroll
      for (int qb = 0; qb < NQ; ++qb) {
        S[qb] = f32x4{0.f, 0.f, 0.f, 0.f};
#pragma unroll
        for (int s = 0; s < 4; ++s) S[qb] = __builtin_amdgcn_mfma_f32_16x16x32_bf16(kf[s], qf[qb][s], S[qb], 0, 0, 0);
      }
      const int lo = t0 - page * 16, hi = t1 - page * 16;
      if (lo > 0 || hi < 16) {
#pragma unroll
        for (int qb = 0; qb < NQ; ++qb)
#pragma unroll
          for (int r = 0; r < 4; ++r) { const int t = 4 * g4 + r; S[qb][r] = (t >= lo && t < hi) ? S[qb][r] : -INFINITY; }
      }
      s16x4 pb[NQ];
      bool grow = false;
      float alpha[NQ];
#pragma unroll
      for (int qb = 0; qb < NQ; ++qb) {
        float mx = fmaxf(fmaxf(S[qb][0], S[qb][1]), fmaxf(S[qb][2], S[qb][3]));
        mx = xg_max(mx);
        const float mn2 = fmaxf(m[qb], mx);
        grow |= mn2 > m[qb];
        alpha[qb] = __builtin_amdgcn_exp2f((m[qb] - mn2) * C2);
        m[qb] = mn2;
        const float mc = -mn2 * C2;
        const float p0 = __builtin_amdgcn_exp2f(fmaf(S[qb][0], C2, mc)), p1 = __builtin_amdgcn_exp2f(fmaf(S[qb][1], C2, mc));
        const float p2 = __builtin_amdgcn_exp2f(fmaf(S[qb][2], C2, mc)), p3 = __builtin_amdgcn_exp2f(fmaf(S[qb][3], C2, mc));
        l[qb] = fmaf(l[qb], alpha[qb], (p0 + p1) + (p2 + p3));
        typedef unsigned u32x2 __attribute__((ext_vector_type(2)));
        pb[qb] = __builtin_bit_cast(s16x4, u32x2{pk_bf16(p0, p1), pk_bf16(p2, p3)});
      }
      if (__any(grow)) {
#pragma unroll
        for (int qb = 0; qb < NQ; ++qb)
#pragma unroll
          for (int k = 0; k < 8; ++k) acc[qb][k] *= alpha[qb];
      }
#pragma unroll
      for (int qb = 0; qb < NQ; ++qb)
#pragma unroll
        for (int k = 0; k < 8; ++k) acc[qb][k] = __builtin_amdgcn_mfma_f32_16x16x16bf16_1k(vf[k], pb[qb], acc[qb][k], 0, 0, 0);
      ++n_comp; c_slot = (c_slot + 1 == R) ? 0 : c_slot + 1;
    }
    if constexpr (NQ == 2) {
      STAMP(6);
      const unsigned slot = WS_SP + ((hg * 64 + j) * 4 + w) * SP_W;
#pragma unroll
      for (int qb = 0; qb < 2; ++qb) {
        const float lt = xg_sum(l[qb]);
        const unsigned rowo = slot + (16 * qb + c16) * 512 + g4 * 16;
#pragma unroll
        for (int k = 0; k < 8; ++k)
          __builtin_amdgcn_raw_buffer_store_b128(__builtin_bit_cast(u32x4, acc[qb][k]), wsr, rowo + k * 64, 0, SHPUB ? 0 : 16);
        if (g4 == 0) {
          typedef unsigned u32x2 __attribute__((ext_vector_type(2)));
          const u32x2 a = {__float_as_uint(m[qb] * C2), __float_as_uint(lt)};
          __builtin_amdgcn_raw_buffer_store_b64(a, wsr, slot + 16384 + (16 * qb + c16) * 8, 0, SHPUB ? 0 : 16);
        }
      }
      if (SHPUB == 1 && c_mask == 0) {
        asm volatile("s_waitcnt vmcnt(0)\n\ts_barrier" ::: "memory");
        if (w == 0) { __builtin_amdgcn_fence(__ATOMIC_RELEASE, "agent"); asm volatile("s_waitcnt vmcnt(0)" ::: "memory"); }
        asm volatile("s_barrier" ::: "memory");
        store_flags();
      } else if (SHPUB == 3 && c_mask == 0) {
        asm volatile("s_waitcnt vmcnt(0)" ::: "memory"); store_flags();
      } else
      if (c_mask == 0) { asm volatile("s_waitcnt vmcnt(0)" ::: "memory"); store_flags(); }
      else flag_wait = 3;
      STAMP(7);
    } else {
      const float lt = xg_sum(l[0]);
      LAS char* sc = lds + LDS_SC + w * 1024;
      if (c16 < 2) {
#pragma unroll
        for (int k = 0; k < 8; ++k) *(LAS f32x4*)(sc + c16 * 512 + (16 * k + 4 * g4) * 4) = acc[0][k];
      }
      asm volatile("s_waitcnt lgkmcnt(0)" ::: "memory");
      const f32x2 a0 = *(LAS f32x2*)(sc + pi_l * 4), a1 = *(LAS f32x2*)(sc + 512 + pi_l * 4);
      if (last_b >= 0) {
        if (held_b >= 0) publish(held_b, ho, hm, hl);
        held_b = last_b;
#pragma unroll
        for (int r = 0; r < 2; ++r) { ho[r][0] = ro[r][0]; ho[r][1] = ro[r][1]; hm[r] = rm[r]; hl[r] = rlsum[r]; }
      }
      ro[0][0] = a0[0]; ro[0][1] = a0[1]; ro[1][0] = a1[0]; ro[1][1] = a1[1];
      rm[0] = rlf(m[0], 0) * C2; rm[1] = rlf(m[0], 1) * C2;
      rlsum[0] = rlf(lt, 0); rlsum[1] = rlf(lt, 1);
      last_b = b;
    }
  };
  if (has_sh) run_piece(Two{}, sh_t0, sh_t1, 0);
  while (c_mask) {
    const int b = __builtin_ctz(c_mask); c_mask &= c_mask - 1;
    int t0, t1; unit_range(b, t0, t1);
    run_piece(One{}, t0, t1, b);
  }
  STAMP(2);
  if (last_b >= 0 && !((own >> last_b) & 1)) { publish(last_b, ro, rm, rlsum); last_b = -1; }
  if (held_b >= 0 && !((own >> held_b) & 1)) { publish(held_b, ho, hm, hl); held_b = -1; }
  if (flag_wait > 0) { asm volatile("s_waitcnt vmcnt(0)" ::: "memory"); store_flags(); }
  STAMP(1);

  while (own) {
    const int b = __builtin_ctz(own); own &= own - 1;
    const unsigned tag = (unsigned)rl((int)gv, b) + 1u;
    const int s = rl(vS, b), L = rl(vL, b);
    unsigned long long pmk = __ballot(max(r_lo, s) < min(r_hi, s + L));
    unsigned long long smk = smask;
    float M0 = -INFINITY, M1 = -INFINITY, L0 = 0.f, L1 = 0.f, o00 = 0.f, o01 = 0.f, o10 = 0.f, o11 = 0.f;
    if (b == last_b) {
      pmk &= ~(1ull << j);
      M0 = rm[0]; M1 = rm[1]; L0 = rlsum[0]; L1 = rlsum[1]; o00 = ro[0][0]; o01 = ro[0][1]; o10 = ro[1][0]; o11 = ro[1][1];
    } else if (b == held_b) {
      pmk &= ~(1ull << j);
      M0 = hm[0]; M1 = hm[1]; L0 = hl[0]; L1 = hl[1]; o00 = ho[0][0]; o01 = ho[0][1]; o10 = ho[1][0]; o11 = ho[1][1];
    }
    auto merge = [&](float m0, float l0, float m1, float l1, float x0, float x1, float y0, float y1) {
      const float n0 = fmaxf(M0, m0), n1 = fmaxf(M1, m1);
      const float a0 = __builtin_amdgcn_exp2f(M0 - n0), b0 = __builtin_amdgcn_exp2f(m0 - n0);
      const float a1 = __builtin_amdgcn_exp2f(M1 - n1), b1 = __builtin_amdgcn_exp2f(m1 - n1);
      L0 = L0 * a0 + l0 * b0; L1 = L1 * a1 + l1 * b1;
      o00 = o00 * a0 + x0 * b0; o01 = o01 * a0 + x1 * b0;
      o10 = o10 * a1 + y0 * b1; o11 = o11 * a1 + y1 * b1;
      M0 = n0; M1 = n1;
    };
    typedef unsigned u32x2 __attribute__((ext_vector_type(2)));
    unsigned spins = 0;
    while (pmk | smk) {
      unsigned long long rdy = 0; bool sh_ready = false;
      const bool single = (pmk & (pmk - 1)) == 0 && smk == 0;
      if (!single || SPEC == 0) {
        u32x4 pml = {0u, 0u, 0u, 0u}; u32x2 fl = {tag, tag};
        if (pmk) {
          const int jj = ((pmk >> lane) & 1) ? lane : __builtin_ctzll(pmk);
          pml = __builtin_amdgcn_raw_buffer_load_b128(wsr, WS_PP + ((hg * 80 + jj + b + 1) * 4 + w) * PP_W + 2048 + 16, 0, 16);
        }
        if (smk) {
          const int jj = ((smk >> lane) & 1) ? lane : __builtin_ctzll(smk);
          fl = __builtin_amdgcn_raw_buffer_load_b64(wsr, WS_SP + ((hg * 64 + jj) * 4 + w) * SP_W + 16640 + b * 8, 0, 16);
        }
        rdy = pmk & __ballot(pml[1] == tag && pml[3] == tag);
        sh_ready = smk && __all(fl[0] == tag && fl[1] == tag);
      } else rdy = pmk;
      bool progress = false;
      if (rdy) {
        constexpr int NB = 4;
        unsigned rows[NB]; unsigned long long bitp[NB];
        unsigned long long tp = rdy;
#pragma unroll
        for (int k = 0; k < NB; ++k) {
          if (tp) { const int jj = __builtin_ctzll(tp); bitp[k] = tp & (~tp + 1); tp &= tp - 1; rows[k] = WS_PP + ((hg * 80 + jj + b + 1) * 4 + w) * PP_W; }
          else { bitp[k] = 0; rows[k] = rows[0]; }
        }
        u32x4 d0[NB], d1[NB], dm[NB];
        if (single) {
          d0[0] = __builtin_amdgcn_raw_buffer_load_b128(wsr, lane * 16, rows[0], 16);
          d1[0] = __builtin_amdgcn_raw_buffer_load_b128(wsr, 1024 + lane * 16, rows[0], 16);
          dm[0] = __builtin_amdgcn_raw_buffer_load_b128(wsr, 2048 + (lane & 1) * 16, rows[0], 16);
#pragma unroll
          for (int k = 1; k < NB; ++k) { d0[k] = d0[0]; d1[k] = d1[0]; dm[k] = dm[0]; }
        } else {
#pragma unroll
          for (int k = 0; k < NB; ++k) {
            d0[k] = __builtin_amdgcn_raw_buffer_load_b128(wsr, lane * 16, rows[k], 16);
            d1[k] = __builtin_amdgcn_raw_buffer_load_b128(wsr, 1024 + lane * 16, rows[k], 16);
            dm[k] = __builtin_amdgcn_raw_buffer_load_b128(wsr, 2048 + (lane & 1) * 16, rows[k], 16);
          }
        }
#pragma unroll
        for (int k = 0; k < NB; ++k) {
          const bool ok = d0[k][1] == tag && d0[k][3] == tag && d1[k][1] == tag && d1[k][3] == tag && dm[k][1] == tag && dm[k][3] == tag;
          if (bitp[k] && __all(ok)) {
            const float mv = __uint_as_float(dm[k][0]), lv = __uint_as_float(dm[k][2]);
            merge(rlf(mv, 0), rlf(lv, 0), rlf(mv, 1), rlf(lv, 1), __uint_as_float(d0[k][0]), __uint_as_float(d0[k][2]), __uint_as_float(d1[k][0]), __uint_as_float(d1[k][2]));
            pmk &= ~bitp[k]; progress = true;
          }
        }
      }
      if (sh_ready) {
        constexpr int NB = 6;
        unsigned rows[NB]; bool val[NB];
        unsigned long long ts = smk;
#pragma unroll
        for (int k = 0; k < NB; ++k) {
          if (ts) { const int jj = __builtin_ctzll(ts); ts &= ts - 1; rows[k] = WS_SP + ((hg * 64 + jj) * 4 + w) * SP_W; val[k] = true; }
          else { rows[k] = rows[0]; val[k] = false; }
        }
        u32x2 d0[NB], d1[NB]; u32x4 dm[NB];
#pragma unroll
        for (int k = 0; k < NB; ++k) {
          d0[k] = __builtin_amdgcn_raw_buffer_load_b64(wsr, b * 1024 + lane * 8, rows[k], 16);
          d1[k] = __builtin_amdgcn_raw_buffer_load_b64(wsr, b * 1024 + 512 + lane * 8, rows[k], 16);
          dm[k] = __builtin_amdgcn_raw_buffer_load_b128(wsr, 16384 + b * 16, rows[k], 16);
        }
#pragma unroll
        for (int k = 0; k < NB; ++k)
          if (val[k]) merge(__uint_as_float(dm[k][0]), __uint_as_float(dm[k][1]), __uint_as_float(dm[k][2]), __uint_as_float(dm[k][3]),
                            __uint_as_float(d0[k][0]), __uint_as_float(d0[k][1]), __uint_as_float(d1[k][0]), __uint_as_float(d1[k][1]));
        smk = ts;
        progress = true;
      }
      if (!progress) {
        if (++spins > (1u << 20)) break;
        __builtin_amdgcn_s_sleep(SLEEP);
      }
    }
    const float i0 = 1.f / L0, i1 = 1.f / L1;
    __hip_bfloat16* orow = p.out + (b * NH + 2 * h) * DH + pi_l;
    *(unsigned*)orow = pk_bf16(o00 * i0, o01 * i0);
    *(unsigned*)(orow + DH) = pk_bf16(o10 * i1, o11 * i1);
    if (lane == 0) __builtin_amdgcn_raw_buffer_store_b32(tag, wsr, WS_G + (h * 16 + b) * 4, 0, 16);
  }
  STAMP(3);
}

}

extern "C" void kernel_launch(void* const* d_in, const int* in_sizes, int n_in, void* d_out, int out_size, void* d_ws, size_t ws_size,
                              hipStream_t stream) {
  using namespace rattn;
  Params p;
  p.q = (const char*)d_in[0]; p.k = (const char*)d_in[1]; p.v = (const char*)d_in[2];
  p.bt = (const int*)d_in[3]; p.lens = (const int*)d_in[4];
  p.out = (__hip_bfloat16*)d_out; p.ws = (char*)d_ws;
  (void)hipFuncSetAttribute((const void*)radix_attn_kernel, hipFuncAttributeMaxDynamicSharedMemorySize, LDS_TOTAL);
  hipLaunchKernelGGL(radix_attn_kernel, dim3(256), dim3(256), LDS_TOTAL, stream, p);
}
```

```cpp
#include <hip/hip_runtime.h>
#include <hip/hip_bf16.h>
#include <stdint.h>

namespace rattn {

constexpr int NSEQ = 16, NH = 32, NKV = 16, DH = 128, NBLK = 256, BS = 16;
constexpr int LK = NBLK * BS;
#define MAPMODE 1
#define KPOL "sc1 nt"
#define VPOL "sc1 nt"
#define RING 3
#define FCOST 56
#define SPEC 1
#define SLEEP 12
#define PACE_LEAD 32
#define SHPUB 0
#define APACE_DELTA 117
constexpr int R = RING;
constexpr int STAGE = 32768;
constexpr int LDS_BT = R * STAGE;
constexpr int LDS_QB = LDS_BT + NSEQ * NBLK * 4;
constexpr int LDS_SC = LDS_QB + 4 * R * 512;
constexpr int LDS_MISC = LDS_SC + 4096;
constexpr int LDS_PROG = LDS_MISC + 64;
constexpr int LDS_TOTAL = LDS_PROG + 512;
static_assert(LDS_TOTAL <= 160 * 1024, "LDS");

constexpr unsigned WS_G = 0;
constexpr unsigned WS_PROG = 2048;
constexpr unsigned WS_PP = 4096;
constexpr unsigned PP_W = 2176;
constexpr unsigned WS_SP = 4u << 20;
constexpr unsigned SP_W = 16384 + 256 + 128;
constexpr unsigned WS_END = WS_SP + 4u * 64u * 4u * SP_W;
constexpr unsigned WS_STAMP = 64u << 20;

typedef unsigned u32x4 __attribute__((ext_vector_type(4)));
typedef float f32x4 __attribute__((ext_vector_type(4)));
typedef float f32x2 __attribute__((ext_vector_type(2)));
typedef __bf16 bf16x8 __attribute__((ext_vector_type(8)));
typedef __bf16 bf16x2 __attribute__((ext_vector_type(2)));
typedef short s16x4 __attribute__((ext_vector_type(4)));
typedef int i32x4 __attribute__((ext_vector_type(4)));
#define LAS __attribute__((address_space(3)))
#define GAS __attribute__((address_space(1)))

constexpr float C2 = 0.088388347648318440f * 1.4426950408889634f;

__device__ __forceinline__ int rfl(int x) { return __builtin_amdgcn_readfirstlane(x); }
__device__ __forceinline__ int rl(int x, int l) { return __builtin_amdgcn_readlane(x, l); }
__device__ __forceinline__ float rlf(float x, int l) { return __builtin_bit_cast(float, __builtin_amdgcn_readlane(__builtin_bit_cast(int, x), l)); }

__device__ __forceinline__ void dma16(const void* sbase, unsigned voff, unsigned dst) {
  unsigned keep;
  asm volatile("s_mov_b32 %0, m0\n\ts_mov_b32 m0, %3\n\ts_nop 0\n\tglobal_load_lds_dwordx4 %1, %2 nt\n\ts_mov_b32 m0, %0"
               : "=&s"(keep) : "v"(voff), "s"(sbase), "s"(dst) : "memory");
}
__device__ __forceinline__ void dma16_keep(const void* sbase, unsigned voff, unsigned dst) {
  unsigned keep;
  asm volatile("s_mov_b32 %0, m0\n\ts_mov_b32 m0, %3\n\ts_nop 0\n\tglobal_load_lds_dwordx4 %1, %2\n\ts_mov_b32 m0, %0"
               : "=&s"(keep) : "v"(voff), "s"(sbase), "s"(dst) : "memory");
}
__device__ __forceinline__ void dma_stage(const void* sK, const void* sV, unsigned dstK, unsigned dstV,
                                          unsigned k0, unsigned k1, unsigned k2, unsigned k3,
                                          unsigned v0, unsigned v1, unsigned v2, unsigned v3) {
  unsigned keep;
  asm volatile("s_mov_b32 %0, m0\n\ts_mov_b32 m0, %3\n\ts_nop 0\n\t"
               "global_load_lds_dwordx4 %5, %1 " KPOL "\n\t"
               "global_load_lds_dwordx4 %6, %1 offset:1024 " KPOL "\n\t"
               "global_load_lds_dwordx4 %7, %1 offset:2048 " KPOL "\n\t"
               "global_load_lds_dwordx4 %8, %1 offset:3072 " KPOL "\n\t"
               "s_mov_b32 m0, %4\n\ts_nop 0\n\t"
               "global_load_lds_dwordx4 %9, %2 " VPOL "\n\t"
               "global_load_lds_dwordx4 %10, %2 offset:1024 " VPOL "\n\t"
               "global_load_lds_dwordx4 %11, %2 offset:2048 " VPOL "\n\t"
               "global_load_lds_dwordx4 %12, %2 offset:3072 " VPOL "\n\t"
               "s_mov_b32 m0, %0"
               : "=&s"(keep)
               : "s"(sK), "s"(sV), "s"(dstK), "s"(dstV), "v"(k0), "v"(k1), "v"(k2), "v"(k3), "v"(v0), "v"(v1), "v"(v2), "v"(v3)
               : "memory");
}
__device__ __forceinline__ void dma16_sc1(const void* sbase, unsigned voff, unsigned dst) {
  unsigned keep;
  asm volatile("s_mov_b32 %0, m0\n\ts_mov_b32 m0, %3\n\ts_nop 0\n\tglobal_load_lds_dwordx4 %1, %2 sc1\n\ts_mov_b32 m0, %0"
               : "=&s"(keep) : "v"(voff), "s"(sbase), "s"(dst) : "memory");
}
template <int CTRL> __device__ __forceinline__ int dppi(int x) { return __builtin_amdgcn_update_dpp(0, x, CTRL, 0xf, 0xf, true); }
template <int CTRL> __device__ __forceinline__ int dppi_keep(int old, int x) { return __builtin_amdgcn_update_dpp(old, x, CTRL, 0xf, 0xf, false); }

__device__ __forceinline__ float xg_max(float x) {
  auto s = __builtin_amdgcn_permlane16_swap(__float_as_uint(x), __float_as_uint(x), false, false);
  x = fmaxf(__uint_as_float(s[0]), __uint_as_float(s[1]));
  auto t = __builtin_amdgcn_permlane32_swap(__float_as_uint(x), __float_as_uint(x), false, false);
  return fmaxf(__uint_as_float(t[0]), __uint_as_float(t[1]));
}
__device__ __forceinline__ float xg_sum(float x) {
  auto s = __builtin_amdgcn_permlane16_swap(__float_as_uint(x), __float_as_uint(x), false, false);
  x = __uint_as_float(s[0]) + __uint_as_float(s[1]);
  auto t = __builtin_amdgcn_permlane32_swap(__float_as_uint(x), __float_as_uint(x), false, false);
  return __uint_as_float(t[0]) + __uint_as_float(t[1]);
}
__device__ __forceinline__ unsigned pk_bf16(float a, float b) {
  bf16x2 v = {(__bf16)a, (__bf16)b};
  return __builtin_bit_cast(unsigned, v);
}

struct One { static constexpr int value = 1; };
struct Two { static constexpr int value = 2; };

struct Params {
  const char* q; const char* k; const char* v; const int* bt; const int* lens; __hip_bfloat16* out; char* ws;
};

__global__ void __launch_bounds__(256, 2) radix_attn_kernel(Params p) {
  extern __shared__ __attribute__((aligned(16))) char smem[];
  LAS char* lds = (LAS char*)smem;
  const int tid = threadIdx.x, lane = tid & 63, w = rfl(tid >> 6);
  const int gidx = blockIdx.x;
  const int hg = (gidx >> 3) & 3, j = (gidx & 7) | ((gidx >> 5) << 3);
  const int h = hg * 4 + w;
  const int c16 = lane & 15, g4 = lane >> 4;
#define STAMP(i) do { } while (0)
  STAMP(0);
#define WSOFF 0
  const auto wsr = __builtin_amdgcn_make_buffer_rsrc(p.ws + (size_t)WSOFF, 0, WS_END, 0x00020000);

  const int lenraw = p.lens[c16];
  const unsigned gv = __builtin_amdgcn_raw_buffer_load_b32(wsr, WS_G + (h * 16 + c16) * 4, 0, 16);
  int pm;
  {
    const i32x4* bt4 = (const i32x4*)p.bt;
    const i32x4 r0 = bt4[lane];
    i32x4 rr[4];
#pragma unroll
    for (int i = 0; i < 4; ++i) rr[i] = bt4[(4 * w + i) * 64 + lane];
    bool n0 = false, n1 = false, n2 = false, n3 = false;
#pragma unroll
    for (int i = 0; i < 4; ++i) {
      n0 |= rr[i][0] != r0[0]; n1 |= rr[i][1] != r0[1]; n2 |= rr[i][2] != r0[2]; n3 |= rr[i][3] != r0[3];
      *(LAS i32x4*)(lds + LDS_BT + ((4 * w + i) * 256 + 4 * lane) * 4) = rr[i];
    }
    const int fc = n0 ? 0 : n1 ? 1 : n2 ? 2 : n3 ? 3 : 4;
    const unsigned long long mm = __ballot(fc < 4);
    int col = NBLK;
    if (mm) { const int fl = __builtin_ctzll(mm); col = 4 * fl + rl(fc, fl); }
    if (lane == 0) *(LAS int*)(lds + LDS_MISC + 4 * w) = col;
    asm volatile("s_waitcnt lgkmcnt(0)\n\ts_barrier" ::: "memory");
    const i32x4 mc = *(LAS i32x4*)(lds + LDS_MISC);
    pm = rfl(min(min(mc[0], mc[1]), min(mc[2], mc[3])));
  }
  asm volatile("" :: "v"(gv));
  const bool zq_l = lenraw <= 0;
  const int len_l = zq_l ? LK : min(lenraw, LK);
  int mn = len_l;
  mn = min(mn, dppi_keep<0x128>(mn, mn)); mn = min(mn, dppi_keep<0x124>(mn, mn));
  mn = min(mn, dppi_keep<0x122>(mn, mn)); mn = min(mn, dppi_keep<0x121>(mn, mn));
  const int P = min(pm, rfl(mn) >> 4);
  const int tsh = P * BS;
  const int vL = len_l - tsh;
  int inc = vL;
  inc += dppi<0x111>(inc); inc += dppi<0x112>(inc); inc += dppi<0x114>(inc); inc += dppi<0x118>(inc);
  const int lsum = rl(inc, 15);
  const int FX = min(FCOST, (tsh + lsum) >> 7);
  int n_sh = 0;
  if (tsh > 0) {
    const int n = lane + 1;
    n_sh = __builtin_ctzll(__ballot(n * (tsh + lsum + n * FX - 64 * FX) >= 64 * tsh)) + 1;
  }
  const int wsh = tsh + n_sh * FX;
  const int vS = wsh + inc - vL;
  const int N = wsh + lsum;
  const int x0 = (j * N) >> 6, x1 = ((j + 1) * N) >> 6;
  const unsigned zmask = (unsigned)(__ballot(zq_l) & 0xffffull);
  const unsigned umask = (unsigned)(__ballot(max(x0, vS) < min(x1, vS + vL)) & 0xffffull);
  const int sh_t0 = min(tsh, x0 - j * FX), sh_t1 = j < n_sh ? min(tsh, x1 - (j + 1) * FX) : 0;
  const bool has_sh = j < n_sh && sh_t1 > sh_t0;

  unsigned voffK[4], voffV[4];
#pragma unroll
  for (int i = 0; i < 4; ++i) {
    const int t = 4 * w + i;
    voffK[i] = g4 * 256 + ((c16 ^ t) << 4) + t * 4096 - i * 1024 + 4096;
    voffV[i] = g4 * 256 + ((c16 ^ (2 * (t & 7))) << 4) + t * 4096 - i * 1024 + 4096;
  }
  const unsigned kaddr0 = w * 256 + c16 * 1024 + ((g4 ^ c16) << 4);
  unsigned vaddr0;
  { const int tq = 4 * g4 + (c16 >> 2), pp = lane & 3; vaddr0 = 16384 + tq * 1024 + w * 256 + ((tq & 7) << 5) + ((pp >> 1) << 4) + ((pp & 1) << 3); }
  const char* kbase = p.k + hg * 1024 - 4096;
  const char* vbase = p.v + hg * 1024 - 4096;

  unsigned i_mask = umask; int i_row, i_t0, i_t1, i_page; bool i_more = true;
  int n_iss = 0, i_slot = 0;
  auto unit_range = [&](int b, int& t0, int& t1) {
    const int s = rl(vS, b), L = rl(vL, b);
    t0 = tsh + max(x0, s) - s; t1 = tsh + min(x1, s + L) - s;
  };
  auto q_dma_private = [&](int b, int slot) {
    if (lane < 32) dma16_keep(p.q + b * 8192 + h * 512, lane * 16, LDS_QB + (w * R + slot) * 512);
  };
  if (has_sh) {
    i_row = 0; i_t0 = sh_t0; i_t1 = sh_t1;
#pragma unroll
    for (int i = 0; i < 8; ++i)
      dma16_keep(p.q + i * 16384 + h * 512, (lane >> 5) * 8192 + (lane & 31) * 16, (R - 1) * STAGE + w * 8192 + i * 1024);
  } else if (i_mask) {
    i_row = __builtin_ctz(i_mask); i_mask &= i_mask - 1; unit_range(i_row, i_t0, i_t1);
    q_dma_private(i_row, 0);
  } else { i_more = false; i_row = 0; i_t0 = 0; i_t1 = 0; }
  i_page = i_t0 >> 4;
  int i_pg0 = i_page, vpg = *(LAS int*)(lds + LDS_BT + (i_row * NBLK + min(i_page + lane, NBLK - 1)) * 4);
  auto issue_stage = [&](bool steady) {
    const int phys = rl(vpg, i_page - i_pg0);
    const int lo = i_t0 - i_page * 16, hi = i_t1 - i_page * 16;
    const size_t pb = (size_t)(unsigned)phys * 65536;
    const unsigned dst = i_slot * STAGE + 4 * w * 1024;
    if (lo <= 0 && hi >= 16) {
      dma_stage(kbase + pb, vbase + pb, dst, dst + 16384, voffK[0], voffK[1], voffK[2], voffK[3], voffV[0], voffV[1], voffV[2], voffV[3]);
    } else {
      unsigned ok[4], ov[4];
#pragma unroll
      for (int i = 0; i < 4; ++i) {
        const int d = (min(max(4 * w + i, lo), hi - 1) - (4 * w + i)) * 4096;
        ok[i] = voffK[i] + d; ov[i] = voffV[i] + d;
      }
      dma_stage(kbase + pb, vbase + pb, dst, dst + 16384, ok[0], ok[1], ok[2], ok[3], ov[0], ov[1], ov[2], ov[3]);
    }
    ++n_iss; i_slot = (i_slot + 1 == R) ? 0 : i_slot + 1;
    ++i_page;
    if (i_page * 16 >= i_t1) {
      if (i_mask) {
        i_row = __builtin_ctz(i_mask); i_mask &= i_mask - 1; unit_range(i_row, i_t0, i_t1); i_page = i_t0 >> 4;
        q_dma_private(i_row, i_slot);
        i_pg0 = i_page; vpg = *(LAS int*)(lds + LDS_BT + (i_row * NBLK + min(i_page + lane, NBLK - 1)) * 4);
      } else i_more = false;
    } else if (i_page - i_pg0 == 64) {
      i_pg0 = i_page; vpg = *(LAS int*)(lds + LDS_BT + (i_row * NBLK + min(i_page + lane, NBLK - 1)) * 4);
    }
  };
#pragma unroll 1
  for (int s = 0; s < R - 1; ++s) if (i_more) issue_stage(false);

  const int r_lo = (lane * N) >> 6, r_hi = ((lane + 1) * N) >> 6;
  unsigned own = 0;
  {
    unsigned mrem = umask;
    while (mrem) {
      const int b = __builtin_ctz(mrem); mrem &= mrem - 1;
      const int s = rl(vS, b), L = rl(vL, b);
      unsigned long long pmk = __ballot(max(r_lo, s) < min(r_hi, s + L));
      const int cnt = __builtin_popcountll(pmk);
      int idx = w % cnt;
      while (idx--) pmk &= pmk - 1;
      if (__builtin_ctzll(pmk) == j) own |= 1u << b;
    }
    const unsigned zl = (unsigned)(__ballot(vL == 0) & 0xffffull);
    if ((j & 3) == 0 && ((zl >> (j >> 2)) & 1)) own |= 1u << (j >> 2);
  }
  const unsigned long long smask = __ballot(lane < n_sh && min(tsh, r_hi - (lane + 1) * FX) > min(tsh, r_lo - lane * FX));

  int n_comp = 0, c_slot = 0;
  unsigned c_mask = umask;
  float ro[2][2] = {{0.f, 0.f}, {0.f, 0.f}}, rm[2] = {-INFINITY, -INFINITY}, rlsum[2] = {0.f, 0.f};
  int last_b = -1;
  float ho[2][2] = {{0.f, 0.f}, {0.f, 0.f}}, hm[2] = {-INFINITY, -INFINITY}, hl[2] = {0.f, 0.f};
  int held_b = -1;
  const int pi_l = 2 * lane;
  int flag_wait = 0;
  auto store_flags = [&]() {
    if (lane < 16) {
      typedef unsigned u32x2 __attribute__((ext_vector_type(2)));
      const u32x2 f = {gv + 1u, gv + 1u};
      __builtin_amdgcn_raw_buffer_store_b64(f, wsr, WS_SP + ((hg * 64 + j) * 4 + w) * SP_W + 16640 + lane * 8, 0, 16);
    }
  };

  auto wait_stage = [&]() {
    const int young = n_iss - n_comp - 1;
    if (young >= R - 2) {
      if (R == 2) asm volatile("s_waitcnt vmcnt(0) lgkmcnt(0)\n\ts_barrier" ::: "memory");
      else if (R == 3) asm volatile("s_waitcnt vmcnt(8) lgkmcnt(0)\n\ts_barrier" ::: "memory");
      else asm volatile("s_waitcnt vmcnt(16) lgkmcnt(0)\n\ts_barrier" ::: "memory");
    } else if (R == 4 && young == 1) asm volatile("s_waitcnt vmcnt(8) lgkmcnt(0)\n\ts_barrier" ::: "memory");
    else asm volatile("s_waitcnt vmcnt(0) lgkmcnt(0)\n\ts_barrier" ::: "memory");
  };

  auto publish = [&](int b, const float (&o)[2][2], const float (&mm)[2], const float (&ll)[2]) {
    const unsigned tag = (unsigned)rl((int)gv, b) + 1u;
    const unsigned slot = WS_PP + ((hg * 80 + j + b + 1) * 4 + w) * PP_W;
    const u32x4 g0 = {__float_as_uint(o[0][0]), tag, __float_as_uint(o[0][1]), tag};
    const u32x4 g1 = {__float_as_uint(o[1][0]), tag, __float_as_uint(o[1][1]), tag};
    __builtin_amdgcn_raw_buffer_store_b128(g0, wsr, slot + lane * 16, 0, 16);
    __builtin_amdgcn_raw_buffer_store_b128(g1, wsr, slot + 1024 + lane * 16, 0, 16);
    if (lane < 2) {
      const u32x4 gm = {__float_as_uint(lane ? mm[1] : mm[0]), tag, __float_as_uint(lane ? ll[1] : ll[0]), tag};
      __builtin_amdgcn_raw_buffer_store_b128(gm, wsr, slot + 2048 + lane * 16, 0, 16);
    }
  };
  auto run_piece = [&](auto nq_tag, int t0, int t1, int b) {
    constexpr int NQ = decltype(nq_tag)::value;
    bf16x8 qf[NQ][4];
    f32x4 acc[NQ][8];
    float m[NQ], l[NQ];
#pragma unroll
    for (int qb = 0; qb < NQ; ++qb) {
      m[qb] = -INFINITY; l[qb] = 0.f;
#pragma unroll
      for (int k = 0; k < 8; ++k) acc[qb][k] = f32x4{0.f, 0.f, 0.f, 0.f};
    }
    if (NQ == 2) {
      if (n_iss >= 2) { if (R == 2) asm volatile("s_waitcnt vmcnt(8)" ::: "memory"); else if (R == 3) asm volatile("s_waitcnt vmcnt(16)" ::: "memory"); else asm volatile("s_waitcnt vmcnt(16)" ::: "memory"); }
      else asm volatile("s_waitcnt vmcnt(8)" ::: "memory");
#pragma unroll
      for (int qb = 0; qb < NQ; ++qb) {
        const bool z = (zmask >> (8 * qb + (c16 >> 1))) & 1;
#pragma unroll
        for (int s = 0; s < 4; ++s) {
          u32x4 x = *(LAS u32x4*)(lds + (R - 1) * STAGE + w * 8192 + (16 * qb + c16) * 256 + (4 * s + g4) * 16);
          if (z) x = u32x4{0u, 0u, 0u, 0u};
          qf[qb][s] = __builtin_bit_cast(bf16x8, x);
        }
      }
    }
    bool first = true;
    for (int page = t0 >> 4; page * 16 < t1; ++page) {
      wait_stage();
      if (NQ == 1 && flag_wait > 0 && --flag_wait == 0) store_flags();
      if (i_more) issue_stage(true);
      if (NQ == 1 && first) {
        const bool z = (c16 >= 2) || ((zmask >> b) & 1);
#pragma unroll
        for (int s = 0; s < 4; ++s) {
          u32x4 x = *(LAS u32x4*)(lds + LDS_QB + (w * R + c_slot) * 512 + (c16 & 1) * 256 + (4 * s + g4) * 16);
          if (z) x = u32x4{0u, 0u, 0u, 0u};
          qf[0][s] = __builtin_bit_cast(bf16x8, x);
        }
      }
      first = false;
      const unsigned sb = c_slot * STAGE;
      bf16x8 kf[4];
#pragma unroll
      for (int s = 0; s < 4; ++s) kf[s] = *(LAS bf16x8*)(lds + sb + (kaddr0 ^ (s << 6)));
      s16x4 vf[8];
#pragma unroll
      for (int k = 0; k < 8; ++k) vf[k] = __builtin_amdgcn_ds_read_tr16_b64_v4i16((LAS s16x4*)(lds + sb + (vaddr0 ^ (k << 5))));
      f32x4 S[NQ];
#pragma unroll
      for (int qb = 0; qb < NQ; ++qb) {
        S[qb] = f32x4{0.f, 0.f, 0.f, 0.f};
#pragma unroll
        for (int s = 0; s < 4; ++s) S[qb] = __builtin_amdgcn_mfma_f32_16x16x32_bf16(kf[s], qf[qb][s], S[qb], 0, 0, 0);
      }
      const int lo = t0 - page * 16, hi = t1 - page * 16;
      if (lo > 0 || hi < 16) {
#pragma unroll
        for (int qb = 0; qb < NQ; ++qb)
#pragma unroll
          for (int r = 0; r < 4; ++r) { const int t = 4 * g4 + r; S[qb][r] = (t >= lo && t < hi) ? S[qb][r] : -INFINITY; }
      }
      s16x4 pb[NQ];
      bool grow = false;
      float alpha[NQ];
#pragma unroll
      for (int qb = 0; qb < NQ; ++qb) {
        float mx = fmaxf(fmaxf(S[qb][0], S[qb][1]), fmaxf(S[qb][2], S[qb][3]));
        mx = xg_max(mx);
        const float mn2 = fmaxf(m[qb], mx);
        grow |= mn2 > m[qb];
        alpha[qb] = __builtin_amdgcn_exp2f((m[qb] - mn2) * C2);
        m[qb] = mn2;
        const float mc = -mn2 * C2;
        const float p0 = __builtin_amdgcn_exp2f(fmaf(S[qb][0], C2, mc)), p1 = __builtin_amdgcn_exp2f(fmaf(S[qb][1], C2, mc));
        const float p2 = __builtin_amdgcn_exp2f(fmaf(S[qb][2], C2, mc)), p3 = __builtin_amdgcn_exp2f(fmaf(S[qb][3], C2, mc));
        l[qb] = fmaf(l[qb], alpha[qb], (p0 + p1) + (p2 + p3));
        typedef unsigned u32x2 __attribute__((ext_vector_type(2)));
        pb[qb] = __builtin_bit_cast(s16x4, u32x2{pk_bf16(p0, p1), pk_bf16(p2, p3)});
      }
      if (__any(grow)) {
#pragma unroll
        for (int qb = 0; qb < NQ; ++qb)
#pragma unroll
          for (int k = 0; k < 8; ++k) acc[qb][k] *= alpha[qb];
      }
#pragma unroll
      for (int qb = 0; qb < NQ; ++qb)
#pragma unroll
        for (int k = 0; k < 8; ++k) acc[qb][k] = __builtin_amdgcn_mfma_f32_16x16x16bf16_1k(vf[k], pb[qb], acc[qb][k], 0, 0, 0);
      ++n_comp; c_slot = (c_slot + 1 == R) ? 0 : c_slot + 1;
    }
    if constexpr (NQ == 2) {
      STAMP(6);
      const unsigned slot = WS_SP + ((hg * 64 + j) * 4 + w) * SP_W;
#pragma unroll
      for (int qb = 0; qb < 2; ++qb) {
        const float lt = xg_sum(l[qb]);
        const unsigned rowo = slot + (16 * qb + c16) * 512 + g4 * 16;
#pragma unroll
        for (int k = 0; k < 8; ++k)
          __builtin_amdgcn_raw_buffer_store_b128(__builtin_bit_cast(u32x4, acc[qb][k]), wsr, rowo + k * 64, 0, SHPUB ? 0 : 16);
        if (g4 == 0) {
          typedef unsigned u32x2 __attribute__((ext_vector_type(2)));
          const u32x2 a = {__float_as_uint(m[qb] * C2), __float_as_uint(lt)};
          __builtin_amdgcn_raw_buffer_store_b64(a, wsr, slot + 16384 + (16 * qb + c16) * 8, 0, SHPUB ? 0 : 16);
        }
      }
      if (SHPUB == 1 && c_mask == 0) {
        asm volatile("s_waitcnt vmcnt(0)\n\ts_barrier" ::: "memory");
        if (w == 0) { __builtin_amdgcn_fence(__ATOMIC_RELEASE, "agent"); asm volatile("s_waitcnt vmcnt(0)" ::: "memory"); }
        asm volatile("s_barrier" ::: "memory");
        store_flags();
      } else if (SHPUB == 3 && c_mask == 0) {
        asm volatile("s_waitcnt vmcnt(0)" ::: "memory"); store_flags();
      } else
      if (c_mask == 0) { asm volatile("s_waitcnt vmcnt(0)" ::: "memory"); store_flags(); }
      else flag_wait = 3;
      STAMP(7);
    } else {
      const float lt = xg_sum(l[0]);
      LAS char* sc = lds + LDS_SC + w * 1024;
      if (c16 < 2) {
#pragma unroll
        for (int k = 0; k < 8; ++k) *(LAS f32x4*)(sc + c16 * 512 + (16 * k + 4 * g4) * 4) = acc[0][k];
      }
      asm volatile("s_waitcnt lgkmcnt(0)" ::: "memory");
      const f32x2 a0 = *(LAS f32x2*)(sc + pi_l * 4), a1 = *(LAS f32x2*)(sc + 512 + pi_l * 4);
      if (last_b >= 0) {
        if (held_b >= 0) publish(held_b, ho, hm, hl);
        held_b = last_b;
#pragma unroll
        for (int r = 0; r < 2; ++r) { ho[r][0] = ro[r][0]; ho[r][1] = ro[r][1]; hm[r] = rm[r]; hl[r] = rlsum[r]; }
      }
      ro[0][0] = a0[0]; ro[0][1] = a0[1]; ro[1][0] = a1[0]; ro[1][1] = a1[1];
      rm[0] = rlf(m[0], 0) * C2; rm[1] = rlf(m[0], 1) * C2;
      rlsum[0] = rlf(lt, 0); rlsum[1] = rlf(lt, 1);
      last_b = b;
    }
  };
  if (has_sh) run_piece(Two{}, sh_t0, sh_t1, 0);
  while (c_mask) {
    const int b = __builtin_ctz(c_mask); c_mask &= c_mask - 1;
    int t0, t1; unit_range(b, t0, t1);
    run_piece(One{}, t0, t1, b);
  }
  STAMP(2);
  if (last_b >= 0 && !((own >> last_b) & 1)) { publish(last_b, ro, rm, rlsum); last_b = -1; }
  if (held_b >= 0 && !((own >> held_b) & 1)) { publish(held_b, ho, hm, hl); held_b = -1; }
  if (flag_wait > 0) { asm volatile("s_waitcnt vmcnt(0)" ::: "memory"); store_flags(); }
  STAMP(1);

  while (own) {
    const int b = __builtin_ctz(own); own &= own - 1;
    const unsigned tag = (unsigned)rl((int)gv, b) + 1u;
    const int s = rl(vS, b), L = rl(vL, b);
    unsigned long long pmk = __ballot(max(r_lo, s) < min(r_hi, s + L));
    unsigned long long smk = smask;
    float M0 = -INFINITY, M1 = -INFINITY, L0 = 0.f, L1 = 0.f, o00 = 0.f, o01 = 0.f, o10 = 0.f, o11 = 0.f;
    if (b == last_b) {
      pmk &= ~(1ull << j);
      M0 = rm[0]; M1 = rm[1]; L0 = rlsum[0]; L1 = rlsum[1]; o00 = ro[0][0]; o01 = ro[0][1]; o10 = ro[1][0]; o11 = ro[1][1];
    } else if (b == held_b) {
      pmk &= ~(1ull << j);
      M0 = hm[0]; M1 = hm[1]; L0 = hl[0]; L1 = hl[1]; o00 = ho[0][0]; o01 = ho[0][1]; o10 = ho[1][0]; o11 = ho[1][1];
    }
    auto merge = [&](float m0, float l0, float m1, float l1, float x0, float x1, float y0, float y1) {
      const float n0 = fmaxf(M0, m0), n1 = fmaxf(M1, m1);
      const float a0 = __builtin_amdgcn_exp2f(M0 - n0), b0 = __builtin_amdgcn_exp2f(m0 - n0);
      const float a1 = __builtin_amdgcn_exp2f(M1 - n1), b1 = __builtin_amdgcn_exp2f(m1 - n1);
      L0 = L0 * a0 + l0 * b0; L1 = L1 * a1 + l1 * b1;
      o00 = o00 * a0 + x0 * b0; o01 = o01 * a0 + x1 * b0;
      o10 = o10 * a1 + y0 * b1; o11 = o11 * a1 + y1 * b1;
      M0 = n0; M1 = n1;
    };
    typedef unsigned u32x2 __attribute__((ext_vector_type(2)));
    unsigned spins = 0;
    while (pmk | smk) {
      unsigned long long rdy = 0; bool sh_ready = false;
      const bool single = (pmk & (pmk - 1)) == 0 && smk == 0;
      if (!single || SPEC == 0) {
        u32x4 pml = {0u, 0u, 0u, 0u}; u32x2 fl = {tag, tag};
        if (pmk) {
          const int jj = ((pmk >> lane) & 1) ? lane : __builtin_ctzll(pmk);
          pml = __builtin_amdgcn_raw_buffer_load_b128(wsr, WS_PP + ((hg * 80 + jj + b + 1) * 4 + w) * PP_W + 2048 + 16, 0, 16);
        }
        if (smk) {
          const int jj = ((smk >> lane) & 1) ? lane : __builtin_ctzll(smk);
          fl = __builtin_amdgcn_raw_buffer_load_b64(wsr, WS_SP + ((hg * 64 + jj) * 4 + w) * SP_W + 16640 + b * 8, 0, 16);
        }
        rdy = pmk & __ballot(pml[1] == tag && pml[3] == tag);
        sh_ready = smk && __all(fl[0] == tag && fl[1] == tag);
      } else rdy = pmk;
      bool progress = false;
      if (rdy) {
        constexpr int NB = 4;
        unsigned rows[NB]; unsigned long long bitp[NB];
        unsigned long long tp = rdy;
#pragma unroll
        for (int k = 0; k < NB; ++k) {
          if (tp) { const int jj = __builtin_ctzll(tp); bitp[k] = tp & (~tp + 1); tp &= tp - 1; rows[k] = WS_PP + ((hg * 80 + jj + b + 1) * 4 + w) * PP_W; }
          else { bitp[k] = 0; rows[k] = rows[0]; }
        }
        u32x4 d0[NB], d1[NB], dm[NB];
        if (single) {
          d0[0] = __builtin_amdgcn_raw_buffer_load_b128(wsr, lane * 16, rows[0], 16);
          d1[0] = __builtin_amdgcn_raw_buffer_load_b128(wsr, 1024 + lane * 16, rows[0], 16);
          dm[0] = __builtin_amdgcn_raw_buffer_load_b128(wsr, 2048 + (lane & 1) * 16, rows[0], 16);
#pragma unroll
          for (int k = 1; k < NB; ++k) { d0[k] = d0[0]; d1[k] = d1[0]; dm[k] = dm[0]; }
        } else {
#pragma unroll
          for (int k = 0; k < NB; ++k) {
            d0[k] = __builtin_amdgcn_raw_buffer_load_b128(wsr, lane * 16, rows[k], 16);
            d1[k] = __builtin_amdgcn_raw_buffer_load_b128(wsr, 1024 + lane * 16, rows[k], 16);
            dm[k] = __builtin_amdgcn_raw_buffer_load_b128(wsr, 2048 + (lane & 1) * 16, rows[k], 16);
          }
        }
#pragma unroll
        for (int k = 0; k < NB; ++k) {
          const bool ok = d0[k][1] == tag && d0[k][3] == tag && d1[k][1] == tag && d1[k][3] == tag && dm[k][1] == tag && dm[k][3] == tag;
          if (bitp[k] && __all(ok)) {
            const float mv = __uint_as_float(dm[k][0]), lv = __uint_as_float(dm[k][2]);
            merge(rlf(mv, 0), rlf(lv, 0), rlf(mv, 1), rlf(lv, 1), __uint_as_float(d0[k][0]), __uint_as_float(d0[k][2]), __uint_as_float(d1[k][0]), __uint_as_float(d1[k][2]));
            pmk &= ~bitp[k]; progress = true;
          }
        }
      }
      if (sh_ready) {
        constexpr int NB = 6;
        unsigned rows[NB]; bool val[NB];
        unsigned long long ts = smk;
#pragma unroll
        for (int k = 0; k < NB; ++k) {
          if (ts) { const int jj = __builtin_ctzll(ts); ts &= ts - 1; rows[k] = WS_SP + ((hg * 64 + jj) * 4 + w) * SP_W; val[k] = true; }
          else { rows[k] = rows[0]; val[k] = false; }
        }
        u32x2 d0[NB], d1[NB]; u32x4 dm[NB];
#pragma unroll
        for (int k = 0; k < NB; ++k) {
          d0[k] = __builtin_amdgcn_raw_buffer_load_b64(wsr, b * 1024 + lane * 8, rows[k], 16);
          d1[k] = __builtin_amdgcn_raw_buffer_load_b64(wsr, b * 1024 + 512 + lane * 8, rows[k], 16);
          dm[k] = __builtin_amdgcn_raw_buffer_load_b128(wsr, 16384 + b * 16, rows[k], 16);
        }
#pragma unroll
        for (int k = 0; k < NB; ++k)
          if (val[k]) merge(__uint_as_float(dm[k][0]), __uint_as_float(dm[k][1]), __uint_as_float(dm[k][2]), __uint_as_float(dm[k][3]),
                            __uint_as_float(d0[k][0]), __uint_as_float(d0[k][1]), __uint_as_float(d1[k][0]), __uint_as_float(d1[k][1]));
        smk = ts;
        progress = true;
      }
      if (!progress) {
        if (++spins > (1u << 20)) break;
        __builtin_amdgcn_s_sleep(SLEEP);
      }
    }
    const float i0 = 1.f / L0, i1 = 1.f / L1;
    __hip_bfloat16* orow = p.out + (b * NH + 2 * h) * DH + pi_l;
    *(unsigned*)orow = pk_bf16(o00 * i0, o01 * i0);
    *(unsigned*)(orow + DH) = pk_bf16(o10 * i1, o11 * i1);
    if (lane == 0) __builtin_amdgcn_raw_buffer_store_b32(tag, wsr, WS_G + (h * 16 + b) * 4, 0, 16);
  }
  STAMP(3);
}

}

extern "C" void kernel_launch(void* const* d_in, const int* in_sizes, int n_in, void* d_out, int out_size, void* d_ws, size_t ws_size,
                              hipStream_t stream) {
  using namespace rattn;
  Params p;
  p.q = (const char*)d_in[0]; p.k = (const char*)d_in[1]; p.v = (const char*)d_in[2];
  p.bt = (const int*)d_in[3]; p.lens = (const int*)d_in[4];
  p.out = (__hip_bfloat16*)d_out; p.ws = (char*)d_ws;
  (void)hipFuncSetAttribute((const void*)radix_attn_kernel, hipFuncAttributeMaxDynamicSharedMemorySize, LDS_TOTAL);
  hipLaunchKernelGGL(radix_attn_kernel, dim3(256), dim3(256), LDS_TOTAL, stream, p);
}
```

```cpp
#include <hip/hip_runtime.h>
#include <hip/hip_bf16.h>
#include <stdint.h>

namespace rattn {

constexpr int NSEQ = 16, NH = 32, NKV = 16, DH = 128, NBLK = 256, BS = 16;
constexpr int LK = NBLK * BS;
#define MAPMODE 1
#define RING 3
#define FCOST 56
#define SPEC 1
#define SLEEP 12
#define PACE_LEAD 32
#define SHPUB 0
#define APACE_DELTA 117
#define KPOL "nt"
#define VPOL "nt"
constexpr int R = RING;
constexpr int STAGE = 32768;
constexpr int LDS_BT = R * STAGE;
constexpr int LDS_QB = LDS_BT + NSEQ * NBLK * 4;
constexpr int LDS_SC = LDS_QB + 4 * R * 512;
constexpr int LDS_MISC = LDS_SC + 4096;
constexpr int LDS_PROG = LDS_MISC + 64;
constexpr int LDS_TOTAL = LDS_PROG + 512;
static_assert(LDS_TOTAL <= 160 * 1024, "LDS");

constexpr unsigned WS_G = 0;
constexpr unsigned WS_PROG = 2048;
constexpr unsigned WS_PP = 4096;
constexpr unsigned PP_W = 2176;
constexpr unsigned WS_SP = 4u << 20;
constexpr unsigned SP_W = 16384 + 256 + 128;
constexpr unsigned WS_END = WS_SP + 4u * 64u * 4u * SP_W;
constexpr unsigned WS_STAMP = 64u << 20;

typedef unsigned u32x4 __attribute__((ext_vector_type(4)));
typedef float f32x4 __attribute__((ext_vector_type(4)));
typedef float f32x2 __attribute__((ext_vector_type(2)));
typedef __bf16 bf16x8 __attribute__((ext_vector_type(8)));
typedef __bf16 bf16x2 __attribute__((ext_vector_type(2)));
typedef short s16x4 __attribute__((ext_vector_type(4)));
typedef int i32x4 __attribute__((ext_vector_type(4)));
#define LAS __attribute__((address_space(3)))
#define GAS __attribute__((address_space(1)))

constexpr float C2 = 0.088388347648318440f * 1.4426950408889634f;

__device__ __forceinline__ int rfl(int x) { return __builtin_amdgcn_readfirstlane(x); }
__device__ __forceinline__ int rl(int x, int l) { return __builtin_amdgcn_readlane(x, l); }
__device__ __forceinline__ float rlf(float x, int l) { return __builtin_bit_cast(float, __builtin_amdgcn_readlane(__builtin_bit_cast(int, x), l)); }

__device__ __forceinline__ void dma16(const void* sbase, unsigned voff, unsigned dst) {
  unsigned keep;
  asm volatile("s_mov_b32 %0, m0\n\ts_mov_b32 m0, %3\n\ts_nop 0\n\tglobal_load_lds_dwordx4 %1, %2 nt\n\ts_mov_b32 m0, %0"
               : "=&s"(keep) : "v"(voff), "s"(sbase), "s"(dst) : "memory");
}
__device__ __forceinline__ void dma16_keep(const void* sbase, unsigned voff, unsigned dst) {
  unsigned keep;
  asm volatile("s_mov_b32 %0, m0\n\ts_mov_b32 m0, %3\n\ts_nop 0\n\tglobal_load_lds_dwordx4 %1, %2\n\ts_mov_b32 m0, %0"
               : "=&s"(keep) : "v"(voff), "s"(sbase), "s"(dst) : "memory");
}
__device__ __forceinline__ void dma_stage(const void* sK, const void* sV, unsigned dstK, unsigned dstV,
                                          unsigned k0, unsigned k1, unsigned k2, unsigned k3,
                                          unsigned v0, unsigned v1, unsigned v2, unsigned v3) {
  unsigned keep;
  asm volatile("s_mov_b32 %0, m0\n\ts_mov_b32 m0, %3\n\ts_nop 0\n\t"
               "global_load_lds_dwordx4 %5, %1 " KPOL "\n\t"
               "global_load_lds_dwordx4 %6, %1 offset:1024 " KPOL "\n\t"
               "global_load_lds_dwordx4 %7, %1 offset:2048 " KPOL "\n\t"
               "global_load_lds_dwordx4 %8, %1 offset:3072 " KPOL "\n\t"
               "s_mov_b32 m0, %4\n\ts_nop 0\n\t"
               "global_load_lds_dwordx4 %9, %2 " VPOL "\n\t"
               "global_load_lds_dwordx4 %10, %2 offset:1024 " VPOL "\n\t"
               "global_load_lds_dwordx4 %11, %2 offset:2048 " VPOL "\n\t"
               "global_load_lds_dwordx4 %12, %2 offset:3072 " VPOL "\n\t"
               "s_mov_b32 m0, %0"
               : "=&s"(keep)
               : "s"(sK), "s"(sV), "s"(dstK), "s"(dstV), "v"(k0), "v"(k1), "v"(k2), "v"(k3), "v"(v0), "v"(v1), "v"(v2), "v"(v3)
               : "memory");
}
__device__ __forceinline__ void dma16_sc1(const void* sbase, unsigned voff, unsigned dst) {
  unsigned keep;
  asm volatile("s_mov_b32 %0, m0\n\ts_mov_b32 m0, %3\n\ts_nop 0\n\tglobal_load_lds_dwordx4 %1, %2 sc1\n\ts_mov_b32 m0, %0"
               : "=&s"(keep) : "v"(voff), "s"(sbase), "s"(dst) : "memory");
}
template <int CTRL> __device__ __forceinline__ int dppi(int x) { return __builtin_amdgcn_update_dpp(0, x, CTRL, 0xf, 0xf, true); }
template <int CTRL> __device__ __forceinline__ int dppi_keep(int old, int x) { return __builtin_amdgcn_update_dpp(old, x, CTRL, 0xf, 0xf, false); }

__device__ __forceinline__ float xg_max(float x) {
  auto s = __builtin_amdgcn_permlane16_swap(__float_as_uint(x), __float_as_uint(x), false, false);
  x = fmaxf(__uint_as_float(s[0]), __uint_as_float(s[1]));
  auto t = __builtin_amdgcn_permlane32_swap(__float_as_uint(x), __float_as_uint(x), false, false);
  return fmaxf(__uint_as_float(t[0]), __uint_as_float(t[1]));
}
__device__ __forceinline__ float xg_sum(float x) {
  auto s = __builtin_amdgcn_permlane16_swap(__float_as_uint(x), __float_as_uint(x), false, false);
  x = __uint_as_float(s[0]) + __uint_as_float(s[1]);
  auto t = __builtin_amdgcn_permlane32_swap(__float_as_uint(x), __float_as_uint(x), false, false);
  return __uint_as_float(t[0]) + __uint_as_float(t[1]);
}
__device__ __forceinline__ unsigned pk_bf16(float a, float b) {
  bf16x2 v = {(__bf16)a, (__bf16)b};
  return __builtin_bit_cast(unsigned, v);
}

struct One { static constexpr int value = 1; };
struct Two { static constexpr int value = 2; };

struct Params {
  const char* q; const char* k; const char* v; const int* bt; const int* lens; __hip_bfloat16* out; char* ws;
};

__global__ void __launch_bounds__(256, 2) radix_attn_kernel(Params p) {
  extern __shared__ __attribute__((aligned(16))) char smem[];
  LAS char* lds = (LAS char*)smem;
  const int tid = threadIdx.x, lane = tid & 63, w = rfl(tid >> 6);
  const int gidx = blockIdx.x;
  const int hg = (gidx >> 3) & 3, j = (gidx & 7) | ((gidx >> 5) << 3);
  const int h = hg * 4 + w;
  const int c16 = lane & 15, g4 = lane >> 4;
#define STAMP(i) do { } while (0)
  STAMP(0);
#define WSOFF 0
  const auto wsr = __builtin_amdgcn_make_buffer_rsrc(p.ws + (size_t)WSOFF, 0, WS_END, 0x00020000);

  const int lenraw = p.lens[c16];
  const unsigned gv = __builtin_amdgcn_raw_buffer_load_b32(wsr, WS_G + (h * 16 + c16) * 4, 0, 16);
  int pm;
  {
    const i32x4* bt4 = (const i32x4*)p.bt;
    const i32x4 r0 = bt4[lane];
    i32x4 rr[4];
#pragma unroll
    for (int i = 0; i < 4; ++i) rr[i] = bt4[(4 * w + i) * 64 + lane];
    bool n0 = false, n1 = false, n2 = false, n3 = false;
#pragma unroll
    for (int i = 0; i < 4; ++i) {
      n0 |= rr[i][0] != r0[0]; n1 |= rr[i][1] != r0[1]; n2 |= rr[i][2] != r0[2]; n3 |= rr[i][3] != r0[3];
      *(LAS i32x4*)(lds + LDS_BT + ((4 * w + i) * 256 + 4 * lane) * 4) = rr[i];
    }
    const int fc = n0 ? 0 : n1 ? 1 : n2 ? 2 : n3 ? 3 : 4;
    const unsigned long long mm = __ballot(fc < 4);
    int col = NBLK;
    if (mm) { const int fl = __builtin_ctzll(mm); col = 4 * fl + rl(fc, fl); }
    if (lane == 0) *(LAS int*)(lds + LDS_MISC + 4 * w) = col;
    asm volatile("s_waitcnt lgkmcnt(0)\n\ts_barrier" ::: "memory");
    const i32x4 mc = *(LAS i32x4*)(lds + LDS_MISC);
    pm = rfl(min(min(mc[0], mc[1]), min(mc[2], mc[3])));
  }
  asm volatile("" :: "v"(gv));
  const bool zq_l = lenraw <= 0;
  const int len_l = zq_l ? LK : min(lenraw, LK);
  int mn = len_l;
  mn = min(mn, dppi_keep<0x128>(mn, mn)); mn = min(mn, dppi_keep<0x124>(mn, mn));
  mn = min(mn, dppi_keep<0x122>(mn, mn)); mn = min(mn, dppi_keep<0x121>(mn, mn));
  const int P = min(pm, rfl(mn) >> 4);
  const int tsh = P * BS;
  const int vL = len_l - tsh;
  int inc = vL;
  inc += dppi<0x111>(inc); inc += dppi<0x112>(inc); inc += dppi<0x114>(inc); inc += dppi<0x118>(inc);
  const int lsum = rl(inc, 15);
  const int FX = min(FCOST, (tsh + lsum) >> 7);
  int n_sh = 0;
  if (tsh > 0) {
    const int n = lane + 1;
    n_sh = __builtin_ctzll(__ballot(n * (tsh + lsum + n * FX - 64 * FX) >= 64 * tsh)) + 1;
  }
  const int wsh = tsh + n_sh * FX;
  const int vS = wsh + inc - vL;
  const int N = wsh + lsum;
  const int x0 = (j * N) >> 6, x1 = ((j + 1) * N) >> 6;
  const unsigned zmask = (unsigned)(__ballot(zq_l) & 0xffffull);
  const unsigned umask = (unsigned)(__ballot(max(x0, vS) < min(x1, vS + vL)) & 0xffffull);
  const int sh_t0 = min(tsh, x0 - j * FX), sh_t1 = j < n_sh ? min(tsh, x1 - (j + 1) * FX) : 0;
  const bool has_sh = j < n_sh && sh_t1 > sh_t0;

  unsigned voffK[4], voffV[4];
#pragma unroll
  for (int i = 0; i < 4; ++i) {
    const int t = 4 * w + i;
    voffK[i] = g4 * 256 + ((c16 ^ t) << 4) + t * 4096 - i * 1024 + 4096;
    voffV[i] = g4 * 256 + ((c16 ^ (2 * (t & 7))) << 4) + t * 4096 - i * 1024 + 4096;
  }
  const unsigned kaddr0 = w * 256 + c16 * 1024 + ((g4 ^ c16) << 4);
  unsigned vaddr0;
  { const int tq = 4 * g4 + (c16 >> 2), pp = lane & 3; vaddr0 = 16384 + tq * 1024 + w * 256 + ((tq & 7) << 5) + ((pp >> 1) << 4) + ((pp & 1) << 3); }
  const char* kbase = p.k + hg * 1024 - 4096;
  const char* vbase = p.v + hg * 1024 - 4096;

  unsigned i_mask = umask; int i_row, i_t0, i_t1, i_page; bool i_more = true;
  int n_iss = 0, i_slot = 0;
  auto unit_range = [&](int b, int& t0, int& t1) {
    const int s = rl(vS, b), L = rl(vL, b);
    t0 = tsh + max(x0, s) - s; t1 = tsh + min(x1, s + L) - s;
  };
  auto q_dma_private = [&](int b, int slot) {
    if (lane < 32) dma16_keep(p.q + b * 8192 + h * 512, lane * 16, LDS_QB + (w * R + slot) * 512);
  };
  if (has_sh) {
    i_row = 0; i_t0 = sh_t0; i_t1 = sh_t1;
#pragma unroll
    for (int i = 0; i < 8; ++i)
      dma16_keep(p.q + i * 16384 + h * 512, (lane >> 5) * 8192 + (lane & 31) * 16, (R - 1) * STAGE + w * 8192 + i * 1024);
  } else if (i_mask) {
    i_row = __builtin_ctz(i_mask); i_mask &= i_mask - 1; unit_range(i_row, i_t0, i_t1);
    q_dma_private(i_row, 0);
  } else { i_more = false; i_row = 0; i_t0 = 0; i_t1 = 0; }
  i_page = i_t0 >> 4;
  int i_pg0 = i_page, vpg = *(LAS int*)(lds + LDS_BT + (i_row * NBLK + min(i_page + lane, NBLK - 1)) * 4);
  auto issue_stage = [&](bool steady) {
    const int phys = rl(vpg, i_page - i_pg0);
    const int lo = i_t0 - i_page * 16, hi = i_t1 - i_page * 16;
    const size_t pb = (size_t)(unsigned)phys * 65536;
    const unsigned dst = i_slot * STAGE + 4 * w * 1024;
    if (lo <= 0 && hi >= 16) {
      dma_stage(kbase + pb, vbase + pb, dst, dst + 16384, voffK[0], voffK[1], voffK[2], voffK[3], voffV[0], voffV[1], voffV[2], voffV[3]);
    } else {
      unsigned ok[4], ov[4];
#pragma unroll
      for (int i = 0; i < 4; ++i) {
        const int d = (min(max(4 * w + i, lo), hi - 1) - (4 * w + i)) * 4096;
        ok[i] = voffK[i] + d; ov[i] = voffV[i] + d;
      }
      dma_stage(kbase + pb, vbase + pb, dst, dst + 16384, ok[0], ok[1], ok[2], ok[3], ov[0], ov[1], ov[2], ov[3]);
    }
    ++n_iss; i_slot = (i_slot + 1 == R) ? 0 : i_slot + 1;
    ++i_page;
    if (i_page * 16 >= i_t1) {
      if (i_mask) {
        i_row = __builtin_ctz(i_mask); i_mask &= i_mask - 1; unit_range(i_row, i_t0, i_t1); i_page = i_t0 >> 4;
        q_dma_private(i_row, i_slot);
        i_pg0 = i_page; vpg = *(LAS int*)(lds + LDS_BT + (i_row * NBLK + min(i_page + lane, NBLK - 1)) * 4);
      } else i_more = false;
    } else if (i_page - i_pg0 == 64) {
      i_pg0 = i_page; vpg = *(LAS int*)(lds + LDS_BT + (i_row * NBLK + min(i_page + lane, NBLK - 1)) * 4);
    }
  };
#pragma unroll 1
  for (int s = 0; s < R - 1; ++s) if (i_more) issue_stage(false);

  const int r_lo = (lane * N) >> 6, r_hi = ((lane + 1) * N) >> 6;
  unsigned own = 0;
  {
    unsigned mrem = umask;
    while (mrem) {
      const int b = __builtin_ctz(mrem); mrem &= mrem - 1;
      const int s = rl(vS, b), L = rl(vL, b);
      unsigned long long pmk = __ballot(max(r_lo, s) < min(r_hi, s + L));
      const int cnt = __builtin_popcountll(pmk);
      int idx = w % cnt;
      while (idx--) pmk &= pmk - 1;
      if (__builtin_ctzll(pmk) == j) own |= 1u << b;
    }
    const unsigned zl = (unsigned)(__ballot(vL == 0) & 0xffffull);
    if ((j & 3) == 0 && ((zl >> (j >> 2)) & 1)) own |= 1u << (j >> 2);
  }
  const unsigned long long smask = __ballot(lane < n_sh && min(tsh, r_hi - (lane + 1) * FX) > min(tsh, r_lo - lane * FX));

  int n_comp = 0, c_slot = 0;
  unsigned c_mask = umask;
  float ro[2][2] = {{0.f, 0.f}, {0.f, 0.f}}, rm[2] = {-INFINITY, -INFINITY}, rlsum[2] = {0.f, 0.f};
  int last_b = -1;
  float ho[2][2] = {{0.f, 0.f}, {0.f, 0.f}}, hm[2] = {-INFINITY, -INFINITY}, hl[2] = {0.f, 0.f};
  int held_b = -1;
  const int pi_l = 2 * lane;
  auto store_flags = [&]() {
    if (lane < 16) {
      typedef unsigned u32x2 __attribute__((ext_vector_type(2)));
      const u32x2 f = {gv + 1u, gv + 1u};
      __builtin_amdgcn_raw_buffer_store_b64(f, wsr, WS_SP + ((hg * 64 + j) * 4 + w) * SP_W + 16640 + lane * 8, 0, 16);
    }
  };

  auto wait_stage = [&]() {
    const int young = n_iss - n_comp - 1;
    if (young >= R - 2) {
      if (R == 2) asm volatile("s_waitcnt vmcnt(0) lgkmcnt(0)\n\ts_barrier" ::: "memory");
      else if (R == 3) asm volatile("s_waitcnt vmcnt(8) lgkmcnt(0)\n\ts_barrier" ::: "memory");
      else asm volatile("s_waitcnt vmcnt(16) lgkmcnt(0)\n\ts_barrier" ::: "memory");
    } else if (R == 4 && young == 1) asm volatile("s_waitcnt vmcnt(8) lgkmcnt(0)\n\ts_barrier" ::: "memory");
    else asm volatile("s_waitcnt vmcnt(0) lgkmcnt(0)\n\ts_barrier" ::: "memory");
  };

  auto publish = [&](int b, const float (&o)[2][2], const float (&mm)[2], const float (&ll)[2]) {
    const unsigned tag = (unsigned)rl((int)gv, b) + 1u;
    const unsigned slot = WS_PP + ((hg * 80 + j + b + 1) * 4 + w) * PP_W;
    const u32x4 g0 = {__float_as_uint(o[0][0]), tag, __float_as_uint(o[0][1]), tag};
    const u32x4 g1 = {__float_as_uint(o[1][0]), tag, __float_as_uint(o[1][1]), tag};
    __builtin_amdgcn_raw_buffer_store_b128(g0, wsr, slot + lane * 16, 0, 16);
    __builtin_amdgcn_raw_buffer_store_b128(g1, wsr, slot + 1024 + lane * 16, 0, 16);
    if (lane < 2) {
      const u32x4 gm = {__float_as_uint(lane ? mm[1] : mm[0]), tag, __float_as_uint(lane ? ll[1] : ll[0]), tag};
      __builtin_amdgcn_raw_buffer_store_b128(gm, wsr, slot + 2048 + lane * 16, 0, 16);
    }
  };
  auto run_piece = [&](auto nq_tag, int t0, int t1, int b) {
    constexpr int NQ = decltype(nq_tag)::value;
    bf16x8 qf[NQ][4];
    f32x4 acc[NQ][8];
    float m[NQ], l[NQ];
#pragma unroll
    for (int qb = 0; qb < NQ; ++qb) {
      m[qb] = -INFINITY; l[qb] = 0.f;
#pragma unroll
      for (int k = 0; k < 8; ++k) acc[qb][k] = f32x4{0.f, 0.f, 0.f, 0.f};
    }
    if (NQ == 2) {
      if (n_iss >= 2) { if (R == 2) asm volatile("s_waitcnt vmcnt(8)" ::: "memory"); else if (R == 3) asm volatile("s_waitcnt vmcnt(16)" ::: "memory"); else asm volatile("s_waitcnt vmcnt(16)" ::: "memory"); }
      else asm volatile("s_waitcnt vmcnt(8)" ::: "memory");
#pragma unroll
      for (int qb = 0; qb < NQ; ++qb) {
        const bool z = (zmask >> (8 * qb + (c16 >> 1))) & 1;
#pragma unroll
        for (int s = 0; s < 4; ++s) {
          u32x4 x = *(LAS u32x4*)(lds + (R - 1) * STAGE + w * 8192 + (16 * qb + c16) * 256 + (4 * s + g4) * 16);
          if (z) x = u32x4{0u, 0u, 0u, 0u};
          qf[qb][s] = __builtin_bit_cast(bf16x8, x);
        }
      }
    }
    bool first = true;
    for (int page = t0 >> 4; page * 16 < t1; ++page) {
      wait_stage();
      if (i_more) issue_stage(true);
      if (NQ == 1 && first) {
        const bool z = (c16 >= 2) || ((zmask >> b) & 1);
#pragma unroll
        for (int s = 0; s < 4; ++s) {
          u32x4 x = *(LAS u32x4*)(lds + LDS_QB + (w * R + c_slot) * 512 + (c16 & 1) * 256 + (4 * s + g4) * 16);
          if (z) x = u32x4{0u, 0u, 0u, 0u};
          qf[0][s] = __builtin_bit_cast(bf16x8, x);
        }
      }
      first = false;
      const unsigned sb = c_slot * STAGE;
      bf16x8 kf[4];
#pragma unroll
      for (int s = 0; s < 4; ++s) kf[s] = *(LAS bf16x8*)(lds + sb + (kaddr0 ^ (s << 6)));
      s16x4 vf[8];
#pragma unroll
      for (int k = 0; k < 8; ++k) vf[k] = __builtin_amdgcn_ds_read_tr16_b64_v4i16((LAS s16x4*)(lds + sb + (vaddr0 ^ (k << 5))));
      f32x4 S[NQ];
#pragma unroll
      for (int qb = 0; qb < NQ; ++qb) {
        S[qb] = f32x4{0.f, 0.f, 0.f, 0.f};
#pragma unroll
        for (int s = 0; s < 4; ++s) S[qb] = __builtin_amdgcn_mfma_f32_16x16x32_bf16(kf[s], qf[qb][s], S[qb], 0, 0, 0);
      }
      const int lo = t0 - page * 16, hi = t1 - page * 16;
      if (lo > 0 || hi < 16) {
#pragma unroll
        for (int qb = 0; qb < NQ; ++qb)
#pragma unroll
          for (int r = 0; r < 4; ++r) { const int t = 4 * g4 + r; S[qb][r] = (t >= lo && t < hi) ? S[qb][r] : -INFINITY; }
      }
      s16x4 pb[NQ];
      bool grow = false;
      float alpha[NQ];
#pragma unroll
      for (int qb = 0; qb < NQ; ++qb) {
        float mx = fmaxf(fmaxf(S[qb][0], S[qb][1]), fmaxf(S[qb][2], S[qb][3]));
        mx = xg_max(mx);
        const float mn2 = fmaxf(m[qb], mx);
        grow |= mn2 > m[qb];
        alpha[qb] = __builtin_amdgcn_exp2f((m[qb] - mn2) * C2);
        m[qb] = mn2;
        const float mc = -mn2 * C2;
        const float p0 = __builtin_amdgcn_exp2f(fmaf(S[qb][0], C2, mc)), p1 = __builtin_amdgcn_exp2f(fmaf(S[qb][1], C2, mc));
        const float p2 = __builtin_amdgcn_exp2f(fmaf(S[qb][2], C2, mc)), p3 = __builtin_amdgcn_exp2f(fmaf(S[qb][3], C2, mc));
        l[qb] = fmaf(l[qb], alpha[qb], (p0 + p1) + (p2 + p3));
        typedef unsigned u32x2 __attribute__((ext_vector_type(2)));
        pb[qb] = __builtin_bit_cast(s16x4, u32x2{pk_bf16(p0, p1), pk_bf16(p2, p3)});
      }
      if (__any(grow)) {
#pragma unroll
        for (int qb = 0; qb < NQ; ++qb)
#pragma unroll
          for (int k = 0; k < 8; ++k) acc[qb][k] *= alpha[qb];
      }
#pragma unroll
      for (int qb = 0; qb < NQ; ++qb)
#pragma unroll
        for (int k = 0; k < 8; ++k) acc[qb][k] = __builtin_amdgcn_mfma_f32_16x16x16bf16_1k(vf[k], pb[qb], acc[qb][k], 0, 0, 0);
      ++n_comp; c_slot = (c_slot + 1 == R) ? 0 : c_slot + 1;
    }
    if constexpr (NQ == 2) {
      STAMP(6);
      const unsigned slot = WS_SP + ((hg * 64 + j) * 4 + w) * SP_W;
#pragma unroll
      for (int qb = 0; qb < 2; ++qb) {
        const float lt = xg_sum(l[qb]);
        const unsigned rowo = slot + (16 * qb + c16) * 512 + g4 * 16;
#pragma unroll
        for (int k = 0; k < 8; ++k)
          __builtin_amdgcn_raw_buffer_store_b128(__builtin_bit_cast(u32x4, acc[qb][k]), wsr, rowo + k * 64, 0, SHPUB ? 0 : 16);
        if (g4 == 0) {
          typedef unsigned u32x2 __attribute__((ext_vector_type(2)));
          const u32x2 a = {__float_as_uint(m[qb] * C2), __float_as_uint(lt)};
          __builtin_amdgcn_raw_buffer_store_b64(a, wsr, slot + 16384 + (16 * qb + c16) * 8, 0, SHPUB ? 0 : 16);
        }
      }
      asm volatile("s_waitcnt vmcnt(0)" ::: "memory");
      store_flags();
      STAMP(7);
    } else {
      const float lt = xg_sum(l[0]);
      LAS char* sc = lds + LDS_SC + w * 1024;
      if (c16 < 2) {
#pragma unroll
        for (int k = 0; k < 8; ++k) *(LAS f32x4*)(sc + c16 * 512 + (16 * k + 4 * g4) * 4) = acc[0][k];
      }
      asm volatile("s_waitcnt lgkmcnt(0)" ::: "memory");
      const f32x2 a0 = *(LAS f32x2*)(sc + pi_l * 4), a1 = *(LAS f32x2*)(sc + 512 + pi_l * 4);
      if (last_b >= 0) {
        if (held_b >= 0) publish(held_b, ho, hm, hl);
        held_b = last_b;
#pragma unroll
        for (int r = 0; r < 2; ++r) { ho[r][0] = ro[r][0]; ho[r][1] = ro[r][1]; hm[r] = rm[r]; hl[r] = rlsum[r]; }
      }
      ro[0][0] = a0[0]; ro[0][1] = a0[1]; ro[1][0] = a1[0]; ro[1][1] = a1[1];
      rm[0] = rlf(m[0], 0) * C2; rm[1] = rlf(m[0], 1) * C2;
      rlsum[0] = rlf(lt, 0); rlsum[1] = rlf(lt, 1);
      last_b = b;
    }
  };
  if (has_sh) run_piece(Two{}, sh_t0, sh_t1, 0);
  while (c_mask) {
    const int b = __builtin_ctz(c_mask); c_mask &= c_mask - 1;
    int t0, t1; unit_range(b, t0, t1);
    run_piece(One{}, t0, t1, b);
  }
  STAMP(2);
  if (last_b >= 0 && !((own >> last_b) & 1)) { publish(last_b, ro, rm, rlsum); last_b = -1; }
  if (held_b >= 0 && !((own >> held_b) & 1)) { publish(held_b, ho, hm, hl); held_b = -1; }
  STAMP(1);

  while (own) {
    const int b = __builtin_ctz(own); own &= own - 1;
    const unsigned tag = (unsigned)rl((int)gv, b) + 1u;
    const int s = rl(vS, b), L = rl(vL, b);
    unsigned long long pmk = __ballot(max(r_lo, s) < min(r_hi, s + L));
    unsigned long long smk = smask;
    float M0 = -INFINITY, M1 = -INFINITY, L0 = 0.f, L1 = 0.f, o00 = 0.f, o01 = 0.f, o10 = 0.f, o11 = 0.f;
    if (b == last_b) {
      pmk &= ~(1ull << j);
      M0 = rm[0]; M1 = rm[1]; L0 = rlsum[0]; L1 = rlsum[1]; o00 = ro[0][0]; o01 = ro[0][1]; o10 = ro[1][0]; o11 = ro[1][1];
    } else if (b == held_b) {
      pmk &= ~(1ull << j);
      M0 = hm[0]; M1 = hm[1]; L0 = hl[0]; L1 = hl[1]; o00 = ho[0][0]; o01 = ho[0][1]; o10 = ho[1][0]; o11 = ho[1][1];
    }
    auto merge = [&](float m0, float l0, float m1, float l1, float x0, float x1, float y0, float y1) {
      const float n0 = fmaxf(M0, m0), n1 = fmaxf(M1, m1);
      const float a0 = __builtin_amdgcn_exp2f(M0 - n0), b0 = __builtin_amdgcn_exp2f(m0 - n0);
      const float a1 = __builtin_amdgcn_exp2f(M1 - n1), b1 = __builtin_amdgcn_exp2f(m1 - n1);
      L0 = L0 * a0 + l0 * b0; L1 = L1 * a1 + l1 * b1;
      o00 = o00 * a0 + x0 * b0; o01 = o01 * a0 + x1 * b0;
      o10 = o10 * a1 + y0 * b1; o11 = o11 * a1 + y1 * b1;
      M0 = n0; M1 = n1;
    };
    typedef unsigned u32x2 __attribute__((ext_vector_type(2)));
    unsigned spins = 0;
    while (pmk | smk) {
      unsigned long long rdy = 0; bool sh_ready = false;
      const bool single = (pmk & (pmk - 1)) == 0 && smk == 0;
      if (!single || SPEC == 0) {
        u32x4 pml = {0u, 0u, 0u, 0u}; u32x2 fl = {tag, tag};
        if (pmk) {
          const int jj = ((pmk >> lane) & 1) ? lane : __builtin_ctzll(pmk);
          pml = __builtin_amdgcn_raw_buffer_load_b128(wsr, WS_PP + ((hg * 80 + jj + b + 1) * 4 + w) * PP_W + 2048 + 16, 0, 16);
        }
        if (smk) {
          const int jj = ((smk >> lane) & 1) ? lane : __builtin_ctzll(smk);
          fl = __builtin_amdgcn_raw_buffer_load_b64(wsr, WS_SP + ((hg * 64 + jj) * 4 + w) * SP_W + 16640 + b * 8, 0, 16);
        }
        rdy = pmk & __ballot(pml[1] == tag && pml[3] == tag);
        sh_ready = smk && __all(fl[0] == tag && fl[1] == tag);
      } else rdy = pmk;
      bool progress = false;
      if (rdy) {
        constexpr int NB = 4;
        unsigned rows[NB]; unsigned long long bitp[NB];
        unsigned long long tp = rdy;
#pragma unroll
        for (int k = 0; k < NB; ++k) {
          if (tp) { const int jj = __builtin_ctzll(tp); bitp[k] = tp & (~tp + 1); tp &= tp - 1; rows[k] = WS_PP + ((hg * 80 + jj + b + 1) * 4 + w) * PP_W; }
          else { bitp[k] = 0; rows[k] = rows[0]; }
        }
        u32x4 d0[NB], d1[NB], dm[NB];
        if (single) {
          d0[0] = __builtin_amdgcn_raw_buffer_load_b128(wsr, lane * 16, rows[0], 16);
          d1[0] = __builtin_amdgcn_raw_buffer_load_b128(wsr, 1024 + lane * 16, rows[0], 16);
          dm[0] = __builtin_amdgcn_raw_buffer_load_b128(wsr, 2048 + (lane & 1) * 16, rows[0], 16);
#pragma unroll
          for (int k = 1; k < NB; ++k) { d0[k] = d0[0]; d1[k] = d1[0]; dm[k] = dm[0]; }
        } else {
#pragma unroll
          for (int k = 0; k < NB; ++k) {
            d0[k] = __builtin_amdgcn_raw_buffer_load_b128(wsr, lane * 16, rows[k], 16);
            d1[k] = __builtin_amdgcn_raw_buffer_load_b128(wsr, 1024 + lane * 16, rows[k], 16);
            dm[k] = __builtin_amdgcn_raw_buffer_load_b128(wsr, 2048 + (lane & 1) * 16, rows[k], 16);
          }
        }
#pragma unroll
        for (int k = 0; k < NB; ++k) {
          const bool ok = d0[k][1] == tag && d0[k][3] == tag && d1[k][1] == tag && d1[k][3] == tag && dm[k][1] == tag && dm[k][3] == tag;
          if (bitp[k] && __all(ok)) {
            const float mv = __uint_as_float(dm[k][0]), lv = __uint_as_float(dm[k][2]);
            merge(rlf(mv, 0), rlf(lv, 0), rlf(mv, 1), rlf(lv, 1), __uint_as_float(d0[k][0]), __uint_as_float(d0[k][2]), __uint_as_float(d1[k][0]), __uint_as_float(d1[k][2]));
            pmk &= ~bitp[k]; progress = true;
          }
        }
      }
      if (sh_ready) {
        constexpr int NB = 6;
        unsigned rows[NB]; bool val[NB];
        unsigned long long ts = smk;
#pragma unroll
        for (int k = 0; k < NB; ++k) {
          if (ts) { const int jj = __builtin_ctzll(ts); ts &= ts - 1; rows[k] = WS_SP + ((hg * 64 + jj) * 4 + w) * SP_W; val[k] = true; }
          else { rows[k] = rows[0]; val[k] = false; }
        }
        u32x2 d0[NB], d1[NB]; u32x4 dm[NB];
#pragma unroll
        for (int k = 0; k < NB; ++k) {
          d0[k] = __builtin_amdgcn_raw_buffer_load_b64(wsr, b * 1024 + lane * 8, rows[k], 16);
          d1[k] = __builtin_amdgcn_raw_buffer_load_b64(wsr, b * 1024 + 512 + lane * 8, rows[k], 16);
          dm[k] = __builtin_amdgcn_raw_buffer_load_b128(wsr, 16384 + b * 16, rows[k], 16);
        }
#pragma unroll
        for (int k = 0; k < NB; ++k)
          if (val[k]) merge(__uint_as_float(dm[k][0]), __uint_as_float(dm[k][1]), __uint_as_float(dm[k][2]), __uint_as_float(dm[k][3]),
                            __uint_as_float(d0[k][0]), __uint_as_float(d0[k][1]), __uint_as_float(d1[k][0]), __uint_as_float(d1[k][1]));
        smk = ts;
        progress = true;
      }
      if (!progress) {
        if (++spins > (1u << 20)) break;
        __builtin_amdgcn_s_sleep(SLEEP);
      }
    }
    const float i0 = 1.f / L0, i1 = 1.f / L1;
    __hip_bfloat16* orow = p.out + (b * NH + 2 * h) * DH + pi_l;
    *(unsigned*)orow = pk_bf16(o00 * i0, o01 * i0);
    *(unsigned*)(orow + DH) = pk_bf16(o10 * i1, o11 * i1);
    if (lane == 0) __builtin_amdgcn_raw_buffer_store_b32(tag, wsr, WS_G + (h * 16 + b) * 4, 0, 16);
  }
  STAMP(3);
}

}

extern "C" void kernel_launch(void* const* d_in, const int* in_sizes, int n_in, void* d_out, int out_size, void* d_ws, size_t ws_size,
                              hipStream_t stream) {
  using namespace rattn;
  Params p;
  p.q = (const char*)d_in[0]; p.k = (const char*)d_in[1]; p.v = (const char*)d_in[2];
  p.bt = (const int*)d_in[3]; p.lens = (const int*)d_in[4];
  p.out = (__hip_bfloat16*)d_out; p.ws = (char*)d_ws;
  (void)hipFuncSetAttribute((const void*)radix_attn_kernel, hipFuncAttributeMaxDynamicSharedMemorySize, LDS_TOTAL);
  hipLaunchKernelGGL(radix_attn_kernel, dim3(256), dim3(256), LDS_TOTAL, stream, p);
}
```

```cpp
#include <hip/hip_runtime.h>
#include <hip/hip_bf16.h>
#include <stdint.h>

namespace rattn {

constexpr int NSEQ = 16, NH = 32, NKV = 16, DH = 128, NBLK = 256, BS = 16;
constexpr int LK = NBLK * BS;
#define MAPMODE 1
#define FCOST 90
#define THK 1152
#define THV 1152
#define RING 3
#define SPEC 1
#define SLEEP 12
#define PACE_LEAD 32
#define SHPUB 0
#define APACE_DELTA 117
#define KPOL "nt"
#define VPOL "nt"
constexpr int R = RING;
constexpr int STAGE = 32768;
constexpr int LDS_BT = R * STAGE;
constexpr int LDS_QB = LDS_BT + NSEQ * NBLK * 4;
constexpr int LDS_SC = LDS_QB + 4 * R * 512;
constexpr int LDS_MISC = LDS_SC + 4096;
constexpr int LDS_PROG = LDS_MISC + 64;
constexpr int LDS_TOTAL = LDS_PROG + 512;
static_assert(LDS_TOTAL <= 160 * 1024, "LDS");

constexpr unsigned WS_G = 0;
constexpr unsigned WS_PROG = 2048;
constexpr unsigned WS_PP = 4096;
constexpr unsigned PP_W = 2176;
constexpr unsigned WS_SP = 4u << 20;
constexpr unsigned SP_W = 16384 + 256 + 128;
constexpr unsigned WS_END = WS_SP + 4u * 64u * 4u * SP_W;
constexpr unsigned WS_STAMP = 64u << 20;

typedef unsigned u32x4 __attribute__((ext_vector_type(4)));
typedef float f32x4 __attribute__((ext_vector_type(4)));
typedef float f32x2 __attribute__((ext_vector_type(2)));
typedef __bf16 bf16x8 __attribute__((ext_vector_type(8)));
typedef __bf16 bf16x2 __attribute__((ext_vector_type(2)));
typedef short s16x4 __attribute__((ext_vector_type(4)));
typedef int i32x4 __attribute__((ext_vector_type(4)));
#define LAS __attribute__((address_space(3)))
#define GAS __attribute__((address_space(1)))

constexpr float C2 = 0.088388347648318440f * 1.4426950408889634f;

__device__ __forceinline__ int rfl(int x) { return __builtin_amdgcn_readfirstlane(x); }
__device__ __forceinline__ int rl(int x, int l) { return __builtin_amdgcn_readlane(x, l); }
__device__ __forceinline__ float rlf(float x, int l) { return __builtin_bit_cast(float, __builtin_amdgcn_readlane(__builtin_bit_cast(int, x), l)); }

__device__ __forceinline__ void dma16(const void* sbase, unsigned voff, unsigned dst) {
  unsigned keep;
  asm volatile("s_mov_b32 %0, m0\n\ts_mov_b32 m0, %3\n\ts_nop 0\n\tglobal_load_lds_dwordx4 %1, %2 nt\n\ts_mov_b32 m0, %0"
               : "=&s"(keep) : "v"(voff), "s"(sbase), "s"(dst) : "memory");
}
__device__ __forceinline__ void dma16_keep(const void* sbase, unsigned voff, unsigned dst) {
  unsigned keep;
  asm volatile("s_mov_b32 %0, m0\n\ts_mov_b32 m0, %3\n\ts_nop 0\n\tglobal_load_lds_dwordx4 %1, %2\n\ts_mov_b32 m0, %0"
               : "=&s"(keep) : "v"(voff), "s"(sbase), "s"(dst) : "memory");
}
#define DMA_STAGE_BODY(KP, VP) \
  asm volatile("s_mov_b32 %0, m0\n\ts_mov_b32 m0, %3\n\ts_nop 0\n\t" \
               "global_load_lds_dwordx4 %5, %1 " KP "\n\t" \
               "global_load_lds_dwordx4 %6, %1 offset:1024 " KP "\n\t" \
               "global_load_lds_dwordx4 %7, %1 offset:2048 " KP "\n\t" \
               "global_load_lds_dwordx4 %8, %1 offset:3072 " KP "\n\t" \
               "s_mov_b32 m0, %4\n\ts_nop 0\n\t" \
               "global_load_lds_dwordx4 %9, %2 " VP "\n\t" \
               "global_load_lds_dwordx4 %10, %2 offset:1024 " VP "\n\t" \
               "global_load_lds_dwordx4 %11, %2 offset:2048 " VP "\n\t" \
               "global_load_lds_dwordx4 %12, %2 offset:3072 " VP "\n\t" \
               "s_mov_b32 m0, %0" \
               : "=&s"(keep) \
               : "s"(sK), "s"(sV), "s"(dstK), "s"(dstV), "v"(k0), "v"(k1), "v"(k2), "v"(k3), "v"(v0), "v"(v1), "v"(v2), "v"(v3) \
               : "memory")
__device__ __forceinline__ void dma_stage(int pol, const void* sK, const void* sV, unsigned dstK, unsigned dstV,
                                          unsigned k0, unsigned k1, unsigned k2, unsigned k3,
                                          unsigned v0, unsigned v1, unsigned v2, unsigned v3) {
  unsigned keep;
  if (pol == 0) DMA_STAGE_BODY("nt", "nt");
  else if (pol == 1) DMA_STAGE_BODY("", "nt");
  else if (pol == 2) DMA_STAGE_BODY("nt", "");
  else DMA_STAGE_BODY("", "");
}
__device__ __forceinline__ void dma16_sc1(const void* sbase, unsigned voff, unsigned dst) {
  unsigned keep;
  asm volatile("s_mov_b32 %0, m0\n\ts_mov_b32 m0, %3\n\ts_nop 0\n\tglobal_load_lds_dwordx4 %1, %2 sc1\n\ts_mov_b32 m0, %0"
               : "=&s"(keep) : "v"(voff), "s"(sbase), "s"(dst) : "memory");
}
template <int CTRL> __device__ __forceinline__ int dppi(int x) { return __builtin_amdgcn_update_dpp(0, x, CTRL, 0xf, 0xf, true); }
template <int CTRL> __device__ __forceinline__ int dppi_keep(int old, int x) { return __builtin_amdgcn_update_dpp(old, x, CTRL, 0xf, 0xf, false); }

__device__ __forceinline__ float xg_max(float x) {
  auto s = __builtin_amdgcn_permlane16_swap(__float_as_uint(x), __float_as_uint(x), false, false);
  x = fmaxf(__uint_as_float(s[0]), __uint_as_float(s[1]));
  auto t = __builtin_amdgcn_permlane32_swap(__float_as_uint(x), __float_as_uint(x), false, false);
  return fmaxf(__uint_as_float(t[0]), __uint_as_float(t[1]));
}
__device__ __forceinline__ float xg_sum(float x) {
  auto s = __builtin_amdgcn_permlane16_swap(__float_as_uint(x), __float_as_uint(x), false, false);
  x = __uint_as_float(s[0]) + __uint_as_float(s[1]);
  auto t = __builtin_amdgcn_permlane32_swap(__float_as_uint(x), __float_as_uint(x), false, false);
  return __uint_as_float(t[0]) + __uint_as_float(t[1]);
}
__device__ __forceinline__ unsigned pk_bf16(float a, float b) {
  bf16x2 v = {(__bf16)a, (__bf16)b};
  return __builtin_bit_cast(unsigned, v);
}

struct One { static constexpr int value = 1; };
struct Two { static constexpr int value = 2; };

struct Params {
  const char* q; const char* k; const char* v; const int* bt; const int* lens; __hip_bfloat16* out; char* ws;
};

__global__ void __launch_bounds__(256, 2) radix_attn_kernel(Params p) {
  extern __shared__ __attribute__((aligned(16))) char smem[];
  LAS char* lds = (LAS char*)smem;
  const int tid = threadIdx.x, lane = tid & 63, w = rfl(tid >> 6);
  const int gidx = blockIdx.x;
  const int hg = (gidx >> 3) & 3, j = (gidx & 7) | ((gidx >> 5) << 3);
  const int h = hg * 4 + w;
  const int c16 = lane & 15, g4 = lane >> 4;
#define STAMP(i) do { } while (0)
  STAMP(0);
#define WSOFF 0
  const auto wsr = __builtin_amdgcn_make_buffer_rsrc(p.ws + (size_t)WSOFF, 0, WS_END, 0x00020000);

  const int lenraw = p.lens[c16];
  const unsigned gv = __builtin_amdgcn_raw_buffer_load_b32(wsr, WS_G + (h * 16 + c16) * 4, 0, 16);
  int pm;
  {
    const i32x4* bt4 = (const i32x4*)p.bt;
    const i32x4 r0 = bt4[lane];
    i32x4 rr[4];
#pragma unroll
    for (int i = 0; i < 4; ++i) rr[i] = bt4[(4 * w + i) * 64 + lane];
    bool n0 = false, n1 = false, n2 = false, n3 = false;
#pragma unroll
    for (int i = 0; i < 4; ++i) {
      n0 |= rr[i][0] != r0[0]; n1 |= rr[i][1] != r0[1]; n2 |= rr[i][2] != r0[2]; n3 |= rr[i][3] != r0[3];
      *(LAS i32x4*)(lds + LDS_BT + ((4 * w + i) * 256 + 4 * lane) * 4) = rr[i];
    }
    const int fc = n0 ? 0 : n1 ? 1 : n2 ? 2 : n3 ? 3 : 4;
    const unsigned long long mm = __ballot(fc < 4);
    int col = NBLK;
    if (mm) { const int fl = __builtin_ctzll(mm); col = 4 * fl + rl(fc, fl); }
    if (lane == 0) *(LAS int*)(lds + LDS_MISC + 4 * w) = col;
    asm volatile("s_waitcnt lgkmcnt(0)\n\ts_barrier" ::: "memory");
    const i32x4 mc = *(LAS i32x4*)(lds + LDS_MISC);
    pm = rfl(min(min(mc[0], mc[1]), min(mc[2], mc[3])));
  }
  asm volatile("" :: "v"(gv));
  const bool zq_l = lenraw <= 0;
  const int len_l = zq_l ? LK : min(lenraw, LK);
  int mn = len_l;
  mn = min(mn, dppi_keep<0x128>(mn, mn)); mn = min(mn, dppi_keep<0x124>(mn, mn));
  mn = min(mn, dppi_keep<0x122>(mn, mn)); mn = min(mn, dppi_keep<0x121>(mn, mn));
  const int P = min(pm, rfl(mn) >> 4);
  const int tsh = P * BS;
  const int vL = len_l - tsh;
  int inc = vL;
  inc += dppi<0x111>(inc); inc += dppi<0x112>(inc); inc += dppi<0x114>(inc); inc += dppi<0x118>(inc);
  const int lsum = rl(inc, 15);
  const int FX = min(FCOST, (tsh + lsum) >> 7);
  int n_sh = 0;
  if (tsh > 0) {
    const int n = lane + 1;
    n_sh = __builtin_ctzll(__ballot(n * (tsh + lsum + n * FX - 64 * FX) >= 64 * tsh)) + 1;
  }
  const int wsh = tsh + n_sh * FX;
  const int vS = wsh + inc - vL;
  const int N = wsh + lsum;
  const int x0 = (j * N) >> 6, x1 = ((j + 1) * N) >> 6;
  const unsigned zmask = (unsigned)(__ballot(zq_l) & 0xffffull);
  const unsigned umask = (unsigned)(__ballot(max(x0, vS) < min(x1, vS + vL)) & 0xffffull);
  const int sh_t0 = min(tsh, x0 - j * FX), sh_t1 = j < n_sh ? min(tsh, x1 - (j + 1) * FX) : 0;
  const bool has_sh = j < n_sh && sh_t1 > sh_t0;

  unsigned voffK[4], voffV[4];
#pragma unroll
  for (int i = 0; i < 4; ++i) {
    const int t = 4 * w + i;
    voffK[i] = g4 * 256 + ((c16 ^ t) << 4) + t * 4096 - i * 1024 + 4096;
    voffV[i] = g4 * 256 + ((c16 ^ (2 * (t & 7))) << 4) + t * 4096 - i * 1024 + 4096;
  }
  const unsigned kaddr0 = w * 256 + c16 * 1024 + ((g4 ^ c16) << 4);
  unsigned vaddr0;
  { const int tq = 4 * g4 + (c16 >> 2), pp = lane & 3; vaddr0 = 16384 + tq * 1024 + w * 256 + ((tq & 7) << 5) + ((pp >> 1) << 4) + ((pp & 1) << 3); }
  const char* kbase = p.k + hg * 1024 - 4096;
  const char* vbase = p.v + hg * 1024 - 4096;

  unsigned i_mask = umask; int i_row, i_t0, i_t1, i_page; bool i_more = true;
  int n_iss = 0, i_slot = 0;
  auto unit_range = [&](int b, int& t0, int& t1) {
    const int s = rl(vS, b), L = rl(vL, b);
    t0 = tsh + max(x0, s) - s; t1 = tsh + min(x1, s + L) - s;
  };
  auto q_dma_private = [&](int b, int slot) {
    if (lane < 32) dma16_keep(p.q + b * 8192 + h * 512, lane * 16, LDS_QB + (w * R + slot) * 512);
  };
  if (has_sh) {
    i_row = 0; i_t0 = sh_t0; i_t1 = sh_t1;
#pragma unroll
    for (int i = 0; i < 8; ++i)
      dma16_keep(p.q + i * 16384 + h * 512, (lane >> 5) * 8192 + (lane & 31) * 16, (R - 1) * STAGE + w * 8192 + i * 1024);
  } else if (i_mask) {
    i_row = __builtin_ctz(i_mask); i_mask &= i_mask - 1; unit_range(i_row, i_t0, i_t1);
    q_dma_private(i_row, 0);
  } else { i_more = false; i_row = 0; i_t0 = 0; i_t1 = 0; }
  i_page = i_t0 >> 4;
  int i_pg0 = i_page, vpg = *(LAS int*)(lds + LDS_BT + (i_row * NBLK + min(i_page + lane, NBLK - 1)) * 4);
  auto issue_stage = [&](bool steady) {
    const int phys = rl(vpg, i_page - i_pg0);
    const int lo = i_t0 - i_page * 16, hi = i_t1 - i_page * 16;
    const size_t pb = (size_t)(unsigned)phys * 65536;
    const int pol = (phys >= THK ? 1 : 0) | (phys >= THV ? 2 : 0);
    const unsigned dst = i_slot * STAGE + 4 * w * 1024;
    if (lo <= 0 && hi >= 16) {
      dma_stage(pol, kbase + pb, vbase + pb, dst, dst + 16384, voffK[0], voffK[1], voffK[2], voffK[3], voffV[0], voffV[1], voffV[2], voffV[3]);
    } else {
      unsigned ok[4], ov[4];
#pragma unroll
      for (int i = 0; i < 4; ++i) {
        const int d = (min(max(4 * w + i, lo), hi - 1) - (4 * w + i)) * 4096;
        ok[i] = voffK[i] + d; ov[i] = voffV[i] + d;
      }
      dma_stage(pol, kbase + pb, vbase + pb, dst, dst + 16384, ok[0], ok[1], ok[2], ok[3], ov[0], ov[1], ov[2], ov[3]);
    }
    ++n_iss; i_slot = (i_slot + 1 == R) ? 0 : i_slot + 1;
    ++i_page;
    if (i_page * 16 >= i_t1) {
      if (i_mask) {
        i_row = __builtin_ctz(i_mask); i_mask &= i_mask - 1; unit_range(i_row, i_t0, i_t1); i_page = i_t0 >> 4;
        q_dma_private(i_row, i_slot);
        i_pg0 = i_page; vpg = *(LAS int*)(lds + LDS_BT + (i_row * NBLK + min(i_page + lane, NBLK - 1)) * 4);
      } else i_more = false;
    } else if (i_page - i_pg0 == 64) {
      i_pg0 = i_page; vpg = *(LAS int*)(lds + LDS_BT + (i_row * NBLK + min(i_page + lane, NBLK - 1)) * 4);
    }
  };
#pragma unroll 1
  for (int s = 0; s < R - 1; ++s) if (i_more) issue_stage(false);

  const int r_lo = (lane * N) >> 6, r_hi = ((lane + 1) * N) >> 6;
  unsigned own = 0;
  {
    unsigned mrem = umask;
    while (mrem) {
      const int b = __builtin_ctz(mrem); mrem &= mrem - 1;
      const int s = rl(vS, b), L = rl(vL, b);
      unsigned long long pmk = __ballot(max(r_lo, s) < min(r_hi, s + L));
      const int cnt = __builtin_popcountll(pmk);
      int idx = w % cnt;
      while (idx--) pmk &= pmk - 1;
      if (__builtin_ctzll(pmk) == j) own |= 1u << b;
    }
    const unsigned zl = (unsigned)(__ballot(vL == 0) & 0xffffull);
    if ((j & 3) == 0 && ((zl >> (j >> 2)) & 1)) own |= 1u << (j >> 2);
  }
  const unsigned long long smask = __ballot(lane < n_sh && min(tsh, r_hi - (lane + 1) * FX) > min(tsh, r_lo - lane * FX));

  int n_comp = 0, c_slot = 0;
  unsigned c_mask = umask;
  float ro[2][2] = {{0.f, 0.f}, {0.f, 0.f}}, rm[2] = {-INFINITY, -INFINITY}, rlsum[2] = {0.f, 0.f};
  int last_b = -1;
  float ho[2][2] = {{0.f, 0.f}, {0.f, 0.f}}, hm[2] = {-INFINITY, -INFINITY}, hl[2] = {0.f, 0.f};
  int held_b = -1;
  const int pi_l = 2 * lane;
  auto store_flags = [&]() {
    if (lane < 16) {
      typedef unsigned u32x2 __attribute__((ext_vector_type(2)));
      const u32x2 f = {gv + 1u, gv + 1u};
      __builtin_amdgcn_raw_buffer_store_b64(f, wsr, WS_SP + ((hg * 64 + j) * 4 + w) * SP_W + 16640 + lane * 8, 0, 16);
    }
  };

  auto wait_stage = [&]() {
    const int young = n_iss - n_comp - 1;
    if (young >= R - 2) {
      if (R == 2) asm volatile("s_waitcnt vmcnt(0) lgkmcnt(0)\n\ts_barrier" ::: "memory");
      else if (R == 3) asm volatile("s_waitcnt vmcnt(8) lgkmcnt(0)\n\ts_barrier" ::: "memory");
      else asm volatile("s_waitcnt vmcnt(16) lgkmcnt(0)\n\ts_barrier" ::: "memory");
    } else if (R == 4 && young == 1) asm volatile("s_waitcnt vmcnt(8) lgkmcnt(0)\n\ts_barrier" ::: "memory");
    else asm volatile("s_waitcnt vmcnt(0) lgkmcnt(0)\n\ts_barrier" ::: "memory");
  };

  auto publish = [&](int b, const float (&o)[2][2], const float (&mm)[2], const float (&ll)[2]) {
    const unsigned tag = (unsigned)rl((int)gv, b) + 1u;
    const unsigned slot = WS_PP + ((hg * 80 + j + b + 1) * 4 + w) * PP_W;
    const u32x4 g0 = {__float_as_uint(o[0][0]), tag, __float_as_uint(o[0][1]), tag};
    const u32x4 g1 = {__float_as_uint(o[1][0]), tag, __float_as_uint(o[1][1]), tag};
    __builtin_amdgcn_raw_buffer_store_b128(g0, wsr, slot + lane * 16, 0, 16);
    __builtin_amdgcn_raw_buffer_store_b128(g1, wsr, slot + 1024 + lane * 16, 0, 16);
    if (lane < 2) {
      const u32x4 gm = {__float_as_uint(lane ? mm[1] : mm[0]), tag, __float_as_uint(lane ? ll[1] : ll[0]), tag};
      __builtin_amdgcn_raw_buffer_store_b128(gm, wsr, slot + 2048 + lane * 16, 0, 16);
    }
  };
  auto run_piece = [&](auto nq_tag, int t0, int t1, int b) {
    constexpr int NQ = decltype(nq_tag)::value;
    bf16x8 qf[NQ][4];
    f32x4 acc[NQ][8];
    float m[NQ], l[NQ];
#pragma unroll
    for (int qb = 0; qb < NQ; ++qb) {
      m[qb] = -INFINITY; l[qb] = 0.f;
#pragma unroll
      for (int k = 0; k < 8; ++k) acc[qb][k] = f32x4{0.f, 0.f, 0.f, 0.f};
    }
    if (NQ == 2) {
      if (n_iss >= 2) { if (R == 2) asm volatile("s_waitcnt vmcnt(8)" ::: "memory"); else if (R == 3) asm volatile("s_waitcnt vmcnt(16)" ::: "memory"); else asm volatile("s_waitcnt vmcnt(16)" ::: "memory"); }
      else asm volatile("s_waitcnt vmcnt(8)" ::: "memory");
#pragma unroll
      for (int qb = 0; qb < NQ; ++qb) {
        const bool z = (zmask >> (8 * qb + (c16 >> 1))) & 1;
#pragma unroll
        for (int s = 0; s < 4; ++s) {
          u32x4 x = *(LAS u32x4*)(lds + (R - 1) * STAGE + w * 8192 + (16 * qb + c16) * 256 + (4 * s + g4) * 16);
          if (z) x = u32x4{0u, 0u, 0u, 0u};
          qf[qb][s] = __builtin_bit_cast(bf16x8, x);
        }
      }
    }
    bool first = true;
    for (int page = t0 >> 4; page * 16 < t1; ++page) {
      wait_stage();
      if (i_more) issue_stage(true);
      if (NQ == 1 && first) {
        const bool z = (c16 >= 2) || ((zmask >> b) & 1);
#pragma unroll
        for (int s = 0; s < 4; ++s) {
          u32x4 x = *(LAS u32x4*)(lds + LDS_QB + (w * R + c_slot) * 512 + (c16 & 1) * 256 + (4 * s + g4) * 16);
          if (z) x = u32x4{0u, 0u, 0u, 0u};
          qf[0][s] = __builtin_bit_cast(bf16x8, x);
        }
      }
      first = false;
      const unsigned sb = c_slot * STAGE;
      bf16x8 kf[4];
#pragma unroll
      for (int s = 0; s < 4; ++s) kf[s] = *(LAS bf16x8*)(lds + sb + (kaddr0 ^ (s << 6)));
      s16x4 vf[8];
#pragma unroll
      for (int k = 0; k < 8; ++k) vf[k] = __builtin_amdgcn_ds_read_tr16_b64_v4i16((LAS s16x4*)(lds + sb + (vaddr0 ^ (k << 5))));
      f32x4 S[NQ];
#pragma unroll
      for (int qb = 0; qb < NQ; ++qb) {
        S[qb] = f32x4{0.f, 0.f, 0.f, 0.f};
#pragma unroll
        for (int s = 0; s < 4; ++s) S[qb] = __builtin_amdgcn_mfma_f32_16x16x32_bf16(kf[s], qf[qb][s], S[qb], 0, 0, 0);
      }
      const int lo = t0 - page * 16, hi = t1 - page * 16;
      if (lo > 0 || hi < 16) {
#pragma unroll
        for (int qb = 0; qb < NQ; ++qb)
#pragma unroll
          for (int r = 0; r < 4; ++r) { const int t = 4 * g4 + r; S[qb][r] = (t >= lo && t < hi) ? S[qb][r] : -INFINITY; }
      }
      s16x4 pb[NQ];
      bool grow = false;
      float alpha[NQ];
#pragma unroll
      for (int qb = 0; qb < NQ; ++qb) {
        float mx = fmaxf(fmaxf(S[qb][0], S[qb][1]), fmaxf(S[qb][2], S[qb][3]));
        mx = xg_max(mx);
        const float mn2 = fmaxf(m[qb], mx);
        grow |= mn2 > m[qb];
        alpha[qb] = __builtin_amdgcn_exp2f((m[qb] - mn2) * C2);
        m[qb] = mn2;
        const float mc = -mn2 * C2;
        const float p0 = __builtin_amdgcn_exp2f(fmaf(S[qb][0], C2, mc)), p1 = __builtin_amdgcn_exp2f(fmaf(S[qb][1], C2, mc));
        const float p2 = __builtin_amdgcn_exp2f(fmaf(S[qb][2], C2, mc)), p3 = __builtin_amdgcn_exp2f(fmaf(S[qb][3], C2, mc));
        l[qb] = fmaf(l[qb], alpha[qb], (p0 + p1) + (p2 + p3));
        typedef unsigned u32x2 __attribute__((ext_vector_type(2)));
        pb[qb] = __builtin_bit_cast(s16x4, u32x2{pk_bf16(p0, p1), pk_bf16(p2, p3)});
      }
      if (__any(grow)) {
#pragma unroll
        for (int qb = 0; qb < NQ; ++qb)
#pragma unroll
          for (int k = 0; k < 8; ++k) acc[qb][k] *= alpha[qb];
      }
#pragma unroll
      for (int qb = 0; qb < NQ; ++qb)
#pragma unroll
        for (int k = 0; k < 8; ++k) acc[qb][k] = __builtin_amdgcn_mfma_f32_16x16x16bf16_1k(vf[k], pb[qb], acc[qb][k], 0, 0, 0);
      ++n_comp; c_slot = (c_slot + 1 == R) ? 0 : c_slot + 1;
    }
    if constexpr (NQ == 2) {
      STAMP(6);
      const unsigned slot = WS_SP + ((hg * 64 + j) * 4 + w) * SP_W;
#pragma unroll
      for (int qb = 0; qb < 2; ++qb) {
        const float lt = xg_sum(l[qb]);
        const unsigned rowo = slot + (16 * qb + c16) * 512 + g4 * 16;
#pragma unroll
        for (int k = 0; k < 8; ++k)
          __builtin_amdgcn_raw_buffer_store_b128(__builtin_bit_cast(u32x4, acc[qb][k]), wsr, rowo + k * 64, 0, SHPUB ? 0 : 16);
        if (g4 == 0) {
          typedef unsigned u32x2 __attribute__((ext_vector_type(2)));
          const u32x2 a = {__float_as_uint(m[qb] * C2), __float_as_uint(lt)};
          __builtin_amdgcn_raw_buffer_store_b64(a, wsr, slot + 16384 + (16 * qb + c16) * 8, 0, SHPUB ? 0 : 16);
        }
      }
      asm volatile("s_waitcnt vmcnt(0)" ::: "memory");
      { const unsigned long long c0 = __builtin_amdgcn_s_memrealtime(); while (__builtin_amdgcn_s_memrealtime() - c0 < 250ull) __builtin_amdgcn_s_sleep(1); }
      store_flags();
      STAMP(7);
    } else {
      const float lt = xg_sum(l[0]);
      LAS char* sc = lds + LDS_SC + w * 1024;
      if (c16 < 2) {
#pragma unroll
        for (int k = 0; k < 8; ++k) *(LAS f32x4*)(sc + c16 * 512 + (16 * k + 4 * g4) * 4) = acc[0][k];
      }
      asm volatile("s_waitcnt lgkmcnt(0)" ::: "memory");
      const f32x2 a0 = *(LAS f32x2*)(sc + pi_l * 4), a1 = *(LAS f32x2*)(sc + 512 + pi_l * 4);
      if (last_b >= 0) {
        if (held_b >= 0) publish(held_b, ho, hm, hl);
        held_b = last_b;
#pragma unroll
        for (int r = 0; r < 2; ++r) { ho[r][0] = ro[r][0]; ho[r][1] = ro[r][1]; hm[r] = rm[r]; hl[r] = rlsum[r]; }
      }
      ro[0][0] = a0[0]; ro[0][1] = a0[1]; ro[1][0] = a1[0]; ro[1][1] = a1[1];
      rm[0] = rlf(m[0], 0) * C2; rm[1] = rlf(m[0], 1) * C2;
      rlsum[0] = rlf(lt, 0); rlsum[1] = rlf(lt, 1);
      last_b = b;
    }
  };
  if (has_sh) run_piece(Two{}, sh_t0, sh_t1, 0);
  while (c_mask) {
    const int b = __builtin_ctz(c_mask); c_mask &= c_mask - 1;
    int t0, t1; unit_range(b, t0, t1);
    run_piece(One{}, t0, t1, b);
  }
  STAMP(2);
  if (last_b >= 0 && !((own >> last_b) & 1)) { publish(last_b, ro, rm, rlsum); last_b = -1; }
  if (held_b >= 0 && !((own >> held_b) & 1)) { publish(held_b, ho, hm, hl); held_b = -1; }
  STAMP(1);

  while (own) {
    const int b = __builtin_ctz(own); own &= own - 1;
    const unsigned tag = (unsigned)rl((int)gv, b) + 1u;
    const int s = rl(vS, b), L = rl(vL, b);
    unsigned long long pmk = __ballot(max(r_lo, s) < min(r_hi, s + L));
    unsigned long long smk = smask;
    float M0 = -INFINITY, M1 = -INFINITY, L0 = 0.f, L1 = 0.f, o00 = 0.f, o01 = 0.f, o10 = 0.f, o11 = 0.f;
    if (b == last_b) {
      pmk &= ~(1ull << j);
      M0 = rm[0]; M1 = rm[1]; L0 = rlsum[0]; L1 = rlsum[1]; o00 = ro[0][0]; o01 = ro[0][1]; o10 = ro[1][0]; o11 = ro[1][1];
    } else if (b == held_b) {
      pmk &= ~(1ull << j);
      M0 = hm[0]; M1 = hm[1]; L0 = hl[0]; L1 = hl[1]; o00 = ho[0][0]; o01 = ho[0][1]; o10 = ho[1][0]; o11 = ho[1][1];
    }
    auto merge = [&](float m0, float l0, float m1, float l1, float x0, float x1, float y0, float y1) {
      const float n0 = fmaxf(M0, m0), n1 = fmaxf(M1, m1);
      const float a0 = __builtin_amdgcn_exp2f(M0 - n0), b0 = __builtin_amdgcn_exp2f(m0 - n0);
      const float a1 = __builtin_amdgcn_exp2f(M1 - n1), b1 = __builtin_amdgcn_exp2f(m1 - n1);
      L0 = L0 * a0 + l0 * b0; L1 = L1 * a1 + l1 * b1;
      o00 = o00 * a0 + x0 * b0; o01 = o01 * a0 + x1 * b0;
      o10 = o10 * a1 + y0 * b1; o11 = o11 * a1 + y1 * b1;
      M0 = n0; M1 = n1;
    };
    typedef unsigned u32x2 __attribute__((ext_vector_type(2)));
    unsigned spins = 0;
    while (pmk | smk) {
      unsigned long long rdy = 0; bool sh_ready = false;
      const bool single = (pmk & (pmk - 1)) == 0 && smk == 0;
      if (!single || SPEC == 0) {
        u32x4 pml = {0u, 0u, 0u, 0u}; u32x2 fl = {tag, tag};
        if (pmk) {
          const int jj = ((pmk >> lane) & 1) ? lane : __builtin_ctzll(pmk);
          pml = __builtin_amdgcn_raw_buffer_load_b128(wsr, WS_PP + ((hg * 80 + jj + b + 1) * 4 + w) * PP_W + 2048 + 16, 0, 16);
        }
        if (smk) {
          const int jj = ((smk >> lane) & 1) ? lane : __builtin_ctzll(smk);
          fl = __builtin_amdgcn_raw_buffer_load_b64(wsr, WS_SP + ((hg * 64 + jj) * 4 + w) * SP_W + 16640 + b * 8, 0, 16);
        }
        rdy = pmk & __ballot(pml[1] == tag && pml[3] == tag);
        sh_ready = smk && __all(fl[0] == tag && fl[1] == tag);
      } else rdy = pmk;
      bool progress = false;
      if (rdy) {
        constexpr int NB = 4;
        unsigned rows[NB]; unsigned long long bitp[NB];
        unsigned long long tp = rdy;
#pragma unroll
        for (int k = 0; k < NB; ++k) {
          if (tp) { const int jj = __builtin_ctzll(tp); bitp[k] = tp & (~tp + 1); tp &= tp - 1; rows[k] = WS_PP + ((hg * 80 + jj + b + 1) * 4 + w) * PP_W; }
          else { bitp[k] = 0; rows[k] = rows[0]; }
        }
        u32x4 d0[NB], d1[NB], dm[NB];
        if (single) {
          d0[0] = __builtin_amdgcn_raw_buffer_load_b128(wsr, lane * 16, rows[0], 16);
          d1[0] = __builtin_amdgcn_raw_buffer_load_b128(wsr, 1024 + lane * 16, rows[0], 16);
          dm[0] = __builtin_amdgcn_raw_buffer_load_b128(wsr, 2048 + (lane & 1) * 16, rows[0], 16);
#pragma unroll
          for (int k = 1; k < NB; ++k) { d0[k] = d0[0]; d1[k] = d1[0]; dm[k] = dm[0]; }
        } else {
#pragma unroll
          for (int k = 0; k < NB; ++k) {
            d0[k] = __builtin_amdgcn_raw_buffer_load_b128(wsr, lane * 16, rows[k], 16);
            d1[k] = __builtin_amdgcn_raw_buffer_load_b128(wsr, 1024 + lane * 16, rows[k], 16);
            dm[k] = __builtin_amdgcn_raw_buffer_load_b128(wsr, 2048 + (lane & 1) * 16, rows[k], 16);
          }
        }
#pragma unroll
        for (int k = 0; k < NB; ++k) {
          const bool ok = d0[k][1] == tag && d0[k][3] == tag && d1[k][1] == tag && d1[k][3] == tag && dm[k][1] == tag && dm[k][3] == tag;
          if (bitp[k] && __all(ok)) {
            const float mv = __uint_as_float(dm[k][0]), lv = __uint_as_float(dm[k][2]);
            merge(rlf(mv, 0), rlf(lv, 0), rlf(mv, 1), rlf(lv, 1), __uint_as_float(d0[k][0]), __uint_as_float(d0[k][2]), __uint_as_float(d1[k][0]), __uint_as_float(d1[k][2]));
            pmk &= ~bitp[k]; progress = true;
          }
        }
      }
      if (sh_ready) {
        constexpr int NB = 6;
        unsigned rows[NB]; bool val[NB];
        unsigned long long ts = smk;
#pragma unroll
        for (int k = 0; k < NB; ++k) {
          if (ts) { const int jj = __builtin_ctzll(ts); ts &= ts - 1; rows[k] = WS_SP + ((hg * 64 + jj) * 4 + w) * SP_W; val[k] = true; }
          else { rows[k] = rows[0]; val[k] = false; }
        }
        u32x2 d0[NB], d1[NB]; u32x4 dm[NB];
#pragma unroll
        for (int k = 0; k < NB; ++k) {
          d0[k] = __builtin_amdgcn_raw_buffer_load_b64(wsr, b * 1024 + lane * 8, rows[k], 16);
          d1[k] = __builtin_amdgcn_raw_buffer_load_b64(wsr, b * 1024 + 512 + lane * 8, rows[k], 16);
          dm[k] = __builtin_amdgcn_raw_buffer_load_b128(wsr, 16384 + b * 16, rows[k], 16);
        }
#pragma unroll
        for (int k = 0; k < NB; ++k)
          if (val[k]) merge(__uint_as_float(dm[k][0]), __uint_as_float(dm[k][1]), __uint_as_float(dm[k][2]), __uint_as_float(dm[k][3]),
                            __uint_as_float(d0[k][0]), __uint_as_float(d0[k][1]), __uint_as_float(d1[k][0]), __uint_as_float(d1[k][1]));
        smk = ts;
        progress = true;
      }
      if (!progress) {
        if (++spins > (1u << 20)) break;
        __builtin_amdgcn_s_sleep(SLEEP);
      }
    }
    const float i0 = 1.f / L0, i1 = 1.f / L1;
    __hip_bfloat16* orow = p.out + (b * NH + 2 * h) * DH + pi_l;
    *(unsigned*)orow = pk_bf16(o00 * i0, o01 * i0);
    *(unsigned*)(orow + DH) = pk_bf16(o10 * i1, o11 * i1);
    if (lane == 0) __builtin_amdgcn_raw_buffer_store_b32(tag, wsr, WS_G + (h * 16 + b) * 4, 0, 16);
  }
  STAMP(3);
}

}

extern "C" void kernel_launch(void* const* d_in, const int* in_sizes, int n_in, void* d_out, int out_size, void* d_ws, size_t ws_size,
                              hipStream_t stream) {
  using namespace rattn;
  Params p;
  p.q = (const char*)d_in[0]; p.k = (const char*)d_in[1]; p.v = (const char*)d_in[2];
  p.bt = (const int*)d_in[3]; p.lens = (const int*)d_in[4];
  p.out = (__hip_bfloat16*)d_out; p.ws = (char*)d_ws;
  (void)hipFuncSetAttribute((const void*)radix_attn_kernel, hipFuncAttributeMaxDynamicSharedMemorySize, LDS_TOTAL);
  hipLaunchKernelGGL(radix_attn_kernel, dim3(256), dim3(256), LDS_TOTAL, stream, p);
}
```

```cpp
#include <hip/hip_runtime.h>
#include <hip/hip_bf16.h>
#include <stdint.h>

namespace rattn {

constexpr int NSEQ = 16, NH = 32, NKV = 16, DH = 128, NBLK = 256, BS = 16;
constexpr int LK = NBLK * BS;
#define HRHO 50
#define MAPMODE 1
#define SLEEP 6
#define KVPOL "nt"
constexpr int R = 3;
constexpr int STAGE = 32768;
constexpr int RH = 4;
constexpr int RING_BYTES = 4 * RH * 8192;
constexpr int LDS_BT = RING_BYTES;
constexpr int LDS_QB = LDS_BT + NSEQ * NBLK * 4;
constexpr int LDS_SC = LDS_QB + 4 * R * 512;
constexpr int LDS_MISC = LDS_SC + 4096;
constexpr int LDS_TOTAL = LDS_MISC + 64;
static_assert(LDS_TOTAL <= 160 * 1024 && LDS_MISC - LDS_QB >= 8192 && RING_BYTES >= R * STAGE, "LDS");

constexpr unsigned WS_G = 0;
constexpr unsigned WS_PP = 4096;
constexpr unsigned PP_W = 2176;
constexpr unsigned WS_HP = 4u << 20;
constexpr unsigned HP_W = 32768 + 512;
constexpr unsigned WS_END = WS_HP + 16u * 16u * HP_W;
constexpr unsigned WS_STAMP = 64u << 20;

typedef unsigned u32x4 __attribute__((ext_vector_type(4)));
typedef unsigned u32x2 __attribute__((ext_vector_type(2)));
typedef float f32x4 __attribute__((ext_vector_type(4)));
typedef float f32x2 __attribute__((ext_vector_type(2)));
typedef __bf16 bf16x8 __attribute__((ext_vector_type(8)));
typedef __bf16 bf16x2 __attribute__((ext_vector_type(2)));
typedef short s16x4 __attribute__((ext_vector_type(4)));
typedef int i32x4 __attribute__((ext_vector_type(4)));
#define LAS __attribute__((address_space(3)))

constexpr float C2 = 0.088388347648318440f * 1.4426950408889634f;

__device__ __forceinline__ int rfl(int x) { return __builtin_amdgcn_readfirstlane(x); }
__device__ __forceinline__ int rl(int x, int l) { return __builtin_amdgcn_readlane(x, l); }
__device__ __forceinline__ float rlf(float x, int l) { return __builtin_bit_cast(float, __builtin_amdgcn_readlane(__builtin_bit_cast(int, x), l)); }

__device__ __forceinline__ void dma16(const void* sbase, unsigned voff, unsigned dst) {
  unsigned keep;
  asm volatile("s_mov_b32 %0, m0\n\ts_mov_b32 m0, %3\n\ts_nop 0\n\tglobal_load_lds_dwordx4 %1, %2\n\ts_mov_b32 m0, %0"
               : "=&s"(keep) : "v"(voff), "s"(sbase), "s"(dst) : "memory");
}
__device__ __forceinline__ void dma_stage(const void* sK, const void* sV, unsigned dstK, unsigned dstV,
                                          unsigned k0, unsigned k1, unsigned k2, unsigned k3,
                                          unsigned v0, unsigned v1, unsigned v2, unsigned v3) {
  unsigned keep;
  asm volatile("s_mov_b32 %0, m0\n\ts_mov_b32 m0, %3\n\ts_nop 0\n\t"
               "global_load_lds_dwordx4 %5, %1 " KVPOL "\n\t"
               "global_load_lds_dwordx4 %6, %1 offset:1024 " KVPOL "\n\t"
               "global_load_lds_dwordx4 %7, %1 offset:2048 " KVPOL "\n\t"
               "global_load_lds_dwordx4 %8, %1 offset:3072 " KVPOL "\n\t"
               "s_mov_b32 m0, %4\n\ts_nop 0\n\t"
               "global_load_lds_dwordx4 %9, %2 " KVPOL "\n\t"
               "global_load_lds_dwordx4 %10, %2 offset:1024 " KVPOL "\n\t"
               "global_load_lds_dwordx4 %11, %2 offset:2048 " KVPOL "\n\t"
               "global_load_lds_dwordx4 %12, %2 offset:3072 " KVPOL "\n\t"
               "s_mov_b32 m0, %0"
               : "=&s"(keep)
               : "s"(sK), "s"(sV), "s"(dstK), "s"(dstV), "v"(k0), "v"(k1), "v"(k2), "v"(k3), "v"(v0), "v"(v1), "v"(v2), "v"(v3)
               : "memory");
}
template <int CTRL> __device__ __forceinline__ int dppi(int x) { return __builtin_amdgcn_update_dpp(0, x, CTRL, 0xf, 0xf, true); }
template <int CTRL> __device__ __forceinline__ int dppi_keep(int old, int x) { return __builtin_amdgcn_update_dpp(old, x, CTRL, 0xf, 0xf, false); }

__device__ __forceinline__ float xg_max(float x) {
  auto s = __builtin_amdgcn_permlane16_swap(__float_as_uint(x), __float_as_uint(x), false, false);
  x = fmaxf(__uint_as_float(s[0]), __uint_as_float(s[1]));
  auto t = __builtin_amdgcn_permlane32_swap(__float_as_uint(x), __float_as_uint(x), false, false);
  return fmaxf(__uint_as_float(t[0]), __uint_as_float(t[1]));
}
__device__ __forceinline__ float xg_sum(float x) {
  auto s = __builtin_amdgcn_permlane16_swap(__float_as_uint(x), __float_as_uint(x), false, false);
  x = __uint_as_float(s[0]) + __uint_as_float(s[1]);
  auto t = __builtin_amdgcn_permlane32_swap(__float_as_uint(x), __float_as_uint(x), false, false);
  return __uint_as_float(t[0]) + __uint_as_float(t[1]);
}
__device__ __forceinline__ unsigned pk_bf16(float a, float b) {
  bf16x2 v = {(__bf16)a, (__bf16)b};
  return __builtin_bit_cast(unsigned, v);
}

template <int NQ>
__device__ __forceinline__ void tile_compute(LAS char* lds, unsigned ka, unsigned va, const bf16x8 (&qf)[NQ][4], f32x4 (&acc)[NQ][8],
                                             float (&m)[NQ], float (&l)[NQ], int lo, int hi, int g4) {
  bf16x8 kf[4];
#pragma unroll
  for (int s = 0; s < 4; ++s) kf[s] = *(LAS bf16x8*)(lds + (ka ^ (s << 6)));
  s16x4 vf[8];
#pragma unroll
  for (int k = 0; k < 8; ++k) vf[k] = __builtin_amdgcn_ds_read_tr16_b64_v4i16((LAS s16x4*)(lds + (va ^ (k << 5))));
  f32x4 S[NQ];
#pragma unroll
  for (int qb = 0; qb < NQ; ++qb) {
    S[qb] = f32x4{0.f, 0.f, 0.f, 0.f};
#pragma unroll
    for (int s = 0; s < 4; ++s) S[qb] = __builtin_amdgcn_mfma_f32_16x16x32_bf16(kf[s], qf[qb][s], S[qb], 0, 0, 0);
  }
  if (lo > 0 || hi < 16) {
#pragma unroll
    for (int qb = 0; qb < NQ; ++qb)
#pragma unroll
      for (int r = 0; r < 4; ++r) { const int t = 4 * g4 + r; S[qb][r] = (t >= lo && t < hi) ? S[qb][r] : -INFINITY; }
  }
  s16x4 pb[NQ];
  bool grow = false;
  float alpha[NQ];
#pragma unroll
  for (int qb = 0; qb < NQ; ++qb) {
    float mx = fmaxf(fmaxf(S[qb][0], S[qb][1]), fmaxf(S[qb][2], S[qb][3]));
    mx = xg_max(mx);
    const float mn2 = fmaxf(m[qb], mx);
    grow |= mn2 > m[qb];
    alpha[qb] = __builtin_amdgcn_exp2f((m[qb] - mn2) * C2);
    m[qb] = mn2;
    const float mc = -mn2 * C2;
    const float p0 = __builtin_amdgcn_exp2f(fmaf(S[qb][0], C2, mc)), p1 = __builtin_amdgcn_exp2f(fmaf(S[qb][1], C2, mc));
    const float p2 = __builtin_amdgcn_exp2f(fmaf(S[qb][2], C2, mc)), p3 = __builtin_amdgcn_exp2f(fmaf(S[qb][3], C2, mc));
    l[qb] = fmaf(l[qb], alpha[qb], (p0 + p1) + (p2 + p3));
    pb[qb] = __builtin_bit_cast(s16x4, u32x2{pk_bf16(p0, p1), pk_bf16(p2, p3)});
  }
  if (__any(grow)) {
#pragma unroll
    for (int qb = 0; qb < NQ; ++qb)
#pragma unroll
      for (int k = 0; k < 8; ++k) acc[qb][k] *= alpha[qb];
  }
#pragma unroll
  for (int qb = 0; qb < NQ; ++qb)
#pragma unroll
    for (int k = 0; k < 8; ++k) acc[qb][k] = __builtin_amdgcn_mfma_f32_16x16x16bf16_1k(vf[k], pb[qb], acc[qb][k], 0, 0, 0);
}

template <int V> struct IntC { static constexpr int value = V; };

struct Params {
  const char* q; const char* k; const char* v; const int* bt; const int* lens; __hip_bfloat16* out; char* ws;
};

__global__ void __launch_bounds__(256, 2) radix_attn_kernel(Params p) {
  extern __shared__ __attribute__((aligned(16))) char smem[];
  LAS char* lds = (LAS char*)smem;
  const int tid = threadIdx.x, lane = tid & 63, w = rfl(tid >> 6);
  const int gidx = blockIdx.x;
  const int hg = (gidx >> 3) & 3;
  const int jj = (((gidx & 7) + 4 * (hg & 1)) & 7) | ((gidx >> 5) << 3);
  const int c16 = lane & 15, g4 = lane >> 4;
#define STAMP(i) do { } while (0)
  STAMP(0);
  const auto wsr = __builtin_amdgcn_make_buffer_rsrc(p.ws, 0, WS_END, 0x00020000);

  const int lenraw = p.lens[c16];
  const unsigned gvw = __builtin_amdgcn_raw_buffer_load_b32(wsr, WS_G + ((hg * 4 + w) * 16 + c16) * 4, 0, 16);
  const unsigned gvh = __builtin_amdgcn_raw_buffer_load_b32(wsr, WS_G + ((hg * 4 + (jj & 3)) * 16 + c16) * 4, 0, 16);
  int pm;
  {
    const i32x4* bt4 = (const i32x4*)p.bt;
    const i32x4 r0 = bt4[lane];
    i32x4 rr[4];
#pragma unroll
    for (int i = 0; i < 4; ++i) rr[i] = bt4[(4 * w + i) * 64 + lane];
    bool n0 = false, n1 = false, n2 = false, n3 = false;
#pragma unroll
    for (int i = 0; i < 4; ++i) {
      n0 |= rr[i][0] != r0[0]; n1 |= rr[i][1] != r0[1]; n2 |= rr[i][2] != r0[2]; n3 |= rr[i][3] != r0[3];
      *(LAS i32x4*)(lds + LDS_BT + ((4 * w + i) * 256 + 4 * lane) * 4) = rr[i];
    }
    const int fc = n0 ? 0 : n1 ? 1 : n2 ? 2 : n3 ? 3 : 4;
    const unsigned long long mm = __ballot(fc < 4);
    int col = NBLK;
    if (mm) { const int fl = __builtin_ctzll(mm); col = 4 * fl + rl(fc, fl); }
    if (lane == 0) *(LAS int*)(lds + LDS_MISC + 4 * w) = col;
    asm volatile("s_waitcnt lgkmcnt(0)\n\ts_barrier" ::: "memory");
    const i32x4 mc = *(LAS i32x4*)(lds + LDS_MISC);
    pm = rfl(min(min(mc[0], mc[1]), min(mc[2], mc[3])));
  }
  asm volatile("" :: "v"(gvw), "v"(gvh));
  const bool zq_l = lenraw <= 0;
  const int len_l = zq_l ? LK : min(lenraw, LK);
  int mn = len_l;
  mn = min(mn, dppi_keep<0x128>(mn, mn)); mn = min(mn, dppi_keep<0x124>(mn, mn));
  mn = min(mn, dppi_keep<0x122>(mn, mn)); mn = min(mn, dppi_keep<0x121>(mn, mn));
  const int P = min(pm, rfl(mn) >> 4);
  const int tsh = P * BS;
  const int vL = len_l - tsh;
  int inc = vL;
  inc += dppi<0x111>(inc); inc += dppi<0x112>(inc); inc += dppi<0x114>(inc); inc += dppi<0x118>(inc);
  const int N = rl(inc, 15);
  const int vS = inc - vL;
  int kH = 0;
  if (P > 0) kH = min(15, max(1, rfl((int)(((unsigned)tsh * 16u * 64u + (unsigned)(tsh + N) * HRHO - 1u) / ((unsigned)(tsh + N) * HRHO)))));
  kH = min(kH, max(P, 0));
  const int nW = 64 - 4 * kH;
  const unsigned zmask = (unsigned)(__ballot(zq_l) & 0xffffull);
  const bool isH = jj < 4 * kH;
  const int r_lo = (int)((unsigned)(lane * N) / (unsigned)nW), r_hi = (int)((unsigned)((lane + 1) * N) / (unsigned)nW);

  if (isH) {
    const int h = hg * 4 + (jj & 3), part = jj >> 2;
    const int p0 = (P * part) / kH, p1 = (P * (part + 1)) / kH;
#pragma unroll
    for (int i = 0; i < 2; ++i)
      dma16(p.q + (2 * w + i) * 16384 + h * 512, (lane >> 5) * 8192 + (lane & 31) * 16, LDS_QB + (2 * w + i) * 1024);
    unsigned oK[4], oV[4];
#pragma unroll
    for (int i = 0; i < 4; ++i) {
      const int t = 4 * i + g4;
      oK[i] = t * 4096 + ((c16 ^ t) << 4) - i * 1024 + 4096;
      oV[i] = t * 4096 + ((c16 ^ (2 * (t & 7))) << 4) - i * 1024 + 4096;
    }
    const char* kb = p.k + h * 256 - 4096;
    const char* vb = p.v + h * 256 - 4096;
    const int nt = max(0, (p1 - p0 - w + 3) >> 2);
    const unsigned ring = w * (RH * 8192);
    int pg0 = 0, vpg = *(LAS int*)(lds + LDS_BT + min(p0 + w + 4 * lane, NBLK - 1) * 4);
    auto issue = [&](int t) {
      if (t - pg0 == 64) { pg0 = t; vpg = *(LAS int*)(lds + LDS_BT + min(p0 + w + 4 * (t + lane), NBLK - 1) * 4); }
      const size_t pb = (size_t)(unsigned)rl(vpg, t - pg0) * 65536;
      const unsigned dst = ring + (t & (RH - 1)) * 8192;
      dma_stage(kb + pb, vb + pb, dst, dst + 4096, oK[0], oK[1], oK[2], oK[3], oV[0], oV[1], oV[2], oV[3]);
    };
    const int npre = min(nt, RH - 1);
#pragma unroll 1
    for (int t = 0; t < npre; ++t) issue(t);
    if (npre == 3) asm volatile("s_waitcnt vmcnt(24)\n\ts_barrier" ::: "memory");
    else asm volatile("s_waitcnt vmcnt(0)\n\ts_barrier" ::: "memory");
    bf16x8 qf[2][4];
#pragma unroll
    for (int qb = 0; qb < 2; ++qb) {
      const bool z = (zmask >> (8 * qb + (c16 >> 1))) & 1;
#pragma unroll
      for (int s = 0; s < 4; ++s) {
        u32x4 x = *(LAS u32x4*)(lds + LDS_QB + (16 * qb + c16) * 256 + (4 * s + g4) * 16);
        if (z) x = u32x4{0u, 0u, 0u, 0u};
        qf[qb][s] = __builtin_bit_cast(bf16x8, x);
      }
    }
    f32x4 acc[2][8];
    float m[2] = {-INFINITY, -INFINITY}, l[2] = {0.f, 0.f};
#pragma unroll
    for (int qb = 0; qb < 2; ++qb)
#pragma unroll
      for (int k = 0; k < 8; ++k) acc[qb][k] = f32x4{0.f, 0.f, 0.f, 0.f};
    const unsigned ka0 = ring + c16 * 256 + ((g4 ^ c16) << 4);
    unsigned va0;
    { const int tq = 4 * g4 + (c16 >> 2), pp = lane & 3; va0 = ring + 4096 + tq * 256 + ((tq & 7) << 5) + ((pp >> 1) << 4) + ((pp & 1) << 3); }
#pragma unroll 1
    for (int t = 0; t < nt; ++t) {
      const int young = min(nt - 1 - t, RH - 2);
      if (young == 2) asm volatile("s_waitcnt vmcnt(16) lgkmcnt(0)" ::: "memory");
      else if (young == 1) asm volatile("s_waitcnt vmcnt(8) lgkmcnt(0)" ::: "memory");
      else asm volatile("s_waitcnt vmcnt(0) lgkmcnt(0)" ::: "memory");
      if (t + RH - 1 < nt) issue(t + RH - 1);
      const unsigned sb = (t & (RH - 1)) * 8192;
      tile_compute<2>(lds, ka0 + sb, va0 + sb, qf, acc, m, l, 0, 16, g4);
    }
    STAMP(2);
    constexpr unsigned ROWB = 528, WAVEB = 32 * ROWB, MLB = 4 * WAVEB;
    asm volatile("s_waitcnt lgkmcnt(0)\n\ts_barrier" ::: "memory");
#pragma unroll
    for (int qb = 0; qb < 2; ++qb) {
      const float lt = xg_sum(l[qb]);
#pragma unroll
      for (int k = 0; k < 8; ++k) *(LAS f32x4*)(lds + w * WAVEB + (16 * qb + c16) * ROWB + (16 * k + 4 * g4) * 4) = acc[qb][k];
      if (g4 == 0) *(LAS f32x2*)(lds + MLB + (w * 32 + 16 * qb + c16) * 8) = f32x2{m[qb] * C2, lt};
    }
    asm volatile("s_waitcnt lgkmcnt(0)\n\ts_barrier" ::: "memory");
    const unsigned slot = WS_HP + (h * 16 + part) * HP_W;
    float mrow = 0.f, lrow = 0.f;
#pragma unroll
    for (int rr = 0; rr < 8; ++rr) {
      const int row = 8 * w + rr;
      f32x2 ml[4], o[4];
#pragma unroll
      for (int v = 0; v < 4; ++v) { ml[v] = *(LAS f32x2*)(lds + MLB + (v * 32 + row) * 8); o[v] = *(LAS f32x2*)(lds + v * WAVEB + row * ROWB + lane * 8); }
      const float M = fmaxf(fmaxf(ml[0][0], ml[1][0]), fmaxf(ml[2][0], ml[3][0]));
      float L = 0.f, o0 = 0.f, o1 = 0.f;
#pragma unroll
      for (int v = 0; v < 4; ++v) {
        const float e = ml[v][1] > 0.f ? __builtin_amdgcn_exp2f(ml[v][0] - M) : 0.f;
        L = fmaf(ml[v][1], e, L); o0 = fmaf(o[v][0], e, o0); o1 = fmaf(o[v][1], e, o1);
      }
      const unsigned tag = (unsigned)rl((int)gvh, row >> 1) + 1u;
      const u32x4 gr = {__float_as_uint(o0), tag, __float_as_uint(o1), tag};
      __builtin_amdgcn_raw_buffer_store_b128(gr, wsr, slot + row * 1024 + lane * 16, 0, 16);
      if (lane == rr) { mrow = M; lrow = L; }
    }
    const unsigned tagr = (unsigned)__shfl((int)gvh, ((8 * w + lane) >> 1) & 15) + 1u;
    if (lane < 8) {
      const unsigned tag = tagr;
      const u32x4 gm = {__float_as_uint(mrow), tag, __float_as_uint(lrow), tag};
      __builtin_amdgcn_raw_buffer_store_b128(gm, wsr, slot + 32768 + (8 * w + lane) * 16, 0, 16);
    }
    STAMP(1);
    STAMP(3);
    return;
  }

  const int j = jj - 4 * kH, h = hg * 4 + w;
  const int x0 = rl(r_lo, j), x1 = rl(r_hi, j);
  const unsigned umask = (unsigned)(__ballot(max(x0, vS) < min(x1, vS + vL)) & 0xffffull);
  unsigned voffK[4], voffV[4];
#pragma unroll
  for (int i = 0; i < 4; ++i) {
    const int t = 4 * w + i;
    voffK[i] = g4 * 256 + ((c16 ^ t) << 4) + t * 4096 - i * 1024 + 4096;
    voffV[i] = g4 * 256 + ((c16 ^ (2 * (t & 7))) << 4) + t * 4096 - i * 1024 + 4096;
  }
  const unsigned kaddr0 = w * 256 + c16 * 1024 + ((g4 ^ c16) << 4);
  unsigned vaddr0;
  { const int tq = 4 * g4 + (c16 >> 2), pp = lane & 3; vaddr0 = 16384 + tq * 1024 + w * 256 + ((tq & 7) << 5) + ((pp >> 1) << 4) + ((pp & 1) << 3); }
  const char* kbase = p.k + hg * 1024 - 4096;
  const char* vbase = p.v + hg * 1024 - 4096;

  unsigned i_mask = umask; int i_row = 0, i_t0 = 0, i_t1 = 0, i_page; bool i_more = false;
  int n_iss = 0, i_slot = 0;
  auto unit_range = [&](int b, int& t0, int& t1) {
    const int s = rl(vS, b), L = rl(vL, b);
    t0 = tsh + max(x0, s) - s; t1 = tsh + min(x1, s + L) - s;
  };
  auto q_dma = [&](int b, int slot) {
    if (lane < 32) dma16(p.q + b * 8192 + h * 512, lane * 16, LDS_QB + (w * R + slot) * 512);
  };
  if (i_mask) {
    i_row = __builtin_ctz(i_mask); i_mask &= i_mask - 1; unit_range(i_row, i_t0, i_t1);
    q_dma(i_row, 0);
    i_more = true;
  }
  i_page = i_t0 >> 4;
  int i_pg0 = i_page, vpg = *(LAS int*)(lds + LDS_BT + (i_row * NBLK + min(i_page + lane, NBLK - 1)) * 4);
  auto issue_stage = [&]() {
    const int phys = rl(vpg, i_page - i_pg0);
    const int lo = i_t0 - i_page * 16, hi = i_t1 - i_page * 16;
    const size_t pb = (size_t)(unsigned)phys * 65536;
    const unsigned dst = i_slot * STAGE + 4 * w * 1024;
    if (lo <= 0 && hi >= 16) {
      dma_stage(kbase + pb, vbase + pb, dst, dst + 16384, voffK[0], voffK[1], voffK[2], voffK[3], voffV[0], voffV[1], voffV[2], voffV[3]);
    } else {
      unsigned ok[4], ov[4];
#pragma unroll
      for (int i = 0; i < 4; ++i) {
        const int d = (min(max(4 * w + i, lo), hi - 1) - (4 * w + i)) * 4096;
        ok[i] = voffK[i] + d; ov[i] = voffV[i] + d;
      }
      dma_stage(kbase + pb, vbase + pb, dst, dst + 16384, ok[0], ok[1], ok[2], ok[3], ov[0], ov[1], ov[2], ov[3]);
    }
    ++n_iss; i_slot = (i_slot + 1 == R) ? 0 : i_slot + 1;
    ++i_page;
    if (i_page * 16 >= i_t1) {
      if (i_mask) {
        i_row = __builtin_ctz(i_mask); i_mask &= i_mask - 1; unit_range(i_row, i_t0, i_t1); i_page = i_t0 >> 4;
        q_dma(i_row, i_slot);
        i_pg0 = i_page; vpg = *(LAS int*)(lds + LDS_BT + (i_row * NBLK + min(i_page + lane, NBLK - 1)) * 4);
      } else i_more = false;
    } else if (i_page - i_pg0 == 64) {
      i_pg0 = i_page; vpg = *(LAS int*)(lds + LDS_BT + (i_row * NBLK + min(i_page + lane, NBLK - 1)) * 4);
    }
  };
#pragma unroll 1
  for (int s = 0; s < R - 1; ++s) if (i_more) issue_stage();

  unsigned own = 0;
  {
    unsigned mrem = umask;
    while (mrem) {
      const int b = __builtin_ctz(mrem); mrem &= mrem - 1;
      const int s = rl(vS, b), L = rl(vL, b);
      unsigned long long pmk = __ballot(lane < nW && max(r_lo, s) < min(r_hi, s + L));
      int idx = w % __builtin_popcountll(pmk);
      while (idx--) pmk &= pmk - 1;
      if (__builtin_ctzll(pmk) == j) own |= 1u << b;
    }
    unsigned zl = (unsigned)(__ballot(vL == 0) & 0xffffull);
    while (zl) { const int b = __builtin_ctz(zl); zl &= zl - 1; if ((b * 4) % nW == j) own |= 1u << b; }
  }

  int n_comp = 0, c_slot = 0;
  unsigned c_mask = umask;
  float ro[2][2] = {{0.f, 0.f}, {0.f, 0.f}}, rm[2] = {-INFINITY, -INFINITY}, rs[2] = {0.f, 0.f};
  float ho[2][2] = {{0.f, 0.f}, {0.f, 0.f}}, hm[2] = {-INFINITY, -INFINITY}, hs[2] = {0.f, 0.f};
  int last_b = -1, held_b = -1;
  auto publish = [&](int b, const float (&o)[2][2], const float (&mm)[2], const float (&ll)[2]) {
    const unsigned tag = (unsigned)rl((int)gvw, b) + 1u;
    const unsigned slot = WS_PP + ((hg * 80 + j + b) * 4 + w) * PP_W;
    const u32x4 g0 = {__float_as_uint(o[0][0]), tag, __float_as_uint(o[0][1]), tag};
    const u32x4 g1 = {__float_as_uint(o[1][0]), tag, __float_as_uint(o[1][1]), tag};
    __builtin_amdgcn_raw_buffer_store_b128(g0, wsr, slot + lane * 16, 0, 16);
    __builtin_amdgcn_raw_buffer_store_b128(g1, wsr, slot + 1024 + lane * 16, 0, 16);
    if (lane < 2) {
      const u32x4 gm = {__float_as_uint(lane ? mm[1] : mm[0]), tag, __float_as_uint(lane ? ll[1] : ll[0]), tag};
      __builtin_amdgcn_raw_buffer_store_b128(gm, wsr, slot + 2048 + lane * 16, 0, 16);
    }
  };
  while (c_mask) {
    const int b = __builtin_ctz(c_mask); c_mask &= c_mask - 1;
    int t0, t1; unit_range(b, t0, t1);
    bf16x8 qf[1][4];
    f32x4 acc[1][8];
    float m[1] = {-INFINITY}, l[1] = {0.f};
#pragma unroll
    for (int k = 0; k < 8; ++k) acc[0][k] = f32x4{0.f, 0.f, 0.f, 0.f};
    bool first = true;
    for (int page = t0 >> 4; page * 16 < t1; ++page) {
      if (n_iss - n_comp > 1) asm volatile("s_waitcnt vmcnt(8) lgkmcnt(0)\n\ts_barrier" ::: "memory");
      else asm volatile("s_waitcnt vmcnt(0) lgkmcnt(0)\n\ts_barrier" ::: "memory");
      if (first) {
        const bool z = (c16 >= 2) || ((zmask >> b) & 1);
#pragma unroll
        for (int s = 0; s < 4; ++s) {
          u32x4 x = *(LAS u32x4*)(lds + LDS_QB + (w * R + c_slot) * 512 + (c16 & 1) * 256 + (4 * s + g4) * 16);
          if (z) x = u32x4{0u, 0u, 0u, 0u};
          qf[0][s] = __builtin_bit_cast(bf16x8, x);
        }
        asm volatile("s_waitcnt lgkmcnt(0)" ::: "memory");
        first = false;
      }
      if (i_more) issue_stage();
      const unsigned sb = c_slot * STAGE;
      tile_compute<1>(lds, kaddr0 + sb, vaddr0 + sb, qf, acc, m, l, t0 - page * 16, t1 - page * 16, g4);
      ++n_comp; c_slot = (c_slot + 1 == R) ? 0 : c_slot + 1;
    }
    const float lt = xg_sum(l[0]);
    LAS char* sc = lds + LDS_SC + w * 1024;
    if (c16 < 2) {
#pragma unroll
      for (int k = 0; k < 8; ++k) *(LAS f32x4*)(sc + c16 * 512 + (16 * k + 4 * g4) * 4) = acc[0][k];
    }
    asm volatile("s_waitcnt lgkmcnt(0)" ::: "memory");
    const f32x2 a0 = *(LAS f32x2*)(sc + lane * 8), a1 = *(LAS f32x2*)(sc + 512 + lane * 8);
    if (last_b >= 0) {
      if (held_b >= 0) publish(held_b, ho, hm, hs);
      held_b = last_b;
#pragma unroll
      for (int r = 0; r < 2; ++r) { ho[r][0] = ro[r][0]; ho[r][1] = ro[r][1]; hm[r] = rm[r]; hs[r] = rs[r]; }
    }
    ro[0][0] = a0[0]; ro[0][1] = a0[1]; ro[1][0] = a1[0]; ro[1][1] = a1[1];
    rm[0] = rlf(m[0], 0) * C2; rm[1] = rlf(m[0], 1) * C2;
    rs[0] = rlf(lt, 0); rs[1] = rlf(lt, 1);
    last_b = b;
  }
  STAMP(2);
  if (last_b >= 0 && !((own >> last_b) & 1)) { publish(last_b, ro, rm, rs); last_b = -1; }
  if (held_b >= 0 && !((own >> held_b) & 1)) { publish(held_b, ho, hm, hs); held_b = -1; }
  STAMP(1);

  while (own) {
    const int b = __builtin_ctz(own); own &= own - 1;
    const unsigned tag = (unsigned)rl((int)gvw, b) + 1u;
    const int s = rl(vS, b), L = rl(vL, b);
    unsigned long long pmk = __ballot(lane < nW && max(r_lo, s) < min(r_hi, s + L));
    unsigned smk = (1u << kH) - 1u;
    float M0 = -INFINITY, M1 = -INFINITY, L0 = 0.f, L1 = 0.f, o00 = 0.f, o01 = 0.f, o10 = 0.f, o11 = 0.f;
    if (b == last_b) {
      pmk &= ~(1ull << j);
      M0 = rm[0]; M1 = rm[1]; L0 = rs[0]; L1 = rs[1]; o00 = ro[0][0]; o01 = ro[0][1]; o10 = ro[1][0]; o11 = ro[1][1];
    } else if (b == held_b) {
      pmk &= ~(1ull << j);
      M0 = hm[0]; M1 = hm[1]; L0 = hs[0]; L1 = hs[1]; o00 = ho[0][0]; o01 = ho[0][1]; o10 = ho[1][0]; o11 = ho[1][1];
    }
    const unsigned p_rows = WS_PP + ((hg * 80 + lane + b) * 4 + w) * PP_W;
    const unsigned s_base = WS_HP + (h * 16 + (lane & 15)) * HP_W;
    unsigned spins = 0;
    bool light = false;
    while (pmk | smk) {
      unsigned long long tp = pmk; unsigned ts = smk;
      const bool single = (__builtin_popcountll(pmk) + __builtin_popcount(smk)) == 1;
      if (light && !single) {
        const int jp = pmk ? (((pmk >> lane) & 1) ? lane : __builtin_ctzll(pmk)) : 0;
        const int js = smk ? (((smk >> (lane & 15)) & 1) ? (lane & 15) : __builtin_ctz(smk)) : 0;
        u32x4 a = {0u, tag, 0u, tag}, c = {0u, tag, 0u, tag};
        if (pmk) a = __builtin_amdgcn_raw_buffer_load_b128(wsr, WS_PP + ((hg * 80 + jp + b) * 4 + w) * PP_W + 2048 + 16, 0, 16);
        if (smk) c = __builtin_amdgcn_raw_buffer_load_b128(wsr, WS_HP + (h * 16 + js) * HP_W + 32768 + (2 * b + 1) * 16, 0, 16);
        tp &= __ballot(a[1] == tag && a[3] == tag);
        ts &= (unsigned)__ballot(c[1] == tag && c[3] == tag);
      }
      bool progress = false;
      if (tp | ts) {
        auto batch = [&](auto nb_tag) {
          constexpr int NB = decltype(nb_tag)::value;
          unsigned rows[NB], mls[NB]; unsigned long long bitp[NB]; unsigned bits[NB];
#pragma unroll
          for (int k = 0; k < NB; ++k) {
            if (tp) { const int q = __builtin_ctzll(tp); bitp[k] = tp & (~tp + 1); bits[k] = 0; tp &= tp - 1; rows[k] = (unsigned)rl((int)p_rows, q); mls[k] = rows[k] + 2048; }
            else if (ts) { const int q = __builtin_ctz(ts); bits[k] = ts & (~ts + 1); bitp[k] = 0; ts &= ts - 1;
                           const unsigned sl = (unsigned)rl((int)s_base, q); rows[k] = sl + b * 2048; mls[k] = sl + 32768 + b * 32; }
            else { bitp[k] = 0; bits[k] = 0; rows[k] = rows[0]; mls[k] = mls[0]; }
          }
          u32x4 d0[NB], d1[NB], dm[NB];
#pragma unroll
          for (int k = 0; k < NB; ++k) {
            d0[k] = __builtin_amdgcn_raw_buffer_load_b128(wsr, lane * 16, rows[k], 16);
            d1[k] = __builtin_amdgcn_raw_buffer_load_b128(wsr, 1024 + lane * 16, rows[k], 16);
            dm[k] = __builtin_amdgcn_raw_buffer_load_b128(wsr, (lane & 1) * 16, mls[k], 16);
          }
#pragma unroll
          for (int k = 0; k < NB; ++k) {
            const bool ok = d0[k][1] == tag && d0[k][3] == tag && d1[k][1] == tag && d1[k][3] == tag && dm[k][1] == tag && dm[k][3] == tag;
            if ((bitp[k] | bits[k]) && __all(ok)) {
              const float mv = __uint_as_float(dm[k][0]), lv = __uint_as_float(dm[k][2]);
              const float m0 = rlf(mv, 0), m1 = rlf(mv, 1), l0 = rlf(lv, 0), l1 = rlf(lv, 1);
              const float n0 = fmaxf(M0, m0), n1 = fmaxf(M1, m1);
              const float a0 = __builtin_amdgcn_exp2f(M0 - n0), b0 = __builtin_amdgcn_exp2f(m0 - n0);
              const float a1 = __builtin_amdgcn_exp2f(M1 - n1), b1 = __builtin_amdgcn_exp2f(m1 - n1);
              L0 = L0 * a0 + l0 * b0; L1 = L1 * a1 + l1 * b1;
              o00 = o00 * a0 + __uint_as_float(d0[k][0]) * b0; o01 = o01 * a0 + __uint_as_float(d0[k][2]) * b0;
              o10 = o10 * a1 + __uint_as_float(d1[k][0]) * b1; o11 = o11 * a1 + __uint_as_float(d1[k][2]) * b1;
              M0 = n0; M1 = n1;
              pmk &= ~bitp[k]; smk &= ~bits[k];
              progress = true;
            }
          }
        };
        const int cnt = __builtin_popcountll(tp) + __builtin_popcount(ts);
        if (cnt == 1) batch(IntC<1>{}); else if (cnt <= 3) batch(IntC<3>{}); else batch(IntC<6>{});
      }
      if (!progress) {
        light = true;
        if (++spins > (1u << 16)) break;
        __builtin_amdgcn_s_sleep(SLEEP);
      }
    }
    const float i0 = 1.f / L0, i1 = 1.f / L1;
    __hip_bfloat16* orow = p.out + (b * NH + 2 * h) * DH + 2 * lane;
    *(unsigned*)orow = pk_bf16(o00 * i0, o01 * i0);
    *(unsigned*)(orow + DH) = pk_bf16(o10 * i1, o11 * i1);
    if (lane == 0) __builtin_amdgcn_raw_buffer_store_b32(tag, wsr, WS_G + (h * 16 + b) * 4, 0, 16);
  }
  STAMP(3);
}

}

extern "C" void kernel_launch(void* const* d_in, const int* in_sizes, int n_in, void* d_out, int out_size, void* d_ws, size_t ws_size,
                              hipStream_t stream) {
  using namespace rattn;
  Params p;
  p.q = (const char*)d_in[0]; p.k = (const char*)d_in[1]; p.v = (const char*)d_in[2];
  p.bt = (const int*)d_in[3]; p.lens = (const int*)d_in[4];
  p.out = (__hip_bfloat16*)d_out; p.ws = (char*)d_ws;
  (void)hipFuncSetAttribute((const void*)radix_attn_kernel, hipFuncAttributeMaxDynamicSharedMemorySize, LDS_TOTAL);
  hipLaunchKernelGGL(radix_attn_kernel, dim3(256), dim3(256), LDS_TOTAL, stream, p);
}
```

```cpp
#include <hip/hip_runtime.h>
#include <hip/hip_bf16.h>
#include <stdint.h>

namespace rattn {

constexpr int NSEQ = 16, NH = 32, NKV = 16, DH = 128, NBLK = 256, BS = 16;
constexpr int LK = NBLK * BS;
#define HRHO 50
#define PROBE_K 0
#define PROBE_WG 200
#define PROBE_WAVE 0
#define PROBE_MUL 1
#define MAPMODE 1
#define SLEEP 6
#define KVPOL "nt"
constexpr int R = 3;
constexpr int STAGE = 32768;
constexpr int RH = 4;
constexpr int RING_BYTES = 4 * RH * 8192;
constexpr int LDS_BT = RING_BYTES;
constexpr int LDS_QB = LDS_BT + NSEQ * NBLK * 4;
constexpr int LDS_SC = LDS_QB + 4 * R * 512;
constexpr int LDS_MISC = LDS_SC + 4096;
constexpr int LDS_JUNK = LDS_MISC + 64;
constexpr int LDS_TOTAL = LDS_JUNK + 1024;
static_assert(LDS_TOTAL <= 160 * 1024 && LDS_MISC - LDS_QB >= 8192 && RING_BYTES >= R * STAGE, "LDS");

constexpr unsigned WS_G = 0;
constexpr unsigned WS_PP = 4096;
constexpr unsigned PP_W = 2176;
constexpr unsigned WS_HP = 4u << 20;
constexpr unsigned HP_W = 32768 + 512;
constexpr unsigned WS_END = WS_HP + 16u * 16u * HP_W;
constexpr unsigned WS_STAMP = 64u << 20;

typedef unsigned u32x4 __attribute__((ext_vector_type(4)));
typedef unsigned u32x2 __attribute__((ext_vector_type(2)));
typedef float f32x4 __attribute__((ext_vector_type(4)));
typedef float f32x2 __attribute__((ext_vector_type(2)));
typedef __bf16 bf16x8 __attribute__((ext_vector_type(8)));
typedef __bf16 bf16x2 __attribute__((ext_vector_type(2)));
typedef short s16x4 __attribute__((ext_vector_type(4)));
typedef int i32x4 __attribute__((ext_vector_type(4)));
#define LAS __attribute__((address_space(3)))

constexpr float C2 = 0.088388347648318440f * 1.4426950408889634f;

__device__ __forceinline__ int rfl(int x) { return __builtin_amdgcn_readfirstlane(x); }
__device__ __forceinline__ int rl(int x, int l) { return __builtin_amdgcn_readlane(x, l); }
__device__ __forceinline__ float rlf(float x, int l) { return __builtin_bit_cast(float, __builtin_amdgcn_readlane(__builtin_bit_cast(int, x), l)); }

__device__ __forceinline__ void dma16(const void* sbase, unsigned voff, unsigned dst) {
  unsigned keep;
  asm volatile("s_mov_b32 %0, m0\n\ts_mov_b32 m0, %3\n\ts_nop 0\n\tglobal_load_lds_dwordx4 %1, %2\n\ts_mov_b32 m0, %0"
               : "=&s"(keep) : "v"(voff), "s"(sbase), "s"(dst) : "memory");
}
__device__ __forceinline__ void dma_stage(const void* sK, const void* sV, unsigned dstK, unsigned dstV,
                                          unsigned k0, unsigned k1, unsigned k2, unsigned k3,
                                          unsigned v0, unsigned v1, unsigned v2, unsigned v3) {
  unsigned keep;
  asm volatile("s_mov_b32 %0, m0\n\ts_mov_b32 m0, %3\n\ts_nop 0\n\t"
               "global_load_lds_dwordx4 %5, %1 " KVPOL "\n\t"
               "global_load_lds_dwordx4 %6, %1 offset:1024 " KVPOL "\n\t"
               "global_load_lds_dwordx4 %7, %1 offset:2048 " KVPOL "\n\t"
               "global_load_lds_dwordx4 %8, %1 offset:3072 " KVPOL "\n\t"
               "s_mov_b32 m0, %4\n\ts_nop 0\n\t"
               "global_load_lds_dwordx4 %9, %2 " KVPOL "\n\t"
               "global_load_lds_dwordx4 %10, %2 offset:1024 " KVPOL "\n\t"
               "global_load_lds_dwordx4 %11, %2 offset:2048 " KVPOL "\n\t"
               "global_load_lds_dwordx4 %12, %2 offset:3072 " KVPOL "\n\t"
               "s_mov_b32 m0, %0"
               : "=&s"(keep)
               : "s"(sK), "s"(sV), "s"(dstK), "s"(dstV), "v"(k0), "v"(k1), "v"(k2), "v"(k3), "v"(v0), "v"(v1), "v"(v2), "v"(v3)
               : "memory");
}
template <int CTRL> __device__ __forceinline__ int dppi(int x) { return __builtin_amdgcn_update_dpp(0, x, CTRL, 0xf, 0xf, true); }
template <int CTRL> __device__ __forceinline__ int dppi_keep(int old, int x) { return __builtin_amdgcn_update_dpp(old, x, CTRL, 0xf, 0xf, false); }

__device__ __forceinline__ float xg_max(float x) {
  auto s = __builtin_amdgcn_permlane16_swap(__float_as_uint(x), __float_as_uint(x), false, false);
  x = fmaxf(__uint_as_float(s[0]), __uint_as_float(s[1]));
  auto t = __builtin_amdgcn_permlane32_swap(__float_as_uint(x), __float_as_uint(x), false, false);
  return fmaxf(__uint_as_float(t[0]), __uint_as_float(t[1]));
}
__device__ __forceinline__ float xg_sum(float x) {
  auto s = __builtin_amdgcn_permlane16_swap(__float_as_uint(x), __float_as_uint(x), false, false);
  x = __uint_as_float(s[0]) + __uint_as_float(s[1]);
  auto t = __builtin_amdgcn_permlane32_swap(__float_as_uint(x), __float_as_uint(x), false, false);
  return __uint_as_float(t[0]) + __uint_as_float(t[1]);
}
__device__ __forceinline__ unsigned pk_bf16(float a, float b) {
  bf16x2 v = {(__bf16)a, (__bf16)b};
  return __builtin_bit_cast(unsigned, v);
}

template <int NQ>
__device__ __forceinline__ void tile_compute(LAS char* lds, unsigned ka, unsigned va, const bf16x8 (&qf)[NQ][4], f32x4 (&acc)[NQ][8],
                                             float (&m)[NQ], float (&l)[NQ], int lo, int hi, int g4) {
  bf16x8 kf[4];
#pragma unroll
  for (int s = 0; s < 4; ++s) kf[s] = *(LAS bf16x8*)(lds + (ka ^ (s << 6)));
  s16x4 vf[8];
#pragma unroll
  for (int k = 0; k < 8; ++k) vf[k] = __builtin_amdgcn_ds_read_tr16_b64_v4i16((LAS s16x4*)(lds + (va ^ (k << 5))));
  f32x4 S[NQ];
#pragma unroll
  for (int qb = 0; qb < NQ; ++qb) {
    S[qb] = f32x4{0.f, 0.f, 0.f, 0.f};
#pragma unroll
    for (int s = 0; s < 4; ++s) S[qb] = __builtin_amdgcn_mfma_f32_16x16x32_bf16(kf[s], qf[qb][s], S[qb], 0, 0, 0);
  }
  if (lo > 0 || hi < 16) {
#pragma unroll
    for (int qb = 0; qb < NQ; ++qb)
#pragma unroll
      for (int r = 0; r < 4; ++r) { const int t = 4 * g4 + r; S[qb][r] = (t >= lo && t < hi) ? S[qb][r] : -INFINITY; }
  }
  s16x4 pb[NQ];
  bool grow = false;
  float alpha[NQ];
#pragma unroll
  for (int qb = 0; qb < NQ; ++qb) {
    float mx = fmaxf(fmaxf(S[qb][0], S[qb][1]), fmaxf(S[qb][2], S[qb][3]));
    mx = xg_max(mx);
    const float mn2 = fmaxf(m[qb], mx);
    grow |= mn2 > m[qb];
    alpha[qb] = __builtin_amdgcn_exp2f((m[qb] - mn2) * C2);
    m[qb] = mn2;
    const float mc = -mn2 * C2;
    const float p0 = __builtin_amdgcn_exp2f(fmaf(S[qb][0], C2, mc)), p1 = __builtin_amdgcn_exp2f(fmaf(S[qb][1], C2, mc));
    const float p2 = __builtin_amdgcn_exp2f(fmaf(S[qb][2], C2, mc)), p3 = __builtin_amdgcn_exp2f(fmaf(S[qb][3], C2, mc));
    l[qb] = fmaf(l[qb], alpha[qb], (p0 + p1) + (p2 + p3));
    pb[qb] = __builtin_bit_cast(s16x4, u32x2{pk_bf16(p0, p1), pk_bf16(p2, p3)});
  }
  if (__any(grow)) {
#pragma unroll
    for (int qb = 0; qb < NQ; ++qb)
#pragma unroll
      for (int k = 0; k < 8; ++k) acc[qb][k] *= alpha[qb];
  }
#pragma unroll
  for (int qb = 0; qb < NQ; ++qb)
#pragma unroll
    for (int k = 0; k < 8; ++k) acc[qb][k] = __builtin_amdgcn_mfma_f32_16x16x16bf16_1k(vf[k], pb[qb], acc[qb][k], 0, 0, 0);
}

template <int V> struct IntC { static constexpr int value = V; };

struct Params {
  const char* q; const char* k; const char* v; const int* bt; const int* lens; __hip_bfloat16* out; char* ws;
};

__global__ void __launch_bounds__(256, 2) radix_attn_kernel(Params p) {
  extern __shared__ __attribute__((aligned(16))) char smem[];
  LAS char* lds = (LAS char*)smem;
  const int tid = threadIdx.x, lane = tid & 63, w = rfl(tid >> 6);
  const int gidx = blockIdx.x;
  const int hg = (gidx >> 3) & 3;
  const int jj = (((gidx & 7) + 4 * (hg & 1)) & 7) | ((gidx >> 5) << 3);
  const int c16 = lane & 15, g4 = lane >> 4;
#define STAMP(i) do { } while (0)
  STAMP(0);
  const unsigned long long pr_t0 = __builtin_amdgcn_s_memrealtime();
  unsigned long long pr_x = pr_t0;
#define PROBE(k) do { if ((k) == PROBE_K && pr_x == pr_t0) pr_x = __builtin_amdgcn_s_memrealtime(); } while (0)
#define PROBE_END() do { if (gidx == PROBE_WG && w == PROBE_WAVE) { const unsigned long long lim = 7000ull + (unsigned long long)PROBE_MUL * (pr_x - pr_t0); while (__builtin_amdgcn_s_memrealtime() - pr_t0 < lim) __builtin_amdgcn_s_sleep(4); } } while (0)
  const auto wsr = __builtin_amdgcn_make_buffer_rsrc(p.ws, 0, WS_END, 0x00020000);

  const int lenraw = p.lens[c16];
  const unsigned gvw = __builtin_amdgcn_raw_buffer_load_b32(wsr, WS_G + ((hg * 4 + w) * 16 + c16) * 4, 0, 16);
  const unsigned gvh = __builtin_amdgcn_raw_buffer_load_b32(wsr, WS_G + ((hg * 4 + (jj & 3)) * 16 + c16) * 4, 0, 16);
  int pm;
  {
    const i32x4* bt4 = (const i32x4*)p.bt;
    const i32x4 r0 = bt4[lane];
    i32x4 rr[4];
#pragma unroll
    for (int i = 0; i < 4; ++i) rr[i] = bt4[(4 * w + i) * 64 + lane];
    bool n0 = false, n1 = false, n2 = false, n3 = false;
#pragma unroll
    for (int i = 0; i < 4; ++i) {
      n0 |= rr[i][0] != r0[0]; n1 |= rr[i][1] != r0[1]; n2 |= rr[i][2] != r0[2]; n3 |= rr[i][3] != r0[3];
      *(LAS i32x4*)(lds + LDS_BT + ((4 * w + i) * 256 + 4 * lane) * 4) = rr[i];
    }
    const int fc = n0 ? 0 : n1 ? 1 : n2 ? 2 : n3 ? 3 : 4;
    const unsigned long long mm = __ballot(fc < 4);
    int col = NBLK;
    if (mm) { const int fl = __builtin_ctzll(mm); col = 4 * fl + rl(fc, fl); }
    if (lane == 0) *(LAS int*)(lds + LDS_MISC + 4 * w) = col;
    asm volatile("s_waitcnt lgkmcnt(0)\n\ts_barrier" ::: "memory");
    PROBE(1);
    const i32x4 mc = *(LAS i32x4*)(lds + LDS_MISC);
    pm = rfl(min(min(mc[0], mc[1]), min(mc[2], mc[3])));
  }
  asm volatile("" :: "v"(gvw), "v"(gvh));
  const bool zq_l = lenraw <= 0;
  const int len_l = zq_l ? LK : min(lenraw, LK);
  int mn = len_l;
  mn = min(mn, dppi_keep<0x128>(mn, mn)); mn = min(mn, dppi_keep<0x124>(mn, mn));
  mn = min(mn, dppi_keep<0x122>(mn, mn)); mn = min(mn, dppi_keep<0x121>(mn, mn));
  const int P = min(pm, rfl(mn) >> 4);
  const int tsh = P * BS;
  const int vL = len_l - tsh;
  int inc = vL;
  inc += dppi<0x111>(inc); inc += dppi<0x112>(inc); inc += dppi<0x114>(inc); inc += dppi<0x118>(inc);
  const int N = rl(inc, 15);
  const int vS = inc - vL;
  int kH = 0;
  if (P > 0) kH = min(15, max(1, rfl((int)(((unsigned)tsh * 16u * 64u + (unsigned)(tsh + N) * HRHO - 1u) / ((unsigned)(tsh + N) * HRHO)))));
  kH = min(kH, max(P, 0));
  const int nW = 64 - 4 * kH;
  const unsigned zmask = (unsigned)(__ballot(zq_l) & 0xffffull);
  const bool isH = jj < 4 * kH;
  const int r_lo = (int)((unsigned)(lane * N) / (unsigned)nW), r_hi = (int)((unsigned)((lane + 1) * N) / (unsigned)nW);

  if (isH) {
    const int h = hg * 4 + (jj & 3), part = jj >> 2;
    const int p0 = (P * part) / kH, p1 = (P * (part + 1)) / kH;
#pragma unroll
    for (int i = 0; i < 2; ++i)
      dma16(p.q + (2 * w + i) * 16384 + h * 512, (lane >> 5) * 8192 + (lane & 31) * 16, LDS_QB + (2 * w + i) * 1024);
    unsigned oK[4], oV[4];
#pragma unroll
    for (int i = 0; i < 4; ++i) {
      const int t = 4 * i + g4;
      oK[i] = t * 4096 + ((c16 ^ t) << 4) - i * 1024 + 4096;
      oV[i] = t * 4096 + ((c16 ^ (2 * (t & 7))) << 4) - i * 1024 + 4096;
    }
    const char* kb = p.k + h * 256 - 4096;
    const char* vb = p.v + h * 256 - 4096;
    const int nt = max(0, (p1 - p0 - w + 3) >> 2);
    const unsigned ring = w * (RH * 8192);
    int pg0 = 0, vpg = *(LAS int*)(lds + LDS_BT + min(p0 + w + 4 * lane, NBLK - 1) * 4);
    auto issue = [&](int t) {
      if (t - pg0 == 64) { pg0 = t; vpg = *(LAS int*)(lds + LDS_BT + min(p0 + w + 4 * (t + lane), NBLK - 1) * 4); }
      const size_t pb = (size_t)(unsigned)rl(vpg, t - pg0) * 65536;
      const unsigned dst = ring + (t & (RH - 1)) * 8192;
      dma_stage(kb + pb, vb + pb, dst, dst + 4096, oK[0], oK[1], oK[2], oK[3], oV[0], oV[1], oV[2], oV[3]);
    };
    const int npre = min(nt, RH - 1);
#pragma unroll 1
    for (int t = 0; t < npre; ++t) issue(t);
    if (npre == 3) asm volatile("s_waitcnt vmcnt(24)\n\ts_barrier" ::: "memory");
    else asm volatile("s_waitcnt vmcnt(0)\n\ts_barrier" ::: "memory");
    bf16x8 qf[2][4];
#pragma unroll
    for (int qb = 0; qb < 2; ++qb) {
      const bool z = (zmask >> (8 * qb + (c16 >> 1))) & 1;
#pragma unroll
      for (int s = 0; s < 4; ++s) {
        u32x4 x = *(LAS u32x4*)(lds + LDS_QB + (16 * qb + c16) * 256 + (4 * s + g4) * 16);
        if (z) x = u32x4{0u, 0u, 0u, 0u};
        qf[qb][s] = __builtin_bit_cast(bf16x8, x);
      }
    }
    f32x4 acc[2][8];
    float m[2] = {-INFINITY, -INFINITY}, l[2] = {0.f, 0.f};
#pragma unroll
    for (int qb = 0; qb < 2; ++qb)
#pragma unroll
      for (int k = 0; k < 8; ++k) acc[qb][k] = f32x4{0.f, 0.f, 0.f, 0.f};
    const unsigned ka0 = ring + c16 * 256 + ((g4 ^ c16) << 4);
    unsigned va0;
    { const int tq = 4 * g4 + (c16 >> 2), pp = lane & 3; va0 = ring + 4096 + tq * 256 + ((tq & 7) << 5) + ((pp >> 1) << 4) + ((pp & 1) << 3); }
#pragma unroll 1
    for (int t = 0; t < nt; ++t) {
      const int young = min(nt - 1 - t, RH - 2);
      if (young == 2) asm volatile("s_waitcnt vmcnt(16) lgkmcnt(0)" ::: "memory");
      else if (young == 1) asm volatile("s_waitcnt vmcnt(8) lgkmcnt(0)" ::: "memory");
      else asm volatile("s_waitcnt vmcnt(0) lgkmcnt(0)" ::: "memory");
      if (t + RH - 1 < nt) issue(t + RH - 1);
      const unsigned sb = (t & (RH - 1)) * 8192;
      tile_compute<2>(lds, ka0 + sb, va0 + sb, qf, acc, m, l, 0, 16, g4);
    }
    STAMP(2);
    constexpr unsigned ROWB = 528, WAVEB = 32 * ROWB, MLB = 4 * WAVEB;
    asm volatile("s_waitcnt lgkmcnt(0)\n\ts_barrier" ::: "memory");
#pragma unroll
    for (int qb = 0; qb < 2; ++qb) {
      const float lt = xg_sum(l[qb]);
#pragma unroll
      for (int k = 0; k < 8; ++k) *(LAS f32x4*)(lds + w * WAVEB + (16 * qb + c16) * ROWB + (16 * k + 4 * g4) * 4) = acc[qb][k];
      if (g4 == 0) *(LAS f32x2*)(lds + MLB + (w * 32 + 16 * qb + c16) * 8) = f32x2{m[qb] * C2, lt};
    }
    asm volatile("s_waitcnt lgkmcnt(0)\n\ts_barrier" ::: "memory");
    const unsigned slot = WS_HP + (h * 16 + part) * HP_W;
    float mrow = 0.f, lrow = 0.f;
#pragma unroll
    for (int rr = 0; rr < 8; ++rr) {
      const int row = 8 * w + rr;
      f32x2 ml[4], o[4];
#pragma unroll
      for (int v = 0; v < 4; ++v) { ml[v] = *(LAS f32x2*)(lds + MLB + (v * 32 + row) * 8); o[v] = *(LAS f32x2*)(lds + v * WAVEB + row * ROWB + lane * 8); }
      const float M = fmaxf(fmaxf(ml[0][0], ml[1][0]), fmaxf(ml[2][0], ml[3][0]));
      float L = 0.f, o0 = 0.f, o1 = 0.f;
#pragma unroll
      for (int v = 0; v < 4; ++v) {
        const float e = ml[v][1] > 0.f ? __builtin_amdgcn_exp2f(ml[v][0] - M) : 0.f;
        L = fmaf(ml[v][1], e, L); o0 = fmaf(o[v][0], e, o0); o1 = fmaf(o[v][1], e, o1);
      }
      const unsigned tag = (unsigned)rl((int)gvh, row >> 1) + 1u;
      const u32x4 gr = {__float_as_uint(o0), tag, __float_as_uint(o1), tag};
      __builtin_amdgcn_raw_buffer_store_b128(gr, wsr, slot + row * 1024 + lane * 16, 0, 16);
      if (lane == rr) { mrow = M; lrow = L; }
    }
    const unsigned tagr = (unsigned)__shfl((int)gvh, ((8 * w + lane) >> 1) & 15) + 1u;
    if (lane < 8) {
      const unsigned tag = tagr;
      const u32x4 gm = {__float_as_uint(mrow), tag, __float_as_uint(lrow), tag};
      __builtin_amdgcn_raw_buffer_store_b128(gm, wsr, slot + 32768 + (8 * w + lane) * 16, 0, 16);
    }
    STAMP(1);
    STAMP(3);
    return;
  }

  const int j = jj - 4 * kH, h = hg * 4 + w;
  const int x0 = rl(r_lo, j), x1 = rl(r_hi, j);
  const unsigned umask = (unsigned)(__ballot(max(x0, vS) < min(x1, vS + vL)) & 0xffffull);
  unsigned voffK[4], voffV[4];
#pragma unroll
  for (int i = 0; i < 4; ++i) {
    const int t = 4 * w + i;
    voffK[i] = g4 * 256 + ((c16 ^ t) << 4) + t * 4096 - i * 1024 + 4096;
    voffV[i] = g4 * 256 + ((c16 ^ (2 * (t & 7))) << 4) + t * 4096 - i * 1024 + 4096;
  }
  const unsigned kaddr0 = w * 256 + c16 * 1024 + ((g4 ^ c16) << 4);
  unsigned vaddr0;
  { const int tq = 4 * g4 + (c16 >> 2), pp = lane & 3; vaddr0 = 16384 + tq * 1024 + w * 256 + ((tq & 7) << 5) + ((pp >> 1) << 4) + ((pp & 1) << 3); }
  const char* kbase = p.k + hg * 1024 - 4096;
  const char* vbase = p.v + hg * 1024 - 4096;

  unsigned i_mask = umask; int i_row = 0, i_t0 = 0, i_t1 = 0, i_page; bool i_more = false;
  int n_iss = 0, i_slot = 0;
  auto unit_range = [&](int b, int& t0, int& t1) {
    const int s = rl(vS, b), L = rl(vL, b);
    t0 = tsh + max(x0, s) - s; t1 = tsh + min(x1, s + L) - s;
  };
  auto q_dma = [&](int b, int slot) {
    if (lane < 32) dma16(p.q + b * 8192 + h * 512, lane * 16, LDS_QB + (w * R + slot) * 512);
  };
  if (i_mask) {
    i_row = __builtin_ctz(i_mask); i_mask &= i_mask - 1; unit_range(i_row, i_t0, i_t1);
    q_dma(i_row, 0);
    i_more = true;
  }
  i_page = i_t0 >> 4;
  int i_pg0 = i_page, vpg = *(LAS int*)(lds + LDS_BT + (i_row * NBLK + min(i_page + lane, NBLK - 1)) * 4);
  auto issue_stage = [&]() {
    const int phys = rl(vpg, i_page - i_pg0);
    const int lo = i_t0 - i_page * 16, hi = i_t1 - i_page * 16;
    const size_t pb = (size_t)(unsigned)phys * 65536;
    const unsigned dst = i_slot * STAGE + 4 * w * 1024;
    if (lo <= 0 && hi >= 16) {
      dma_stage(kbase + pb, vbase + pb, dst, dst + 16384, voffK[0], voffK[1], voffK[2], voffK[3], voffV[0], voffV[1], voffV[2], voffV[3]);
    } else {
      unsigned ok[4], ov[4];
#pragma unroll
      for (int i = 0; i < 4; ++i) {
        const int d = (min(max(4 * w + i, lo), hi - 1) - (4 * w + i)) * 4096;
        ok[i] = voffK[i] + d; ov[i] = voffV[i] + d;
      }
      dma_stage(kbase + pb, vbase + pb, dst, dst + 16384, ok[0], ok[1], ok[2], ok[3], ov[0], ov[1], ov[2], ov[3]);
    }
    ++n_iss; i_slot = (i_slot + 1 == R) ? 0 : i_slot + 1;
    ++i_page;
    if (i_page * 16 >= i_t1) {
      if (i_mask) {
        i_row = __builtin_ctz(i_mask); i_mask &= i_mask - 1; unit_range(i_row, i_t0, i_t1); i_page = i_t0 >> 4;
        q_dma(i_row, i_slot);
        i_pg0 = i_page; vpg = *(LAS int*)(lds + LDS_BT + (i_row * NBLK + min(i_page + lane, NBLK - 1)) * 4);
      } else i_more = false;
    } else if (i_page - i_pg0 == 64) {
      i_pg0 = i_page; vpg = *(LAS int*)(lds + LDS_BT + (i_row * NBLK + min(i_page + lane, NBLK - 1)) * 4);
    }
  };
  PROBE(2);
#pragma unroll 1
  for (int s = 0; s < R - 1; ++s) if (i_more) issue_stage();

  unsigned own = 0;
  {
    unsigned mrem = umask;
    while (mrem) {
      const int b = __builtin_ctz(mrem); mrem &= mrem - 1;
      const int s = rl(vS, b), L = rl(vL, b);
      unsigned long long pmk = __ballot(lane < nW && max(r_lo, s) < min(r_hi, s + L));
      int idx = w % __builtin_popcountll(pmk);
      while (idx--) pmk &= pmk - 1;
      if (__builtin_ctzll(pmk) == j) own |= 1u << b;
    }
    unsigned zl = (unsigned)(__ballot(vL == 0) & 0xffffull);
    while (zl) { const int b = __builtin_ctz(zl); zl &= zl - 1; if ((b * 4) % nW == j) own |= 1u << b; }
  }

  int n_comp = 0, c_slot = 0;
  unsigned c_mask = umask;
  float ro[2][2] = {{0.f, 0.f}, {0.f, 0.f}}, rm[2] = {-INFINITY, -INFINITY}, rs[2] = {0.f, 0.f};
  float ho[2][2] = {{0.f, 0.f}, {0.f, 0.f}}, hm[2] = {-INFINITY, -INFINITY}, hs[2] = {0.f, 0.f};
  int last_b = -1, held_b = -1;
  auto publish = [&](int b, const float (&o)[2][2], const float (&mm)[2], const float (&ll)[2]) {
    const unsigned tag = (unsigned)rl((int)gvw, b) + 1u;
    const unsigned slot = WS_PP + ((hg * 80 + j + b) * 4 + w) * PP_W;
    const u32x4 g0 = {__float_as_uint(o[0][0]), tag, __float_as_uint(o[0][1]), tag};
    const u32x4 g1 = {__float_as_uint(o[1][0]), tag, __float_as_uint(o[1][1]), tag};
    __builtin_amdgcn_raw_buffer_store_b128(g0, wsr, slot + lane * 16, 0, 16);
    __builtin_amdgcn_raw_buffer_store_b128(g1, wsr, slot + 1024 + lane * 16, 0, 16);
    if (lane < 2) {
      const u32x4 gm = {__float_as_uint(lane ? mm[1] : mm[0]), tag, __float_as_uint(lane ? ll[1] : ll[0]), tag};
      __builtin_amdgcn_raw_buffer_store_b128(gm, wsr, slot + 2048 + lane * 16, 0, 16);
    }
  };
  while (c_mask) {
    const int b = __builtin_ctz(c_mask); c_mask &= c_mask - 1;
    int t0, t1; unit_range(b, t0, t1);
    bf16x8 qf[1][4];
    f32x4 acc[1][8];
    float m[1] = {-INFINITY}, l[1] = {0.f};
#pragma unroll
    for (int k = 0; k < 8; ++k) acc[0][k] = f32x4{0.f, 0.f, 0.f, 0.f};
    bool first = true;
    for (int page = t0 >> 4; page * 16 < t1; ++page) {
      if (n_iss - n_comp > 1) asm volatile("s_waitcnt vmcnt(8) lgkmcnt(0)\n\ts_barrier" ::: "memory");
      else asm volatile("s_waitcnt vmcnt(0) lgkmcnt(0)\n\ts_barrier" ::: "memory");
      PROBE(3);
      if (n_comp == 12) PROBE(4);
      if (first) {
        const bool z = (c16 >= 2) || ((zmask >> b) & 1);
#pragma unroll
        for (int s = 0; s < 4; ++s) {
          u32x4 x = *(LAS u32x4*)(lds + LDS_QB + (w * R + c_slot) * 512 + (c16 & 1) * 256 + (4 * s + g4) * 16);
          if (z) x = u32x4{0u, 0u, 0u, 0u};
          qf[0][s] = __builtin_bit_cast(bf16x8, x);
        }
        asm volatile("s_waitcnt lgkmcnt(0)" ::: "memory");
        first = false;
      }
      if (i_more) issue_stage();
      const unsigned sb = c_slot * STAGE;
      tile_compute<1>(lds, kaddr0 + sb, vaddr0 + sb, qf, acc, m, l, t0 - page * 16, t1 - page * 16, g4);
      ++n_comp; c_slot = (c_slot + 1 == R) ? 0 : c_slot + 1;
    }
    const float lt = xg_sum(l[0]);
    LAS char* sc = lds + LDS_SC + w * 1024;
    if (c16 < 2) {
#pragma unroll
      for (int k = 0; k < 8; ++k) *(LAS f32x4*)(sc + c16 * 512 + (16 * k + 4 * g4) * 4) = acc[0][k];
    }
    asm volatile("s_waitcnt lgkmcnt(0)" ::: "memory");
    const f32x2 a0 = *(LAS f32x2*)(sc + lane * 8), a1 = *(LAS f32x2*)(sc + 512 + lane * 8);
    if (last_b >= 0) {
      if (held_b >= 0) publish(held_b, ho, hm, hs);
      held_b = last_b;
#pragma unroll
      for (int r = 0; r < 2; ++r) { ho[r][0] = ro[r][0]; ho[r][1] = ro[r][1]; hm[r] = rm[r]; hs[r] = rs[r]; }
    }
    ro[0][0] = a0[0]; ro[0][1] = a0[1]; ro[1][0] = a1[0]; ro[1][1] = a1[1];
    rm[0] = rlf(m[0], 0) * C2; rm[1] = rlf(m[0], 1) * C2;
    rs[0] = rlf(lt, 0); rs[1] = rlf(lt, 1);
    last_b = b;
  }
  STAMP(2);
  PROBE(5);
  if (last_b >= 0 && !((own >> last_b) & 1)) { publish(last_b, ro, rm, rs); last_b = -1; }
  if (held_b >= 0 && !((own >> held_b) & 1)) { publish(held_b, ho, hm, hs); held_b = -1; }
  STAMP(1);

  while (own) {
    const int b = __builtin_ctz(own); own &= own - 1;
    const unsigned tag = (unsigned)rl((int)gvw, b) + 1u;
    const int s = rl(vS, b), L = rl(vL, b);
    unsigned long long pmk = __ballot(lane < nW && max(r_lo, s) < min(r_hi, s + L));
    unsigned smk = (1u << kH) - 1u;
    float M0 = -INFINITY, M1 = -INFINITY, L0 = 0.f, L1 = 0.f, o00 = 0.f, o01 = 0.f, o10 = 0.f, o11 = 0.f;
    if (b == last_b) {
      pmk &= ~(1ull << j);
      M0 = rm[0]; M1 = rm[1]; L0 = rs[0]; L1 = rs[1]; o00 = ro[0][0]; o01 = ro[0][1]; o10 = ro[1][0]; o11 = ro[1][1];
    } else if (b == held_b) {
      pmk &= ~(1ull << j);
      M0 = hm[0]; M1 = hm[1]; L0 = hs[0]; L1 = hs[1]; o00 = ho[0][0]; o01 = ho[0][1]; o10 = ho[1][0]; o11 = ho[1][1];
    }
    const unsigned p_rows = WS_PP + ((hg * 80 + lane + b) * 4 + w) * PP_W;
    const unsigned s_base = WS_HP + (h * 16 + (lane & 15)) * HP_W;
    unsigned spins = 0;
    bool light = false;
    while (pmk | smk) {
      unsigned long long tp = pmk; unsigned ts = smk;
      const bool single = (__builtin_popcountll(pmk) + __builtin_popcount(smk)) == 1;
      if (light && !single) {
        const int jp = pmk ? (((pmk >> lane) & 1) ? lane : __builtin_ctzll(pmk)) : 0;
        const int js = smk ? (((smk >> (lane & 15)) & 1) ? (lane & 15) : __builtin_ctz(smk)) : 0;
        u32x4 a = {0u, tag, 0u, tag}, c = {0u, tag, 0u, tag};
        if (pmk) a = __builtin_amdgcn_raw_buffer_load_b128(wsr, WS_PP + ((hg * 80 + jp + b) * 4 + w) * PP_W + 2048 + 16, 0, 16);
        if (smk) c = __builtin_amdgcn_raw_buffer_load_b128(wsr, WS_HP + (h * 16 + js) * HP_W + 32768 + (2 * b + 1) * 16, 0, 16);
        tp &= __ballot(a[1] == tag && a[3] == tag);
        ts &= (unsigned)__ballot(c[1] == tag && c[3] == tag);
      }
      bool progress = false;
      if (tp | ts) {
        auto batch = [&](auto nb_tag) {
          constexpr int NB = decltype(nb_tag)::value;
          unsigned rows[NB], mls[NB]; unsigned long long bitp[NB]; unsigned bits[NB];
#pragma unroll
          for (int k = 0; k < NB; ++k) {
            if (tp) { const int q = __builtin_ctzll(tp); bitp[k] = tp & (~tp + 1); bits[k] = 0; tp &= tp - 1; rows[k] = (unsigned)rl((int)p_rows, q); mls[k] = rows[k] + 2048; }
            else if (ts) { const int q = __builtin_ctz(ts); bits[k] = ts & (~ts + 1); bitp[k] = 0; ts &= ts - 1;
                           const unsigned sl = (unsigned)rl((int)s_base, q); rows[k] = sl + b * 2048; mls[k] = sl + 32768 + b * 32; }
            else { bitp[k] = 0; bits[k] = 0; rows[k] = rows[0]; mls[k] = mls[0]; }
          }
          u32x4 d0[NB], d1[NB], dm[NB];
#pragma unroll
          for (int k = 0; k < NB; ++k) {
            d0[k] = __builtin_amdgcn_raw_buffer_load_b128(wsr, lane * 16, rows[k], 16);
            d1[k] = __builtin_amdgcn_raw_buffer_load_b128(wsr, 1024 + lane * 16, rows[k], 16);
            dm[k] = __builtin_amdgcn_raw_buffer_load_b128(wsr, (lane & 1) * 16, mls[k], 16);
          }
#pragma unroll
          for (int k = 0; k < NB; ++k) {
            const bool ok = d0[k][1] == tag && d0[k][3] == tag && d1[k][1] == tag && d1[k][3] == tag && dm[k][1] == tag && dm[k][3] == tag;
            if ((bitp[k] | bits[k]) && __all(ok)) {
              const float mv = __uint_as_float(dm[k][0]), lv = __uint_as_float(dm[k][2]);
              const float m0 = rlf(mv, 0), m1 = rlf(mv, 1), l0 = rlf(lv, 0), l1 = rlf(lv, 1);
              const float n0 = fmaxf(M0, m0), n1 = fmaxf(M1, m1);
              const float a0 = __builtin_amdgcn_exp2f(M0 - n0), b0 = __builtin_amdgcn_exp2f(m0 - n0);
              const float a1 = __builtin_amdgcn_exp2f(M1 - n1), b1 = __builtin_amdgcn_exp2f(m1 - n1);
              L0 = L0 * a0 + l0 * b0; L1 = L1 * a1 + l1 * b1;
              o00 = o00 * a0 + __uint_as_float(d0[k][0]) * b0; o01 = o01 * a0 + __uint_as_float(d0[k][2]) * b0;
              o10 = o10 * a1 + __uint_as_float(d1[k][0]) * b1; o11 = o11 * a1 + __uint_as_float(d1[k][2]) * b1;
              M0 = n0; M1 = n1;
              pmk &= ~bitp[k]; smk &= ~bits[k];
              progress = true;
            }
          }
        };
        const int cnt = __builtin_popcountll(tp) + __builtin_popcount(ts);
        if (cnt == 1) batch(IntC<1>{}); else if (cnt <= 3) batch(IntC<3>{}); else batch(IntC<6>{});
      }
      if (!progress) {
        light = true;
        if (++spins > (1u << 16)) break;
        __builtin_amdgcn_s_sleep(SLEEP);
      }
    }
    const float i0 = 1.f / L0, i1 = 1.f / L1;
    __hip_bfloat16* orow = p.out + (b * NH + 2 * h) * DH + 2 * lane;
    *(unsigned*)orow = pk_bf16(o00 * i0, o01 * i0);
    *(unsigned*)(orow + DH) = pk_bf16(o10 * i1, o11 * i1);
    if (lane == 0) __builtin_amdgcn_raw_buffer_store_b32(tag, wsr, WS_G + (h * 16 + b) * 4, 0, 16);
  }
  STAMP(3);
  PROBE(6);
  PROBE_END();
}

}

extern "C" void kernel_launch(void* const* d_in, const int* in_sizes, int n_in, void* d_out, int out_size, void* d_ws, size_t ws_size,
                              hipStream_t stream) {
  using namespace rattn;
  Params p;
  p.q = (const char*)d_in[0]; p.k = (const char*)d_in[1]; p.v = (const char*)d_in[2];
  p.bt = (const int*)d_in[3]; p.lens = (const int*)d_in[4];
  p.out = (__hip_bfloat16*)d_out; p.ws = (char*)d_ws;
  (void)hipFuncSetAttribute((const void*)radix_attn_kernel, hipFuncAttributeMaxDynamicSharedMemorySize, LDS_TOTAL);
  hipLaunchKernelGGL(radix_attn_kernel, dim3(256), dim3(256), LDS_TOTAL, stream, p);
}
```

```cpp
#include <hip/hip_runtime.h>
#include <hip/hip_bf16.h>
#include <stdint.h>

namespace rattn {

constexpr int NSEQ = 16, NH = 32, NKV = 16, DH = 128, NBLK = 256, BS = 16;
constexpr int LK = NBLK * BS;
#define HRHO 50

#define MAPMODE 1
#define SLEEP 6
#define KVPOL "nt"
constexpr int R = 3;
constexpr int STAGE = 32768;
constexpr int RH = 4;
constexpr int RING_BYTES = 4 * RH * 8192;
constexpr int LDS_BT = RING_BYTES;
constexpr int LDS_QB = LDS_BT + NSEQ * NBLK * 4;
constexpr int LDS_SC = LDS_QB + 4 * R * 512;
constexpr int LDS_MISC = LDS_SC + 4096;
constexpr int LDS_JUNK = LDS_MISC + 64;
constexpr int LDS_TOTAL = LDS_JUNK + 1024;
static_assert(LDS_TOTAL <= 160 * 1024 && LDS_MISC - LDS_QB >= 8192 && RING_BYTES >= R * STAGE, "LDS");

constexpr unsigned WS_G = 0;
constexpr unsigned WS_PP = 4096;
constexpr unsigned PP_W = 2176;
constexpr unsigned WS_HP = 4u << 20;
constexpr unsigned HP_W = 32768 + 512;
constexpr unsigned WS_END = WS_HP + 16u * 16u * HP_W;
constexpr unsigned WS_STAMP = 64u << 20;

typedef unsigned u32x4 __attribute__((ext_vector_type(4)));
typedef unsigned u32x2 __attribute__((ext_vector_type(2)));
typedef float f32x4 __attribute__((ext_vector_type(4)));
typedef float f32x2 __attribute__((ext_vector_type(2)));
typedef __bf16 bf16x8 __attribute__((ext_vector_type(8)));
typedef __bf16 bf16x2 __attribute__((ext_vector_type(2)));
typedef short s16x4 __attribute__((ext_vector_type(4)));
typedef int i32x4 __attribute__((ext_vector_type(4)));
#define LAS __attribute__((address_space(3)))

constexpr float C2 = 0.088388347648318440f * 1.4426950408889634f;

__device__ __forceinline__ int rfl(int x) { return __builtin_amdgcn_readfirstlane(x); }
__device__ __forceinline__ int rl(int x, int l) { return __builtin_amdgcn_readlane(x, l); }
__device__ __forceinline__ float rlf(float x, int l) { return __builtin_bit_cast(float, __builtin_amdgcn_readlane(__builtin_bit_cast(int, x), l)); }

__device__ __forceinline__ void dma16(const void* sbase, unsigned voff, unsigned dst) {
  unsigned keep;
  asm volatile("s_mov_b32 %0, m0\n\ts_mov_b32 m0, %3\n\ts_nop 0\n\tglobal_load_lds_dwordx4 %1, %2\n\ts_mov_b32 m0, %0"
               : "=&s"(keep) : "v"(voff), "s"(sbase), "s"(dst) : "memory");
}
__device__ __forceinline__ void dma_stage(const void* sK, const void* sV, unsigned dstK, unsigned dstV,
                                          unsigned k0, unsigned k1, unsigned k2, unsigned k3,
                                          unsigned v0, unsigned v1, unsigned v2, unsigned v3) {
  unsigned keep;
  asm volatile("s_mov_b32 %0, m0\n\ts_mov_b32 m0, %3\n\ts_nop 0\n\t"
               "global_load_lds_dwordx4 %5, %1 " KVPOL "\n\t"
               "global_load_lds_dwordx4 %6, %1 offset:1024 " KVPOL "\n\t"
               "global_load_lds_dwordx4 %7, %1 offset:2048 " KVPOL "\n\t"
               "global_load_lds_dwordx4 %8, %1 offset:3072 " KVPOL "\n\t"
               "s_mov_b32 m0, %4\n\ts_nop 0\n\t"
               "global_load_lds_dwordx4 %9, %2 " KVPOL "\n\t"
               "global_load_lds_dwordx4 %10, %2 offset:1024 " KVPOL "\n\t"
               "global_load_lds_dwordx4 %11, %2 offset:2048 " KVPOL "\n\t"
               "global_load_lds_dwordx4 %12, %2 offset:3072 " KVPOL "\n\t"
               "s_mov_b32 m0, %0"
               : "=&s"(keep)
               : "s"(sK), "s"(sV), "s"(dstK), "s"(dstV), "v"(k0), "v"(k1), "v"(k2), "v"(k3), "v"(v0), "v"(v1), "v"(v2), "v"(v3)
               : "memory");
}
template <int CTRL> __device__ __forceinline__ int dppi(int x) { return __builtin_amdgcn_update_dpp(0, x, CTRL, 0xf, 0xf, true); }
template <int CTRL> __device__ __forceinline__ int dppi_keep(int old, int x) { return __builtin_amdgcn_update_dpp(old, x, CTRL, 0xf, 0xf, false); }

__device__ __forceinline__ float xg_max(float x) {
  auto s = __builtin_amdgcn_permlane16_swap(__float_as_uint(x), __float_as_uint(x), false, false);
  x = fmaxf(__uint_as_float(s[0]), __uint_as_float(s[1]));
  auto t = __builtin_amdgcn_permlane32_swap(__float_as_uint(x), __float_as_uint(x), false, false);
  return fmaxf(__uint_as_float(t[0]), __uint_as_float(t[1]));
}
__device__ __forceinline__ float xg_sum(float x) {
  auto s = __builtin_amdgcn_permlane16_swap(__float_as_uint(x), __float_as_uint(x), false, false);
  x = __uint_as_float(s[0]) + __uint_as_float(s[1]);
  auto t = __builtin_amdgcn_permlane32_swap(__float_as_uint(x), __float_as_uint(x), false, false);
  return __uint_as_float(t[0]) + __uint_as_float(t[1]);
}
__device__ __forceinline__ unsigned pk_bf16(float a, float b) {
  bf16x2 v = {(__bf16)a, (__bf16)b};
  return __builtin_bit_cast(unsigned, v);
}

template <int NQ>
__device__ __forceinline__ void tile_compute(LAS char* lds, unsigned ka, unsigned va, const bf16x8 (&qf)[NQ][4], f32x4 (&acc)[NQ][8],
                                             float (&m)[NQ], float (&l)[NQ], int lo, int hi, int g4) {
  bf16x8 kf[4];
#pragma unroll
  for (int s = 0; s < 4; ++s) kf[s] = *(LAS bf16x8*)(lds + (ka ^ (s << 6)));
  s16x4 vf[8];
#pragma unroll
  for (int k = 0; k < 8; ++k) vf[k] = __builtin_amdgcn_ds_read_tr16_b64_v4i16((LAS s16x4*)(lds + (va ^ (k << 5))));
  f32x4 S[NQ];
#pragma unroll
  for (int qb = 0; qb < NQ; ++qb) {
    S[qb] = f32x4{0.f, 0.f, 0.f, 0.f};
#pragma unroll
    for (int s = 0; s < 4; ++s) S[qb] = __builtin_amdgcn_mfma_f32_16x16x32_bf16(kf[s], qf[qb][s], S[qb], 0, 0, 0);
  }
  if (lo > 0 || hi < 16) {
#pragma unroll
    for (int qb = 0; qb < NQ; ++qb)
#pragma unroll
      for (int r = 0; r < 4; ++r) { const int t = 4 * g4 + r; S[qb][r] = (t >= lo && t < hi) ? S[qb][r] : -INFINITY; }
  }
  s16x4 pb[NQ];
  bool grow = false;
  float alpha[NQ];
#pragma unroll
  for (int qb = 0; qb < NQ; ++qb) {
    float mx = fmaxf(fmaxf(S[qb][0], S[qb][1]), fmaxf(S[qb][2], S[qb][3]));
    mx = xg_max(mx);
    const float mn2 = fmaxf(m[qb], mx);
    grow |= mn2 > m[qb];
    alpha[qb] = __builtin_amdgcn_exp2f((m[qb] - mn2) * C2);
    m[qb] = mn2;
    const float mc = -mn2 * C2;
    const float p0 = __builtin_amdgcn_exp2f(fmaf(S[qb][0], C2, mc)), p1 = __builtin_amdgcn_exp2f(fmaf(S[qb][1], C2, mc));
    const float p2 = __builtin_amdgcn_exp2f(fmaf(S[qb][2], C2, mc)), p3 = __builtin_amdgcn_exp2f(fmaf(S[qb][3], C2, mc));
    l[qb] = fmaf(l[qb], alpha[qb], (p0 + p1) + (p2 + p3));
    pb[qb] = __builtin_bit_cast(s16x4, u32x2{pk_bf16(p0, p1), pk_bf16(p2, p3)});
  }
  if (__any(grow)) {
#pragma unroll
    for (int qb = 0; qb < NQ; ++qb)
#pragma unroll
      for (int k = 0; k < 8; ++k) acc[qb][k] *= alpha[qb];
  }
#pragma unroll
  for (int qb = 0; qb < NQ; ++qb)
#pragma unroll
    for (int k = 0; k < 8; ++k) acc[qb][k] = __builtin_amdgcn_mfma_f32_16x16x16bf16_1k(vf[k], pb[qb], acc[qb][k], 0, 0, 0);
}

template <int V> struct IntC { static constexpr int value = V; };

struct Params {
  const char* q; const char* k; const char* v; const int* bt; const int* lens; __hip_bfloat16* out; char* ws;
};

__global__ void __launch_bounds__(256, 2) radix_attn_kernel(Params p) {
  extern __shared__ __attribute__((aligned(16))) char smem[];
  LAS char* lds = (LAS char*)smem;
  const int tid = threadIdx.x, lane = tid & 63, w = rfl(tid >> 6);
  const int gidx = blockIdx.x;
  const int hg = (gidx >> 3) & 3;
  const int jj = (((gidx & 7) + 4 * (hg & 1)) & 7) | ((gidx >> 5) << 3);
  const int c16 = lane & 15, g4 = lane >> 4;
#define STAMP(i) do { } while (0)
  STAMP(0);
  const auto wsr = __builtin_amdgcn_make_buffer_rsrc(p.ws, 0, WS_END, 0x00020000);

  const int lenraw = p.lens[c16];
  const unsigned gvw = __builtin_amdgcn_raw_buffer_load_b32(wsr, WS_G + ((hg * 4 + w) * 16 + c16) * 4, 0, 16);
  const unsigned gvh = __builtin_amdgcn_raw_buffer_load_b32(wsr, WS_G + ((hg * 4 + (jj & 3)) * 16 + c16) * 4, 0, 16);
  int pm;
  {
    const i32x4* bt4 = (const i32x4*)p.bt;
    const i32x4 r0 = bt4[lane];
    i32x4 rr[4];
#pragma unroll
    for (int i = 0; i < 4; ++i) rr[i] = bt4[(4 * w + i) * 64 + lane];
    bool n0 = false, n1 = false, n2 = false, n3 = false;
#pragma unroll
    for (int i = 0; i < 4; ++i) {
      n0 |= rr[i][0] != r0[0]; n1 |= rr[i][1] != r0[1]; n2 |= rr[i][2] != r0[2]; n3 |= rr[i][3] != r0[3];
      *(LAS i32x4*)(lds + LDS_BT + ((4 * w + i) * 256 + 4 * lane) * 4) = rr[i];
    }
    const int fc = n0 ? 0 : n1 ? 1 : n2 ? 2 : n3 ? 3 : 4;
    const unsigned long long mm = __ballot(fc < 4);
    int col = NBLK;
    if (mm) { const int fl = __builtin_ctzll(mm); col = 4 * fl + rl(fc, fl); }
    if (lane == 0) *(LAS int*)(lds + LDS_MISC + 4 * w) = col;
    asm volatile("s_waitcnt lgkmcnt(0)\n\ts_barrier" ::: "memory");
    const i32x4 mc = *(LAS i32x4*)(lds + LDS_MISC);
    pm = rfl(min(min(mc[0], mc[1]), min(mc[2], mc[3])));
  }
  asm volatile("" :: "v"(gvw), "v"(gvh));
  const bool zq_l = lenraw <= 0;
  const int len_l = zq_l ? LK : min(lenraw, LK);
  int mn = len_l;
  mn = min(mn, dppi_keep<0x128>(mn, mn)); mn = min(mn, dppi_keep<0x124>(mn, mn));
  mn = min(mn, dppi_keep<0x122>(mn, mn)); mn = min(mn, dppi_keep<0x121>(mn, mn));
  const int P = min(pm, rfl(mn) >> 4);
  const int tsh = P * BS;
  const int vL = len_l - tsh;
  int inc = vL;
  inc += dppi<0x111>(inc); inc += dppi<0x112>(inc); inc += dppi<0x114>(inc); inc += dppi<0x118>(inc);
  const int N = rl(inc, 15);
  const int vS = inc - vL;
  int kH = 0;
  if (P > 0) kH = min(15, max(1, rfl((int)(((unsigned)tsh * 16u * 64u + (unsigned)(tsh + N) * HRHO - 1u) / ((unsigned)(tsh + N) * HRHO)))));
  kH = min(kH, max(P, 0));
  const int nW = 64 - 4 * kH;
  const unsigned zmask = (unsigned)(__ballot(zq_l) & 0xffffull);
  const bool isH = jj < 4 * kH;
  const int r_lo = (int)((unsigned)(lane * N) / (unsigned)nW), r_hi = (int)((unsigned)((lane + 1) * N) / (unsigned)nW);

  if (isH) {
    const int h = hg * 4 + (jj & 3), part = jj >> 2;
    const int p0 = (P * part) / kH, p1 = (P * (part + 1)) / kH;
#pragma unroll
    for (int i = 0; i < 2; ++i)
      dma16(p.q + (2 * w + i) * 16384 + h * 512, (lane >> 5) * 8192 + (lane & 31) * 16, LDS_QB + (2 * w + i) * 1024);
    unsigned oK[4], oV[4];
#pragma unroll
    for (int i = 0; i < 4; ++i) {
      const int t = 4 * i + g4;
      oK[i] = t * 4096 + ((c16 ^ t) << 4) - i * 1024 + 4096;
      oV[i] = t * 4096 + ((c16 ^ (2 * (t & 7))) << 4) - i * 1024 + 4096;
    }
    const char* kb = p.k + h * 256 - 4096;
    const char* vb = p.v + h * 256 - 4096;
    const int nt = max(0, (p1 - p0 - w + 3) >> 2);
    const unsigned ring = w * (RH * 8192);
    int pg0 = 0, vpg = *(LAS int*)(lds + LDS_BT + min(p0 + w + 4 * lane, NBLK - 1) * 4);
    auto issue = [&](int t) {
      if (t - pg0 == 64) { pg0 = t; vpg = *(LAS int*)(lds + LDS_BT + min(p0 + w + 4 * (t + lane), NBLK - 1) * 4); }
      const size_t pb = (size_t)(unsigned)rl(vpg, t - pg0) * 65536;
      const unsigned dst = ring + (t & (RH - 1)) * 8192;
      dma_stage(kb + pb, vb + pb, dst, dst + 4096, oK[0], oK[1], oK[2], oK[3], oV[0], oV[1], oV[2], oV[3]);
    };
    const int npre = min(nt, RH - 1);
#pragma unroll 1
    for (int t = 0; t < npre; ++t) issue(t);
    if (npre == 3) asm volatile("s_waitcnt vmcnt(24)\n\ts_barrier" ::: "memory");
    else asm volatile("s_waitcnt vmcnt(0)\n\ts_barrier" ::: "memory");
    bf16x8 qf[2][4];
#pragma unroll
    for (int qb = 0; qb < 2; ++qb) {
      const bool z = (zmask >> (8 * qb + (c16 >> 1))) & 1;
#pragma unroll
      for (int s = 0; s < 4; ++s) {
        u32x4 x = *(LAS u32x4*)(lds + LDS_QB + (16 * qb + c16) * 256 + (4 * s + g4) * 16);
        if (z) x = u32x4{0u, 0u, 0u, 0u};
        qf[qb][s] = __builtin_bit_cast(bf16x8, x);
      }
    }
    f32x4 acc[2][8];
    float m[2] = {-INFINITY, -INFINITY}, l[2] = {0.f, 0.f};
#pragma unroll
    for (int qb = 0; qb < 2; ++qb)
#pragma unroll
      for (int k = 0; k < 8; ++k) acc[qb][k] = f32x4{0.f, 0.f, 0.f, 0.f};
    const unsigned ka0 = ring + c16 * 256 + ((g4 ^ c16) << 4);
    unsigned va0;
    { const int tq = 4 * g4 + (c16 >> 2), pp = lane & 3; va0 = ring + 4096 + tq * 256 + ((tq & 7) << 5) + ((pp >> 1) << 4) + ((pp & 1) << 3); }
#pragma unroll 1
    for (int t = 0; t < nt; ++t) {
      const int young = min(nt - 1 - t, RH - 2);
      if (young == 2) asm volatile("s_waitcnt vmcnt(16) lgkmcnt(0)" ::: "memory");
      else if (young == 1) asm volatile("s_waitcnt vmcnt(8) lgkmcnt(0)" ::: "memory");
      else asm volatile("s_waitcnt vmcnt(0) lgkmcnt(0)" ::: "memory");
      if (t + RH - 1 < nt) issue(t + RH - 1);
      const unsigned sb = (t & (RH - 1)) * 8192;
      tile_compute<2>(lds, ka0 + sb, va0 + sb, qf, acc, m, l, 0, 16, g4);
    }
    STAMP(2);
    constexpr unsigned ROWB = 528, WAVEB = 32 * ROWB, MLB = 4 * WAVEB;
    asm volatile("s_waitcnt lgkmcnt(0)\n\ts_barrier" ::: "memory");
#pragma unroll
    for (int qb = 0; qb < 2; ++qb) {
      const float lt = xg_sum(l[qb]);
#pragma unroll
      for (int k = 0; k < 8; ++k) *(LAS f32x4*)(lds + w * WAVEB + (16 * qb + c16) * ROWB + (16 * k + 4 * g4) * 4) = acc[qb][k];
      if (g4 == 0) *(LAS f32x2*)(lds + MLB + (w * 32 + 16 * qb + c16) * 8) = f32x2{m[qb] * C2, lt};
    }
    asm volatile("s_waitcnt lgkmcnt(0)\n\ts_barrier" ::: "memory");
    const unsigned slot = WS_HP + (h * 16 + part) * HP_W;
    float mrow = 0.f, lrow = 0.f;
#pragma unroll
    for (int rr = 0; rr < 8; ++rr) {
      const int row = 8 * w + rr;
      f32x2 ml[4], o[4];
#pragma unroll
      for (int v = 0; v < 4; ++v) { ml[v] = *(LAS f32x2*)(lds + MLB + (v * 32 + row) * 8); o[v] = *(LAS f32x2*)(lds + v * WAVEB + row * ROWB + lane * 8); }
      const float M = fmaxf(fmaxf(ml[0][0], ml[1][0]), fmaxf(ml[2][0], ml[3][0]));
      float L = 0.f, o0 = 0.f, o1 = 0.f;
#pragma unroll
      for (int v = 0; v < 4; ++v) {
        const float e = ml[v][1] > 0.f ? __builtin_amdgcn_exp2f(ml[v][0] - M) : 0.f;
        L = fmaf(ml[v][1], e, L); o0 = fmaf(o[v][0], e, o0); o1 = fmaf(o[v][1], e, o1);
      }
      const unsigned tag = (unsigned)rl((int)gvh, row >> 1) + 1u;
      const u32x4 gr = {__float_as_uint(o0), tag, __float_as_uint(o1), tag};
      __builtin_amdgcn_raw_buffer_store_b128(gr, wsr, slot + row * 1024 + lane * 16, 0, 16);
      if (lane == rr) { mrow = M; lrow = L; }
    }
    const unsigned tagr = (unsigned)__shfl((int)gvh, ((8 * w + lane) >> 1) & 15) + 1u;
    if (lane < 8) {
      const unsigned tag = tagr;
      const u32x4 gm = {__float_as_uint(mrow), tag, __float_as_uint(lrow), tag};
      __builtin_amdgcn_raw_buffer_store_b128(gm, wsr, slot + 32768 + (8 * w + lane) * 16, 0, 16);
    }
    STAMP(1);
    STAMP(3);
    return;
  }

  const int j = jj - 4 * kH, h = hg * 4 + w;
  const int x0 = rl(r_lo, j), x1 = rl(r_hi, j);
  const unsigned umask = (unsigned)(__ballot(max(x0, vS) < min(x1, vS + vL)) & 0xffffull);
  unsigned voffK[4], voffV[4];
#pragma unroll
  for (int i = 0; i < 4; ++i) {
    const int t = 4 * w + i;
    voffK[i] = g4 * 256 + ((c16 ^ t) << 4) + t * 4096 - i * 1024 + 4096;
    voffV[i] = g4 * 256 + ((c16 ^ (2 * (t & 7))) << 4) + t * 4096 - i * 1024 + 4096;
  }
  const unsigned kaddr0 = w * 256 + c16 * 1024 + ((g4 ^ c16) << 4);
  unsigned vaddr0;
  { const int tq = 4 * g4 + (c16 >> 2), pp = lane & 3; vaddr0 = 16384 + tq * 1024 + w * 256 + ((tq & 7) << 5) + ((pp >> 1) << 4) + ((pp & 1) << 3); }
  const char* kbase = p.k + hg * 1024 - 4096;
  const char* vbase = p.v + hg * 1024 - 4096;

  unsigned i_mask = umask; int i_row = 0, i_t0 = 0, i_t1 = 0, i_page; bool i_more = false;
  int n_iss = 0, i_slot = 0;
  auto unit_range = [&](int b, int& t0, int& t1) {
    const int s = rl(vS, b), L = rl(vL, b);
    t0 = tsh + max(x0, s) - s; t1 = tsh + min(x1, s + L) - s;
  };
  auto q_dma = [&](int b, int slot) {
    if (lane < 32) dma16(p.q + b * 8192 + h * 512, lane * 16, LDS_QB + (w * R + slot) * 512);
  };
  if (i_mask) {
    i_row = __builtin_ctz(i_mask); i_mask &= i_mask - 1; unit_range(i_row, i_t0, i_t1);
    q_dma(i_row, 0);
    i_more = true;
  }
  i_page = i_t0 >> 4;
  int i_pg0 = i_page, vpg = *(LAS int*)(lds + LDS_BT + (i_row * NBLK + min(i_page + lane, NBLK - 1)) * 4);
  auto issue_stage = [&]() {
    const int phys = rl(vpg, i_page - i_pg0);
    const int lo = i_t0 - i_page * 16, hi = i_t1 - i_page * 16;
    const size_t pb = (size_t)(unsigned)phys * 65536;
    const unsigned dst = i_slot * STAGE + 4 * w * 1024;
    if (lo <= 0 && hi >= 16) {
      dma_stage(kbase + pb, vbase + pb, dst, dst + 16384, voffK[0], voffK[1], voffK[2], voffK[3], voffV[0], voffV[1], voffV[2], voffV[3]);
    } else {
      unsigned ok[4], ov[4];
#pragma unroll
      for (int i = 0; i < 4; ++i) {
        const int d = (min(max(4 * w + i, lo), hi - 1) - (4 * w + i)) * 4096;
        ok[i] = voffK[i] + d; ov[i] = voffV[i] + d;
      }
      dma_stage(kbase + pb, vbase + pb, dst, dst + 16384, ok[0], ok[1], ok[2], ok[3], ov[0], ov[1], ov[2], ov[3]);
    }
    ++n_iss; i_slot = (i_slot + 1 == R) ? 0 : i_slot + 1;
    ++i_page;
    if (i_page * 16 >= i_t1) {
      if (i_mask) {
        i_row = __builtin_ctz(i_mask); i_mask &= i_mask - 1; unit_range(i_row, i_t0, i_t1); i_page = i_t0 >> 4;
        q_dma(i_row, i_slot);
        i_pg0 = i_page; vpg = *(LAS int*)(lds + LDS_BT + (i_row * NBLK + min(i_page + lane, NBLK - 1)) * 4);
      } else i_more = false;
    } else if (i_page - i_pg0 == 64) {
      i_pg0 = i_page; vpg = *(LAS int*)(lds + LDS_BT + (i_row * NBLK + min(i_page + lane, NBLK - 1)) * 4);
    }
  };
#pragma unroll 1
  for (int s = 0; s < R - 1; ++s) if (i_more) issue_stage();

  unsigned own = 0;
  {
    unsigned mrem = umask;
    while (mrem) {
      const int b = __builtin_ctz(mrem); mrem &= mrem - 1;
      const int s = rl(vS, b), L = rl(vL, b);
      unsigned long long pmk = __ballot(lane < nW && max(r_lo, s) < min(r_hi, s + L));
      int idx = w % __builtin_popcountll(pmk);
      while (idx--) pmk &= pmk - 1;
      if (__builtin_ctzll(pmk) == j) own |= 1u << b;
    }
    unsigned zl = (unsigned)(__ballot(vL == 0) & 0xffffull);
    while (zl) { const int b = __builtin_ctz(zl); zl &= zl - 1; if ((b * 4) % nW == j) own |= 1u << b; }
  }

  int n_comp = 0, c_slot = 0;
  unsigned c_mask = umask;
  float ro[2][2] = {{0.f, 0.f}, {0.f, 0.f}}, rm[2] = {-INFINITY, -INFINITY}, rs[2] = {0.f, 0.f};
  float ho[2][2] = {{0.f, 0.f}, {0.f, 0.f}}, hm[2] = {-INFINITY, -INFINITY}, hs[2] = {0.f, 0.f};
  int last_b = -1, held_b = -1;
  auto publish = [&](int b, const float (&o)[2][2], const float (&mm)[2], const float (&ll)[2]) {
    const unsigned tag = (unsigned)rl((int)gvw, b) + 1u;
    const unsigned slot = WS_PP + ((hg * 80 + j + b) * 4 + w) * PP_W;
    const u32x4 g0 = {__float_as_uint(o[0][0]), tag, __float_as_uint(o[0][1]), tag};
    const u32x4 g1 = {__float_as_uint(o[1][0]), tag, __float_as_uint(o[1][1]), tag};
    __builtin_amdgcn_raw_buffer_store_b128(g0, wsr, slot + lane * 16, 0, 16);
    __builtin_amdgcn_raw_buffer_store_b128(g1, wsr, slot + 1024 + lane * 16, 0, 16);
    if (lane < 2) {
      const u32x4 gm = {__float_as_uint(lane ? mm[1] : mm[0]), tag, __float_as_uint(lane ? ll[1] : ll[0]), tag};
      __builtin_amdgcn_raw_buffer_store_b128(gm, wsr, slot + 2048 + lane * 16, 0, 16);
    }
  };
  while (c_mask) {
    const int b = __builtin_ctz(c_mask); c_mask &= c_mask - 1;
    int t0, t1; unit_range(b, t0, t1);
    bf16x8 qf[1][4];
    f32x4 acc[1][8];
    float m[1] = {-INFINITY}, l[1] = {0.f};
#pragma unroll
    for (int k = 0; k < 8; ++k) acc[0][k] = f32x4{0.f, 0.f, 0.f, 0.f};
    bool first = true;
    for (int page = t0 >> 4; page * 16 < t1; ++page) {
      if (n_iss - n_comp > 1) asm volatile("s_waitcnt vmcnt(8) lgkmcnt(0)\n\ts_barrier" ::: "memory");
      else asm volatile("s_waitcnt vmcnt(0) lgkmcnt(0)\n\ts_barrier" ::: "memory");
      if (first) {
        const bool z = (c16 >= 2) || ((zmask >> b) & 1);
#pragma unroll
        for (int s = 0; s < 4; ++s) {
          u32x4 x = *(LAS u32x4*)(lds + LDS_QB + (w * R + c_slot) * 512 + (c16 & 1) * 256 + (4 * s + g4) * 16);
          if (z) x = u32x4{0u, 0u, 0u, 0u};
          qf[0][s] = __builtin_bit_cast(bf16x8, x);
        }
        asm volatile("s_waitcnt lgkmcnt(0)" ::: "memory");
        first = false;
      }
      if (i_more) issue_stage();
      const unsigned sb = c_slot * STAGE;
      tile_compute<1>(lds, kaddr0 + sb, vaddr0 + sb, qf, acc, m, l, t0 - page * 16, t1 - page * 16, g4);
      ++n_comp; c_slot = (c_slot + 1 == R) ? 0 : c_slot + 1;
    }
    const float lt = xg_sum(l[0]);
    LAS char* sc = lds + LDS_SC + w * 1024;
    if (c16 < 2) {
#pragma unroll
      for (int k = 0; k < 8; ++k) *(LAS f32x4*)(sc + c16 * 512 + (16 * k + 4 * g4) * 4) = acc[0][k];
    }
    asm volatile("s_waitcnt lgkmcnt(0)" ::: "memory");
    const f32x2 a0 = *(LAS f32x2*)(sc + lane * 8), a1 = *(LAS f32x2*)(sc + 512 + lane * 8);
    if (last_b >= 0) {
      if (held_b >= 0) publish(held_b, ho, hm, hs);
      held_b = last_b;
#pragma unroll
      for (int r = 0; r < 2; ++r) { ho[r][0] = ro[r][0]; ho[r][1] = ro[r][1]; hm[r] = rm[r]; hs[r] = rs[r]; }
    }
    ro[0][0] = a0[0]; ro[0][1] = a0[1]; ro[1][0] = a1[0]; ro[1][1] = a1[1];
    rm[0] = rlf(m[0], 0) * C2; rm[1] = rlf(m[0], 1) * C2;
    rs[0] = rlf(lt, 0); rs[1] = rlf(lt, 1);
    last_b = b;
  }
  STAMP(2);
  if (last_b >= 0 && !((own >> last_b) & 1)) { publish(last_b, ro, rm, rs); last_b = -1; }
  if (held_b >= 0 && !((own >> held_b) & 1)) { publish(held_b, ho, hm, hs); held_b = -1; }
  STAMP(1);

  while (own) {
    const int b = __builtin_ctz(own); own &= own - 1;
    const unsigned tag = (unsigned)rl((int)gvw, b) + 1u;
    const int s = rl(vS, b), L = rl(vL, b);
    unsigned long long pmk = __ballot(lane < nW && max(r_lo, s) < min(r_hi, s + L));
    unsigned smk = (1u << kH) - 1u;
    float M0 = -INFINITY, M1 = -INFINITY, L0 = 0.f, L1 = 0.f, o00 = 0.f, o01 = 0.f, o10 = 0.f, o11 = 0.f;
    if (b == last_b) {
      pmk &= ~(1ull << j);
      M0 = rm[0]; M1 = rm[1]; L0 = rs[0]; L1 = rs[1]; o00 = ro[0][0]; o01 = ro[0][1]; o10 = ro[1][0]; o11 = ro[1][1];
    } else if (b == held_b) {
      pmk &= ~(1ull << j);
      M0 = hm[0]; M1 = hm[1]; L0 = hs[0]; L1 = hs[1]; o00 = ho[0][0]; o01 = ho[0][1]; o10 = ho[1][0]; o11 = ho[1][1];
    }
    const unsigned p_rows = WS_PP + ((hg * 80 + lane + b) * 4 + w) * PP_W;
    const unsigned s_base = WS_HP + (h * 16 + (lane & 15)) * HP_W;
    unsigned spins = 0;
    bool light = false;
    while (pmk | smk) {
      unsigned long long tp = pmk; unsigned ts = smk;
      const bool single = (__builtin_popcountll(pmk) + __builtin_popcount(smk)) == 1;
      if (light && !single) {
        const int jp = pmk ? (((pmk >> lane) & 1) ? lane : __builtin_ctzll(pmk)) : 0;
        const int js = smk ? (((smk >> (lane & 15)) & 1) ? (lane & 15) : __builtin_ctz(smk)) : 0;
        u32x4 a = {0u, tag, 0u, tag}, c = {0u, tag, 0u, tag};
        if (pmk) a = __builtin_amdgcn_raw_buffer_load_b128(wsr, WS_PP + ((hg * 80 + jp + b) * 4 + w) * PP_W + 2048 + 16, 0, 16);
        if (smk) c = __builtin_amdgcn_raw_buffer_load_b128(wsr, WS_HP + (h * 16 + js) * HP_W + 32768 + (2 * b + 1) * 16, 0, 16);
        tp &= __ballot(a[1] == tag && a[3] == tag);
        ts &= (unsigned)__ballot(c[1] == tag && c[3] == tag);
      }
      bool progress = false;
      if (tp | ts) {
        auto batch = [&](auto nb_tag) {
          constexpr int NB = decltype(nb_tag)::value;
          unsigned rows[NB], mls[NB]; unsigned long long bitp[NB]; unsigned bits[NB];
#pragma unroll
          for (int k = 0; k < NB; ++k) {
            if (tp) { const int q = __builtin_ctzll(tp); bitp[k] = tp & (~tp + 1); bits[k] = 0; tp &= tp - 1; rows[k] = (unsigned)rl((int)p_rows, q); mls[k] = rows[k] + 2048; }
            else if (ts) { const int q = __builtin_ctz(ts); bits[k] = ts & (~ts + 1); bitp[k] = 0; ts &= ts - 1;
                           const unsigned sl = (unsigned)rl((int)s_base, q); rows[k] = sl + b * 2048; mls[k] = sl + 32768 + b * 32; }
            else { bitp[k] = 0; bits[k] = 0; rows[k] = rows[0]; mls[k] = mls[0]; }
          }
          u32x4 d0[NB], d1[NB], dm[NB];
#pragma unroll
          for (int k = 0; k < NB; ++k) {
            d0[k] = __builtin_amdgcn_raw_buffer_load_b128(wsr, lane * 16, rows[k], 16);
            d1[k] = __builtin_amdgcn_raw_buffer_load_b128(wsr, 1024 + lane * 16, rows[k], 16);
            dm[k] = __builtin_amdgcn_raw_buffer_load_b128(wsr, (lane & 1) * 16, mls[k], 16);
          }
#pragma unroll
          for (int k = 0; k < NB; ++k) {
            const bool ok = d0[k][1] == tag && d0[k][3] == tag && d1[k][1] == tag && d1[k][3] == tag && dm[k][1] == tag && dm[k][3] == tag;
            if ((bitp[k] | bits[k]) && __all(ok)) {
              const float mv = __uint_as_float(dm[k][0]), lv = __uint_as_float(dm[k][2]);
              const float m0 = rlf(mv, 0), m1 = rlf(mv, 1), l0 = rlf(lv, 0), l1 = rlf(lv, 1);
              const float n0 = fmaxf(M0, m0), n1 = fmaxf(M1, m1);
              const float a0 = __builtin_amdgcn_exp2f(M0 - n0), b0 = __builtin_amdgcn_exp2f(m0 - n0);
              const float a1 = __builtin_amdgcn_exp2f(M1 - n1), b1 = __builtin_amdgcn_exp2f(m1 - n1);
              L0 = L0 * a0 + l0 * b0; L1 = L1 * a1 + l1 * b1;
              o00 = o00 * a0 + __uint_as_float(d0[k][0]) * b0; o01 = o01 * a0 + __uint_as_float(d0[k][2]) * b0;
              o10 = o10 * a1 + __uint_as_float(d1[k][0]) * b1; o11 = o11 * a1 + __uint_as_float(d1[k][2]) * b1;
              M0 = n0; M1 = n1;
              pmk &= ~bitp[k]; smk &= ~bits[k];
              progress = true;
            }
          }
        };
        const int cnt = __builtin_popcountll(tp) + __builtin_popcount(ts);
        if (cnt == 1) batch(IntC<1>{}); else if (cnt <= 3) batch(IntC<3>{}); else batch(IntC<6>{});
      }
      if (!progress) {
        light = true;
        if (++spins > (1u << 16)) break;
        __builtin_amdgcn_s_sleep(SLEEP);
      }
    }
    const float i0 = 1.f / L0, i1 = 1.f / L1;
    __hip_bfloat16* orow = p.out + (b * NH + 2 * h) * DH + 2 * lane;
    *(unsigned*)orow = pk_bf16(o00 * i0, o01 * i0);
    *(unsigned*)(orow + DH) = pk_bf16(o10 * i1, o11 * i1);
    if (lane == 0) __builtin_amdgcn_raw_buffer_store_b32(tag, wsr, WS_G + (h * 16 + b) * 4, 0, 16);
  }
  STAMP(3);
}

}

extern "C" void kernel_launch(void* const* d_in, const int* in_sizes, int n_in, void* d_out, int out_size, void* d_ws, size_t ws_size,
                              hipStream_t stream) {
  using namespace rattn;
  Params p;
  p.q = (const char*)d_in[0]; p.k = (const char*)d_in[1]; p.v = (const char*)d_in[2];
  p.bt = (const int*)d_in[3]; p.lens = (const int*)d_in[4];
  p.out = (__hip_bfloat16*)d_out; p.ws = (char*)d_ws;
  (void)hipFuncSetAttribute((const void*)radix_attn_kernel, hipFuncAttributeMaxDynamicSharedMemorySize, LDS_TOTAL);
  hipLaunchKernelGGL(radix_attn_kernel, dim3(256), dim3(256), LDS_TOTAL, stream, p);
}
```

```cpp
#include <hip/hip_runtime.h>
#include <hip/hip_bf16.h>
#include <stdint.h>

namespace rattn {

constexpr int NSEQ = 16, NH = 32, NKV = 16, DH = 128, NBLK = 256, BS = 16;
constexpr int LK = NBLK * BS;
#define PACE_LEAD 32
#define PACE_H 88
#define HRHO 50
#define PACE 6700
#define MAPMODE 1
#define SLEEP 6
#define KVPOL "nt"
constexpr int R = 3;
constexpr int STAGE = 32768;
constexpr int RH = 4;
constexpr int RING_BYTES = 4 * RH * 8192;
constexpr int LDS_BT = RING_BYTES;
constexpr int LDS_QB = LDS_BT + NSEQ * NBLK * 4;
constexpr int LDS_SC = LDS_QB + 4 * R * 512;
constexpr int LDS_MISC = LDS_SC + 4096;
constexpr int LDS_JUNK = LDS_MISC + 64;
constexpr int LDS_TOTAL = LDS_JUNK + 1024;
static_assert(LDS_TOTAL <= 160 * 1024 && LDS_MISC - LDS_QB >= 8192 && RING_BYTES >= R * STAGE, "LDS");

constexpr unsigned WS_G = 0;
constexpr unsigned WS_PP = 4096;
constexpr unsigned PP_W = 2176;
constexpr unsigned WS_HP = 4u << 20;
constexpr unsigned HP_W = 32768 + 512;
constexpr unsigned WS_END = WS_HP + 16u * 16u * HP_W;
constexpr unsigned WS_STAMP = 64u << 20;

typedef unsigned u32x4 __attribute__((ext_vector_type(4)));
typedef unsigned u32x2 __attribute__((ext_vector_type(2)));
typedef float f32x4 __attribute__((ext_vector_type(4)));
typedef float f32x2 __attribute__((ext_vector_type(2)));
typedef __bf16 bf16x8 __attribute__((ext_vector_type(8)));
typedef __bf16 bf16x2 __attribute__((ext_vector_type(2)));
typedef short s16x4 __attribute__((ext_vector_type(4)));
typedef int i32x4 __attribute__((ext_vector_type(4)));
#define LAS __attribute__((address_space(3)))

constexpr float C2 = 0.088388347648318440f * 1.4426950408889634f;

__device__ __forceinline__ int rfl(int x) { return __builtin_amdgcn_readfirstlane(x); }
__device__ __forceinline__ int rl(int x, int l) { return __builtin_amdgcn_readlane(x, l); }
__device__ __forceinline__ float rlf(float x, int l) { return __builtin_bit_cast(float, __builtin_amdgcn_readlane(__builtin_bit_cast(int, x), l)); }

__device__ __forceinline__ void dma16(const void* sbase, unsigned voff, unsigned dst) {
  unsigned keep;
  asm volatile("s_mov_b32 %0, m0\n\ts_mov_b32 m0, %3\n\ts_nop 0\n\tglobal_load_lds_dwordx4 %1, %2\n\ts_mov_b32 m0, %0"
               : "=&s"(keep) : "v"(voff), "s"(sbase), "s"(dst) : "memory");
}
__device__ __forceinline__ void dma_stage(const void* sK, const void* sV, unsigned dstK, unsigned dstV,
                                          unsigned k0, unsigned k1, unsigned k2, unsigned k3,
                                          unsigned v0, unsigned v1, unsigned v2, unsigned v3) {
  unsigned keep;
  asm volatile("s_mov_b32 %0, m0\n\ts_mov_b32 m0, %3\n\ts_nop 0\n\t"
               "global_load_lds_dwordx4 %5, %1 " KVPOL "\n\t"
               "global_load_lds_dwordx4 %6, %1 offset:1024 " KVPOL "\n\t"
               "global_load_lds_dwordx4 %7, %1 offset:2048 " KVPOL "\n\t"
               "global_load_lds_dwordx4 %8, %1 offset:3072 " KVPOL "\n\t"
               "s_mov_b32 m0, %4\n\ts_nop 0\n\t"
               "global_load_lds_dwordx4 %9, %2 " KVPOL "\n\t"
               "global_load_lds_dwordx4 %10, %2 offset:1024 " KVPOL "\n\t"
               "global_load_lds_dwordx4 %11, %2 offset:2048 " KVPOL "\n\t"
               "global_load_lds_dwordx4 %12, %2 offset:3072 " KVPOL "\n\t"
               "s_mov_b32 m0, %0"
               : "=&s"(keep)
               : "s"(sK), "s"(sV), "s"(dstK), "s"(dstV), "v"(k0), "v"(k1), "v"(k2), "v"(k3), "v"(v0), "v"(v1), "v"(v2), "v"(v3)
               : "memory");
}
template <int CTRL> __device__ __forceinline__ int dppi(int x) { return __builtin_amdgcn_update_dpp(0, x, CTRL, 0xf, 0xf, true); }
template <int CTRL> __device__ __forceinline__ int dppi_keep(int old, int x) { return __builtin_amdgcn_update_dpp(old, x, CTRL, 0xf, 0xf, false); }

__device__ __forceinline__ float xg_max(float x) {
  auto s = __builtin_amdgcn_permlane16_swap(__float_as_uint(x), __float_as_uint(x), false, false);
  x = fmaxf(__uint_as_float(s[0]), __uint_as_float(s[1]));
  auto t = __builtin_amdgcn_permlane32_swap(__float_as_uint(x), __float_as_uint(x), false, false);
  return fmaxf(__uint_as_float(t[0]), __uint_as_float(t[1]));
}
__device__ __forceinline__ float xg_sum(float x) {
  auto s = __builtin_amdgcn_permlane16_swap(__float_as_uint(x), __float_as_uint(x), false, false);
  x = __uint_as_float(s[0]) + __uint_as_float(s[1]);
  auto t = __builtin_amdgcn_permlane32_swap(__float_as_uint(x), __float_as_uint(x), false, false);
  return __uint_as_float(t[0]) + __uint_as_float(t[1]);
}
__device__ __forceinline__ unsigned pk_bf16(float a, float b) {
  bf16x2 v = {(__bf16)a, (__bf16)b};
  return __builtin_bit_cast(unsigned, v);
}

template <int NQ>
__device__ __forceinline__ void tile_compute(LAS char* lds, unsigned ka, unsigned va, const bf16x8 (&qf)[NQ][4], f32x4 (&acc)[NQ][8],
                                             float (&m)[NQ], float (&l)[NQ], int lo, int hi, int g4) {
  bf16x8 kf[4];
#pragma unroll
  for (int s = 0; s < 4; ++s) kf[s] = *(LAS bf16x8*)(lds + (ka ^ (s << 6)));
  s16x4 vf[8];
#pragma unroll
  for (int k = 0; k < 8; ++k) vf[k] = __builtin_amdgcn_ds_read_tr16_b64_v4i16((LAS s16x4*)(lds + (va ^ (k << 5))));
  f32x4 S[NQ];
#pragma unroll
  for (int qb = 0; qb < NQ; ++qb) {
    S[qb] = f32x4{0.f, 0.f, 0.f, 0.f};
#pragma unroll
    for (int s = 0; s < 4; ++s) S[qb] = __builtin_amdgcn_mfma_f32_16x16x32_bf16(kf[s], qf[qb][s], S[qb], 0, 0, 0);
  }
  if (lo > 0 || hi < 16) {
#pragma unroll
    for (int qb = 0; qb < NQ; ++qb)
#pragma unroll
      for (int r = 0; r < 4; ++r) { const int t = 4 * g4 + r; S[qb][r] = (t >= lo && t < hi) ? S[qb][r] : -INFINITY; }
  }
  s16x4 pb[NQ];
  bool grow = false;
  float alpha[NQ];
#pragma unroll
  for (int qb = 0; qb < NQ; ++qb) {
    float mx = fmaxf(fmaxf(S[qb][0], S[qb][1]), fmaxf(S[qb][2], S[qb][3]));
    mx = xg_max(mx);
    const float mn2 = fmaxf(m[qb], mx);
    grow |= mn2 > m[qb];
    alpha[qb] = __builtin_amdgcn_exp2f((m[qb] - mn2) * C2);
    m[qb] = mn2;
    const float mc = -mn2 * C2;
    const float p0 = __builtin_amdgcn_exp2f(fmaf(S[qb][0], C2, mc)), p1 = __builtin_amdgcn_exp2f(fmaf(S[qb][1], C2, mc));
    const float p2 = __builtin_amdgcn_exp2f(fmaf(S[qb][2], C2, mc)), p3 = __builtin_amdgcn_exp2f(fmaf(S[qb][3], C2, mc));
    l[qb] = fmaf(l[qb], alpha[qb], (p0 + p1) + (p2 + p3));
    pb[qb] = __builtin_bit_cast(s16x4, u32x2{pk_bf16(p0, p1), pk_bf16(p2, p3)});
  }
  if (__any(grow)) {
#pragma unroll
    for (int qb = 0; qb < NQ; ++qb)
#pragma unroll
      for (int k = 0; k < 8; ++k) acc[qb][k] *= alpha[qb];
  }
#pragma unroll
  for (int qb = 0; qb < NQ; ++qb)
#pragma unroll
    for (int k = 0; k < 8; ++k) acc[qb][k] = __builtin_amdgcn_mfma_f32_16x16x16bf16_1k(vf[k], pb[qb], acc[qb][k], 0, 0, 0);
}

template <int V> struct IntC { static constexpr int value = V; };

struct Params {
  const char* q; const char* k; const char* v; const int* bt; const int* lens; __hip_bfloat16* out; char* ws;
};

__global__ void __launch_bounds__(256, 2) radix_attn_kernel(Params p) {
  extern __shared__ __attribute__((aligned(16))) char smem[];
  LAS char* lds = (LAS char*)smem;
  const int tid = threadIdx.x, lane = tid & 63, w = rfl(tid >> 6);
  const int gidx = blockIdx.x;
  const int hg = (gidx >> 3) & 3;
  const int jj = (((gidx & 7) + 4 * (hg & 1)) & 7) | ((gidx >> 5) << 3);
  const int c16 = lane & 15, g4 = lane >> 4;
#define STAMP(i) do { } while (0)
  STAMP(0);
  const auto wsr = __builtin_amdgcn_make_buffer_rsrc(p.ws, 0, WS_END, 0x00020000);

  const int lenraw = p.lens[c16];
  const unsigned gvw = __builtin_amdgcn_raw_buffer_load_b32(wsr, WS_G + ((hg * 4 + w) * 16 + c16) * 4, 0, 16);
  const unsigned gvh = __builtin_amdgcn_raw_buffer_load_b32(wsr, WS_G + ((hg * 4 + (jj & 3)) * 16 + c16) * 4, 0, 16);
  int pm;
  {
    const i32x4* bt4 = (const i32x4*)p.bt;
    const i32x4 r0 = bt4[lane];
    i32x4 rr[4];
#pragma unroll
    for (int i = 0; i < 4; ++i) rr[i] = bt4[(4 * w + i) * 64 + lane];
    bool n0 = false, n1 = false, n2 = false, n3 = false;
#pragma unroll
    for (int i = 0; i < 4; ++i) {
      n0 |= rr[i][0] != r0[0]; n1 |= rr[i][1] != r0[1]; n2 |= rr[i][2] != r0[2]; n3 |= rr[i][3] != r0[3];
      *(LAS i32x4*)(lds + LDS_BT + ((4 * w + i) * 256 + 4 * lane) * 4) = rr[i];
    }
    const int fc = n0 ? 0 : n1 ? 1 : n2 ? 2 : n3 ? 3 : 4;
    const unsigned long long mm = __ballot(fc < 4);
    int col = NBLK;
    if (mm) { const int fl = __builtin_ctzll(mm); col = 4 * fl + rl(fc, fl); }
    if (lane == 0) *(LAS int*)(lds + LDS_MISC + 4 * w) = col;
    asm volatile("s_waitcnt lgkmcnt(0)\n\ts_barrier" ::: "memory");
    const i32x4 mc = *(LAS i32x4*)(lds + LDS_MISC);
    pm = rfl(min(min(mc[0], mc[1]), min(mc[2], mc[3])));
  }
  asm volatile("" :: "v"(gvw), "v"(gvh));
  const bool zq_l = lenraw <= 0;
  const int len_l = zq_l ? LK : min(lenraw, LK);
  int mn = len_l;
  mn = min(mn, dppi_keep<0x128>(mn, mn)); mn = min(mn, dppi_keep<0x124>(mn, mn));
  mn = min(mn, dppi_keep<0x122>(mn, mn)); mn = min(mn, dppi_keep<0x121>(mn, mn));
  const int P = min(pm, rfl(mn) >> 4);
  const int tsh = P * BS;
  const int vL = len_l - tsh;
  int inc = vL;
  inc += dppi<0x111>(inc); inc += dppi<0x112>(inc); inc += dppi<0x114>(inc); inc += dppi<0x118>(inc);
  const int N = rl(inc, 15);
  const int vS = inc - vL;
  int kH = 0;
  if (P > 0) kH = min(15, max(1, rfl((int)(((unsigned)tsh * 16u * 64u + (unsigned)(tsh + N) * HRHO - 1u) / ((unsigned)(tsh + N) * HRHO)))));
  kH = min(kH, max(P, 0));
  const int nW = 64 - 4 * kH;
  const unsigned zmask = (unsigned)(__ballot(zq_l) & 0xffffull);
  const bool isH = jj < 4 * kH;
  const float t_all = (float)(tsh + N) * (819.2f / (float)PACE);
  const unsigned long long t_ref = __builtin_amdgcn_s_memrealtime();
  const int r_lo = (int)((unsigned)(lane * N) / (unsigned)nW), r_hi = (int)((unsigned)((lane + 1) * N) / (unsigned)nW);

  if (isH) {
    const int h = hg * 4 + (jj & 3), part = jj >> 2;
    const int p0 = (P * part) / kH, p1 = (P * (part + 1)) / kH;
#pragma unroll
    for (int i = 0; i < 2; ++i)
      dma16(p.q + (2 * w + i) * 16384 + h * 512, (lane >> 5) * 8192 + (lane & 31) * 16, LDS_QB + (2 * w + i) * 1024);
    unsigned oK[4], oV[4];
#pragma unroll
    for (int i = 0; i < 4; ++i) {
      const int t = 4 * i + g4;
      oK[i] = t * 4096 + ((c16 ^ t) << 4) - i * 1024 + 4096;
      oV[i] = t * 4096 + ((c16 ^ (2 * (t & 7))) << 4) - i * 1024 + 4096;
    }
    const char* kb = p.k + h * 256 - 4096;
    const char* vb = p.v + h * 256 - 4096;
    const int nt = max(0, (p1 - p0 - w + 3) >> 2);
    const unsigned ring = w * (RH * 8192);
    int pg0 = 0, vpg = *(LAS int*)(lds + LDS_BT + min(p0 + w + 4 * lane, NBLK - 1) * 4);
    const float pace_h = t_all * (PACE_H * 0.01f) / (float)max(nt, 1);
    auto issue = [&](int t) {
      { const int due = (int)((float)(t - 3) * pace_h); while ((int)(__builtin_amdgcn_s_memrealtime() - t_ref) < due) __builtin_amdgcn_s_sleep(1); }
      if (t - pg0 == 64) { pg0 = t; vpg = *(LAS int*)(lds + LDS_BT + min(p0 + w + 4 * (t + lane), NBLK - 1) * 4); }
      const size_t pb = (size_t)(unsigned)rl(vpg, t - pg0) * 65536;
      const unsigned dst = ring + (t & (RH - 1)) * 8192;
      dma_stage(kb + pb, vb + pb, dst, dst + 4096, oK[0], oK[1], oK[2], oK[3], oV[0], oV[1], oV[2], oV[3]);
    };
    const int npre = min(nt, RH - 1);
#pragma unroll 1
    for (int t = 0; t < npre; ++t) issue(t);
    if (npre == 3) asm volatile("s_waitcnt vmcnt(24)\n\ts_barrier" ::: "memory");
    else asm volatile("s_waitcnt vmcnt(0)\n\ts_barrier" ::: "memory");
    bf16x8 qf[2][4];
#pragma unroll
    for (int qb = 0; qb < 2; ++qb) {
      const bool z = (zmask >> (8 * qb + (c16 >> 1))) & 1;
#pragma unroll
      for (int s = 0; s < 4; ++s) {
        u32x4 x = *(LAS u32x4*)(lds + LDS_QB + (16 * qb + c16) * 256 + (4 * s + g4) * 16);
        if (z) x = u32x4{0u, 0u, 0u, 0u};
        qf[qb][s] = __builtin_bit_cast(bf16x8, x);
      }
    }
    f32x4 acc[2][8];
    float m[2] = {-INFINITY, -INFINITY}, l[2] = {0.f, 0.f};
#pragma unroll
    for (int qb = 0; qb < 2; ++qb)
#pragma unroll
      for (int k = 0; k < 8; ++k) acc[qb][k] = f32x4{0.f, 0.f, 0.f, 0.f};
    const unsigned ka0 = ring + c16 * 256 + ((g4 ^ c16) << 4);
    unsigned va0;
    { const int tq = 4 * g4 + (c16 >> 2), pp = lane & 3; va0 = ring + 4096 + tq * 256 + ((tq & 7) << 5) + ((pp >> 1) << 4) + ((pp & 1) << 3); }
#pragma unroll 1
    for (int t = 0; t < nt; ++t) {
      const int young = min(nt - 1 - t, RH - 2);
      if (young == 2) asm volatile("s_waitcnt vmcnt(16) lgkmcnt(0)" ::: "memory");
      else if (young == 1) asm volatile("s_waitcnt vmcnt(8) lgkmcnt(0)" ::: "memory");
      else asm volatile("s_waitcnt vmcnt(0) lgkmcnt(0)" ::: "memory");
      if (t + RH - 1 < nt) issue(t + RH - 1);
      const unsigned sb = (t & (RH - 1)) * 8192;
      tile_compute<2>(lds, ka0 + sb, va0 + sb, qf, acc, m, l, 0, 16, g4);
    }
    STAMP(2);
    constexpr unsigned ROWB = 528, WAVEB = 32 * ROWB, MLB = 4 * WAVEB;
    asm volatile("s_waitcnt lgkmcnt(0)\n\ts_barrier" ::: "memory");
#pragma unroll
    for (int qb = 0; qb < 2; ++qb) {
      const float lt = xg_sum(l[qb]);
#pragma unroll
      for (int k = 0; k < 8; ++k) *(LAS f32x4*)(lds + w * WAVEB + (16 * qb + c16) * ROWB + (16 * k + 4 * g4) * 4) = acc[qb][k];
      if (g4 == 0) *(LAS f32x2*)(lds + MLB + (w * 32 + 16 * qb + c16) * 8) = f32x2{m[qb] * C2, lt};
    }
    asm volatile("s_waitcnt lgkmcnt(0)\n\ts_barrier" ::: "memory");
    const unsigned slot = WS_HP + (h * 16 + part) * HP_W;
    float mrow = 0.f, lrow = 0.f;
#pragma unroll
    for (int rr = 0; rr < 8; ++rr) {
      const int row = 8 * w + rr;
      f32x2 ml[4], o[4];
#pragma unroll
      for (int v = 0; v < 4; ++v) { ml[v] = *(LAS f32x2*)(lds + MLB + (v * 32 + row) * 8); o[v] = *(LAS f32x2*)(lds + v * WAVEB + row * ROWB + lane * 8); }
      const float M = fmaxf(fmaxf(ml[0][0], ml[1][0]), fmaxf(ml[2][0], ml[3][0]));
      float L = 0.f, o0 = 0.f, o1 = 0.f;
#pragma unroll
      for (int v = 0; v < 4; ++v) {
        const float e = ml[v][1] > 0.f ? __builtin_amdgcn_exp2f(ml[v][0] - M) : 0.f;
        L = fmaf(ml[v][1], e, L); o0 = fmaf(o[v][0], e, o0); o1 = fmaf(o[v][1], e, o1);
      }
      const unsigned tag = (unsigned)rl((int)gvh, row >> 1) + 1u;
      const u32x4 gr = {__float_as_uint(o0), tag, __float_as_uint(o1), tag};
      __builtin_amdgcn_raw_buffer_store_b128(gr, wsr, slot + row * 1024 + lane * 16, 0, 16);
      if (lane == rr) { mrow = M; lrow = L; }
    }
    const unsigned tagr = (unsigned)__shfl((int)gvh, ((8 * w + lane) >> 1) & 15) + 1u;
    if (lane < 8) {
      const unsigned tag = tagr;
      const u32x4 gm = {__float_as_uint(mrow), tag, __float_as_uint(lrow), tag};
      __builtin_amdgcn_raw_buffer_store_b128(gm, wsr, slot + 32768 + (8 * w + lane) * 16, 0, 16);
    }
    STAMP(1);
    STAMP(3);
    return;
  }

  const int j = jj - 4 * kH, h = hg * 4 + w;
  const int x0 = rl(r_lo, j), x1 = rl(r_hi, j);
  const unsigned umask = (unsigned)(__ballot(max(x0, vS) < min(x1, vS + vL)) & 0xffffull);
  unsigned voffK[4], voffV[4];
#pragma unroll
  for (int i = 0; i < 4; ++i) {
    const int t = 4 * w + i;
    voffK[i] = g4 * 256 + ((c16 ^ t) << 4) + t * 4096 - i * 1024 + 4096;
    voffV[i] = g4 * 256 + ((c16 ^ (2 * (t & 7))) << 4) + t * 4096 - i * 1024 + 4096;
  }
  const unsigned kaddr0 = w * 256 + c16 * 1024 + ((g4 ^ c16) << 4);
  unsigned vaddr0;
  { const int tq = 4 * g4 + (c16 >> 2), pp = lane & 3; vaddr0 = 16384 + tq * 1024 + w * 256 + ((tq & 7) << 5) + ((pp >> 1) << 4) + ((pp & 1) << 3); }
  const char* kbase = p.k + hg * 1024 - 4096;
  const char* vbase = p.v + hg * 1024 - 4096;

  unsigned i_mask = umask; int i_row = 0, i_t0 = 0, i_t1 = 0, i_page; bool i_more = false;
  int n_iss = 0, i_slot = 0;
  auto unit_range = [&](int b, int& t0, int& t1) {
    const int s = rl(vS, b), L = rl(vL, b);
    t0 = tsh + max(x0, s) - s; t1 = tsh + min(x1, s + L) - s;
  };
  auto q_dma = [&](int b, int slot) {
    if (lane < 32) dma16(p.q + b * 8192 + h * 512, lane * 16, LDS_QB + (w * R + slot) * 512);
  };
  if (i_mask) {
    i_row = __builtin_ctz(i_mask); i_mask &= i_mask - 1; unit_range(i_row, i_t0, i_t1);
    q_dma(i_row, 0);
    i_more = true;
  }
  i_page = i_t0 >> 4;
  int i_pg0 = i_page, vpg = *(LAS int*)(lds + LDS_BT + (i_row * NBLK + min(i_page + lane, NBLK - 1)) * 4);
  int pace_tok = 0;
  const float pace_d = t_all / (float)max(x1 - x0, 1);
  auto issue_stage = [&]() {
    const int phys = rl(vpg, i_page - i_pg0);
    const int lo = i_t0 - i_page * 16, hi = i_t1 - i_page * 16;
    {
      const int due = (int)((float)(pace_tok - PACE_LEAD) * pace_d);
      pace_tok += min(hi, 16) - max(lo, 0);
      while ((int)(__builtin_amdgcn_s_memrealtime() - t_ref) < due) __builtin_amdgcn_s_sleep(1);
    }
    const size_t pb = (size_t)(unsigned)phys * 65536;
    const unsigned dst = i_slot * STAGE + 4 * w * 1024;
    if (lo <= 0 && hi >= 16) {
      dma_stage(kbase + pb, vbase + pb, dst, dst + 16384, voffK[0], voffK[1], voffK[2], voffK[3], voffV[0], voffV[1], voffV[2], voffV[3]);
    } else {
      unsigned ok[4], ov[4];
#pragma unroll
      for (int i = 0; i < 4; ++i) {
        const int d = (min(max(4 * w + i, lo), hi - 1) - (4 * w + i)) * 4096;
        ok[i] = voffK[i] + d; ov[i] = voffV[i] + d;
      }
      dma_stage(kbase + pb, vbase + pb, dst, dst + 16384, ok[0], ok[1], ok[2], ok[3], ov[0], ov[1], ov[2], ov[3]);
    }
    ++n_iss; i_slot = (i_slot + 1 == R) ? 0 : i_slot + 1;
    ++i_page;
    if (i_page * 16 >= i_t1) {
      if (i_mask) {
        i_row = __builtin_ctz(i_mask); i_mask &= i_mask - 1; unit_range(i_row, i_t0, i_t1); i_page = i_t0 >> 4;
        q_dma(i_row, i_slot);
        i_pg0 = i_page; vpg = *(LAS int*)(lds + LDS_BT + (i_row * NBLK + min(i_page + lane, NBLK - 1)) * 4);
      } else i_more = false;
    } else if (i_page - i_pg0 == 64) {
      i_pg0 = i_page; vpg = *(LAS int*)(lds + LDS_BT + (i_row * NBLK + min(i_page + lane, NBLK - 1)) * 4);
    }
  };
#pragma unroll 1
  for (int s = 0; s < R - 1; ++s) if (i_more) issue_stage();

  unsigned own = 0;
  {
    unsigned mrem = umask;
    while (mrem) {
      const int b = __builtin_ctz(mrem); mrem &= mrem - 1;
      const int s = rl(vS, b), L = rl(vL, b);
      unsigned long long pmk = __ballot(lane < nW && max(r_lo, s) < min(r_hi, s + L));
      int idx = w % __builtin_popcountll(pmk);
      while (idx--) pmk &= pmk - 1;
      if (__builtin_ctzll(pmk) == j) own |= 1u << b;
    }
    unsigned zl = (unsigned)(__ballot(vL == 0) & 0xffffull);
    while (zl) { const int b = __builtin_ctz(zl); zl &= zl - 1; if ((b * 4) % nW == j) own |= 1u << b; }
  }

  int n_comp = 0, c_slot = 0;
  unsigned c_mask = umask;
  float ro[2][2] = {{0.f, 0.f}, {0.f, 0.f}}, rm[2] = {-INFINITY, -INFINITY}, rs[2] = {0.f, 0.f};
  float ho[2][2] = {{0.f, 0.f}, {0.f, 0.f}}, hm[2] = {-INFINITY, -INFINITY}, hs[2] = {0.f, 0.f};
  int last_b = -1, held_b = -1;
  auto publish = [&](int b, const float (&o)[2][2], const float (&mm)[2], const float (&ll)[2]) {
    const unsigned tag = (unsigned)rl((int)gvw, b) + 1u;
    const unsigned slot = WS_PP + ((hg * 80 + j + b) * 4 + w) * PP_W;
    const u32x4 g0 = {__float_as_uint(o[0][0]), tag, __float_as_uint(o[0][1]), tag};
    const u32x4 g1 = {__float_as_uint(o[1][0]), tag, __float_as_uint(o[1][1]), tag};
    __builtin_amdgcn_raw_buffer_store_b128(g0, wsr, slot + lane * 16, 0, 16);
    __builtin_amdgcn_raw_buffer_store_b128(g1, wsr, slot + 1024 + lane * 16, 0, 16);
    if (lane < 2) {
      const u32x4 gm = {__float_as_uint(lane ? mm[1] : mm[0]), tag, __float_as_uint(lane ? ll[1] : ll[0]), tag};
      __builtin_amdgcn_raw_buffer_store_b128(gm, wsr, slot + 2048 + lane * 16, 0, 16);
    }
  };
  while (c_mask) {
    const int b = __builtin_ctz(c_mask); c_mask &= c_mask - 1;
    int t0, t1; unit_range(b, t0, t1);
    bf16x8 qf[1][4];
    f32x4 acc[1][8];
    float m[1] = {-INFINITY}, l[1] = {0.f};
#pragma unroll
    for (int k = 0; k < 8; ++k) acc[0][k] = f32x4{0.f, 0.f, 0.f, 0.f};
    bool first = true;
    for (int page = t0 >> 4; page * 16 < t1; ++page) {
      if (n_iss - n_comp > 1) asm volatile("s_waitcnt vmcnt(8) lgkmcnt(0)\n\ts_barrier" ::: "memory");
      else asm volatile("s_waitcnt vmcnt(0) lgkmcnt(0)\n\ts_barrier" ::: "memory");
      if (first) {
        const bool z = (c16 >= 2) || ((zmask >> b) & 1);
#pragma unroll
        for (int s = 0; s < 4; ++s) {
          u32x4 x = *(LAS u32x4*)(lds + LDS_QB + (w * R + c_slot) * 512 + (c16 & 1) * 256 + (4 * s + g4) * 16);
          if (z) x = u32x4{0u, 0u, 0u, 0u};
          qf[0][s] = __builtin_bit_cast(bf16x8, x);
        }
        asm volatile("s_waitcnt lgkmcnt(0)" ::: "memory");
        first = false;
      }
      if (i_more) issue_stage();
      const unsigned sb = c_slot * STAGE;
      tile_compute<1>(lds, kaddr0 + sb, vaddr0 + sb, qf, acc, m, l, t0 - page * 16, t1 - page * 16, g4);
      ++n_comp; c_slot = (c_slot + 1 == R) ? 0 : c_slot + 1;
    }
    const float lt = xg_sum(l[0]);
    LAS char* sc = lds + LDS_SC + w * 1024;
    if (c16 < 2) {
#pragma unroll
      for (int k = 0; k < 8; ++k) *(LAS f32x4*)(sc + c16 * 512 + (16 * k + 4 * g4) * 4) = acc[0][k];
    }
    asm volatile("s_waitcnt lgkmcnt(0)" ::: "memory");
    const f32x2 a0 = *(LAS f32x2*)(sc + lane * 8), a1 = *(LAS f32x2*)(sc + 512 + lane * 8);
    if (last_b >= 0) {
      if (held_b >= 0) publish(held_b, ho, hm, hs);
      held_b = last_b;
#pragma unroll
      for (int r = 0; r < 2; ++r) { ho[r][0] = ro[r][0]; ho[r][1] = ro[r][1]; hm[r] = rm[r]; hs[r] = rs[r]; }
    }
    ro[0][0] = a0[0]; ro[0][1] = a0[1]; ro[1][0] = a1[0]; ro[1][1] = a1[1];
    rm[0] = rlf(m[0], 0) * C2; rm[1] = rlf(m[0], 1) * C2;
    rs[0] = rlf(lt, 0); rs[1] = rlf(lt, 1);
    last_b = b;
  }
  STAMP(2);
  if (last_b >= 0 && !((own >> last_b) & 1)) { publish(last_b, ro, rm, rs); last_b = -1; }
  if (held_b >= 0 && !((own >> held_b) & 1)) { publish(held_b, ho, hm, hs); held_b = -1; }
  STAMP(1);

  while (own) {
    const int b = __builtin_ctz(own); own &= own - 1;
    const unsigned tag = (unsigned)rl((int)gvw, b) + 1u;
    const int s = rl(vS, b), L = rl(vL, b);
    unsigned long long pmk = __ballot(lane < nW && max(r_lo, s) < min(r_hi, s + L));
    unsigned smk = (1u << kH) - 1u;
    float M0 = -INFINITY, M1 = -INFINITY, L0 = 0.f, L1 = 0.f, o00 = 0.f, o01 = 0.f, o10 = 0.f, o11 = 0.f;
    if (b == last_b) {
      pmk &= ~(1ull << j);
      M0 = rm[0]; M1 = rm[1]; L0 = rs[0]; L1 = rs[1]; o00 = ro[0][0]; o01 = ro[0][1]; o10 = ro[1][0]; o11 = ro[1][1];
    } else if (b == held_b) {
      pmk &= ~(1ull << j);
      M0 = hm[0]; M1 = hm[1]; L0 = hs[0]; L1 = hs[1]; o00 = ho[0][0]; o01 = ho[0][1]; o10 = ho[1][0]; o11 = ho[1][1];
    }
    const unsigned p_rows = WS_PP + ((hg * 80 + lane + b) * 4 + w) * PP_W;
    const unsigned s_base = WS_HP + (h * 16 + (lane & 15)) * HP_W;
    unsigned spins = 0;
    bool light = false;
    while (pmk | smk) {
      unsigned long long tp = pmk; unsigned ts = smk;
      const bool single = (__builtin_popcountll(pmk) + __builtin_popcount(smk)) == 1;
      if (light && !single) {
        const int jp = pmk ? (((pmk >> lane) & 1) ? lane : __builtin_ctzll(pmk)) : 0;
        const int js = smk ? (((smk >> (lane & 15)) & 1) ? (lane & 15) : __builtin_ctz(smk)) : 0;
        u32x4 a = {0u, tag, 0u, tag}, c = {0u, tag, 0u, tag};
        if (pmk) a = __builtin_amdgcn_raw_buffer_load_b128(wsr, WS_PP + ((hg * 80 + jp + b) * 4 + w) * PP_W + 2048 + 16, 0, 16);
        if (smk) c = __builtin_amdgcn_raw_buffer_load_b128(wsr, WS_HP + (h * 16 + js) * HP_W + 32768 + (2 * b + 1) * 16, 0, 16);
        tp &= __ballot(a[1] == tag && a[3] == tag);
        ts &= (unsigned)__ballot(c[1] == tag && c[3] == tag);
      }
      bool progress = false;
      if (tp | ts) {
        auto batch = [&](auto nb_tag) {
          constexpr int NB = decltype(nb_tag)::value;
          unsigned rows[NB], mls[NB]; unsigned long long bitp[NB]; unsigned bits[NB];
#pragma unroll
          for (int k = 0; k < NB; ++k) {
            if (tp) { const int q = __builtin_ctzll(tp); bitp[k] = tp & (~tp + 1); bits[k] = 0; tp &= tp - 1; rows[k] = (unsigned)rl((int)p_rows, q); mls[k] = rows[k] + 2048; }
            else if (ts) { const int q = __builtin_ctz(ts); bits[k] = ts & (~ts + 1); bitp[k] = 0; ts &= ts - 1;
                           const unsigned sl = (unsigned)rl((int)s_base, q); rows[k] = sl + b * 2048; mls[k] = sl + 32768 + b * 32; }
            else { bitp[k] = 0; bits[k] = 0; rows[k] = rows[0]; mls[k] = mls[0]; }
          }
          u32x4 d0[NB], d1[NB], dm[NB];
#pragma unroll
          for (int k = 0; k < NB; ++k) {
            d0[k] = __builtin_amdgcn_raw_buffer_load_b128(wsr, lane * 16, rows[k], 16);
            d1[k] = __builtin_amdgcn_raw_buffer_load_b128(wsr, 1024 + lane * 16, rows[k], 16);
            dm[k] = __builtin_amdgcn_raw_buffer_load_b128(wsr, (lane & 1) * 16, mls[k], 16);
          }
#pragma unroll
          for (int k = 0; k < NB; ++k) {
            const bool ok = d0[k][1] == tag && d0[k][3] == tag && d1[k][1] == tag && d1[k][3] == tag && dm[k][1] == tag && dm[k][3] == tag;
            if ((bitp[k] | bits[k]) && __all(ok)) {
              const float mv = __uint_as_float(dm[k][0]), lv = __uint_as_float(dm[k][2]);
              const float m0 = rlf(mv, 0), m1 = rlf(mv, 1), l0 = rlf(lv, 0), l1 = rlf(lv, 1);
              const float n0 = fmaxf(M0, m0), n1 = fmaxf(M1, m1);
              const float a0 = __builtin_amdgcn_exp2f(M0 - n0), b0 = __builtin_amdgcn_exp2f(m0 - n0);
              const float a1 = __builtin_amdgcn_exp2f(M1 - n1), b1 = __builtin_amdgcn_exp2f(m1 - n1);
              L0 = L0 * a0 + l0 * b0; L1 = L1 * a1 + l1 * b1;
              o00 = o00 * a0 + __uint_as_float(d0[k][0]) * b0; o01 = o01 * a0 + __uint_as_float(d0[k][2]) * b0;
              o10 = o10 * a1 + __uint_as_float(d1[k][0]) * b1; o11 = o11 * a1 + __uint_as_float(d1[k][2]) * b1;
              M0 = n0; M1 = n1;
              pmk &= ~bitp[k]; smk &= ~bits[k];
              progress = true;
            }
          }
        };
        const int cnt = __builtin_popcountll(tp) + __builtin_popcount(ts);
        if (cnt == 1) batch(IntC<1>{}); else if (cnt <= 3) batch(IntC<3>{}); else batch(IntC<6>{});
      }
      if (!progress) {
        light = true;
        if (++spins > (1u << 16)) break;
        __builtin_amdgcn_s_sleep(SLEEP);
      }
    }
    const float i0 = 1.f / L0, i1 = 1.f / L1;
    __hip_bfloat16* orow = p.out + (b * NH + 2 * h) * DH + 2 * lane;
    *(unsigned*)orow = pk_bf16(o00 * i0, o01 * i0);
    *(unsigned*)(orow + DH) = pk_bf16(o10 * i1, o11 * i1);
    if (lane == 0) __builtin_amdgcn_raw_buffer_store_b32(tag, wsr, WS_G + (h * 16 + b) * 4, 0, 16);
  }
  STAMP(3);
}

}

extern "C" void kernel_launch(void* const* d_in, const int* in_sizes, int n_in, void* d_out, int out_size, void* d_ws, size_t ws_size,
                              hipStream_t stream) {
  using namespace rattn;
  Params p;
  p.q = (const char*)d_in[0]; p.k = (const char*)d_in[1]; p.v = (const char*)d_in[2];
  p.bt = (const int*)d_in[3]; p.lens = (const int*)d_in[4];
  p.out = (__hip_bfloat16*)d_out; p.ws = (char*)d_ws;
  (void)hipFuncSetAttribute((const void*)radix_attn_kernel, hipFuncAttributeMaxDynamicSharedMemorySize, LDS_TOTAL);
  hipLaunchKernelGGL(radix_attn_kernel, dim3(256), dim3(256), LDS_TOTAL, stream, p);
}
```

```cpp
#include <hip/hip_runtime.h>
#include <hip/hip_bf16.h>
#include <stdint.h>

namespace rattn {

constexpr int NSEQ = 16, NH = 32, NKV = 16, DH = 128, NBLK = 256, BS = 16;
constexpr int LK = NBLK * BS;
#define HRHO 50
#define MDELAY 30
#define PACE 7300
#define ADAPT 1
#define A_F 1008
#define FASTTRY 2
#define PACE_LEAD 2
#define PACE_H 88
#define FULLR 0
#define R0AUX 16
#define HPUB_AT 85
#define A_LEAD 0.f
#define A_HMARGIN 350.f
#define A_HPUB 150.f
#define SLEEP2 2
#define MAPMODE 1
#define SLEEP 6
#define KVPOL "nt"
#define RING 3
constexpr int R = RING;
constexpr int STAGE = 32768;
constexpr int RH = 4;
constexpr int RING_BYTES = 4 * RH * 8192;
constexpr int LDS_BT = RING_BYTES;
constexpr int LDS_QB = LDS_BT + NSEQ * NBLK * 4;
constexpr int LDS_SC = LDS_QB + 4 * R * 512;
constexpr int LDS_MISC = LDS_SC + 4096;
constexpr int LDS_PROG = LDS_MISC + 64;
constexpr int LDS_JUNK = LDS_PROG;
constexpr int LDS_TOTAL = LDS_PROG + 1024;
static_assert(LDS_TOTAL <= 160 * 1024 && LDS_MISC - LDS_QB >= 8192 && RING_BYTES >= R * STAGE, "LDS");

constexpr unsigned WS_G = 0;
constexpr unsigned WS_PROG = 2048;
constexpr unsigned WS_PP = 4096;
constexpr unsigned PP_W = 2176;
constexpr unsigned WS_HP = WS_PP + 320u * 4u * PP_W + 4096u;
constexpr unsigned HP_W = 32768 + 512;
constexpr unsigned WS_END = WS_HP + 16u * 16u * HP_W;
constexpr unsigned WS_STAMP = 64u << 20;

typedef unsigned u32x4 __attribute__((ext_vector_type(4)));
typedef unsigned u32x2 __attribute__((ext_vector_type(2)));
typedef float f32x4 __attribute__((ext_vector_type(4)));
typedef float f32x2 __attribute__((ext_vector_type(2)));
typedef __bf16 bf16x8 __attribute__((ext_vector_type(8)));
typedef __bf16 bf16x2 __attribute__((ext_vector_type(2)));
typedef short s16x4 __attribute__((ext_vector_type(4)));
typedef int i32x4 __attribute__((ext_vector_type(4)));
#define LAS __attribute__((address_space(3)))

constexpr float C2 = 0.088388347648318440f * 1.4426950408889634f;

__device__ __forceinline__ int rfl(int x) { return __builtin_amdgcn_readfirstlane(x); }
__device__ __forceinline__ int rl(int x, int l) { return __builtin_amdgcn_readlane(x, l); }
__device__ __forceinline__ float rlf(float x, int l) { return __builtin_bit_cast(float, __builtin_amdgcn_readlane(__builtin_bit_cast(int, x), l)); }

__device__ __forceinline__ void dma16(const void* sbase, unsigned voff, unsigned dst) {
  unsigned keep;
  asm volatile("s_mov_b32 %0, m0\n\ts_mov_b32 m0, %3\n\ts_nop 0\n\tglobal_load_lds_dwordx4 %1, %2\n\ts_mov_b32 m0, %0"
               : "=&s"(keep) : "v"(voff), "s"(sbase), "s"(dst) : "memory");
}
__device__ __forceinline__ void dma16_sc1(const void* sbase, unsigned voff, unsigned dst) {
  unsigned keep;
  asm volatile("s_mov_b32 %0, m0\n\ts_mov_b32 m0, %3\n\ts_nop 0\n\tglobal_load_lds_dwordx4 %1, %2 sc1\n\ts_mov_b32 m0, %0"
               : "=&s"(keep) : "v"(voff), "s"(sbase), "s"(dst) : "memory");
}
__device__ __forceinline__ void dma_stage(const void* sK, const void* sV, unsigned dstK, unsigned dstV,
                                          unsigned k0, unsigned k1, unsigned k2, unsigned k3,
                                          unsigned v0, unsigned v1, unsigned v2, unsigned v3) {
  unsigned keep;
  asm volatile("s_mov_b32 %0, m0\n\ts_mov_b32 m0, %3\n\ts_nop 0\n\t"
               "global_load_lds_dwordx4 %5, %1 " KVPOL "\n\t"
               "global_load_lds_dwordx4 %6, %1 offset:1024 " KVPOL "\n\t"
               "global_load_lds_dwordx4 %7, %1 offset:2048 " KVPOL "\n\t"
               "global_load_lds_dwordx4 %8, %1 offset:3072 " KVPOL "\n\t"
               "s_mov_b32 m0, %4\n\ts_nop 0\n\t"
               "global_load_lds_dwordx4 %9, %2 " KVPOL "\n\t"
               "global_load_lds_dwordx4 %10, %2 offset:1024 " KVPOL "\n\t"
               "global_load_lds_dwordx4 %11, %2 offset:2048 " KVPOL "\n\t"
               "global_load_lds_dwordx4 %12, %2 offset:3072 " KVPOL "\n\t"
               "s_mov_b32 m0, %0"
               : "=&s"(keep)
               : "s"(sK), "s"(sV), "s"(dstK), "s"(dstV), "v"(k0), "v"(k1), "v"(k2), "v"(k3), "v"(v0), "v"(v1), "v"(v2), "v"(v3)
               : "memory");
}
template <int CTRL> __device__ __forceinline__ int dppi(int x) { return __builtin_amdgcn_update_dpp(0, x, CTRL, 0xf, 0xf, true); }
template <int CTRL> __device__ __forceinline__ int dppi_keep(int old, int x) { return __builtin_amdgcn_update_dpp(old, x, CTRL, 0xf, 0xf, false); }

__device__ __forceinline__ float xg_max(float x) {
  auto s = __builtin_amdgcn_permlane16_swap(__float_as_uint(x), __float_as_uint(x), false, false);
  x = fmaxf(__uint_as_float(s[0]), __uint_as_float(s[1]));
  auto t = __builtin_amdgcn_permlane32_swap(__float_as_uint(x), __float_as_uint(x), false, false);
  return fmaxf(__uint_as_float(t[0]), __uint_as_float(t[1]));
}
__device__ __forceinline__ float xg_sum(float x) {
  auto s = __builtin_amdgcn_permlane16_swap(__float_as_uint(x), __float_as_uint(x), false, false);
  x = __uint_as_float(s[0]) + __uint_as_float(s[1]);
  auto t = __builtin_amdgcn_permlane32_swap(__float_as_uint(x), __float_as_uint(x), false, false);
  return __uint_as_float(t[0]) + __uint_as_float(t[1]);
}
__device__ __forceinline__ unsigned pk_bf16(float a, float b) {
  bf16x2 v = {(__bf16)a, (__bf16)b};
  return __builtin_bit_cast(unsigned, v);
}

template <int NQ>
__device__ __forceinline__ void tile_compute(LAS char* lds, unsigned ka, unsigned va, const bf16x8 (&qf)[NQ][4], f32x4 (&acc)[NQ][8],
                                             float (&m)[NQ], float (&l)[NQ], int lo, int hi, int g4) {
  bf16x8 kf[4];
#pragma unroll
  for (int s = 0; s < 4; ++s) kf[s] = *(LAS bf16x8*)(lds + (ka ^ (s << 6)));
  s16x4 vf[8];
#pragma unroll
  for (int k = 0; k < 8; ++k) vf[k] = __builtin_amdgcn_ds_read_tr16_b64_v4i16((LAS s16x4*)(lds + (va ^ (k << 5))));
  f32x4 S[NQ];
#pragma unroll
  for (int qb = 0; qb < NQ; ++qb) {
    S[qb] = f32x4{0.f, 0.f, 0.f, 0.f};
#pragma unroll
    for (int s = 0; s < 4; ++s) S[qb] = __builtin_amdgcn_mfma_f32_16x16x32_bf16(kf[s], qf[qb][s], S[qb], 0, 0, 0);
  }
  if (lo > 0 || hi < 16) {
#pragma unroll
    for (int qb = 0; qb < NQ; ++qb)
#pragma unroll
      for (int r = 0; r < 4; ++r) { const int t = 4 * g4 + r; S[qb][r] = (t >= lo && t < hi) ? S[qb][r] : -INFINITY; }
  }
  s16x4 pb[NQ];
  bool grow = false;
  float alpha[NQ];
#pragma unroll
  for (int qb = 0; qb < NQ; ++qb) {
    float mx = fmaxf(fmaxf(S[qb][0], S[qb][1]), fmaxf(S[qb][2], S[qb][3]));
    mx = xg_max(mx);
    const float mn2 = fmaxf(m[qb], mx);
    grow |= mn2 > m[qb];
    alpha[qb] = __builtin_amdgcn_exp2f((m[qb] - mn2) * C2);
    m[qb] = mn2;
    const float mc = -mn2 * C2;
    const float p0 = __builtin_amdgcn_exp2f(fmaf(S[qb][0], C2, mc)), p1 = __builtin_amdgcn_exp2f(fmaf(S[qb][1], C2, mc));
    const float p2 = __builtin_amdgcn_exp2f(fmaf(S[qb][2], C2, mc)), p3 = __builtin_amdgcn_exp2f(fmaf(S[qb][3], C2, mc));
    l[qb] = fmaf(l[qb], alpha[qb], (p0 + p1) + (p2 + p3));
    pb[qb] = __builtin_bit_cast(s16x4, u32x2{pk_bf16(p0, p1), pk_bf16(p2, p3)});
  }
  if (__any(grow)) {
#pragma unroll
    for (int qb = 0; qb < NQ; ++qb)
#pragma unroll
      for (int k = 0; k < 8; ++k) acc[qb][k] *= alpha[qb];
  }
#pragma unroll
  for (int qb = 0; qb < NQ; ++qb)
#pragma unroll
    for (int k = 0; k < 8; ++k) acc[qb][k] = __builtin_amdgcn_mfma_f32_16x16x16bf16_1k(vf[k], pb[qb], acc[qb][k], 0, 0, 0);
}

template <int V> struct IntC { static constexpr int value = V; };

struct Params {
  const char* q; const char* k; const char* v; const int* bt; const int* lens; __hip_bfloat16* out; char* ws;
};

__global__ void __launch_bounds__(256, 2) radix_attn_kernel(Params p) {
  extern __shared__ __attribute__((aligned(16))) char smem[];
  LAS char* lds = (LAS char*)smem;
  const int tid = threadIdx.x, lane = tid & 63, w = rfl(tid >> 6);
  const int gidx = blockIdx.x;
  const int hg = (gidx >> 3) & 3;
  const int jj = (((gidx & 7) + 4 * (hg & 1)) & 7) | ((gidx >> 5) << 3);
  const int c16 = lane & 15, g4 = lane >> 4;
#define STAMP(i) do { } while (0)
#define STAMP_FLUSH() do { } while (0)
  STAMP(0);
  const auto wsr = __builtin_amdgcn_make_buffer_rsrc(p.ws, 0, WS_END, 0x00020000);

  const int lenraw = p.lens[c16];
  const unsigned gvw = __builtin_amdgcn_raw_buffer_load_b32(wsr, WS_G + ((hg * 4 + w) * 16 + c16) * 4, 0, 16);
  const unsigned gvh = __builtin_amdgcn_raw_buffer_load_b32(wsr, WS_G + ((hg * 4 + (jj & 3)) * 16 + c16) * 4, 0, 16);
  const unsigned gp = __builtin_amdgcn_raw_buffer_load_b32(wsr, WS_G, 0, 16);
  int pm;
  {
    const i32x4* bt4 = (const i32x4*)p.bt;
    const i32x4 r0 = bt4[lane];
    i32x4 rr[4];
#pragma unroll
    for (int i = 0; i < 4; ++i) rr[i] = bt4[(4 * w + i) * 64 + lane];
    bool n0 = false, n1 = false, n2 = false, n3 = false;
#pragma unroll
    for (int i = 0; i < 4; ++i) {
      n0 |= rr[i][0] != r0[0]; n1 |= rr[i][1] != r0[1]; n2 |= rr[i][2] != r0[2]; n3 |= rr[i][3] != r0[3];
      *(LAS i32x4*)(lds + LDS_BT + ((4 * w + i) * 256 + 4 * lane) * 4) = rr[i];
    }
    const int fc = n0 ? 0 : n1 ? 1 : n2 ? 2 : n3 ? 3 : 4;
    const unsigned long long mm = __ballot(fc < 4);
    int col = NBLK;
    if (mm) { const int fl = __builtin_ctzll(mm); col = 4 * fl + rl(fc, fl); }
    if (lane == 0) *(LAS int*)(lds + LDS_MISC + 4 * w) = col;
    asm volatile("s_waitcnt lgkmcnt(0)\n\ts_barrier" ::: "memory");
    const i32x4 mc = *(LAS i32x4*)(lds + LDS_MISC);
    pm = rfl(min(min(mc[0], mc[1]), min(mc[2], mc[3])));
  }
  asm volatile("" :: "v"(gp));
  const unsigned ptag = (unsigned)rfl((int)gp) + 1u;
  asm volatile("" :: "v"(gvw), "v"(gvh));
  const bool zq_l = lenraw <= 0;
  const int len_l = zq_l ? LK : min(lenraw, LK);
  int mn = len_l;
  mn = min(mn, dppi_keep<0x128>(mn, mn)); mn = min(mn, dppi_keep<0x124>(mn, mn));
  mn = min(mn, dppi_keep<0x122>(mn, mn)); mn = min(mn, dppi_keep<0x121>(mn, mn));
  const int P = min(pm, rfl(mn) >> 4);
  const int tsh = P * BS;
  const int vL = ((len_l + BS - 1) >> 4) - P;
  int inc = vL;
  inc += dppi<0x111>(inc); inc += dppi<0x112>(inc); inc += dppi<0x114>(inc); inc += dppi<0x118>(inc);
  const int N = rl(inc, 15);
  const int vS = inc - vL;
  int kH = 0;
  if (P > 0) kH = min(15, max(1, rfl((int)(((unsigned)P * 16u * 64u + (unsigned)(P + N) * HRHO - 1u) / ((unsigned)(P + N) * HRHO)))));
  kH = min(kH, max(P, 0));
  const int nW = 64 - 4 * kH;
  const unsigned zmask = (unsigned)(__ballot(zq_l) & 0xffffull);
  const bool isH = jj < 4 * kH;
  const float t_all = (float)(P + N) * (16.f * 819.2f / (float)PACE);
  const unsigned long long t_ref = __builtin_amdgcn_s_memrealtime();
  constexpr int A_S0 = 3, A_S1 = 11;
  const int m_avg = (N + nW - 1) / nW;
  const bool adapt_on = nW >= 24 && m_avg >= A_S1 + 8;
  auto read_progress = [&](unsigned ldsoff, float& t1, float& d) -> bool {
    const u32x4 e = *(LAS u32x4*)(lds + ldsoff + c16 * 16);
    const bool okv = e[1] == ptag && e[3] == ptag;
    const int cnt = __builtin_popcount((unsigned)__ballot(okv) & 0xffffu);
    float a = okv ? (float)(int)(e[0] - (unsigned)t_ref) : 0.f, c = okv ? (float)e[2] : 0.f;
    a += __builtin_bit_cast(float, dppi_keep<0x128>(0, __builtin_bit_cast(int, a))); c += __builtin_bit_cast(float, dppi_keep<0x128>(0, __builtin_bit_cast(int, c)));
    a += __builtin_bit_cast(float, dppi_keep<0x124>(0, __builtin_bit_cast(int, a))); c += __builtin_bit_cast(float, dppi_keep<0x124>(0, __builtin_bit_cast(int, c)));
    a += __builtin_bit_cast(float, dppi_keep<0x122>(0, __builtin_bit_cast(int, a))); c += __builtin_bit_cast(float, dppi_keep<0x122>(0, __builtin_bit_cast(int, c)));
    a += __builtin_bit_cast(float, dppi_keep<0x121>(0, __builtin_bit_cast(int, a))); c += __builtin_bit_cast(float, dppi_keep<0x121>(0, __builtin_bit_cast(int, c)));
    if (cnt < 8) return false;
    const float ic = 1.f / (float)cnt;
    t1 = rlf(a, 0) * ic; d = rlf(c, 0) * ic * (1.f / (A_S1 - A_S0));
    return true;
  };
  const int r_lo = (int)((unsigned)(lane * N) / (unsigned)nW), r_hi = (int)((unsigned)((lane + 1) * N) / (unsigned)nW);

  if (isH) {
    const int h = hg * 4 + (jj & 3), part = jj >> 2;
    const int p0 = (P * part) / kH, p1 = (P * (part + 1)) / kH;
#pragma unroll
    for (int i = 0; i < 2; ++i)
      dma16(p.q + (2 * w + i) * 16384 + h * 512, (lane >> 5) * 8192 + (lane & 31) * 16, LDS_QB + (2 * w + i) * 1024);
    unsigned oK[4], oV[4];
#pragma unroll
    for (int i = 0; i < 4; ++i) {
      const int t = 4 * i + g4;
      oK[i] = t * 4096 + ((c16 ^ t) << 4) - i * 1024 + 4096;
      oV[i] = t * 4096 + ((c16 ^ (2 * (t & 7))) << 4) - i * 1024 + 4096;
    }
    const char* kb = p.k + h * 256 - 4096;
    const char* vb = p.v + h * 256 - 4096;
    const int nt = max(0, (p1 - p0 - w + 3) >> 2);
    const unsigned ring = w * (RH * 8192);
    int pg0 = 0, vpg = *(LAS int*)(lds + LDS_BT + min(p0 + w + 4 * lane, NBLK - 1) * 4);
    float pace_h = t_all * (PACE_H * 0.01f) / (float)max(nt, 1), pace_hb = -3.f * pace_h;
    float t_pub = t_all * (HPUB_AT * 0.01f);
    auto issue = [&](int t) {
      { const int due = (int)(pace_hb + (float)t * pace_h); while ((int)(__builtin_amdgcn_s_memrealtime() - t_ref) < due) __builtin_amdgcn_s_sleep(1); }
      if (t - pg0 == 64) { pg0 = t; vpg = *(LAS int*)(lds + LDS_BT + min(p0 + w + 4 * (t + lane), NBLK - 1) * 4); }
      const size_t pb = (size_t)(unsigned)rl(vpg, t - pg0) * 65536;
      const unsigned dst = ring + (t & (RH - 1)) * 8192;
      dma_stage(kb + pb, vb + pb, dst, dst + 4096, oK[0], oK[1], oK[2], oK[3], oV[0], oV[1], oV[2], oV[3]);
    };
    const int npre = min(nt, RH - 1);
#pragma unroll 1
    for (int t = 0; t < npre; ++t) issue(t);
    if (npre == 3) asm volatile("s_waitcnt vmcnt(24)\n\ts_barrier" ::: "memory");
    else asm volatile("s_waitcnt vmcnt(0)\n\ts_barrier" ::: "memory");
    bf16x8 qf[2][4];
#pragma unroll
    for (int qb = 0; qb < 2; ++qb) {
      const bool z = (zmask >> (8 * qb + (c16 >> 1))) & 1;
#pragma unroll
      for (int s = 0; s < 4; ++s) {
        u32x4 x = *(LAS u32x4*)(lds + LDS_QB + (16 * qb + c16) * 256 + (4 * s + g4) * 16);
        if (z) x = u32x4{0u, 0u, 0u, 0u};
        qf[qb][s] = __builtin_bit_cast(bf16x8, x);
      }
    }
    f32x4 acc[2][8];
    float m[2] = {-INFINITY, -INFINITY}, l[2] = {0.f, 0.f};
#pragma unroll
    for (int qb = 0; qb < 2; ++qb)
#pragma unroll
      for (int k = 0; k < 8; ++k) acc[qb][k] = f32x4{0.f, 0.f, 0.f, 0.f};
    const unsigned ka0 = ring + c16 * 256 + ((g4 ^ c16) << 4);
    unsigned va0;
    { const int tq = 4 * g4 + (c16 >> 2), pp = lane & 3; va0 = ring + 4096 + tq * 256 + ((tq & 7) << 5) + ((pp >> 1) << 4) + ((pp & 1) << 3); }
#pragma unroll 1
    for (int t = 0; t < nt; ++t) {
      const int young = min(nt - 1 - t, RH - 2);
      if (young == 2) asm volatile("s_waitcnt vmcnt(16) lgkmcnt(0)" ::: "memory");
      else if (young == 1) asm volatile("s_waitcnt vmcnt(8) lgkmcnt(0)" ::: "memory");
      else asm volatile("s_waitcnt vmcnt(0) lgkmcnt(0)" ::: "memory");
      if (adapt_on && nt >= 12) {
        const int tp = (nt * 9) >> 4;
        if (t == tp + 3) {
          float t1, d;
          if (read_progress(LDS_PROG + w * 256, t1, d)) {
            const float t_end = t1 + (float)(m_avg - A_S1) * d * (A_F * 0.001f);
            const float now = (float)(int)(__builtin_amdgcn_s_memrealtime() - t_ref);
            const int t_i = t + RH - 1;
            pace_h = fmaxf((t_end - A_HMARGIN - now) / (float)max(nt - t_i, 1), 0.f); pace_hb = now - (float)t_i * pace_h;
            t_pub = t_end - A_HPUB;
          }
        }
      }
      if (t + RH - 1 < nt) issue(t + RH - 1);
      if (adapt_on && nt >= 12 && t == ((nt * 9) >> 4) && lane < 16) dma16_sc1(p.ws + WS_PROG, lane * 16, LDS_PROG + w * 256);
      const unsigned sb = (t & (RH - 1)) * 8192;
      tile_compute<2>(lds, ka0 + sb, va0 + sb, qf, acc, m, l, 0, 16, g4);
    }
    STAMP(2);
    constexpr unsigned ROWB = 528, WAVEB = 32 * ROWB, MLB = 4 * WAVEB;
    asm volatile("s_waitcnt lgkmcnt(0)\n\ts_barrier" ::: "memory");
#pragma unroll
    for (int qb = 0; qb < 2; ++qb) {
      const float lt = xg_sum(l[qb]);
#pragma unroll
      for (int k = 0; k < 8; ++k) *(LAS f32x4*)(lds + w * WAVEB + (16 * qb + c16) * ROWB + (16 * k + 4 * g4) * 4) = acc[qb][k];
      if (g4 == 0) *(LAS f32x2*)(lds + MLB + (w * 32 + 16 * qb + c16) * 8) = f32x2{m[qb] * C2, lt};
    }
    asm volatile("s_waitcnt lgkmcnt(0)\n\ts_barrier" ::: "memory");
    while ((int)(__builtin_amdgcn_s_memrealtime() - t_ref) < (int)t_pub) __builtin_amdgcn_s_sleep(8);
    const unsigned slot = WS_HP + (part * 16 + h) * HP_W;
    float mrow = 0.f, lrow = 0.f;
#pragma unroll
    for (int rr = 0; rr < 8; ++rr) {
      const int row = 8 * w + rr;
      f32x2 ml[4], o[4];
#pragma unroll
      for (int v = 0; v < 4; ++v) { ml[v] = *(LAS f32x2*)(lds + MLB + (v * 32 + row) * 8); o[v] = *(LAS f32x2*)(lds + v * WAVEB + row * ROWB + lane * 8); }
      const float M = fmaxf(fmaxf(ml[0][0], ml[1][0]), fmaxf(ml[2][0], ml[3][0]));
      float L = 0.f, o0 = 0.f, o1 = 0.f;
#pragma unroll
      for (int v = 0; v < 4; ++v) {
        const float e = ml[v][1] > 0.f ? __builtin_amdgcn_exp2f(ml[v][0] - M) : 0.f;
        L = fmaf(ml[v][1], e, L); o0 = fmaf(o[v][0], e, o0); o1 = fmaf(o[v][1], e, o1);
      }
      const unsigned tag = (unsigned)rl((int)gvh, row >> 1) + 1u;
      const u32x4 gr = {__float_as_uint(o0), tag, __float_as_uint(o1), tag};
      __builtin_amdgcn_raw_buffer_store_b128(gr, wsr, slot + row * 1024 + lane * 16, 0, 16);
      if (lane == rr) { mrow = M; lrow = L; }
    }
    const unsigned tagr = (unsigned)__shfl((int)gvh, ((8 * w + lane) >> 1) & 15) + 1u;
    if (lane < 8) {
      const unsigned tag = tagr;
      const u32x4 gm = {__float_as_uint(mrow), tag, __float_as_uint(lrow), tag};
      __builtin_amdgcn_raw_buffer_store_b128(gm, wsr, slot + 32768 + (8 * w + lane) * 16, 0, 16);
    }
    STAMP(1);
    STAMP(3);
    STAMP_FLUSH();
    return;
  }

  const int j = jj - 4 * kH, h = hg * 4 + w;
  const int x0 = rl(r_lo, j), x1 = rl(r_hi, j);
  const unsigned umask = (unsigned)(__ballot(max(x0, vS) < min(x1, vS + vL)) & 0xffffull);
  unsigned voffK[4], voffV[4];
#pragma unroll
  for (int i = 0; i < 4; ++i) {
    const int t = 4 * w + i;
    voffK[i] = g4 * 256 + ((c16 ^ t) << 4) + t * 4096 - i * 1024 + 4096;
    voffV[i] = g4 * 256 + ((c16 ^ (2 * (t & 7))) << 4) + t * 4096 - i * 1024 + 4096;
  }
  const unsigned kaddr0 = w * 256 + c16 * 1024 + ((g4 ^ c16) << 4);
  unsigned vaddr0;
  { const int tq = 4 * g4 + (c16 >> 2), pp = lane & 3; vaddr0 = 16384 + tq * 1024 + w * 256 + ((tq & 7) << 5) + ((pp >> 1) << 4) + ((pp & 1) << 3); }
  const char* kbase = p.k + hg * 1024 - 4096;
  const char* vbase = p.v + hg * 1024 - 4096;

  unsigned i_mask = umask; int i_row = 0, i_t0 = 0, i_t1 = 0, i_page; bool i_more = false;
  int n_iss = 0, i_slot = 0;
  auto unit_range = [&](int b, int& t0, int& t1) {
    const int s = rl(vS, b), L = rl(vL, b);
    t0 = (P + max(x0, s) - s) * BS; t1 = min((P + min(x1, s + L) - s) * BS, rl(len_l, b));
  };
  auto q_dma = [&](int b, int slot) {
    if (lane < 32) dma16(p.q + b * 8192 + h * 512, lane * 16, LDS_QB + (w * R + slot) * 512);
  };
  if (i_mask) {
    i_row = __builtin_ctz(i_mask); i_mask &= i_mask - 1; unit_range(i_row, i_t0, i_t1);
    q_dma(i_row, 0);
    i_more = true;
  }
  i_page = i_t0 >> 4;
  int i_pg0 = i_page, vpg = *(LAS int*)(lds + LDS_BT + (i_row * NBLK + min(i_page + lane, NBLK - 1)) * 4);
  int pace_tok = 0;
  float pace_d = t_all / (float)max(x1 - x0, 1), pace_b = -(float)PACE_LEAD * pace_d;
  const int rep = (adapt_on && hg < 2 && j >= (nW >> 1) && j < (nW >> 1) + 8) ? hg * 8 + j - (nW >> 1) : -1;
  unsigned t_s0 = 0;
  auto issue_stage = [&]() {
    const int phys = rl(vpg, i_page - i_pg0);
    const int hi = i_t1 - i_page * 16;
    {
      if (adapt_on && pace_tok == A_S1 + 3 + R - 1) {
        float t1, d;
        if (read_progress(LDS_PROG, t1, d)) { pace_d = d * (A_F * 0.001f); pace_b = t1 - (float)A_S1 * pace_d - A_LEAD; }
      }
      const int due = (int)(pace_b + (float)pace_tok * pace_d);
      pace_tok += 1;
      while ((int)(__builtin_amdgcn_s_memrealtime() - t_ref) < due) __builtin_amdgcn_s_sleep(1);
    }
    const size_t pb = (size_t)(unsigned)phys * 65536;
    const unsigned dst = i_slot * STAGE + 4 * w * 1024;
    if (hi >= 16) {
      dma_stage(kbase + pb, vbase + pb, dst, dst + 16384, voffK[0], voffK[1], voffK[2], voffK[3], voffV[0], voffV[1], voffV[2], voffV[3]);
    } else {
      unsigned ok[4], ov[4];
#pragma unroll
      for (int i = 0; i < 4; ++i) {
        const int d = (min(4 * w + i, hi - 1) - (4 * w + i)) * 4096;
        ok[i] = voffK[i] + d; ov[i] = voffV[i] + d;
      }
      dma_stage(kbase + pb, vbase + pb, dst, dst + 16384, ok[0], ok[1], ok[2], ok[3], ov[0], ov[1], ov[2], ov[3]);
    }
    if (adapt_on) {
      const int k = pace_tok - 1;
      if (rep >= 0 && w == 3) {
        if (k == A_S0) t_s0 = (unsigned)__builtin_amdgcn_s_memrealtime();
        if (k == A_S1 && lane == 0) {
          const unsigned now = (unsigned)__builtin_amdgcn_s_memrealtime();
          const u32x4 e = {now, ptag, now - t_s0, ptag};
          __builtin_amdgcn_raw_buffer_store_b128(e, wsr, WS_PROG + rep * 16, 0, 16);
        }
      }
      if (k == A_S1 + 3 && w == 0 && lane < 16) dma16_sc1(p.ws + WS_PROG, lane * 16, LDS_PROG);
    }
    ++n_iss; i_slot = (i_slot + 1 == R) ? 0 : i_slot + 1;
    ++i_page;
    if (i_page * 16 >= i_t1) {
      if (i_mask) {
        i_row = __builtin_ctz(i_mask); i_mask &= i_mask - 1; unit_range(i_row, i_t0, i_t1); i_page = i_t0 >> 4;
        q_dma(i_row, i_slot);
        i_pg0 = i_page; vpg = *(LAS int*)(lds + LDS_BT + (i_row * NBLK + min(i_page + lane, NBLK - 1)) * 4);
      } else i_more = false;
    } else if (i_page - i_pg0 == 64) {
      i_pg0 = i_page; vpg = *(LAS int*)(lds + LDS_BT + (i_row * NBLK + min(i_page + lane, NBLK - 1)) * 4);
    }
  };
#pragma unroll 1
  for (int s = 0; s < R - 1; ++s) if (i_more) issue_stage();

  unsigned own = 0;
  {
    unsigned mrem = umask;
    while (mrem) {
      const int b = __builtin_ctz(mrem); mrem &= mrem - 1;
      const int s = rl(vS, b), L = rl(vL, b);
      unsigned long long pmk = __ballot(lane < nW && max(r_lo, s) < min(r_hi, s + L));
      int idx = w % __builtin_popcountll(pmk);
      while (idx--) pmk &= pmk - 1;
      if (__builtin_ctzll(pmk) == j) own |= 1u << b;
    }
    unsigned zl = (unsigned)(__ballot(vL == 0) & 0xffffull);
    while (zl) { const int b = __builtin_ctz(zl); zl &= zl - 1; if ((b * 4) % nW == j) own |= 1u << b; }
  }

  int fb = -1, fcnt = 0; unsigned vrow = 0, vmlo = 0;
  if (own && (own & (own - 1)) == 0) {
    const int b = __builtin_ctz(own);
    const unsigned above = umask >> b;
    if ((umask >> b & 1) && (above == 1 || __builtin_popcount(above) == 2)) {
      const int s = rl(vS, b), L = rl(vL, b);
      const unsigned long long pmk = __ballot(lane < nW && max(r_lo, s) < min(r_hi, s + L)) & ~(1ull << j);
      const int np = __builtin_popcountll(pmk);
      if (np + kH <= 8 && np + kH > 0) {
        fb = b; fcnt = np + kH;
        const int rank = __builtin_amdgcn_mbcnt_hi((unsigned)(pmk >> 32), __builtin_amdgcn_mbcnt_lo((unsigned)pmk, 0));
        const unsigned prow = WS_PP + ((hg * 80 + lane + b) * 4 + w) * PP_W;
        const unsigned pushed = (unsigned)__builtin_amdgcn_ds_permute((((pmk >> lane) & 1) ? rank : 63) << 2, (int)prow);
        const unsigned hb = WS_HP + (min(max(lane - np, 0), 15) * 16 + h) * HP_W;
        vrow = lane < np ? pushed : hb + b * 2048;
        const unsigned mlb = lane < np ? pushed + 2048 : hb + 32768 + b * 32;
        vmlo = (unsigned)__shfl((int)mlb, min(lane >> 1, fcnt - 1)) + (lane & 1) * 16;
      }
    }
  }

  int n_comp = 0, c_slot = 0;
  unsigned c_mask = umask;
  float ro[2][2] = {{0.f, 0.f}, {0.f, 0.f}}, rm[2] = {-INFINITY, -INFINITY}, rs[2] = {0.f, 0.f};
  float ho[2][2] = {{0.f, 0.f}, {0.f, 0.f}}, hm[2] = {-INFINITY, -INFINITY}, hs[2] = {0.f, 0.f};
  int last_b = -1, held_b = -1;
  auto publish = [&](int b, const float (&o)[2][2], const float (&mm)[2], const float (&ll)[2]) {
    const unsigned tag = (unsigned)rl((int)gvw, b) + 1u;
    const unsigned slot = WS_PP + ((hg * 80 + j + b) * 4 + w) * PP_W;
    const u32x4 g0 = {__float_as_uint(o[0][0]), tag, __float_as_uint(o[0][1]), tag};
    const u32x4 g1 = {__float_as_uint(o[1][0]), tag, __float_as_uint(o[1][1]), tag};
    __builtin_amdgcn_raw_buffer_store_b128(g0, wsr, slot + lane * 16, 0, 16);
    __builtin_amdgcn_raw_buffer_store_b128(g1, wsr, slot + 1024 + lane * 16, 0, 16);
    if (lane < 2) {
      const u32x4 gm = {__float_as_uint(lane ? mm[1] : mm[0]), tag, __float_as_uint(lane ? ll[1] : ll[0]), tag};
      __builtin_amdgcn_raw_buffer_store_b128(gm, wsr, slot + 2048 + lane * 16, 0, 16);
    }
  };
  while (c_mask) {
    const int b = __builtin_ctz(c_mask); c_mask &= c_mask - 1;
    int t0, t1; unit_range(b, t0, t1);
    bf16x8 qf[1][4];
    f32x4 acc[1][8];
    float m[1] = {-INFINITY}, l[1] = {0.f};
#pragma unroll
    for (int k = 0; k < 8; ++k) acc[0][k] = f32x4{0.f, 0.f, 0.f, 0.f};
    bool first = true;
    for (int page = t0 >> 4; page * 16 < t1; ++page) {
      {
        const int young = n_iss - n_comp - 1;
        if (R == 4 && young >= 2) asm volatile("s_waitcnt vmcnt(16) lgkmcnt(0)\n\ts_barrier" ::: "memory");
        else if (young >= 1) asm volatile("s_waitcnt vmcnt(8) lgkmcnt(0)\n\ts_barrier" ::: "memory");
        else asm volatile("s_waitcnt vmcnt(0) lgkmcnt(0)\n\ts_barrier" ::: "memory");
      }
      if (first) {
        const bool z = (c16 >= 2) || ((zmask >> b) & 1);
#pragma unroll
        for (int s = 0; s < 4; ++s) {
          u32x4 x = *(LAS u32x4*)(lds + LDS_QB + (w * R + c_slot) * 512 + (c16 & 1) * 256 + (4 * s + g4) * 16);
          if (z) x = u32x4{0u, 0u, 0u, 0u};
          qf[0][s] = __builtin_bit_cast(bf16x8, x);
        }
        asm volatile("s_waitcnt lgkmcnt(0)" ::: "memory");
        first = false;
      }
      if (i_more) issue_stage();
      const unsigned sb = c_slot * STAGE;
      tile_compute<1>(lds, kaddr0 + sb, vaddr0 + sb, qf, acc, m, l, 0, t1 - page * 16, g4);
      ++n_comp; c_slot = (c_slot + 1 == R) ? 0 : c_slot + 1;
    }
    const float lt = xg_sum(l[0]);
    LAS char* sc = lds + LDS_SC + w * 1024;
    if (c16 < 2) {
#pragma unroll
      for (int k = 0; k < 8; ++k) *(LAS f32x4*)(sc + c16 * 512 + (16 * k + 4 * g4) * 4) = acc[0][k];
    }
    asm volatile("s_waitcnt lgkmcnt(0)" ::: "memory");
    const f32x2 a0 = *(LAS f32x2*)(sc + lane * 8), a1 = *(LAS f32x2*)(sc + 512 + lane * 8);
    if (last_b >= 0) {
      if (held_b >= 0) publish(held_b, ho, hm, hs);
      held_b = last_b;
#pragma unroll
      for (int r = 0; r < 2; ++r) { ho[r][0] = ro[r][0]; ho[r][1] = ro[r][1]; hm[r] = rm[r]; hs[r] = rs[r]; }
    }
    ro[0][0] = a0[0]; ro[0][1] = a0[1]; ro[1][0] = a1[0]; ro[1][1] = a1[1];
    rm[0] = rlf(m[0], 0) * C2; rm[1] = rlf(m[0], 1) * C2;
    rs[0] = rlf(lt, 0); rs[1] = rlf(lt, 1);
    last_b = b;
  }
  STAMP(2);
  if (last_b >= 0 && !((own >> last_b) & 1)) { publish(last_b, ro, rm, rs); last_b = -1; }
  if (held_b >= 0 && !((own >> held_b) & 1)) { publish(held_b, ho, hm, hs); held_b = -1; }
  STAMP(1);

  if (own) { const unsigned long long c0 = __builtin_amdgcn_s_memrealtime(); while (__builtin_amdgcn_s_memrealtime() - c0 < MDELAY) __builtin_amdgcn_s_sleep(1); }
  if (fb >= 0) {
    const int b = fb;
    const unsigned tag = (unsigned)rl((int)gvw, b) + 1u;
    const bool mine_last = b == last_b;
    const float om0 = mine_last ? rm[0] : hm[0], om1 = mine_last ? rm[1] : hm[1], ol0 = mine_last ? rs[0] : hs[0], ol1 = mine_last ? rs[1] : hs[1];
    const float oo00 = mine_last ? ro[0][0] : ho[0][0], oo01 = mine_last ? ro[0][1] : ho[0][1];
    const float oo10 = mine_last ? ro[1][0] : ho[1][0], oo11 = mine_last ? ro[1][1] : ho[1][1];
    auto attempt = [&](auto nb_tag) -> bool {
      constexpr int NB = decltype(nb_tag)::value;
      u32x4 d0[NB], d1[NB];
      unsigned so[NB];
#pragma unroll
      for (int k = 0; k < NB; ++k) so[k] = (unsigned)rl((int)vrow, min(k, fcnt - 1));
      asm volatile("" ::: "memory");
      const u32x4 dm = __builtin_amdgcn_raw_buffer_load_b128(wsr, vmlo, 0, 16);
#pragma unroll
      for (int k = 0; k < NB; ++k) {
        d0[k] = __builtin_amdgcn_raw_buffer_load_b128(wsr, lane * 16, so[k], 16);
        d1[k] = __builtin_amdgcn_raw_buffer_load_b128(wsr, 1024 + lane * 16, so[k], 16);
      }
      __builtin_amdgcn_sched_barrier(0);
      const bool live = lane < 2 * fcnt;
      const float mk = live ? __uint_as_float(dm[0]) : -INFINITY, lk = live ? __uint_as_float(dm[2]) : 0.f;
      const float om = (lane & 1) ? om1 : om0, ol = (lane & 1) ? ol1 : ol0;
      float M = fmaxf(mk, om);
      M = fmaxf(M, __builtin_bit_cast(float, dppi_keep<0x122>(__builtin_bit_cast(int, M), __builtin_bit_cast(int, M))));
      M = fmaxf(M, __builtin_bit_cast(float, dppi_keep<0x124>(__builtin_bit_cast(int, M), __builtin_bit_cast(int, M))));
      M = fmaxf(M, __builtin_bit_cast(float, dppi_keep<0x128>(__builtin_bit_cast(int, M), __builtin_bit_cast(int, M))));
      const float e = __builtin_amdgcn_exp2f(mk - M), eo = __builtin_amdgcn_exp2f(om - M);
      float Ls = lk * e;
      Ls += __builtin_bit_cast(float, dppi_keep<0x122>(0, __builtin_bit_cast(int, Ls)));
      Ls += __builtin_bit_cast(float, dppi_keep<0x124>(0, __builtin_bit_cast(int, Ls)));
      Ls += __builtin_bit_cast(float, dppi_keep<0x128>(0, __builtin_bit_cast(int, Ls)));
      Ls = fmaf(ol, eo, Ls);
      const float inv = __builtin_amdgcn_rcpf(Ls);
      const float en = e * inv, eon = eo * inv;
      const float eo0 = rlf(eon, 0), eo1 = rlf(eon, 1);
      float o00 = oo00 * eo0, o01 = oo01 * eo0, o10 = oo10 * eo1, o11 = oo11 * eo1;
      unsigned bad = (dm[1] ^ tag) | (dm[3] ^ tag);
#pragma unroll
      for (int k = 0; k < NB; ++k) {
        const float w0 = rlf(en, 2 * k), w1 = rlf(en, 2 * k + 1);
        bad |= ((d0[k][1] ^ tag) | (d0[k][3] ^ tag)) | ((d1[k][1] ^ tag) | (d1[k][3] ^ tag));
        o00 = fmaf(__uint_as_float(d0[k][0]), w0, o00); o01 = fmaf(__uint_as_float(d0[k][2]), w0, o01);
        o10 = fmaf(__uint_as_float(d1[k][0]), w1, o10); o11 = fmaf(__uint_as_float(d1[k][2]), w1, o11);
      }
      if (__any(bad != 0)) return false;
      __hip_bfloat16* orow = p.out + (b * NH + 2 * h) * DH + 2 * lane;
      *(unsigned*)orow = pk_bf16(o00, o01);
      *(unsigned*)(orow + DH) = pk_bf16(o10, o11);
      if (lane == 0) __builtin_amdgcn_raw_buffer_store_b32(tag, wsr, WS_G + (h * 16 + b) * 4, 0, 16);
      return true;
    };
#pragma unroll 1
    for (int t = 0; t < 16; ++t) {
      if (t >= FASTTRY) {
        unsigned spins = 0;
        while (true) {
          asm volatile("" ::: "memory");
          const u32x4 x = __builtin_amdgcn_raw_buffer_load_b128(wsr, vmlo, 0, 16);
          if (!__any(x[1] != tag || x[3] != tag) || ++spins > (1u << 16)) break;
          __builtin_amdgcn_s_sleep(SLEEP2);
        }
      }
      const bool ok = fcnt <= 5 ? attempt(IntC<5>{}) : attempt(IntC<8>{});
      if (ok) { own = 0; break; }
    }
  }
  while (own) {
    const int b = __builtin_ctz(own); own &= own - 1;
    const unsigned tag = (unsigned)rl((int)gvw, b) + 1u;
    const int s = rl(vS, b), L = rl(vL, b);
    unsigned long long pmk = __ballot(lane < nW && max(r_lo, s) < min(r_hi, s + L));
    unsigned smk = (1u << kH) - 1u;
    float M0 = -INFINITY, M1 = -INFINITY, L0 = 0.f, L1 = 0.f, o00 = 0.f, o01 = 0.f, o10 = 0.f, o11 = 0.f;
    if (b == last_b) {
      pmk &= ~(1ull << j);
      M0 = rm[0]; M1 = rm[1]; L0 = rs[0]; L1 = rs[1]; o00 = ro[0][0]; o01 = ro[0][1]; o10 = ro[1][0]; o11 = ro[1][1];
    } else if (b == held_b) {
      pmk &= ~(1ull << j);
      M0 = hm[0]; M1 = hm[1]; L0 = hs[0]; L1 = hs[1]; o00 = ho[0][0]; o01 = ho[0][1]; o10 = ho[1][0]; o11 = ho[1][1];
    }
    const unsigned p_rows = WS_PP + ((hg * 80 + lane + b) * 4 + w) * PP_W;
    const unsigned s_base = WS_HP + ((lane & 15) * 16 + h) * HP_W;
    unsigned spins = 0;
    bool light = false;
    while (pmk | smk) {
      asm volatile("" ::: "memory");
      unsigned long long tp = pmk; unsigned ts = smk;
      const bool single = (__builtin_popcountll(pmk) + __builtin_popcount(smk)) == 1;
      if (light && !single) {
        const int jp = pmk ? (((pmk >> lane) & 1) ? lane : __builtin_ctzll(pmk)) : 0;
        const int js = smk ? (((smk >> (lane & 15)) & 1) ? (lane & 15) : __builtin_ctz(smk)) : 0;
        u32x4 a = {0u, tag, 0u, tag}, c = {0u, tag, 0u, tag};
        if (pmk) a = __builtin_amdgcn_raw_buffer_load_b128(wsr, WS_PP + ((hg * 80 + jp + b) * 4 + w) * PP_W + 2048 + 16, 0, 16);
        if (smk) c = __builtin_amdgcn_raw_buffer_load_b128(wsr, WS_HP + (js * 16 + h) * HP_W + 32768 + (2 * b + 1) * 16, 0, 16);
        tp &= __ballot(a[1] == tag && a[3] == tag);
        ts &= (unsigned)__ballot(c[1] == tag && c[3] == tag);
      }
      bool progress = false;
      if (tp | ts) {
        auto batch = [&](auto nb_tag) {
          constexpr int NB = decltype(nb_tag)::value;
          unsigned rows[NB], mls[NB]; unsigned long long bitp[NB]; unsigned bits[NB];
#pragma unroll
          for (int k = 0; k < NB; ++k) {
            if (tp) { const int q = __builtin_ctzll(tp); bitp[k] = tp & (~tp + 1); bits[k] = 0; tp &= tp - 1; rows[k] = (unsigned)rl((int)p_rows, q); mls[k] = rows[k] + 2048; }
            else if (ts) { const int q = __builtin_ctz(ts); bits[k] = ts & (~ts + 1); bitp[k] = 0; ts &= ts - 1;
                           const unsigned sl = (unsigned)rl((int)s_base, q); rows[k] = sl + b * 2048; mls[k] = sl + 32768 + b * 32; }
            else { bitp[k] = 0; bits[k] = 0; rows[k] = rows[0]; mls[k] = mls[0]; }
          }
          u32x4 d0[NB], d1[NB], dm[NB];
          if (R0AUX != 16 && spins == 0) {
#pragma unroll
            for (int k = 0; k < NB; ++k) {
              d0[k] = __builtin_amdgcn_raw_buffer_load_b128(wsr, lane * 16, rows[k], R0AUX);
              d1[k] = __builtin_amdgcn_raw_buffer_load_b128(wsr, 1024 + lane * 16, rows[k], R0AUX);
              dm[k] = __builtin_amdgcn_raw_buffer_load_b128(wsr, (lane & 1) * 16, mls[k], R0AUX);
            }
          } else {
#pragma unroll
            for (int k = 0; k < NB; ++k) {
              d0[k] = __builtin_amdgcn_raw_buffer_load_b128(wsr, lane * 16, rows[k], 16);
              d1[k] = __builtin_amdgcn_raw_buffer_load_b128(wsr, 1024 + lane * 16, rows[k], 16);
              dm[k] = __builtin_amdgcn_raw_buffer_load_b128(wsr, (lane & 1) * 16, mls[k], 16);
            }
          }
#pragma unroll
          for (int k = 0; k < NB; ++k) {
            const bool ok = d0[k][1] == tag && d0[k][3] == tag && d1[k][1] == tag && d1[k][3] == tag && dm[k][1] == tag && dm[k][3] == tag;
            if ((bitp[k] | bits[k]) && __all(ok)) {
              const float mv = __uint_as_float(dm[k][0]), lv = __uint_as_float(dm[k][2]);
              const float m0 = rlf(mv, 0), m1 = rlf(mv, 1), l0 = rlf(lv, 0), l1 = rlf(lv, 1);
              const float n0 = fmaxf(M0, m0), n1 = fmaxf(M1, m1);
              const float a0 = __builtin_amdgcn_exp2f(M0 - n0), b0 = __builtin_amdgcn_exp2f(m0 - n0);
              const float a1 = __builtin_amdgcn_exp2f(M1 - n1), b1 = __builtin_amdgcn_exp2f(m1 - n1);
              L0 = L0 * a0 + l0 * b0; L1 = L1 * a1 + l1 * b1;
              o00 = o00 * a0 + __uint_as_float(d0[k][0]) * b0; o01 = o01 * a0 + __uint_as_float(d0[k][2]) * b0;
              o10 = o10 * a1 + __uint_as_float(d1[k][0]) * b1; o11 = o11 * a1 + __uint_as_float(d1[k][2]) * b1;
              M0 = n0; M1 = n1;
              pmk &= ~bitp[k]; smk &= ~bits[k];
              progress = true;
            }
          }
        };
        const int cnt = __builtin_popcountll(tp) + __builtin_popcount(ts);
        if (cnt == 1) batch(IntC<1>{}); else if (cnt <= 3) batch(IntC<3>{}); else batch(IntC<6>{});

      }
      if (!progress) {
        if (++spins > (1u << 16)) break;
        if (spins > FULLR) { light = true; __builtin_amdgcn_s_sleep(SLEEP); }
      }
    }
    const float i0 = 1.f / L0, i1 = 1.f / L1;
    __hip_bfloat16* orow = p.out + (b * NH + 2 * h) * DH + 2 * lane;
    *(unsigned*)orow = pk_bf16(o00 * i0, o01 * i0);
    *(unsigned*)(orow + DH) = pk_bf16(o10 * i1, o11 * i1);
    if (lane == 0) __builtin_amdgcn_raw_buffer_store_b32(tag, wsr, WS_G + (h * 16 + b) * 4, 0, 16);
  }
  STAMP(3);
  STAMP_FLUSH();
}

}

extern "C" void kernel_launch(void* const* d_in, const int* in_sizes, int n_in, void* d_out, int out_size, void* d_ws, size_t ws_size,
                              hipStream_t stream) {
  using namespace rattn;
  Params p;
  p.q = (const char*)d_in[0]; p.k = (const char*)d_in[1]; p.v = (const char*)d_in[2];
  p.bt = (const int*)d_in[3]; p.lens = (const int*)d_in[4];
  p.out = (__hip_bfloat16*)d_out; p.ws = (char*)d_ws;
  (void)hipFuncSetAttribute((const void*)radix_attn_kernel, hipFuncAttributeMaxDynamicSharedMemorySize, LDS_TOTAL);
  hipLaunchKernelGGL(radix_attn_kernel, dim3(256), dim3(256), LDS_TOTAL, stream, p);
}
```

```cpp
#include <hip/hip_runtime.h>
#include <hip/hip_bf16.h>
#include <stdint.h>

namespace rattn {

constexpr int NSEQ = 16, NH = 32, NKV = 16, DH = 128, NBLK = 256, BS = 16;
constexpr int LK = NBLK * BS;
#define GAUX 16
#define HRHO 50
#define MDELAY 30
#define PACE 7300
#define ADAPT 1
#define A_F 1008
#define FASTTRY 2

#define PACE_LEAD 2
#define PACE_H 88
#define FULLR 0
#define R0AUX 16
#define HPUB_AT 85
#define A_LEAD 0.f
#define A_HMARGIN 350.f
#define A_HPUB 150.f
#define SLEEP2 2
#define MAPMODE 1
#define SLEEP 6
#define KVPOL "nt"
#define RING 3
constexpr int R = RING;
constexpr int STAGE = 32768;
constexpr int RH = 4;
constexpr int RING_BYTES = 4 * RH * 8192;
constexpr int LDS_BT = RING_BYTES;
constexpr int LDS_QB = LDS_BT + NSEQ * NBLK * 4;
constexpr int LDS_SC = LDS_QB + 4 * R * 512;
constexpr int LDS_MISC = LDS_SC + 4096;
constexpr int LDS_PROG = LDS_MISC + 64;
constexpr int LDS_JUNK = LDS_PROG;
constexpr int LDS_TOTAL = LDS_PROG + 1024;
static_assert(LDS_TOTAL <= 160 * 1024 && LDS_MISC - LDS_QB >= 8192 && RING_BYTES >= R * STAGE, "LDS");

constexpr unsigned WS_G = 0;
constexpr unsigned WS_PROG = 2048;
constexpr unsigned WS_PP = 4096;
constexpr unsigned PP_W = 2176;
constexpr unsigned WS_HP = WS_PP + 320u * 4u * PP_W + 4096u;
constexpr unsigned HP_W = 32768 + 512;
constexpr unsigned WS_END = WS_HP + 16u * 16u * HP_W;
constexpr unsigned WS_STAMP = 64u << 20;

typedef unsigned u32x4 __attribute__((ext_vector_type(4)));
typedef unsigned u32x2 __attribute__((ext_vector_type(2)));
typedef float f32x4 __attribute__((ext_vector_type(4)));
typedef float f32x2 __attribute__((ext_vector_type(2)));
typedef __bf16 bf16x8 __attribute__((ext_vector_type(8)));
typedef __bf16 bf16x2 __attribute__((ext_vector_type(2)));
typedef short s16x4 __attribute__((ext_vector_type(4)));
typedef int i32x4 __attribute__((ext_vector_type(4)));
#define LAS __attribute__((address_space(3)))

constexpr float C2 = 0.088388347648318440f * 1.4426950408889634f;

__device__ __forceinline__ int rfl(int x) { return __builtin_amdgcn_readfirstlane(x); }
__device__ __forceinline__ int rl(int x, int l) { return __builtin_amdgcn_readlane(x, l); }
__device__ __forceinline__ float rlf(float x, int l) { return __builtin_bit_cast(float, __builtin_amdgcn_readlane(__builtin_bit_cast(int, x), l)); }

__device__ __forceinline__ void dma16(const void* sbase, unsigned voff, unsigned dst) {
  unsigned keep;
  asm volatile("s_mov_b32 %0, m0\n\ts_mov_b32 m0, %3\n\ts_nop 0\n\tglobal_load_lds_dwordx4 %1, %2\n\ts_mov_b32 m0, %0"
               : "=&s"(keep) : "v"(voff), "s"(sbase), "s"(dst) : "memory");
}
__device__ __forceinline__ void dma16_sc1(const void* sbase, unsigned voff, unsigned dst) {
  unsigned keep;
  asm volatile("s_mov_b32 %0, m0\n\ts_mov_b32 m0, %3\n\ts_nop 0\n\tglobal_load_lds_dwordx4 %1, %2 sc1\n\ts_mov_b32 m0, %0"
               : "=&s"(keep) : "v"(voff), "s"(sbase), "s"(dst) : "memory");
}
__device__ __forceinline__ void dma_stage(const void* sK, const void* sV, unsigned dstK, unsigned dstV,
                                          unsigned k0, unsigned k1, unsigned k2, unsigned k3,
                                          unsigned v0, unsigned v1, unsigned v2, unsigned v3) {
  unsigned keep;
  asm volatile("s_mov_b32 %0, m0\n\ts_mov_b32 m0, %3\n\ts_nop 0\n\t"
               "global_load_lds_dwordx4 %5, %1 " KVPOL "\n\t"
               "global_load_lds_dwordx4 %6, %1 offset:1024 " KVPOL "\n\t"
               "global_load_lds_dwordx4 %7, %1 offset:2048 " KVPOL "\n\t"
               "global_load_lds_dwordx4 %8, %1 offset:3072 " KVPOL "\n\t"
               "s_mov_b32 m0, %4\n\ts_nop 0\n\t"
               "global_load_lds_dwordx4 %9, %2 " KVPOL "\n\t"
               "global_load_lds_dwordx4 %10, %2 offset:1024 " KVPOL "\n\t"
               "global_load_lds_dwordx4 %11, %2 offset:2048 " KVPOL "\n\t"
               "global_load_lds_dwordx4 %12, %2 offset:3072 " KVPOL "\n\t"
               "s_mov_b32 m0, %0"
               : "=&s"(keep)
               : "s"(sK), "s"(sV), "s"(dstK), "s"(dstV), "v"(k0), "v"(k1), "v"(k2), "v"(k3), "v"(v0), "v"(v1), "v"(v2), "v"(v3)
               : "memory");
}
template <int CTRL> __device__ __forceinline__ int dppi(int x) { return __builtin_amdgcn_update_dpp(0, x, CTRL, 0xf, 0xf, true); }
template <int CTRL> __device__ __forceinline__ int dppi_keep(int old, int x) { return __builtin_amdgcn_update_dpp(old, x, CTRL, 0xf, 0xf, false); }

__device__ __forceinline__ float xg_max(float x) {
  auto s = __builtin_amdgcn_permlane16_swap(__float_as_uint(x), __float_as_uint(x), false, false);
  x = fmaxf(__uint_as_float(s[0]), __uint_as_float(s[1]));
  auto t = __builtin_amdgcn_permlane32_swap(__float_as_uint(x), __float_as_uint(x), false, false);
  return fmaxf(__uint_as_float(t[0]), __uint_as_float(t[1]));
}
__device__ __forceinline__ float xg_sum(float x) {
  auto s = __builtin_amdgcn_permlane16_swap(__float_as_uint(x), __float_as_uint(x), false, false);
  x = __uint_as_float(s[0]) + __uint_as_float(s[1]);
  auto t = __builtin_amdgcn_permlane32_swap(__float_as_uint(x), __float_as_uint(x), false, false);
  return __uint_as_float(t[0]) + __uint_as_float(t[1]);
}
__device__ __forceinline__ unsigned pk_bf16(float a, float b) {
  bf16x2 v = {(__bf16)a, (__bf16)b};
  return __builtin_bit_cast(unsigned, v);
}

template <int NQ>
__device__ __forceinline__ void tile_compute(LAS char* lds, unsigned ka, unsigned va, const bf16x8 (&qf)[NQ][4], f32x4 (&acc)[NQ][8],
                                             float (&m)[NQ], float (&l)[NQ], int lo, int hi, int g4) {
  bf16x8 kf[4];
#pragma unroll
  for (int s = 0; s < 4; ++s) kf[s] = *(LAS bf16x8*)(lds + (ka ^ (s << 6)));
  s16x4 vf[8];
#pragma unroll
  for (int k = 0; k < 8; ++k) vf[k] = __builtin_amdgcn_ds_read_tr16_b64_v4i16((LAS s16x4*)(lds + (va ^ (k << 5))));
  f32x4 S[NQ];
#pragma unroll
  for (int qb = 0; qb < NQ; ++qb) {
    S[qb] = f32x4{0.f, 0.f, 0.f, 0.f};
#pragma unroll
    for (int s = 0; s < 4; ++s) S[qb] = __builtin_amdgcn_mfma_f32_16x16x32_bf16(kf[s], qf[qb][s], S[qb], 0, 0, 0);
  }
  if (lo > 0 || hi < 16) {
#pragma unroll
    for (int qb = 0; qb < NQ; ++qb)
#pragma unroll
      for (int r = 0; r < 4; ++r) { const int t = 4 * g4 + r; S[qb][r] = (t >= lo && t < hi) ? S[qb][r] : -INFINITY; }
  }
  s16x4 pb[NQ];
  bool grow = false;
  float alpha[NQ];
#pragma unroll
  for (int qb = 0; qb < NQ; ++qb) {
    float mx = fmaxf(fmaxf(S[qb][0], S[qb][1]), fmaxf(S[qb][2], S[qb][3]));
    mx = xg_max(mx);
    const float mn2 = fmaxf(m[qb], mx);
    grow |= mn2 > m[qb];
    alpha[qb] = __builtin_amdgcn_exp2f((m[qb] - mn2) * C2);
    m[qb] = mn2;
    const float mc = -mn2 * C2;
    const float p0 = __builtin_amdgcn_exp2f(fmaf(S[qb][0], C2, mc)), p1 = __builtin_amdgcn_exp2f(fmaf(S[qb][1], C2, mc));
    const float p2 = __builtin_amdgcn_exp2f(fmaf(S[qb][2], C2, mc)), p3 = __builtin_amdgcn_exp2f(fmaf(S[qb][3], C2, mc));
    l[qb] = fmaf(l[qb], alpha[qb], (p0 + p1) + (p2 + p3));
    pb[qb] = __builtin_bit_cast(s16x4, u32x2{pk_bf16(p0, p1), pk_bf16(p2, p3)});
  }
  if (__any(grow)) {
#pragma unroll
    for (int qb = 0; qb < NQ; ++qb)
#pragma unroll
      for (int k = 0; k < 8; ++k) acc[qb][k] *= alpha[qb];
  }
#pragma unroll
  for (int qb = 0; qb < NQ; ++qb)
#pragma unroll
    for (int k = 0; k < 8; ++k) acc[qb][k] = __builtin_amdgcn_mfma_f32_16x16x16bf16_1k(vf[k], pb[qb], acc[qb][k], 0, 0, 0);
}

template <int V> struct IntC { static constexpr int value = V; };

struct Params {
  const char* q; const char* k; const char* v; const int* bt; const int* lens; __hip_bfloat16* out; char* ws;
  unsigned kvbytes; unsigned unused;
};

__global__ void __launch_bounds__(256, 2) radix_attn_kernel(Params p) {
  extern __shared__ __attribute__((aligned(16))) char smem[];
  LAS char* lds = (LAS char*)smem;
  const int tid = threadIdx.x, lane = tid & 63, w = rfl(tid >> 6);
  const int gidx = blockIdx.x;
  const int hg = (gidx >> 3) & 3;
  const int jj = (((gidx & 7) + 4 * (hg & 1)) & 7) | ((gidx >> 5) << 3);
  const int c16 = lane & 15, g4 = lane >> 4;
#define STAMP(i) do { } while (0)
#define STAMP_FLUSH() do { } while (0)
  STAMP(0);
  const auto wsr = __builtin_amdgcn_make_buffer_rsrc(p.ws, 0, WS_END, 0x00020000);

  const int lenraw = p.lens[c16];
  const unsigned gvw = __builtin_amdgcn_raw_buffer_load_b32(wsr, WS_G + ((hg * 4 + w) * 16 + c16) * 4, 0, 16);
  const unsigned gvh = __builtin_amdgcn_raw_buffer_load_b32(wsr, WS_G + ((hg * 4 + (jj & 3)) * 16 + c16) * 4, 0, 16);
  const unsigned gp = __builtin_amdgcn_raw_buffer_load_b32(wsr, WS_G, 0, 16);
  int pm;
  {
    const i32x4* bt4 = (const i32x4*)p.bt;
    const i32x4 r0 = bt4[lane];
    i32x4 rr[4];
#pragma unroll
    for (int i = 0; i < 4; ++i) rr[i] = bt4[(4 * w + i) * 64 + lane];
    bool n0 = false, n1 = false, n2 = false, n3 = false;
#pragma unroll
    for (int i = 0; i < 4; ++i) {
      n0 |= rr[i][0] != r0[0]; n1 |= rr[i][1] != r0[1]; n2 |= rr[i][2] != r0[2]; n3 |= rr[i][3] != r0[3];
      *(LAS i32x4*)(lds + LDS_BT + ((4 * w + i) * 256 + 4 * lane) * 4) = rr[i];
    }
    const int fc = n0 ? 0 : n1 ? 1 : n2 ? 2 : n3 ? 3 : 4;
    const unsigned long long mm = __ballot(fc < 4);
    int col = NBLK;
    if (mm) { const int fl = __builtin_ctzll(mm); col = 4 * fl + rl(fc, fl); }
    if (lane == 0) *(LAS int*)(lds + LDS_MISC + 4 * w) = col;
    asm volatile("s_waitcnt lgkmcnt(0)\n\ts_barrier" ::: "memory");
    const i32x4 mc = *(LAS i32x4*)(lds + LDS_MISC);
    pm = rfl(min(min(mc[0], mc[1]), min(mc[2], mc[3])));
  }
  asm volatile("" :: "v"(gp));
  const unsigned ptag = (unsigned)rfl((int)gp) + 1u;
  asm volatile("" :: "v"(gvw), "v"(gvh));
  const bool zq_l = lenraw <= 0;
  const int len_l = zq_l ? LK : min(lenraw, LK);
  int mn = len_l;
  mn = min(mn, dppi_keep<0x128>(mn, mn)); mn = min(mn, dppi_keep<0x124>(mn, mn));
  mn = min(mn, dppi_keep<0x122>(mn, mn)); mn = min(mn, dppi_keep<0x121>(mn, mn));
  const int P = min(pm, rfl(mn) >> 4);
  const int tsh = P * BS;
  const int vL = ((len_l + BS - 1) >> 4) - P;
  int inc = vL;
  inc += dppi<0x111>(inc); inc += dppi<0x112>(inc); inc += dppi<0x114>(inc); inc += dppi<0x118>(inc);
  const int N = rl(inc, 15);
  const int vS = inc - vL;
  int kH = 0;
  if (P > 0) kH = min(15, max(1, rfl((int)(((unsigned)P * 16u * 64u + (unsigned)(P + N) * HRHO - 1u) / ((unsigned)(P + N) * HRHO)))));
  kH = min(kH, max(P, 0));
  const int nW = 64 - 4 * kH;
  const unsigned zmask = (unsigned)(__ballot(zq_l) & 0xffffull);
  const bool isH = jj < 4 * kH;
  const float t_all = (float)(P + N) * (16.f * 819.2f / (float)PACE);
  const unsigned long long t_ref = __builtin_amdgcn_s_memrealtime();
  constexpr int A_S0 = 3, A_S1 = 11;
  const int m_avg = (N + nW - 1) / nW;
  const bool adapt_on = nW >= 24 && m_avg >= A_S1 + 8;
  auto read_progress = [&](unsigned ldsoff, float& t1, float& d) -> bool {
    const u32x4 e = *(LAS u32x4*)(lds + ldsoff + c16 * 16);
    const bool okv = e[1] == ptag && e[3] == ptag;
    const int cnt = __builtin_popcount((unsigned)__ballot(okv) & 0xffffu);
    float a = okv ? (float)(int)(e[0] - (unsigned)t_ref) : 0.f, c = okv ? (float)e[2] : 0.f;
    a += __builtin_bit_cast(float, dppi_keep<0x128>(0, __builtin_bit_cast(int, a))); c += __builtin_bit_cast(float, dppi_keep<0x128>(0, __builtin_bit_cast(int, c)));
    a += __builtin_bit_cast(float, dppi_keep<0x124>(0, __builtin_bit_cast(int, a))); c += __builtin_bit_cast(float, dppi_keep<0x124>(0, __builtin_bit_cast(int, c)));
    a += __builtin_bit_cast(float, dppi_keep<0x122>(0, __builtin_bit_cast(int, a))); c += __builtin_bit_cast(float, dppi_keep<0x122>(0, __builtin_bit_cast(int, c)));
    a += __builtin_bit_cast(float, dppi_keep<0x121>(0, __builtin_bit_cast(int, a))); c += __builtin_bit_cast(float, dppi_keep<0x121>(0, __builtin_bit_cast(int, c)));
    if (cnt < 8) return false;
    const float ic = 1.f / (float)cnt;
    t1 = rlf(a, 0) * ic; d = rlf(c, 0) * ic * (1.f / (A_S1 - A_S0));
    return true;
  };
  const int r_lo = (int)((unsigned)(lane * N) / (unsigned)nW), r_hi = (int)((unsigned)((lane + 1) * N) / (unsigned)nW);

  if (isH) {
    const int h = hg * 4 + (jj & 3), part = jj >> 2;
    const int p0 = (P * part) / kH, p1 = (P * (part + 1)) / kH;
#pragma unroll
    for (int i = 0; i < 2; ++i)
      dma16(p.q + (2 * w + i) * 16384 + h * 512, (lane >> 5) * 8192 + (lane & 31) * 16, LDS_QB + (2 * w + i) * 1024);
    unsigned oK[4], oV[4];
#pragma unroll
    for (int i = 0; i < 4; ++i) {
      const int t = 4 * i + g4;
      oK[i] = t * 4096 + ((c16 ^ t) << 4) - i * 1024 + 4096;
      oV[i] = t * 4096 + ((c16 ^ (2 * (t & 7))) << 4) - i * 1024 + 4096;
    }
    const char* kb = p.k + h * 256 - 4096;
    const char* vb = p.v + h * 256 - 4096;
    const int nt = max(0, (p1 - p0 - w + 3) >> 2);
    const unsigned ring = w * (RH * 8192);
    int pg0 = 0, vpg = *(LAS int*)(lds + LDS_BT + min(p0 + w + 4 * lane, NBLK - 1) * 4);
    float pace_h = t_all * (PACE_H * 0.01f) / (float)max(nt, 1), pace_hb = -3.f * pace_h;
    float t_pub = t_all * (HPUB_AT * 0.01f);
    auto issue = [&](int t) {
      { const int due = (int)(pace_hb + (float)t * pace_h); while ((int)(__builtin_amdgcn_s_memrealtime() - t_ref) < due) __builtin_amdgcn_s_sleep(1); }
      if (t - pg0 == 64) { pg0 = t; vpg = *(LAS int*)(lds + LDS_BT + min(p0 + w + 4 * (t + lane), NBLK - 1) * 4); }
      const size_t pb = (size_t)(unsigned)rl(vpg, t - pg0) * 65536;
      const unsigned dst = ring + (t & (RH - 1)) * 8192;
      dma_stage(kb + pb, vb + pb, dst, dst + 4096, oK[0], oK[1], oK[2], oK[3], oV[0], oV[1], oV[2], oV[3]);
    };
    const int npre = min(nt, RH - 1);
#pragma unroll 1
    for (int t = 0; t < npre; ++t) issue(t);
    if (npre == 3) asm volatile("s_waitcnt vmcnt(24)\n\ts_barrier" ::: "memory");
    else asm volatile("s_waitcnt vmcnt(0)\n\ts_barrier" ::: "memory");
    bf16x8 qf[2][4];
#pragma unroll
    for (int qb = 0; qb < 2; ++qb) {
      const bool z = (zmask >> (8 * qb + (c16 >> 1))) & 1;
#pragma unroll
      for (int s = 0; s < 4; ++s) {
        u32x4 x = *(LAS u32x4*)(lds + LDS_QB + (16 * qb + c16) * 256 + (4 * s + g4) * 16);
        if (z) x = u32x4{0u, 0u, 0u, 0u};
        qf[qb][s] = __builtin_bit_cast(bf16x8, x);
      }
    }
    f32x4 acc[2][8];
    float m[2] = {-INFINITY, -INFINITY}, l[2] = {0.f, 0.f};
#pragma unroll
    for (int qb = 0; qb < 2; ++qb)
#pragma unroll
      for (int k = 0; k < 8; ++k) acc[qb][k] = f32x4{0.f, 0.f, 0.f, 0.f};
    const unsigned ka0 = ring + c16 * 256 + ((g4 ^ c16) << 4);
    unsigned va0;
    { const int tq = 4 * g4 + (c16 >> 2), pp = lane & 3; va0 = ring + 4096 + tq * 256 + ((tq & 7) << 5) + ((pp >> 1) << 4) + ((pp & 1) << 3); }
#pragma unroll 1
    for (int t = 0; t < nt; ++t) {
      const int young = min(nt - 1 - t, RH - 2);
      if (young == 2) asm volatile("s_waitcnt vmcnt(16) lgkmcnt(0)" ::: "memory");
      else if (young == 1) asm volatile("s_waitcnt vmcnt(8) lgkmcnt(0)" ::: "memory");
      else asm volatile("s_waitcnt vmcnt(0) lgkmcnt(0)" ::: "memory");
      if (adapt_on && nt >= 12) {
        const int tp = (nt * 9) >> 4;
        if (t == tp + 3) {
          float t1, d;
          if (read_progress(LDS_PROG + w * 256, t1, d)) {
            const float t_end = t1 + (float)(m_avg - A_S1) * d * (A_F * 0.001f);
            const float now = (float)(int)(__builtin_amdgcn_s_memrealtime() - t_ref);
            const int t_i = t + RH - 1;
            pace_h = fmaxf((t_end - A_HMARGIN - now) / (float)max(nt - t_i, 1), 0.f); pace_hb = now - (float)t_i * pace_h;
            t_pub = t_end - A_HPUB;
          }
        }
      }
      if (t + RH - 1 < nt) issue(t + RH - 1);
      if (adapt_on && nt >= 12 && t == ((nt * 9) >> 4) && lane < 16) dma16_sc1(p.ws + WS_PROG, lane * 16, LDS_PROG + w * 256);
      const unsigned sb = (t & (RH - 1)) * 8192;
      tile_compute<2>(lds, ka0 + sb, va0 + sb, qf, acc, m, l, 0, 16, g4);
    }
    STAMP(2);
    constexpr unsigned ROWB = 528, WAVEB = 32 * ROWB, MLB = 4 * WAVEB;
    asm volatile("s_waitcnt lgkmcnt(0)\n\ts_barrier" ::: "memory");
#pragma unroll
    for (int qb = 0; qb < 2; ++qb) {
      const float lt = xg_sum(l[qb]);
#pragma unroll
      for (int k = 0; k < 8; ++k) *(LAS f32x4*)(lds + w * WAVEB + (16 * qb + c16) * ROWB + (16 * k + 4 * g4) * 4) = acc[qb][k];
      if (g4 == 0) *(LAS f32x2*)(lds + MLB + (w * 32 + 16 * qb + c16) * 8) = f32x2{m[qb] * C2, lt};
    }
    asm volatile("s_waitcnt lgkmcnt(0)\n\ts_barrier" ::: "memory");
    while ((int)(__builtin_amdgcn_s_memrealtime() - t_ref) < (int)t_pub) __builtin_amdgcn_s_sleep(8);
    const unsigned slot = WS_HP + (part * 16 + h) * HP_W;
    float mrow = 0.f, lrow = 0.f;
#pragma unroll
    for (int rr = 0; rr < 8; ++rr) {
      const int row = 8 * w + rr;
      f32x2 ml[4], o[4];
#pragma unroll
      for (int v = 0; v < 4; ++v) { ml[v] = *(LAS f32x2*)(lds + MLB + (v * 32 + row) * 8); o[v] = *(LAS f32x2*)(lds + v * WAVEB + row * ROWB + lane * 8); }
      const float M = fmaxf(fmaxf(ml[0][0], ml[1][0]), fmaxf(ml[2][0], ml[3][0]));
      float L = 0.f, o0 = 0.f, o1 = 0.f;
#pragma unroll
      for (int v = 0; v < 4; ++v) {
        const float e = ml[v][1] > 0.f ? __builtin_amdgcn_exp2f(ml[v][0] - M) : 0.f;
        L = fmaf(ml[v][1], e, L); o0 = fmaf(o[v][0], e, o0); o1 = fmaf(o[v][1], e, o1);
      }
      const unsigned tag = (unsigned)rl((int)gvh, row >> 1) + 1u;
      const u32x4 gr = {__float_as_uint(o0), tag, __float_as_uint(o1), tag};
      __builtin_amdgcn_raw_buffer_store_b128(gr, wsr, slot + row * 1024 + lane * 16, 0, 16);
      if (lane == rr) { mrow = M; lrow = L; }
    }
    const unsigned tagr = (unsigned)__shfl((int)gvh, ((8 * w + lane) >> 1) & 15) + 1u;
    if (lane < 8) {
      const unsigned tag = tagr;
      const u32x4 gm = {__float_as_uint(mrow), tag, __float_as_uint(lrow), tag};
      __builtin_amdgcn_raw_buffer_store_b128(gm, wsr, slot + 32768 + (8 * w + lane) * 16, 0, 16);
    }
    STAMP(1);
    STAMP(3);
    STAMP_FLUSH();
    return;
  }

  const int j = jj - 4 * kH, h = hg * 4 + w;
  const int x0 = rl(r_lo, j), x1 = rl(r_hi, j);
  const unsigned umask = (unsigned)(__ballot(max(x0, vS) < min(x1, vS + vL)) & 0xffffull);
  unsigned voffK[4], voffV[4];
#pragma unroll
  for (int i = 0; i < 4; ++i) {
    const int t = 4 * w + i;
    voffK[i] = g4 * 256 + ((c16 ^ t) << 4) + t * 4096 - i * 1024 + 4096;
    voffV[i] = g4 * 256 + ((c16 ^ (2 * (t & 7))) << 4) + t * 4096 - i * 1024 + 4096;
  }
  const unsigned kaddr0 = w * 256 + c16 * 1024 + ((g4 ^ c16) << 4);
  unsigned vaddr0;
  { const int tq = 4 * g4 + (c16 >> 2), pp = lane & 3; vaddr0 = 16384 + tq * 1024 + w * 256 + ((tq & 7) << 5) + ((pp >> 1) << 4) + ((pp & 1) << 3); }
  const char* kbase = p.k + hg * 1024 - 4096;
  const char* vbase = p.v + hg * 1024 - 4096;

  unsigned i_mask = umask; int i_row = 0, i_t0 = 0, i_t1 = 0, i_page; bool i_more = false;
  int n_iss = 0, i_slot = 0;
  auto unit_range = [&](int b, int& t0, int& t1) {
    const int s = rl(vS, b), L = rl(vL, b);
    t0 = (P + max(x0, s) - s) * BS; t1 = min((P + min(x1, s + L) - s) * BS, rl(len_l, b));
  };
  auto q_dma = [&](int b, int slot) {
    if (lane < 32) dma16(p.q + b * 8192 + h * 512, lane * 16, LDS_QB + (w * R + slot) * 512);
  };
  if (i_mask) {
    i_row = __builtin_ctz(i_mask); i_mask &= i_mask - 1; unit_range(i_row, i_t0, i_t1);
    q_dma(i_row, 0);
    i_more = true;
  }
  i_page = i_t0 >> 4;
  int i_pg0 = i_page, vpg = *(LAS int*)(lds + LDS_BT + (i_row * NBLK + min(i_page + lane, NBLK - 1)) * 4);
  int pace_tok = 0;
  float pace_d = t_all / (float)max(x1 - x0, 1), pace_b = -(float)PACE_LEAD * pace_d;
  const int rep = (adapt_on && hg < 2 && j >= (nW >> 1) && j < (nW >> 1) + 8) ? hg * 8 + j - (nW >> 1) : -1;
  unsigned t_s0 = 0;
  auto issue_stage = [&]() {
    const int phys = rl(vpg, i_page - i_pg0);
    const int hi = i_t1 - i_page * 16;
    {
      if (adapt_on && pace_tok == A_S1 + 3 + R - 1) {
        float t1, d;
        if (read_progress(LDS_PROG, t1, d)) { pace_d = d * (A_F * 0.001f); pace_b = t1 - (float)A_S1 * pace_d - A_LEAD; }
      }
      const int due = (int)(pace_b + (float)pace_tok * pace_d);
      pace_tok += 1;
      while ((int)(__builtin_amdgcn_s_memrealtime() - t_ref) < due) __builtin_amdgcn_s_sleep(1);
    }
    const size_t pb = (size_t)(unsigned)phys * 65536;
    const unsigned dst = i_slot * STAGE + 4 * w * 1024;
    if (hi >= 16) {
      dma_stage(kbase + pb, vbase + pb, dst, dst + 16384, voffK[0], voffK[1], voffK[2], voffK[3], voffV[0], voffV[1], voffV[2], voffV[3]);
    } else {
      unsigned ok[4], ov[4];
#pragma unroll
      for (int i = 0; i < 4; ++i) {
        const int d = (min(4 * w + i, hi - 1) - (4 * w + i)) * 4096;
        ok[i] = voffK[i] + d; ov[i] = voffV[i] + d;
      }
      dma_stage(kbase + pb, vbase + pb, dst, dst + 16384, ok[0], ok[1], ok[2], ok[3], ov[0], ov[1], ov[2], ov[3]);
    }
    if (adapt_on) {
      const int k = pace_tok - 1;
      if (rep >= 0 && w == 3) {
        if (k == A_S0) t_s0 = (unsigned)__builtin_amdgcn_s_memrealtime();
        if (k == A_S1 && lane == 0) {
          const unsigned now = (unsigned)__builtin_amdgcn_s_memrealtime();
          const u32x4 e = {now, ptag, now - t_s0, ptag};
          __builtin_amdgcn_raw_buffer_store_b128(e, wsr, WS_PROG + rep * 16, 0, 16);
        }
      }
      if (k == A_S1 + 3 && w == 0 && lane < 16) dma16_sc1(p.ws + WS_PROG, lane * 16, LDS_PROG);
    }
    ++n_iss; i_slot = (i_slot + 1 == R) ? 0 : i_slot + 1;
    ++i_page;
    if (i_page * 16 >= i_t1) {
      if (i_mask) {
        i_row = __builtin_ctz(i_mask); i_mask &= i_mask - 1; unit_range(i_row, i_t0, i_t1); i_page = i_t0 >> 4;
        q_dma(i_row, i_slot);
        i_pg0 = i_page; vpg = *(LAS int*)(lds + LDS_BT + (i_row * NBLK + min(i_page + lane, NBLK - 1)) * 4);
      } else i_more = false;
    } else if (i_page - i_pg0 == 64) {
      i_pg0 = i_page; vpg = *(LAS int*)(lds + LDS_BT + (i_row * NBLK + min(i_page + lane, NBLK - 1)) * 4);
    }
  };
#pragma unroll 1
  for (int s = 0; s < R - 1; ++s) if (i_more) issue_stage();

  unsigned own = 0;
  {
    unsigned mrem = umask;
    while (mrem) {
      const int b = __builtin_ctz(mrem); mrem &= mrem - 1;
      const int s = rl(vS, b), L = rl(vL, b);
      unsigned long long pmk = __ballot(lane < nW && max(r_lo, s) < min(r_hi, s + L));
      int idx = w % __builtin_popcountll(pmk);
      while (idx--) pmk &= pmk - 1;
      if (__builtin_ctzll(pmk) == j) own |= 1u << b;
    }
    unsigned zl = (unsigned)(__ballot(vL == 0) & 0xffffull);
    while (zl) { const int b = __builtin_ctz(zl); zl &= zl - 1; if ((b * 4) % nW == j) own |= 1u << b; }
  }

  int fb = -1, fcnt = 0; unsigned vrow = 0, vmlo = 0;
  if (own && (own & (own - 1)) == 0) {
    const int b = __builtin_ctz(own);
    const unsigned above = umask >> b;
    if ((umask >> b & 1) && (above == 1 || __builtin_popcount(above) == 2)) {
      const int s = rl(vS, b), L = rl(vL, b);
      const unsigned long long pmk = __ballot(lane < nW && max(r_lo, s) < min(r_hi, s + L)) & ~(1ull << j);
      const int np = __builtin_popcountll(pmk);
      if (np + kH <= 8 && np + kH > 0) {
        fb = b; fcnt = np + kH;
        const int rank = __builtin_amdgcn_mbcnt_hi((unsigned)(pmk >> 32), __builtin_amdgcn_mbcnt_lo((unsigned)pmk, 0));
        const unsigned prow = WS_PP + ((hg * 80 + lane + b) * 4 + w) * PP_W;
        const unsigned pushed = (unsigned)__builtin_amdgcn_ds_permute((((pmk >> lane) & 1) ? rank : 63) << 2, (int)prow);
        const unsigned hb = WS_HP + (min(max(lane - np, 0), 15) * 16 + h) * HP_W;
        vrow = lane < np ? pushed : hb + b * 2048;
        const unsigned mlb = lane < np ? pushed + 2048 : hb + 32768 + b * 32;
        vmlo = (unsigned)__shfl((int)mlb, min(lane >> 1, fcnt - 1)) + (lane & 1) * 16;
      }
    }
  }

  int n_comp = 0, c_slot = 0;
  unsigned c_mask = umask;
  float ro[2][2] = {{0.f, 0.f}, {0.f, 0.f}}, rm[2] = {-INFINITY, -INFINITY}, rs[2] = {0.f, 0.f};
  float ho[2][2] = {{0.f, 0.f}, {0.f, 0.f}}, hm[2] = {-INFINITY, -INFINITY}, hs[2] = {0.f, 0.f};
  int last_b = -1, held_b = -1;
  auto publish = [&](int b, const float (&o)[2][2], const float (&mm)[2], const float (&ll)[2]) {
    const unsigned tag = (unsigned)rl((int)gvw, b) + 1u;
    const unsigned slot = WS_PP + ((hg * 80 + j + b) * 4 + w) * PP_W;
    const u32x4 g0 = {__float_as_uint(o[0][0]), tag, __float_as_uint(o[0][1]), tag};
    const u32x4 g1 = {__float_as_uint(o[1][0]), tag, __float_as_uint(o[1][1]), tag};
    __builtin_amdgcn_raw_buffer_store_b128(g0, wsr, slot + lane * 16, 0, 16);
    __builtin_amdgcn_raw_buffer_store_b128(g1, wsr, slot + 1024 + lane * 16, 0, 16);
    if (lane < 2) {
      const u32x4 gm = {__float_as_uint(lane ? mm[1] : mm[0]), tag, __float_as_uint(lane ? ll[1] : ll[0]), tag};
      __builtin_amdgcn_raw_buffer_store_b128(gm, wsr, slot + 2048 + lane * 16, 0, 16);
    }
  };
  while (c_mask) {
    const int b = __builtin_ctz(c_mask); c_mask &= c_mask - 1;
    int t0, t1; unit_range(b, t0, t1);
    bf16x8 qf[1][4];
    f32x4 acc[1][8];
    float m[1] = {-INFINITY}, l[1] = {0.f};
#pragma unroll
    for (int k = 0; k < 8; ++k) acc[0][k] = f32x4{0.f, 0.f, 0.f, 0.f};
    bool first = true;
    for (int page = t0 >> 4; page * 16 < t1; ++page) {
      {
        const int young = n_iss - n_comp - 1;
        if (R == 4 && young >= 2) asm volatile("s_waitcnt vmcnt(16) lgkmcnt(0)\n\ts_barrier" ::: "memory");
        else if (young >= 1) asm volatile("s_waitcnt vmcnt(8) lgkmcnt(0)\n\ts_barrier" ::: "memory");
        else asm volatile("s_waitcnt vmcnt(0) lgkmcnt(0)\n\ts_barrier" ::: "memory");
      }
      if (first) {
        const bool z = (c16 >= 2) || ((zmask >> b) & 1);
#pragma unroll
        for (int s = 0; s < 4; ++s) {
          u32x4 x = *(LAS u32x4*)(lds + LDS_QB + (w * R + c_slot) * 512 + (c16 & 1) * 256 + (4 * s + g4) * 16);
          if (z) x = u32x4{0u, 0u, 0u, 0u};
          qf[0][s] = __builtin_bit_cast(bf16x8, x);
        }
        asm volatile("s_waitcnt lgkmcnt(0)" ::: "memory");
        first = false;
      }
      if (i_more) issue_stage();
      const unsigned sb = c_slot * STAGE;
      tile_compute<1>(lds, kaddr0 + sb, vaddr0 + sb, qf, acc, m, l, 0, t1 - page * 16, g4);
      ++n_comp; c_slot = (c_slot + 1 == R) ? 0 : c_slot + 1;
    }
    const float lt = xg_sum(l[0]);
    LAS char* sc = lds + LDS_SC + w * 1024;
    if (c16 < 2) {
#pragma unroll
      for (int k = 0; k < 8; ++k) *(LAS f32x4*)(sc + c16 * 512 + (16 * k + 4 * g4) * 4) = acc[0][k];
    }
    asm volatile("s_waitcnt lgkmcnt(0)" ::: "memory");
    const f32x2 a0 = *(LAS f32x2*)(sc + lane * 8), a1 = *(LAS f32x2*)(sc + 512 + lane * 8);
    if (last_b >= 0) {
      if (held_b >= 0) publish(held_b, ho, hm, hs);
      held_b = last_b;
#pragma unroll
      for (int r = 0; r < 2; ++r) { ho[r][0] = ro[r][0]; ho[r][1] = ro[r][1]; hm[r] = rm[r]; hs[r] = rs[r]; }
    }
    ro[0][0] = a0[0]; ro[0][1] = a0[1]; ro[1][0] = a1[0]; ro[1][1] = a1[1];
    rm[0] = rlf(m[0], 0) * C2; rm[1] = rlf(m[0], 1) * C2;
    rs[0] = rlf(lt, 0); rs[1] = rlf(lt, 1);
    last_b = b;
  }
  STAMP(2);
  if (last_b >= 0 && !((own >> last_b) & 1)) { publish(last_b, ro, rm, rs); last_b = -1; }
  if (held_b >= 0 && !((own >> held_b) & 1)) { publish(held_b, ho, hm, hs); held_b = -1; }
  STAMP(1);

  if (own) { const unsigned long long c0 = __builtin_amdgcn_s_memrealtime(); while (__builtin_amdgcn_s_memrealtime() - c0 < MDELAY) __builtin_amdgcn_s_sleep(1); }
  if (fb >= 0) {
    const int b = fb;
    const unsigned tag = (unsigned)rl((int)gvw, b) + 1u;
    const bool mine_last = b == last_b;
    const float om0 = mine_last ? rm[0] : hm[0], om1 = mine_last ? rm[1] : hm[1], ol0 = mine_last ? rs[0] : hs[0], ol1 = mine_last ? rs[1] : hs[1];
    const float oo00 = mine_last ? ro[0][0] : ho[0][0], oo01 = mine_last ? ro[0][1] : ho[0][1];
    const float oo10 = mine_last ? ro[1][0] : ho[1][0], oo11 = mine_last ? ro[1][1] : ho[1][1];
    auto attempt = [&](auto nb_tag) -> bool {
      constexpr int NB = decltype(nb_tag)::value;
      u32x4 d0[NB], d1[NB];
      unsigned so[NB];
#pragma unroll
      for (int k = 0; k < NB; ++k) so[k] = (unsigned)rl((int)vrow, min(k, fcnt - 1));
      asm volatile("" ::: "memory");
      const u32x4 dm = __builtin_amdgcn_raw_buffer_load_b128(wsr, vmlo, 0, 16);
#pragma unroll
      for (int k = 0; k < NB; ++k) {
        d0[k] = __builtin_amdgcn_raw_buffer_load_b128(wsr, lane * 16, so[k], 16);
        d1[k] = __builtin_amdgcn_raw_buffer_load_b128(wsr, 1024 + lane * 16, so[k], 16);
      }
      __builtin_amdgcn_sched_barrier(0);
      const bool live = lane < 2 * fcnt;
      const float mk = live ? __uint_as_float(dm[0]) : -INFINITY, lk = live ? __uint_as_float(dm[2]) : 0.f;
      const float om = (lane & 1) ? om1 : om0, ol = (lane & 1) ? ol1 : ol0;
      float M = fmaxf(mk, om);
      M = fmaxf(M, __builtin_bit_cast(float, dppi_keep<0x122>(__builtin_bit_cast(int, M), __builtin_bit_cast(int, M))));
      M = fmaxf(M, __builtin_bit_cast(float, dppi_keep<0x124>(__builtin_bit_cast(int, M), __builtin_bit_cast(int, M))));
      M = fmaxf(M, __builtin_bit_cast(float, dppi_keep<0x128>(__builtin_bit_cast(int, M), __builtin_bit_cast(int, M))));
      const float e = __builtin_amdgcn_exp2f(mk - M), eo = __builtin_amdgcn_exp2f(om - M);
      float Ls = lk * e;
      Ls += __builtin_bit_cast(float, dppi_keep<0x122>(0, __builtin_bit_cast(int, Ls)));
      Ls += __builtin_bit_cast(float, dppi_keep<0x124>(0, __builtin_bit_cast(int, Ls)));
      Ls += __builtin_bit_cast(float, dppi_keep<0x128>(0, __builtin_bit_cast(int, Ls)));
      Ls = fmaf(ol, eo, Ls);
      const float inv = __builtin_amdgcn_rcpf(Ls);
      const float en = e * inv, eon = eo * inv;
      const float eo0 = rlf(eon, 0), eo1 = rlf(eon, 1);
      float o00 = oo00 * eo0, o01 = oo01 * eo0, o10 = oo10 * eo1, o11 = oo11 * eo1;
      unsigned bad = (dm[1] ^ tag) | (dm[3] ^ tag);
#pragma unroll
      for (int k = 0; k < NB; ++k) {
        const float w0 = rlf(en, 2 * k), w1 = rlf(en, 2 * k + 1);
        bad |= ((d0[k][1] ^ tag) | (d0[k][3] ^ tag)) | ((d1[k][1] ^ tag) | (d1[k][3] ^ tag));
        o00 = fmaf(__uint_as_float(d0[k][0]), w0, o00); o01 = fmaf(__uint_as_float(d0[k][2]), w0, o01);
        o10 = fmaf(__uint_as_float(d1[k][0]), w1, o10); o11 = fmaf(__uint_as_float(d1[k][2]), w1, o11);
      }
      if (__any(bad != 0)) return false;
      __hip_bfloat16* orow = p.out + (b * NH + 2 * h) * DH + 2 * lane;
      *(unsigned*)orow = pk_bf16(o00, o01);
      *(unsigned*)(orow + DH) = pk_bf16(o10, o11);
      if (lane == 0) __builtin_amdgcn_raw_buffer_store_b32(tag, wsr, WS_G + (h * 16 + b) * 4, 0, GAUX);
      return true;
    };
#pragma unroll 1
    for (int t = 0; t < 16; ++t) {
      if (t >= FASTTRY) {
        unsigned spins = 0;
        while (true) {
          asm volatile("" ::: "memory");
          const u32x4 x = __builtin_amdgcn_raw_buffer_load_b128(wsr, vmlo, 0, 16);
          if (!__any(x[1] != tag || x[3] != tag) || ++spins > (1u << 16)) break;
          __builtin_amdgcn_s_sleep(SLEEP2);
        }
      }
      const bool ok = fcnt <= 5 ? attempt(IntC<5>{}) : attempt(IntC<8>{});
      if (ok) { own = 0; break; }
    }
  }
  while (own) {
    const int b = __builtin_ctz(own); own &= own - 1;
    const unsigned tag = (unsigned)rl((int)gvw, b) + 1u;
    const int s = rl(vS, b), L = rl(vL, b);
    unsigned long long pmk = __ballot(lane < nW && max(r_lo, s) < min(r_hi, s + L));
    unsigned smk = (1u << kH) - 1u;
    float M0 = -INFINITY, M1 = -INFINITY, L0 = 0.f, L1 = 0.f, o00 = 0.f, o01 = 0.f, o10 = 0.f, o11 = 0.f;
    if (b == last_b) {
      pmk &= ~(1ull << j);
      M0 = rm[0]; M1 = rm[1]; L0 = rs[0]; L1 = rs[1]; o00 = ro[0][0]; o01 = ro[0][1]; o10 = ro[1][0]; o11 = ro[1][1];
    } else if (b == held_b) {
      pmk &= ~(1ull << j);
      M0 = hm[0]; M1 = hm[1]; L0 = hs[0]; L1 = hs[1]; o00 = ho[0][0]; o01 = ho[0][1]; o10 = ho[1][0]; o11 = ho[1][1];
    }
    const unsigned p_rows = WS_PP + ((hg * 80 + lane + b) * 4 + w) * PP_W;
    const unsigned s_base = WS_HP + ((lane & 15) * 16 + h) * HP_W;
    unsigned spins = 0;
    bool light = false;
    while (pmk | smk) {
      asm volatile("" ::: "memory");
      unsigned long long tp = pmk; unsigned ts = smk;
      const bool single = (__builtin_popcountll(pmk) + __builtin_popcount(smk)) == 1;
      if (light && !single) {
        const int jp = pmk ? (((pmk >> lane) & 1) ? lane : __builtin_ctzll(pmk)) : 0;
        const int js = smk ? (((smk >> (lane & 15)) & 1) ? (lane & 15) : __builtin_ctz(smk)) : 0;
        u32x4 a = {0u, tag, 0u, tag}, c = {0u, tag, 0u, tag};
        if (pmk) a = __builtin_amdgcn_raw_buffer_load_b128(wsr, WS_PP + ((hg * 80 + jp + b) * 4 + w) * PP_W + 2048 + 16, 0, 16);
        if (smk) c = __builtin_amdgcn_raw_buffer_load_b128(wsr, WS_HP + (js * 16 + h) * HP_W + 32768 + (2 * b + 1) * 16, 0, 16);
        tp &= __ballot(a[1] == tag && a[3] == tag);
        ts &= (unsigned)__ballot(c[1] == tag && c[3] == tag);
      }
      bool progress = false;
      if (tp | ts) {
        auto batch = [&](auto nb_tag) {
          constexpr int NB = decltype(nb_tag)::value;
          unsigned rows[NB], mls[NB]; unsigned long long bitp[NB]; unsigned bits[NB];
#pragma unroll
          for (int k = 0; k < NB; ++k) {
            if (tp) { const int q = __builtin_ctzll(tp); bitp[k] = tp & (~tp + 1); bits[k] = 0; tp &= tp - 1; rows[k] = (unsigned)rl((int)p_rows, q); mls[k] = rows[k] + 2048; }
            else if (ts) { const int q = __builtin_ctz(ts); bits[k] = ts & (~ts + 1); bitp[k] = 0; ts &= ts - 1;
                           const unsigned sl = (unsigned)rl((int)s_base, q); rows[k] = sl + b * 2048; mls[k] = sl + 32768 + b * 32; }
            else { bitp[k] = 0; bits[k] = 0; rows[k] = rows[0]; mls[k] = mls[0]; }
          }
          u32x4 d0[NB], d1[NB], dm[NB];
          if (R0AUX != 16 && spins == 0) {
#pragma unroll
            for (int k = 0; k < NB; ++k) {
              d0[k] = __builtin_amdgcn_raw_buffer_load_b128(wsr, lane * 16, rows[k], R0AUX);
              d1[k] = __builtin_amdgcn_raw_buffer_load_b128(wsr, 1024 + lane * 16, rows[k], R0AUX);
              dm[k] = __builtin_amdgcn_raw_buffer_load_b128(wsr, (lane & 1) * 16, mls[k], R0AUX);
            }
          } else {
#pragma unroll
            for (int k = 0; k < NB; ++k) {
              d0[k] = __builtin_amdgcn_raw_buffer_load_b128(wsr, lane * 16, rows[k], 16);
              d1[k] = __builtin_amdgcn_raw_buffer_load_b128(wsr, 1024 + lane * 16, rows[k], 16);
              dm[k] = __builtin_amdgcn_raw_buffer_load_b128(wsr, (lane & 1) * 16, mls[k], 16);
            }
          }
#pragma unroll
          for (int k = 0; k < NB; ++k) {
            const bool ok = d0[k][1] == tag && d0[k][3] == tag && d1[k][1] == tag && d1[k][3] == tag && dm[k][1] == tag && dm[k][3] == tag;
            if ((bitp[k] | bits[k]) && __all(ok)) {
              const float mv = __uint_as_float(dm[k][0]), lv = __uint_as_float(dm[k][2]);
              const float m0 = rlf(mv, 0), m1 = rlf(mv, 1), l0 = rlf(lv, 0), l1 = rlf(lv, 1);
              const float n0 = fmaxf(M0, m0), n1 = fmaxf(M1, m1);
              const float a0 = __builtin_amdgcn_exp2f(M0 - n0), b0 = __builtin_amdgcn_exp2f(m0 - n0);
              const float a1 = __builtin_amdgcn_exp2f(M1 - n1), b1 = __builtin_amdgcn_exp2f(m1 - n1);
              L0 = L0 * a0 + l0 * b0; L1 = L1 * a1 + l1 * b1;
              o00 = o00 * a0 + __uint_as_float(d0[k][0]) * b0; o01 = o01 * a0 + __uint_as_float(d0[k][2]) * b0;
              o10 = o10 * a1 + __uint_as_float(d1[k][0]) * b1; o11 = o11 * a1 + __uint_as_float(d1[k][2]) * b1;
              M0 = n0; M1 = n1;
              pmk &= ~bitp[k]; smk &= ~bits[k];
              progress = true;
            }
          }
        };
        const int cnt = __builtin_popcountll(tp) + __builtin_popcount(ts);
        if (cnt == 1) batch(IntC<1>{}); else if (cnt <= 3) batch(IntC<3>{}); else batch(IntC<6>{});

      }
      if (!progress) {
        if (++spins > (1u << 16)) break;
        if (spins > FULLR) { light = true; __builtin_amdgcn_s_sleep(SLEEP); }
      }
    }
    const float i0 = 1.f / L0, i1 = 1.f / L1;
    __hip_bfloat16* orow = p.out + (b * NH + 2 * h) * DH + 2 * lane;
    *(unsigned*)orow = pk_bf16(o00 * i0, o01 * i0);
    *(unsigned*)(orow + DH) = pk_bf16(o10 * i1, o11 * i1);
    if (lane == 0) __builtin_amdgcn_raw_buffer_store_b32(tag, wsr, WS_G + (h * 16 + b) * 4, 0, GAUX);
  }
  STAMP(3);
  STAMP_FLUSH();
}

}

extern "C" void kernel_launch(void* const* d_in, const int* in_sizes, int n_in, void* d_out, int out_size, void* d_ws, size_t ws_size,
                              hipStream_t stream) {
  using namespace rattn;
  Params p;
  p.q = (const char*)d_in[0]; p.k = (const char*)d_in[1]; p.v = (const char*)d_in[2];
  p.bt = (const int*)d_in[3]; p.lens = (const int*)d_in[4];
  p.out = (__hip_bfloat16*)d_out; p.ws = (char*)d_ws;
  p.kvbytes = (unsigned)in_sizes[1] * 2u; p.unused = 0;
  (void)hipFuncSetAttribute((const void*)radix_attn_kernel, hipFuncAttributeMaxDynamicSharedMemorySize, LDS_TOTAL);
  hipLaunchKernelGGL(radix_attn_kernel, dim3(256), dim3(256), LDS_TOTAL, stream, p);
}
```

```cpp
#include <hip/hip_runtime.h>
#include <hip/hip_bf16.h>
#include <stdint.h>

namespace rattn {

constexpr int NSEQ = 16, NH = 32, NKV = 16, DH = 128, NBLK = 256, BS = 16;
constexpr int LK = NBLK * BS;
#define GAUX 0
#define KVW_LANES 3
#define KVW_SHIFT 21
#define HRHO 50
#define MDELAY 30
#define PACE 7300
#define ADAPT 1
#define A_F 1008
#define FASTTRY 2
#define PROBE_WG 200
#define PROBE_WAVE 0
#define PROBE_K 1
#define PROBE_MUL 20
#define PACE_LEAD 2
#define PACE_H 88
#define FULLR 0
#define R0AUX 16
#define HPUB_AT 85
#define A_LEAD 0.f
#define A_HMARGIN 350.f
#define A_HPUB 150.f
#define SLEEP2 2
#define MAPMODE 1
#define SLEEP 6
#define KVPOL "nt"
#define RING 3
constexpr int R = RING;
constexpr int STAGE = 32768;
constexpr int RH = 4;
constexpr int RING_BYTES = 4 * RH * 8192;
constexpr int LDS_BT = RING_BYTES;
constexpr int LDS_QB = LDS_BT + NSEQ * NBLK * 4;
constexpr int LDS_SC = LDS_QB + 4 * R * 512;
constexpr int LDS_MISC = LDS_SC + 4096;
constexpr int LDS_PROG = LDS_MISC + 64;
constexpr int LDS_JUNK = LDS_PROG;
constexpr int LDS_TOTAL_JUNK = LDS_PROG + 1024;
constexpr int LDS_TOTAL = LDS_TOTAL_JUNK + 1024;
static_assert(LDS_TOTAL <= 160 * 1024 && LDS_MISC - LDS_QB >= 8192 && RING_BYTES >= R * STAGE, "LDS");

constexpr unsigned WS_G = 0;
constexpr unsigned WS_PROG = 2048;
constexpr unsigned WS_PP = 4096;
constexpr unsigned PP_W = 2176;
constexpr unsigned WS_HP = WS_PP + 320u * 4u * PP_W + 4096u;
constexpr unsigned HP_W = 32768 + 512;
constexpr unsigned WS_END = WS_HP + 16u * 16u * HP_W;
constexpr unsigned WS_STAMP = 64u << 20;

typedef unsigned u32x4 __attribute__((ext_vector_type(4)));
typedef unsigned u32x2 __attribute__((ext_vector_type(2)));
typedef float f32x4 __attribute__((ext_vector_type(4)));
typedef float f32x2 __attribute__((ext_vector_type(2)));
typedef __bf16 bf16x8 __attribute__((ext_vector_type(8)));
typedef __bf16 bf16x2 __attribute__((ext_vector_type(2)));
typedef short s16x4 __attribute__((ext_vector_type(4)));
typedef int i32x4 __attribute__((ext_vector_type(4)));
#define LAS __attribute__((address_space(3)))

constexpr float C2 = 0.088388347648318440f * 1.4426950408889634f;

__device__ __forceinline__ int rfl(int x) { return __builtin_amdgcn_readfirstlane(x); }
__device__ __forceinline__ int rl(int x, int l) { return __builtin_amdgcn_readlane(x, l); }
__device__ __forceinline__ float rlf(float x, int l) { return __builtin_bit_cast(float, __builtin_amdgcn_readlane(__builtin_bit_cast(int, x), l)); }

__device__ __forceinline__ void dma16(const void* sbase, unsigned voff, unsigned dst) {
  unsigned keep;
  asm volatile("s_mov_b32 %0, m0\n\ts_mov_b32 m0, %3\n\ts_nop 0\n\tglobal_load_lds_dwordx4 %1, %2\n\ts_mov_b32 m0, %0"
               : "=&s"(keep) : "v"(voff), "s"(sbase), "s"(dst) : "memory");
}
__device__ __forceinline__ void dma16_sc1(const void* sbase, unsigned voff, unsigned dst) {
  unsigned keep;
  asm volatile("s_mov_b32 %0, m0\n\ts_mov_b32 m0, %3\n\ts_nop 0\n\tglobal_load_lds_dwordx4 %1, %2 sc1\n\ts_mov_b32 m0, %0"
               : "=&s"(keep) : "v"(voff), "s"(sbase), "s"(dst) : "memory");
}
__device__ __forceinline__ void dma_stage(const void* sK, const void* sV, unsigned dstK, unsigned dstV,
                                          unsigned k0, unsigned k1, unsigned k2, unsigned k3,
                                          unsigned v0, unsigned v1, unsigned v2, unsigned v3) {
  unsigned keep;
  asm volatile("s_mov_b32 %0, m0\n\ts_mov_b32 m0, %3\n\ts_nop 0\n\t"
               "global_load_lds_dwordx4 %5, %1 " KVPOL "\n\t"
               "global_load_lds_dwordx4 %6, %1 offset:1024 " KVPOL "\n\t"
               "global_load_lds_dwordx4 %7, %1 offset:2048 " KVPOL "\n\t"
               "global_load_lds_dwordx4 %8, %1 offset:3072 " KVPOL "\n\t"
               "s_mov_b32 m0, %4\n\ts_nop 0\n\t"
               "global_load_lds_dwordx4 %9, %2 " KVPOL "\n\t"
               "global_load_lds_dwordx4 %10, %2 offset:1024 " KVPOL "\n\t"
               "global_load_lds_dwordx4 %11, %2 offset:2048 " KVPOL "\n\t"
               "global_load_lds_dwordx4 %12, %2 offset:3072 " KVPOL "\n\t"
               "s_mov_b32 m0, %0"
               : "=&s"(keep)
               : "s"(sK), "s"(sV), "s"(dstK), "s"(dstV), "v"(k0), "v"(k1), "v"(k2), "v"(k3), "v"(v0), "v"(v1), "v"(v2), "v"(v3)
               : "memory");
}
template <int CTRL> __device__ __forceinline__ int dppi(int x) { return __builtin_amdgcn_update_dpp(0, x, CTRL, 0xf, 0xf, true); }
template <int CTRL> __device__ __forceinline__ int dppi_keep(int old, int x) { return __builtin_amdgcn_update_dpp(old, x, CTRL, 0xf, 0xf, false); }

__device__ __forceinline__ float xg_max(float x) {
  auto s = __builtin_amdgcn_permlane16_swap(__float_as_uint(x), __float_as_uint(x), false, false);
  x = fmaxf(__uint_as_float(s[0]), __uint_as_float(s[1]));
  auto t = __builtin_amdgcn_permlane32_swap(__float_as_uint(x), __float_as_uint(x), false, false);
  return fmaxf(__uint_as_float(t[0]), __uint_as_float(t[1]));
}
__device__ __forceinline__ float xg_sum(float x) {
  auto s = __builtin_amdgcn_permlane16_swap(__float_as_uint(x), __float_as_uint(x), false, false);
  x = __uint_as_float(s[0]) + __uint_as_float(s[1]);
  auto t = __builtin_amdgcn_permlane32_swap(__float_as_uint(x), __float_as_uint(x), false, false);
  return __uint_as_float(t[0]) + __uint_as_float(t[1]);
}
__device__ __forceinline__ unsigned pk_bf16(float a, float b) {
  bf16x2 v = {(__bf16)a, (__bf16)b};
  return __builtin_bit_cast(unsigned, v);
}

template <int NQ>
__device__ __forceinline__ void tile_compute(LAS char* lds, unsigned ka, unsigned va, const bf16x8 (&qf)[NQ][4], f32x4 (&acc)[NQ][8],
                                             float (&m)[NQ], float (&l)[NQ], int lo, int hi, int g4) {
  bf16x8 kf[4];
#pragma unroll
  for (int s = 0; s < 4; ++s) kf[s] = *(LAS bf16x8*)(lds + (ka ^ (s << 6)));
  s16x4 vf[8];
#pragma unroll
  for (int k = 0; k < 8; ++k) vf[k] = __builtin_amdgcn_ds_read_tr16_b64_v4i16((LAS s16x4*)(lds + (va ^ (k << 5))));
  f32x4 S[NQ];
#pragma unroll
  for (int qb = 0; qb < NQ; ++qb) {
    S[qb] = f32x4{0.f, 0.f, 0.f, 0.f};
#pragma unroll
    for (int s = 0; s < 4; ++s) S[qb] = __builtin_amdgcn_mfma_f32_16x16x32_bf16(kf[s], qf[qb][s], S[qb], 0, 0, 0);
  }
  if (lo > 0 || hi < 16) {
#pragma unroll
    for (int qb = 0; qb < NQ; ++qb)
#pragma unroll
      for (int r = 0; r < 4; ++r) { const int t = 4 * g4 + r; S[qb][r] = (t >= lo && t < hi) ? S[qb][r] : -INFINITY; }
  }
  s16x4 pb[NQ];
  bool grow = false;
  float alpha[NQ];
#pragma unroll
  for (int qb = 0; qb < NQ; ++qb) {
    float mx = fmaxf(fmaxf(S[qb][0], S[qb][1]), fmaxf(S[qb][2], S[qb][3]));
    mx = xg_max(mx);
    const float mn2 = fmaxf(m[qb], mx);
    grow |= mn2 > m[qb];
    alpha[qb] = __builtin_amdgcn_exp2f((m[qb] - mn2) * C2);
    m[qb] = mn2;
    const float mc = -mn2 * C2;
    const float p0 = __builtin_amdgcn_exp2f(fmaf(S[qb][0], C2, mc)), p1 = __builtin_amdgcn_exp2f(fmaf(S[qb][1], C2, mc));
    const float p2 = __builtin_amdgcn_exp2f(fmaf(S[qb][2], C2, mc)), p3 = __builtin_amdgcn_exp2f(fmaf(S[qb][3], C2, mc));
    l[qb] = fmaf(l[qb], alpha[qb], (p0 + p1) + (p2 + p3));
    pb[qb] = __builtin_bit_cast(s16x4, u32x2{pk_bf16(p0, p1), pk_bf16(p2, p3)});
  }
  if (__any(grow)) {
#pragma unroll
    for (int qb = 0; qb < NQ; ++qb)
#pragma unroll
      for (int k = 0; k < 8; ++k) acc[qb][k] *= alpha[qb];
  }
#pragma unroll
  for (int qb = 0; qb < NQ; ++qb)
#pragma unroll
    for (int k = 0; k < 8; ++k) acc[qb][k] = __builtin_amdgcn_mfma_f32_16x16x16bf16_1k(vf[k], pb[qb], acc[qb][k], 0, 0, 0);
}

template <int V> struct IntC { static constexpr int value = V; };

struct Params {
  const char* q; const char* k; const char* v; const int* bt; const int* lens; __hip_bfloat16* out; char* ws;
  unsigned kvbytes; unsigned unused;
};

__global__ void __launch_bounds__(256, 2) radix_attn_kernel(Params p) {
  extern __shared__ __attribute__((aligned(16))) char smem[];
  LAS char* lds = (LAS char*)smem;
  const int tid = threadIdx.x, lane = tid & 63, w = rfl(tid >> 6);
  const int gidx = blockIdx.x;
  const int hg = (gidx >> 3) & 3;
  const int jj = (((gidx & 7) + 4 * (hg & 1)) & 7) | ((gidx >> 5) << 3);
  const int c16 = lane & 15, g4 = lane >> 4;
#define STAMP(i) do { } while (0)
#define STAMP_FLUSH() do { } while (0)
  STAMP(0);
  const unsigned long long pr_t0 = __builtin_amdgcn_s_memrealtime();
  unsigned long long pr_x = pr_t0;
#define PROBE(k) do { if ((k) == PROBE_K && pr_x == pr_t0) pr_x = __builtin_amdgcn_s_memrealtime(); } while (0)
#define PROBE_END() do { if (gidx == PROBE_WG && w == PROBE_WAVE) { const unsigned long long lim = 7000ull + (unsigned long long)PROBE_MUL * (pr_x - pr_t0); while (__builtin_amdgcn_s_memrealtime() - pr_t0 < lim) __builtin_amdgcn_s_sleep(4); } } while (0)
  const auto wsr = __builtin_amdgcn_make_buffer_rsrc(p.ws, 0, WS_END, 0x00020000);

  const int lenraw = p.lens[c16];
  const unsigned gvw = __builtin_amdgcn_raw_buffer_load_b32(wsr, WS_G + ((hg * 4 + w) * 16 + c16) * 4, 0, 16);
  const unsigned gvh = __builtin_amdgcn_raw_buffer_load_b32(wsr, WS_G + ((hg * 4 + (jj & 3)) * 16 + c16) * 4, 0, 16);
  const unsigned gp = __builtin_amdgcn_raw_buffer_load_b32(wsr, WS_G, 0, 16);
  int pm;
  {
    const i32x4* bt4 = (const i32x4*)p.bt;
    const i32x4 r0 = bt4[lane];
    i32x4 rr[4];
#pragma unroll
    for (int i = 0; i < 4; ++i) rr[i] = bt4[(4 * w + i) * 64 + lane];
    bool n0 = false, n1 = false, n2 = false, n3 = false;
#pragma unroll
    for (int i = 0; i < 4; ++i) {
      n0 |= rr[i][0] != r0[0]; n1 |= rr[i][1] != r0[1]; n2 |= rr[i][2] != r0[2]; n3 |= rr[i][3] != r0[3];
      *(LAS i32x4*)(lds + LDS_BT + ((4 * w + i) * 256 + 4 * lane) * 4) = rr[i];
    }
    const int fc = n0 ? 0 : n1 ? 1 : n2 ? 2 : n3 ? 3 : 4;
    const unsigned long long mm = __ballot(fc < 4);
    int col = NBLK;
    if (mm) { const int fl = __builtin_ctzll(mm); col = 4 * fl + rl(fc, fl); }
    if (lane == 0) *(LAS int*)(lds + LDS_MISC + 4 * w) = col;
    asm volatile("s_waitcnt lgkmcnt(0)\n\ts_barrier" ::: "memory");
    PROBE(1);
    const i32x4 mc = *(LAS i32x4*)(lds + LDS_MISC);
    pm = rfl(min(min(mc[0], mc[1]), min(mc[2], mc[3])));
  }
  asm volatile("" :: "v"(gp));
  const unsigned ptag = (unsigned)rfl((int)gp) + 1u;
  asm volatile("" :: "v"(gvw), "v"(gvh));
  const bool zq_l = lenraw <= 0;
  const int len_l = zq_l ? LK : min(lenraw, LK);
  int mn = len_l;
  mn = min(mn, dppi_keep<0x128>(mn, mn)); mn = min(mn, dppi_keep<0x124>(mn, mn));
  mn = min(mn, dppi_keep<0x122>(mn, mn)); mn = min(mn, dppi_keep<0x121>(mn, mn));
  const int P = min(pm, rfl(mn) >> 4);
  const int tsh = P * BS;
  const int vL = ((len_l + BS - 1) >> 4) - P;
  int inc = vL;
  inc += dppi<0x111>(inc); inc += dppi<0x112>(inc); inc += dppi<0x114>(inc); inc += dppi<0x118>(inc);
  const int N = rl(inc, 15);
  const int vS = inc - vL;
  int kH = 0;
  if (P > 0) kH = min(15, max(1, rfl((int)(((unsigned)P * 16u * 64u + (unsigned)(P + N) * HRHO - 1u) / ((unsigned)(P + N) * HRHO)))));
  kH = min(kH, max(P, 0));
  const int nW = 64 - 4 * kH;
  const unsigned zmask = (unsigned)(__ballot(zq_l) & 0xffffull);
  const bool isH = jj < 4 * kH;
  const float t_all = (float)(P + N) * (16.f * 819.2f / (float)PACE);
  const unsigned long long t_ref = __builtin_amdgcn_s_memrealtime();
  constexpr int A_S0 = 3, A_S1 = 11;
  const int m_avg = (N + nW - 1) / nW;
  const bool adapt_on = nW >= 24 && m_avg >= A_S1 + 8;
  auto read_progress = [&](unsigned ldsoff, float& t1, float& d) -> bool {
    const u32x4 e = *(LAS u32x4*)(lds + ldsoff + c16 * 16);
    const bool okv = e[1] == ptag && e[3] == ptag;
    const int cnt = __builtin_popcount((unsigned)__ballot(okv) & 0xffffu);
    float a = okv ? (float)(int)(e[0] - (unsigned)t_ref) : 0.f, c = okv ? (float)e[2] : 0.f;
    a += __builtin_bit_cast(float, dppi_keep<0x128>(0, __builtin_bit_cast(int, a))); c += __builtin_bit_cast(float, dppi_keep<0x128>(0, __builtin_bit_cast(int, c)));
    a += __builtin_bit_cast(float, dppi_keep<0x124>(0, __builtin_bit_cast(int, a))); c += __builtin_bit_cast(float, dppi_keep<0x124>(0, __builtin_bit_cast(int, c)));
    a += __builtin_bit_cast(float, dppi_keep<0x122>(0, __builtin_bit_cast(int, a))); c += __builtin_bit_cast(float, dppi_keep<0x122>(0, __builtin_bit_cast(int, c)));
    a += __builtin_bit_cast(float, dppi_keep<0x121>(0, __builtin_bit_cast(int, a))); c += __builtin_bit_cast(float, dppi_keep<0x121>(0, __builtin_bit_cast(int, c)));
    if (cnt < 8) return false;
    const float ic = 1.f / (float)cnt;
    t1 = rlf(a, 0) * ic; d = rlf(c, 0) * ic * (1.f / (A_S1 - A_S0));
    return true;
  };
  const int r_lo = (int)((unsigned)(lane * N) / (unsigned)nW), r_hi = (int)((unsigned)((lane + 1) * N) / (unsigned)nW);

  if (isH) {
    const int h = hg * 4 + (jj & 3), part = jj >> 2;
    const int p0 = (P * part) / kH, p1 = (P * (part + 1)) / kH;
#pragma unroll
    for (int i = 0; i < 2; ++i)
      dma16(p.q + (2 * w + i) * 16384 + h * 512, (lane >> 5) * 8192 + (lane & 31) * 16, LDS_QB + (2 * w + i) * 1024);
    unsigned oK[4], oV[4];
#pragma unroll
    for (int i = 0; i < 4; ++i) {
      const int t = 4 * i + g4;
      oK[i] = t * 4096 + ((c16 ^ t) << 4) - i * 1024 + 4096;
      oV[i] = t * 4096 + ((c16 ^ (2 * (t & 7))) << 4) - i * 1024 + 4096;
    }
    const char* kb = p.k + h * 256 - 4096;
    const char* vb = p.v + h * 256 - 4096;
    const int nt = max(0, (p1 - p0 - w + 3) >> 2);
    const unsigned ring = w * (RH * 8192);
    int pg0 = 0, vpg = *(LAS int*)(lds + LDS_BT + min(p0 + w + 4 * lane, NBLK - 1) * 4);
    float pace_h = t_all * (PACE_H * 0.01f) / (float)max(nt, 1), pace_hb = -3.f * pace_h;
    float t_pub = t_all * (HPUB_AT * 0.01f);
    auto issue = [&](int t) {
      { const int due = (int)(pace_hb + (float)t * pace_h); while ((int)(__builtin_amdgcn_s_memrealtime() - t_ref) < due) __builtin_amdgcn_s_sleep(1); }
      if (t - pg0 == 64) { pg0 = t; vpg = *(LAS int*)(lds + LDS_BT + min(p0 + w + 4 * (t + lane), NBLK - 1) * 4); }
      const size_t pb = (size_t)(unsigned)rl(vpg, t - pg0) * 65536;
      const unsigned dst = ring + (t & (RH - 1)) * 8192;
      dma_stage(kb + pb, vb + pb, dst, dst + 4096, oK[0], oK[1], oK[2], oK[3], oV[0], oV[1], oV[2], oV[3]);
    };
    const int npre = min(nt, RH - 1);
#pragma unroll 1
    for (int t = 0; t < npre; ++t) issue(t);
    if (npre == 3) asm volatile("s_waitcnt vmcnt(24)\n\ts_barrier" ::: "memory");
    else asm volatile("s_waitcnt vmcnt(0)\n\ts_barrier" ::: "memory");
    bf16x8 qf[2][4];
#pragma unroll
    for (int qb = 0; qb < 2; ++qb) {
      const bool z = (zmask >> (8 * qb + (c16 >> 1))) & 1;
#pragma unroll
      for (int s = 0; s < 4; ++s) {
        u32x4 x = *(LAS u32x4*)(lds + LDS_QB + (16 * qb + c16) * 256 + (4 * s + g4) * 16);
        if (z) x = u32x4{0u, 0u, 0u, 0u};
        qf[qb][s] = __builtin_bit_cast(bf16x8, x);
      }
    }
    f32x4 acc[2][8];
    float m[2] = {-INFINITY, -INFINITY}, l[2] = {0.f, 0.f};
#pragma unroll
    for (int qb = 0; qb < 2; ++qb)
#pragma unroll
      for (int k = 0; k < 8; ++k) acc[qb][k] = f32x4{0.f, 0.f, 0.f, 0.f};
    const unsigned ka0 = ring + c16 * 256 + ((g4 ^ c16) << 4);
    unsigned va0;
    { const int tq = 4 * g4 + (c16 >> 2), pp = lane & 3; va0 = ring + 4096 + tq * 256 + ((tq & 7) << 5) + ((pp >> 1) << 4) + ((pp & 1) << 3); }
#pragma unroll 1
    for (int t = 0; t < nt; ++t) {
      const int young = min(nt - 1 - t, RH - 2);
      if (young == 2) asm volatile("s_waitcnt vmcnt(16) lgkmcnt(0)" ::: "memory");
      else if (young == 1) asm volatile("s_waitcnt vmcnt(8) lgkmcnt(0)" ::: "memory");
      else asm volatile("s_waitcnt vmcnt(0) lgkmcnt(0)" ::: "memory");
      if (adapt_on && nt >= 12) {
        const int tp = (nt * 9) >> 4;
        if (t == tp + 3) {
          float t1, d;
          if (read_progress(LDS_PROG + w * 256, t1, d)) {
            const float t_end = t1 + (float)(m_avg - A_S1) * d * (A_F * 0.001f);
            const float now = (float)(int)(__builtin_amdgcn_s_memrealtime() - t_ref);
            const int t_i = t + RH - 1;
            pace_h = fmaxf((t_end - A_HMARGIN - now) / (float)max(nt - t_i, 1), 0.f); pace_hb = now - (float)t_i * pace_h;
            t_pub = t_end - A_HPUB;
          }
        }
      }
      if (t + RH - 1 < nt) issue(t + RH - 1);
      if (adapt_on && nt >= 12 && t == ((nt * 9) >> 4) && lane < 16) dma16_sc1(p.ws + WS_PROG, lane * 16, LDS_PROG + w * 256);
      const unsigned sb = (t & (RH - 1)) * 8192;
      tile_compute<2>(lds, ka0 + sb, va0 + sb, qf, acc, m, l, 0, 16, g4);
    }
    STAMP(2);
    constexpr unsigned ROWB = 528, WAVEB = 32 * ROWB, MLB = 4 * WAVEB;
    asm volatile("s_waitcnt lgkmcnt(0)\n\ts_barrier" ::: "memory");
#pragma unroll
    for (int qb = 0; qb < 2; ++qb) {
      const float lt = xg_sum(l[qb]);
#pragma unroll
      for (int k = 0; k < 8; ++k) *(LAS f32x4*)(lds + w * WAVEB + (16 * qb + c16) * ROWB + (16 * k + 4 * g4) * 4) = acc[qb][k];
      if (g4 == 0) *(LAS f32x2*)(lds + MLB + (w * 32 + 16 * qb + c16) * 8) = f32x2{m[qb] * C2, lt};
    }
    asm volatile("s_waitcnt lgkmcnt(0)\n\ts_barrier" ::: "memory");
    while ((int)(__builtin_amdgcn_s_memrealtime() - t_ref) < (int)t_pub) __builtin_amdgcn_s_sleep(8);
    const unsigned slot = WS_HP + (part * 16 + h) * HP_W;
    float mrow = 0.f, lrow = 0.f;
#pragma unroll
    for (int rr = 0; rr < 8; ++rr) {
      const int row = 8 * w + rr;
      f32x2 ml[4], o[4];
#pragma unroll
      for (int v = 0; v < 4; ++v) { ml[v] = *(LAS f32x2*)(lds + MLB + (v * 32 + row) * 8); o[v] = *(LAS f32x2*)(lds + v * WAVEB + row * ROWB + lane * 8); }
      const float M = fmaxf(fmaxf(ml[0][0], ml[1][0]), fmaxf(ml[2][0], ml[3][0]));
      float L = 0.f, o0 = 0.f, o1 = 0.f;
#pragma unroll
      for (int v = 0; v < 4; ++v) {
        const float e = ml[v][1] > 0.f ? __builtin_amdgcn_exp2f(ml[v][0] - M) : 0.f;
        L = fmaf(ml[v][1], e, L); o0 = fmaf(o[v][0], e, o0); o1 = fmaf(o[v][1], e, o1);
      }
      const unsigned tag = (unsigned)rl((int)gvh, row >> 1) + 1u;
      const u32x4 gr = {__float_as_uint(o0), tag, __float_as_uint(o1), tag};
      __builtin_amdgcn_raw_buffer_store_b128(gr, wsr, slot + row * 1024 + lane * 16, 0, 16);
      if (lane == rr) { mrow = M; lrow = L; }
    }
    const unsigned tagr = (unsigned)__shfl((int)gvh, ((8 * w + lane) >> 1) & 15) + 1u;
    if (lane < 8) {
      const unsigned tag = tagr;
      const u32x4 gm = {__float_as_uint(mrow), tag, __float_as_uint(lrow), tag};
      __builtin_amdgcn_raw_buffer_store_b128(gm, wsr, slot + 32768 + (8 * w + lane) * 16, 0, 16);
    }
    STAMP(1);
    STAMP(3);
    STAMP_FLUSH();
    return;
  }

  const int j = jj - 4 * kH, h = hg * 4 + w;
  const int x0 = rl(r_lo, j), x1 = rl(r_hi, j);
  const unsigned umask = (unsigned)(__ballot(max(x0, vS) < min(x1, vS + vL)) & 0xffffull);
  unsigned voffK[4], voffV[4];
#pragma unroll
  for (int i = 0; i < 4; ++i) {
    const int t = 4 * w + i;
    voffK[i] = g4 * 256 + ((c16 ^ t) << 4) + t * 4096 - i * 1024 + 4096;
    voffV[i] = g4 * 256 + ((c16 ^ (2 * (t & 7))) << 4) + t * 4096 - i * 1024 + 4096;
  }
  const unsigned kaddr0 = w * 256 + c16 * 1024 + ((g4 ^ c16) << 4);
  unsigned vaddr0;
  { const int tq = 4 * g4 + (c16 >> 2), pp = lane & 3; vaddr0 = 16384 + tq * 1024 + w * 256 + ((tq & 7) << 5) + ((pp >> 1) << 4) + ((pp & 1) << 3); }
  const char* kbase = p.k + hg * 1024 - 4096;
  const char* vbase = p.v + hg * 1024 - 4096;

  unsigned i_mask = umask; int i_row = 0, i_t0 = 0, i_t1 = 0, i_page; bool i_more = false;
  int n_iss = 0, i_slot = 0;
  auto unit_range = [&](int b, int& t0, int& t1) {
    const int s = rl(vS, b), L = rl(vL, b);
    t0 = (P + max(x0, s) - s) * BS; t1 = min((P + min(x1, s + L) - s) * BS, rl(len_l, b));
  };
  auto q_dma = [&](int b, int slot) {
    if (lane < 32) dma16(p.q + b * 8192 + h * 512, lane * 16, LDS_QB + (w * R + slot) * 512);
  };
  if (i_mask) {
    i_row = __builtin_ctz(i_mask); i_mask &= i_mask - 1; unit_range(i_row, i_t0, i_t1);
    q_dma(i_row, 0);
    i_more = true;
  }
  i_page = i_t0 >> 4;
  int i_pg0 = i_page, vpg = *(LAS int*)(lds + LDS_BT + (i_row * NBLK + min(i_page + lane, NBLK - 1)) * 4);
  int pace_tok = 0;
  float pace_d = t_all / (float)max(x1 - x0, 1), pace_b = -(float)PACE_LEAD * pace_d;
  const int rep = (adapt_on && hg < 2 && j >= (nW >> 1) && j < (nW >> 1) + 8) ? hg * 8 + j - (nW >> 1) : -1;
  unsigned t_s0 = 0;
  auto issue_stage = [&]() {
    const int phys = rl(vpg, i_page - i_pg0);
    const int hi = i_t1 - i_page * 16;
    {
      if (adapt_on && pace_tok == A_S1 + 3 + R - 1) {
        float t1, d;
        if (read_progress(LDS_PROG, t1, d)) { pace_d = d * (A_F * 0.001f); pace_b = t1 - (float)A_S1 * pace_d - A_LEAD; }
      }
      const int due = (int)(pace_b + (float)pace_tok * pace_d);
      pace_tok += 1;
      while ((int)(__builtin_amdgcn_s_memrealtime() - t_ref) < due) __builtin_amdgcn_s_sleep(1);
    }
    const size_t pb = (size_t)(unsigned)phys * 65536;
    const unsigned dst = i_slot * STAGE + 4 * w * 1024;
    if (hi >= 16) {
      dma_stage(kbase + pb, vbase + pb, dst, dst + 16384, voffK[0], voffK[1], voffK[2], voffK[3], voffV[0], voffV[1], voffV[2], voffV[3]);
    } else {
      unsigned ok[4], ov[4];
#pragma unroll
      for (int i = 0; i < 4; ++i) {
        const int d = (min(4 * w + i, hi - 1) - (4 * w + i)) * 4096;
        ok[i] = voffK[i] + d; ov[i] = voffV[i] + d;
      }
      dma_stage(kbase + pb, vbase + pb, dst, dst + 16384, ok[0], ok[1], ok[2], ok[3], ov[0], ov[1], ov[2], ov[3]);
    }
    if (adapt_on) {
      const int k = pace_tok - 1;
      if (rep >= 0 && w == 3) {
        if (k == A_S0) t_s0 = (unsigned)__builtin_amdgcn_s_memrealtime();
        if (k == A_S1 && lane == 0) {
          const unsigned now = (unsigned)__builtin_amdgcn_s_memrealtime();
          const u32x4 e = {now, ptag, now - t_s0, ptag};
          __builtin_amdgcn_raw_buffer_store_b128(e, wsr, WS_PROG + rep * 16, 0, 16);
        }
      }
      if (k == A_S1 + 3 && w == 0 && lane < 16) dma16_sc1(p.ws + WS_PROG, lane * 16, LDS_PROG);
    }
    ++n_iss; i_slot = (i_slot + 1 == R) ? 0 : i_slot + 1;
    ++i_page;
    if (i_page * 16 >= i_t1) {
      if (i_mask) {
        i_row = __builtin_ctz(i_mask); i_mask &= i_mask - 1; unit_range(i_row, i_t0, i_t1); i_page = i_t0 >> 4;
        q_dma(i_row, i_slot);
        i_pg0 = i_page; vpg = *(LAS int*)(lds + LDS_BT + (i_row * NBLK + min(i_page + lane, NBLK - 1)) * 4);
      } else i_more = false;
    } else if (i_page - i_pg0 == 64) {
      i_pg0 = i_page; vpg = *(LAS int*)(lds + LDS_BT + (i_row * NBLK + min(i_page + lane, NBLK - 1)) * 4);
    }
  };
  PROBE(2);
#pragma unroll 1
  for (int s = 0; s < R - 1; ++s) if (i_more) issue_stage();

  unsigned own = 0;
  {
    unsigned mrem = umask;
    while (mrem) {
      const int b = __builtin_ctz(mrem); mrem &= mrem - 1;
      const int s = rl(vS, b), L = rl(vL, b);
      unsigned long long pmk = __ballot(lane < nW && max(r_lo, s) < min(r_hi, s + L));
      int idx = w % __builtin_popcountll(pmk);
      while (idx--) pmk &= pmk - 1;
      if (__builtin_ctzll(pmk) == j) own |= 1u << b;
    }
    unsigned zl = (unsigned)(__ballot(vL == 0) & 0xffffull);
    while (zl) { const int b = __builtin_ctz(zl); zl &= zl - 1; if ((b * 4) % nW == j) own |= 1u << b; }
  }

  int fb = -1, fcnt = 0; unsigned vrow = 0, vmlo = 0;
  if (own && (own & (own - 1)) == 0) {
    const int b = __builtin_ctz(own);
    const unsigned above = umask >> b;
    if ((umask >> b & 1) && (above == 1 || __builtin_popcount(above) == 2)) {
      const int s = rl(vS, b), L = rl(vL, b);
      const unsigned long long pmk = __ballot(lane < nW && max(r_lo, s) < min(r_hi, s + L)) & ~(1ull << j);
      const int np = __builtin_popcountll(pmk);
      if (np + kH <= 8 && np + kH > 0) {
        fb = b; fcnt = np + kH;
        const int rank = __builtin_amdgcn_mbcnt_hi((unsigned)(pmk >> 32), __builtin_amdgcn_mbcnt_lo((unsigned)pmk, 0));
        const unsigned prow = WS_PP + ((hg * 80 + lane + b) * 4 + w) * PP_W;
        const unsigned pushed = (unsigned)__builtin_amdgcn_ds_permute((((pmk >> lane) & 1) ? rank : 63) << 2, (int)prow);
        const unsigned hb = WS_HP + (min(max(lane - np, 0), 15) * 16 + h) * HP_W;
        vrow = lane < np ? pushed : hb + b * 2048;
        const unsigned mlb = lane < np ? pushed + 2048 : hb + 32768 + b * 32;
        vmlo = (unsigned)__shfl((int)mlb, min(lane >> 1, fcnt - 1)) + (lane & 1) * 16;
      }
    }
  }

  int n_comp = 0, c_slot = 0;
  unsigned c_mask = umask;
  float ro[2][2] = {{0.f, 0.f}, {0.f, 0.f}}, rm[2] = {-INFINITY, -INFINITY}, rs[2] = {0.f, 0.f};
  float ho[2][2] = {{0.f, 0.f}, {0.f, 0.f}}, hm[2] = {-INFINITY, -INFINITY}, hs[2] = {0.f, 0.f};
  int last_b = -1, held_b = -1;
  auto publish = [&](int b, const float (&o)[2][2], const float (&mm)[2], const float (&ll)[2]) {
    const unsigned tag = (unsigned)rl((int)gvw, b) + 1u;
    const unsigned slot = WS_PP + ((hg * 80 + j + b) * 4 + w) * PP_W;
    const u32x4 g0 = {__float_as_uint(o[0][0]), tag, __float_as_uint(o[0][1]), tag};
    const u32x4 g1 = {__float_as_uint(o[1][0]), tag, __float_as_uint(o[1][1]), tag};
    __builtin_amdgcn_raw_buffer_store_b128(g0, wsr, slot + lane * 16, 0, 16);
    __builtin_amdgcn_raw_buffer_store_b128(g1, wsr, slot + 1024 + lane * 16, 0, 16);
    if (lane < 2) {
      const u32x4 gm = {__float_as_uint(lane ? mm[1] : mm[0]), tag, __float_as_uint(lane ? ll[1] : ll[0]), tag};
      __builtin_amdgcn_raw_buffer_store_b128(gm, wsr, slot + 2048 + lane * 16, 0, 16);
    }
  };
  while (c_mask) {
    const int b = __builtin_ctz(c_mask); c_mask &= c_mask - 1;
    int t0, t1; unit_range(b, t0, t1);
    bf16x8 qf[1][4];
    f32x4 acc[1][8];
    float m[1] = {-INFINITY}, l[1] = {0.f};
#pragma unroll
    for (int k = 0; k < 8; ++k) acc[0][k] = f32x4{0.f, 0.f, 0.f, 0.f};
    bool first = true;
    for (int page = t0 >> 4; page * 16 < t1; ++page) {
      {
        const int young = n_iss - n_comp - 1;
        if (R == 4 && young >= 2) asm volatile("s_waitcnt vmcnt(16) lgkmcnt(0)\n\ts_barrier" ::: "memory");
        else if (young >= 1) asm volatile("s_waitcnt vmcnt(8) lgkmcnt(0)\n\ts_barrier" ::: "memory");
        else asm volatile("s_waitcnt vmcnt(0) lgkmcnt(0)\n\ts_barrier" ::: "memory");
      }
      PROBE(3);
      if (first) {
        const bool z = (c16 >= 2) || ((zmask >> b) & 1);
#pragma unroll
        for (int s = 0; s < 4; ++s) {
          u32x4 x = *(LAS u32x4*)(lds + LDS_QB + (w * R + c_slot) * 512 + (c16 & 1) * 256 + (4 * s + g4) * 16);
          if (z) x = u32x4{0u, 0u, 0u, 0u};
          qf[0][s] = __builtin_bit_cast(bf16x8, x);
        }
        asm volatile("s_waitcnt lgkmcnt(0)" ::: "memory");
        first = false;
      }
      if (i_more) issue_stage();
      const unsigned sb = c_slot * STAGE;
      tile_compute<1>(lds, kaddr0 + sb, vaddr0 + sb, qf, acc, m, l, 0, t1 - page * 16, g4);
      ++n_comp; c_slot = (c_slot + 1 == R) ? 0 : c_slot + 1;
    }
    const float lt = xg_sum(l[0]);
    LAS char* sc = lds + LDS_SC + w * 1024;
    if (c16 < 2) {
#pragma unroll
      for (int k = 0; k < 8; ++k) *(LAS f32x4*)(sc + c16 * 512 + (16 * k + 4 * g4) * 4) = acc[0][k];
    }
    asm volatile("s_waitcnt lgkmcnt(0)" ::: "memory");
    const f32x2 a0 = *(LAS f32x2*)(sc + lane * 8), a1 = *(LAS f32x2*)(sc + 512 + lane * 8);
    if (last_b >= 0) {
      if (held_b >= 0) publish(held_b, ho, hm, hs);
      held_b = last_b;
#pragma unroll
      for (int r = 0; r < 2; ++r) { ho[r][0] = ro[r][0]; ho[r][1] = ro[r][1]; hm[r] = rm[r]; hs[r] = rs[r]; }
    }
    ro[0][0] = a0[0]; ro[0][1] = a0[1]; ro[1][0] = a1[0]; ro[1][1] = a1[1];
    rm[0] = rlf(m[0], 0) * C2; rm[1] = rlf(m[0], 1) * C2;
    rs[0] = rlf(lt, 0); rs[1] = rlf(lt, 1);
    last_b = b;
  }
  STAMP(2);
  PROBE(5);
  if (last_b >= 0 && !((own >> last_b) & 1)) { publish(last_b, ro, rm, rs); last_b = -1; }
  if (held_b >= 0 && !((own >> held_b) & 1)) { publish(held_b, ho, hm, hs); held_b = -1; }
  STAMP(1);

  if (own) { const unsigned long long c0 = __builtin_amdgcn_s_memrealtime(); while (__builtin_amdgcn_s_memrealtime() - c0 < MDELAY) __builtin_amdgcn_s_sleep(1); }
  if (fb >= 0) {
    const int b = fb;
    const unsigned tag = (unsigned)rl((int)gvw, b) + 1u;
    const bool mine_last = b == last_b;
    const float om0 = mine_last ? rm[0] : hm[0], om1 = mine_last ? rm[1] : hm[1], ol0 = mine_last ? rs[0] : hs[0], ol1 = mine_last ? rs[1] : hs[1];
    const float oo00 = mine_last ? ro[0][0] : ho[0][0], oo01 = mine_last ? ro[0][1] : ho[0][1];
    const float oo10 = mine_last ? ro[1][0] : ho[1][0], oo11 = mine_last ? ro[1][1] : ho[1][1];
    auto attempt = [&](auto nb_tag) -> bool {
      constexpr int NB = decltype(nb_tag)::value;
      u32x4 d0[NB], d1[NB];
      unsigned so[NB];
#pragma unroll
      for (int k = 0; k < NB; ++k) so[k] = (unsigned)rl((int)vrow, min(k, fcnt - 1));
      asm volatile("" ::: "memory");
      const u32x4 dm = __builtin_amdgcn_raw_buffer_load_b128(wsr, vmlo, 0, 16);
#pragma unroll
      for (int k = 0; k < NB; ++k) {
        d0[k] = __builtin_amdgcn_raw_buffer_load_b128(wsr, lane * 16, so[k], 16);
        d1[k] = __builtin_amdgcn_raw_buffer_load_b128(wsr, 1024 + lane * 16, so[k], 16);
      }
      __builtin_amdgcn_sched_barrier(0);
      const bool live = lane < 2 * fcnt;
      const float mk = live ? __uint_as_float(dm[0]) : -INFINITY, lk = live ? __uint_as_float(dm[2]) : 0.f;
      const float om = (lane & 1) ? om1 : om0, ol = (lane & 1) ? ol1 : ol0;
      float M = fmaxf(mk, om);
      M = fmaxf(M, __builtin_bit_cast(float, dppi_keep<0x122>(__builtin_bit_cast(int, M), __builtin_bit_cast(int, M))));
      M = fmaxf(M, __builtin_bit_cast(float, dppi_keep<0x124>(__builtin_bit_cast(int, M), __builtin_bit_cast(int, M))));
      M = fmaxf(M, __builtin_bit_cast(float, dppi_keep<0x128>(__builtin_bit_cast(int, M), __builtin_bit_cast(int, M))));
      const float e = __builtin_amdgcn_exp2f(mk - M), eo = __builtin_amdgcn_exp2f(om - M);
      float Ls = lk * e;
      Ls += __builtin_bit_cast(float, dppi_keep<0x122>(0, __builtin_bit_cast(int, Ls)));
      Ls += __builtin_bit_cast(float, dppi_keep<0x124>(0, __builtin_bit_cast(int, Ls)));
      Ls += __builtin_bit_cast(float, dppi_keep<0x128>(0, __builtin_bit_cast(int, Ls)));
      Ls = fmaf(ol, eo, Ls);
      const float inv = __builtin_amdgcn_rcpf(Ls);
      const float en = e * inv, eon = eo * inv;
      const float eo0 = rlf(eon, 0), eo1 = rlf(eon, 1);
      float o00 = oo00 * eo0, o01 = oo01 * eo0, o10 = oo10 * eo1, o11 = oo11 * eo1;
      unsigned bad = (dm[1] ^ tag) | (dm[3] ^ tag);
#pragma unroll
      for (int k = 0; k < NB; ++k) {
        const float w0 = rlf(en, 2 * k), w1 = rlf(en, 2 * k + 1);
        bad |= ((d0[k][1] ^ tag) | (d0[k][3] ^ tag)) | ((d1[k][1] ^ tag) | (d1[k][3] ^ tag));
        o00 = fmaf(__uint_as_float(d0[k][0]), w0, o00); o01 = fmaf(__uint_as_float(d0[k][2]), w0, o01);
        o10 = fmaf(__uint_as_float(d1[k][0]), w1, o10); o11 = fmaf(__uint_as_float(d1[k][2]), w1, o11);
      }
      if (__any(bad != 0)) return false;
      __hip_bfloat16* orow = p.out + (b * NH + 2 * h) * DH + 2 * lane;
      *(unsigned*)orow = pk_bf16(o00, o01);
      *(unsigned*)(orow + DH) = pk_bf16(o10, o11);
      if (lane == 0) __builtin_amdgcn_raw_buffer_store_b32(tag, wsr, WS_G + (h * 16 + b) * 4, 0, GAUX);
      return true;
    };
#pragma unroll 1
    for (int t = 0; t < 16; ++t) {
      if (t >= FASTTRY) {
        unsigned spins = 0;
        while (true) {
          asm volatile("" ::: "memory");
          const u32x4 x = __builtin_amdgcn_raw_buffer_load_b128(wsr, vmlo, 0, 16);
          if (!__any(x[1] != tag || x[3] != tag) || ++spins > (1u << 16)) break;
          __builtin_amdgcn_s_sleep(SLEEP2);
        }
      }
      const bool ok = fcnt <= 5 ? attempt(IntC<5>{}) : attempt(IntC<8>{});
      if (ok) { own = 0; break; }
    }
  }
  while (own) {
    const int b = __builtin_ctz(own); own &= own - 1;
    const unsigned tag = (unsigned)rl((int)gvw, b) + 1u;
    const int s = rl(vS, b), L = rl(vL, b);
    unsigned long long pmk = __ballot(lane < nW && max(r_lo, s) < min(r_hi, s + L));
    unsigned smk = (1u << kH) - 1u;
    float M0 = -INFINITY, M1 = -INFINITY, L0 = 0.f, L1 = 0.f, o00 = 0.f, o01 = 0.f, o10 = 0.f, o11 = 0.f;
    if (b == last_b) {
      pmk &= ~(1ull << j);
      M0 = rm[0]; M1 = rm[1]; L0 = rs[0]; L1 = rs[1]; o00 = ro[0][0]; o01 = ro[0][1]; o10 = ro[1][0]; o11 = ro[1][1];
    } else if (b == held_b) {
      pmk &= ~(1ull << j);
      M0 = hm[0]; M1 = hm[1]; L0 = hs[0]; L1 = hs[1]; o00 = ho[0][0]; o01 = ho[0][1]; o10 = ho[1][0]; o11 = ho[1][1];
    }
    const unsigned p_rows = WS_PP + ((hg * 80 + lane + b) * 4 + w) * PP_W;
    const unsigned s_base = WS_HP + ((lane & 15) * 16 + h) * HP_W;
    unsigned spins = 0;
    bool light = false;
    while (pmk | smk) {
      asm volatile("" ::: "memory");
      unsigned long long tp = pmk; unsigned ts = smk;
      const bool single = (__builtin_popcountll(pmk) + __builtin_popcount(smk)) == 1;
      if (light && !single) {
        const int jp = pmk ? (((pmk >> lane) & 1) ? lane : __builtin_ctzll(pmk)) : 0;
        const int js = smk ? (((smk >> (lane & 15)) & 1) ? (lane & 15) : __builtin_ctz(smk)) : 0;
        u32x4 a = {0u, tag, 0u, tag}, c = {0u, tag, 0u, tag};
        if (pmk) a = __builtin_amdgcn_raw_buffer_load_b128(wsr, WS_PP + ((hg * 80 + jp + b) * 4 + w) * PP_W + 2048 + 16, 0, 16);
        if (smk) c = __builtin_amdgcn_raw_buffer_load_b128(wsr, WS_HP + (js * 16 + h) * HP_W + 32768 + (2 * b + 1) * 16, 0, 16);
        tp &= __ballot(a[1] == tag && a[3] == tag);
        ts &= (unsigned)__ballot(c[1] == tag && c[3] == tag);
      }
      bool progress = false;
      if (tp | ts) {
        auto batch = [&](auto nb_tag) {
          constexpr int NB = decltype(nb_tag)::value;
          unsigned rows[NB], mls[NB]; unsigned long long bitp[NB]; unsigned bits[NB];
#pragma unroll
          for (int k = 0; k < NB; ++k) {
            if (tp) { const int q = __builtin_ctzll(tp); bitp[k] = tp & (~tp + 1); bits[k] = 0; tp &= tp - 1; rows[k] = (unsigned)rl((int)p_rows, q); mls[k] = rows[k] + 2048; }
            else if (ts) { const int q = __builtin_ctz(ts); bits[k] = ts & (~ts + 1); bitp[k] = 0; ts &= ts - 1;
                           const unsigned sl = (unsigned)rl((int)s_base, q); rows[k] = sl + b * 2048; mls[k] = sl + 32768 + b * 32; }
            else { bitp[k] = 0; bits[k] = 0; rows[k] = rows[0]; mls[k] = mls[0]; }
          }
          u32x4 d0[NB], d1[NB], dm[NB];
          if (R0AUX != 16 && spins == 0) {
#pragma unroll
            for (int k = 0; k < NB; ++k) {
              d0[k] = __builtin_amdgcn_raw_buffer_load_b128(wsr, lane * 16, rows[k], R0AUX);
              d1[k] = __builtin_amdgcn_raw_buffer_load_b128(wsr, 1024 + lane * 16, rows[k], R0AUX);
              dm[k] = __builtin_amdgcn_raw_buffer_load_b128(wsr, (lane & 1) * 16, mls[k], R0AUX);
            }
          } else {
#pragma unroll
            for (int k = 0; k < NB; ++k) {
              d0[k] = __builtin_amdgcn_raw_buffer_load_b128(wsr, lane * 16, rows[k], 16);
              d1[k] = __builtin_amdgcn_raw_buffer_load_b128(wsr, 1024 + lane * 16, rows[k], 16);
              dm[k] = __builtin_amdgcn_raw_buffer_load_b128(wsr, (lane & 1) * 16, mls[k], 16);
            }
          }
#pragma unroll
          for (int k = 0; k < NB; ++k) {
            const bool ok = d0[k][1] == tag && d0[k][3] == tag && d1[k][1] == tag && d1[k][3] == tag && dm[k][1] == tag && dm[k][3] == tag;
            if ((bitp[k] | bits[k]) && __all(ok)) {
              const float mv = __uint_as_float(dm[k][0]), lv = __uint_as_float(dm[k][2]);
              const float m0 = rlf(mv, 0), m1 = rlf(mv, 1), l0 = rlf(lv, 0), l1 = rlf(lv, 1);
              const float n0 = fmaxf(M0, m0), n1 = fmaxf(M1, m1);
              const float a0 = __builtin_amdgcn_exp2f(M0 - n0), b0 = __builtin_amdgcn_exp2f(m0 - n0);
              const float a1 = __builtin_amdgcn_exp2f(M1 - n1), b1 = __builtin_amdgcn_exp2f(m1 - n1);
              L0 = L0 * a0 + l0 * b0; L1 = L1 * a1 + l1 * b1;
              o00 = o00 * a0 + __uint_as_float(d0[k][0]) * b0; o01 = o01 * a0 + __uint_as_float(d0[k][2]) * b0;
              o10 = o10 * a1 + __uint_as_float(d1[k][0]) * b1; o11 = o11 * a1 + __uint_as_float(d1[k][2]) * b1;
              M0 = n0; M1 = n1;
              pmk &= ~bitp[k]; smk &= ~bits[k];
              progress = true;
            }
          }
        };
        const int cnt = __builtin_popcountll(tp) + __builtin_popcount(ts);
        if (cnt == 1) batch(IntC<1>{}); else if (cnt <= 3) batch(IntC<3>{}); else batch(IntC<6>{});

      }
      if (!progress) {
        if (++spins > (1u << 16)) break;
        if (spins > FULLR) { light = true; __builtin_amdgcn_s_sleep(SLEEP); }
      }
    }
    const float i0 = 1.f / L0, i1 = 1.f / L1;
    __hip_bfloat16* orow = p.out + (b * NH + 2 * h) * DH + 2 * lane;
    *(unsigned*)orow = pk_bf16(o00 * i0, o01 * i0);
    *(unsigned*)(orow + DH) = pk_bf16(o10 * i1, o11 * i1);
    if (lane == 0) __builtin_amdgcn_raw_buffer_store_b32(tag, wsr, WS_G + (h * 16 + b) * 4, 0, GAUX);
  }
  STAMP(3);
  STAMP_FLUSH();
  PROBE(6);
  PROBE_END();
}

}

extern "C" void kernel_launch(void* const* d_in, const int* in_sizes, int n_in, void* d_out, int out_size, void* d_ws, size_t ws_size,
                              hipStream_t stream) {
  using namespace rattn;
  Params p;
  p.q = (const char*)d_in[0]; p.k = (const char*)d_in[1]; p.v = (const char*)d_in[2];
  p.bt = (const int*)d_in[3]; p.lens = (const int*)d_in[4];
  p.out = (__hip_bfloat16*)d_out; p.ws = (char*)d_ws;
  p.kvbytes = (unsigned)in_sizes[1] * 2u; p.unused = 0;
  (void)hipFuncSetAttribute((const void*)radix_attn_kernel, hipFuncAttributeMaxDynamicSharedMemorySize, LDS_TOTAL);
  hipLaunchKernelGGL(radix_attn_kernel, dim3(256), dim3(256), LDS_TOTAL, stream, p);
}
```

```cpp
#include <hip/hip_runtime.h>
#include <hip/hip_bf16.h>
#include <stdint.h>

namespace rattn {

constexpr int NSEQ = 16, NH = 32, NKV = 16, DH = 128, NBLK = 256, BS = 16;
constexpr int LK = NBLK * BS;
#define GAUX 0
#define KVW_LANES 3
#define KVW_SHIFT 21
#define HRHO 50
#define MDELAY 30
#define PACE 7300
#define ADAPT 1
#define A_F 1008
#define FASTTRY 2
#define PROBE_WG 200
#define PROBE_WAVE 0
#define PROBE_K 1
#define PROBE_MUL 20
#define PACE_LEAD 2
#define PACE_H 88
#define FULLR 0
#define R0AUX 16
#define HPUB_AT 85
#define A_LEAD 0.f
#define A_HMARGIN 350.f
#define A_HPUB 150.f
#define SLEEP2 2
#define MAPMODE 1
#define SLEEP 6
#define KVPOL "nt"
#define RING 3
constexpr int R = RING;
constexpr int STAGE = 32768;
constexpr int RH = 4;
constexpr int RING_BYTES = 4 * RH * 8192;
constexpr int LDS_BT = RING_BYTES;
constexpr int LDS_QB = LDS_BT + NSEQ * NBLK * 4;
constexpr int LDS_SC = LDS_QB + 4 * R * 512;
constexpr int LDS_MISC = LDS_SC + 4096;
constexpr int LDS_PROG = LDS_MISC + 64;
constexpr int LDS_JUNK = LDS_PROG;
constexpr int LDS_TOTAL_JUNK = LDS_PROG + 1024;
constexpr int LDS_TOTAL = LDS_TOTAL_JUNK + 1024;
static_assert(LDS_TOTAL <= 160 * 1024 && LDS_MISC - LDS_QB >= 8192 && RING_BYTES >= R * STAGE, "LDS");

constexpr unsigned WS_G = 0;
constexpr unsigned WS_PROG = 2048;
constexpr unsigned WS_PP = 4096;
constexpr unsigned PP_W = 2176;
constexpr unsigned WS_HP = WS_PP + 320u * 4u * PP_W + 4096u;
constexpr unsigned HP_W = 32768 + 512;
constexpr unsigned WS_END = WS_HP + 16u * 16u * HP_W;
constexpr unsigned WS_STAMP = 64u << 20;

typedef unsigned u32x4 __attribute__((ext_vector_type(4)));
typedef unsigned u32x2 __attribute__((ext_vector_type(2)));
typedef float f32x4 __attribute__((ext_vector_type(4)));
typedef float f32x2 __attribute__((ext_vector_type(2)));
typedef __bf16 bf16x8 __attribute__((ext_vector_type(8)));
typedef __bf16 bf16x2 __attribute__((ext_vector_type(2)));
typedef short s16x4 __attribute__((ext_vector_type(4)));
typedef int i32x4 __attribute__((ext_vector_type(4)));
#define LAS __attribute__((address_space(3)))

constexpr float C2 = 0.088388347648318440f * 1.4426950408889634f;

__device__ __forceinline__ int rfl(int x) { return __builtin_amdgcn_readfirstlane(x); }
__device__ __forceinline__ int rl(int x, int l) { return __builtin_amdgcn_readlane(x, l); }
__device__ __forceinline__ float rlf(float x, int l) { return __builtin_bit_cast(float, __builtin_amdgcn_readlane(__builtin_bit_cast(int, x), l)); }

__device__ __forceinline__ void dma16(const void* sbase, unsigned voff, unsigned dst) {
  unsigned keep;
  asm volatile("s_mov_b32 %0, m0\n\ts_mov_b32 m0, %3\n\ts_nop 0\n\tglobal_load_lds_dwordx4 %1, %2\n\ts_mov_b32 m0, %0"
               : "=&s"(keep) : "v"(voff), "s"(sbase), "s"(dst) : "memory");
}
__device__ __forceinline__ void dma16_sc1(const void* sbase, unsigned voff, unsigned dst) {
  unsigned keep;
  asm volatile("s_mov_b32 %0, m0\n\ts_mov_b32 m0, %3\n\ts_nop 0\n\tglobal_load_lds_dwordx4 %1, %2 sc1\n\ts_mov_b32 m0, %0"
               : "=&s"(keep) : "v"(voff), "s"(sbase), "s"(dst) : "memory");
}
__device__ __forceinline__ void dma_stage(const void* sK, const void* sV, unsigned dstK, unsigned dstV,
                                          unsigned k0, unsigned k1, unsigned k2, unsigned k3,
                                          unsigned v0, unsigned v1, unsigned v2, unsigned v3) {
  unsigned keep;
  asm volatile("s_mov_b32 %0, m0\n\ts_mov_b32 m0, %3\n\ts_nop 0\n\t"
               "global_load_lds_dwordx4 %5, %1 " KVPOL "\n\t"
               "global_load_lds_dwordx4 %6, %1 offset:1024 " KVPOL "\n\t"
               "global_load_lds_dwordx4 %7, %1 offset:2048 " KVPOL "\n\t"
               "global_load_lds_dwordx4 %8, %1 offset:3072 " KVPOL "\n\t"
               "s_mov_b32 m0, %4\n\ts_nop 0\n\t"
               "global_load_lds_dwordx4 %9, %2 " KVPOL "\n\t"
               "global_load_lds_dwordx4 %10, %2 offset:1024 " KVPOL "\n\t"
               "global_load_lds_dwordx4 %11, %2 offset:2048 " KVPOL "\n\t"
               "global_load_lds_dwordx4 %12, %2 offset:3072 " KVPOL "\n\t"
               "s_mov_b32 m0, %0"
               : "=&s"(keep)
               : "s"(sK), "s"(sV), "s"(dstK), "s"(dstV), "v"(k0), "v"(k1), "v"(k2), "v"(k3), "v"(v0), "v"(v1), "v"(v2), "v"(v3)
               : "memory");
}
template <int CTRL> __device__ __forceinline__ int dppi(int x) { return __builtin_amdgcn_update_dpp(0, x, CTRL, 0xf, 0xf, true); }
template <int CTRL> __device__ __forceinline__ int dppi_keep(int old, int x) { return __builtin_amdgcn_update_dpp(old, x, CTRL, 0xf, 0xf, false); }

__device__ __forceinline__ float xg_max(float x) {
  auto s = __builtin_amdgcn_permlane16_swap(__float_as_uint(x), __float_as_uint(x), false, false);
  x = fmaxf(__uint_as_float(s[0]), __uint_as_float(s[1]));
  auto t = __builtin_amdgcn_permlane32_swap(__float_as_uint(x), __float_as_uint(x), false, false);
  return fmaxf(__uint_as_float(t[0]), __uint_as_float(t[1]));
}
__device__ __forceinline__ float xg_sum(float x) {
  auto s = __builtin_amdgcn_permlane16_swap(__float_as_uint(x), __float_as_uint(x), false, false);
  x = __uint_as_float(s[0]) + __uint_as_float(s[1]);
  auto t = __builtin_amdgcn_permlane32_swap(__float_as_uint(x), __float_as_uint(x), false, false);
  return __uint_as_float(t[0]) + __uint_as_float(t[1]);
}
__device__ __forceinline__ unsigned pk_bf16(float a, float b) {
  bf16x2 v = {(__bf16)a, (__bf16)b};
  return __builtin_bit_cast(unsigned, v);
}

template <int NQ>
__device__ __forceinline__ void tile_compute(LAS char* lds, unsigned ka, unsigned va, const bf16x8 (&qf)[NQ][4], f32x4 (&acc)[NQ][8],
                                             float (&m)[NQ], float (&l)[NQ], int lo, int hi, int g4) {
  bf16x8 kf[4];
#pragma unroll
  for (int s = 0; s < 4; ++s) kf[s] = *(LAS bf16x8*)(lds + (ka ^ (s << 6)));
  s16x4 vf[8];
#pragma unroll
  for (int k = 0; k < 8; ++k) vf[k] = __builtin_amdgcn_ds_read_tr16_b64_v4i16((LAS s16x4*)(lds + (va ^ (k << 5))));
  f32x4 S[NQ];
#pragma unroll
  for (int qb = 0; qb < NQ; ++qb) {
    S[qb] = f32x4{0.f, 0.f, 0.f, 0.f};
#pragma unroll
    for (int s = 0; s < 4; ++s) S[qb] = __builtin_amdgcn_mfma_f32_16x16x32_bf16(kf[s], qf[qb][s], S[qb], 0, 0, 0);
  }
  if (lo > 0 || hi < 16) {
#pragma unroll
    for (int qb = 0; qb < NQ; ++qb)
#pragma unroll
      for (int r = 0; r < 4; ++r) { const int t = 4 * g4 + r; S[qb][r] = (t >= lo && t < hi) ? S[qb][r] : -INFINITY; }
  }
  s16x4 pb[NQ];
  bool grow = false;
  float alpha[NQ];
#pragma unroll
  for (int qb = 0; qb < NQ; ++qb) {
    float mx = fmaxf(fmaxf(S[qb][0], S[qb][1]), fmaxf(S[qb][2], S[qb][3]));
    mx = xg_max(mx);
    const float mn2 = fmaxf(m[qb], mx);
    grow |= mn2 > m[qb];
    alpha[qb] = __builtin_amdgcn_exp2f((m[qb] - mn2) * C2);
    m[qb] = mn2;
    const float mc = -mn2 * C2;
    const float p0 = __builtin_amdgcn_exp2f(fmaf(S[qb][0], C2, mc)), p1 = __builtin_amdgcn_exp2f(fmaf(S[qb][1], C2, mc));
    const float p2 = __builtin_amdgcn_exp2f(fmaf(S[qb][2], C2, mc)), p3 = __builtin_amdgcn_exp2f(fmaf(S[qb][3], C2, mc));
    l[qb] = fmaf(l[qb], alpha[qb], (p0 + p1) + (p2 + p3));
    pb[qb] = __builtin_bit_cast(s16x4, u32x2{pk_bf16(p0, p1), pk_bf16(p2, p3)});
  }
  if (__any(grow)) {
#pragma unroll
    for (int qb = 0; qb < NQ; ++qb)
#pragma unroll
      for (int k = 0; k < 8; ++k) acc[qb][k] *= alpha[qb];
  }
#pragma unroll
  for (int qb = 0; qb < NQ; ++qb)
#pragma unroll
    for (int k = 0; k < 8; ++k) acc[qb][k] = __builtin_amdgcn_mfma_f32_16x16x16bf16_1k(vf[k], pb[qb], acc[qb][k], 0, 0, 0);
}

template <int V> struct IntC { static constexpr int value = V; };

struct Params {
  const char* q; const char* k; const char* v; const int* bt; const int* lens; __hip_bfloat16* out; char* ws;
  unsigned kvbytes; unsigned unused;
};

__global__ void __launch_bounds__(256, 2) radix_attn_kernel(Params p) {
  extern __shared__ __attribute__((aligned(16))) char smem[];
  LAS char* lds = (LAS char*)smem;
  const int tid = threadIdx.x, lane = tid & 63, w = rfl(tid >> 6);
  const int gidx = blockIdx.x;
  const int hg = (gidx >> 3) & 3;
  const int jj = (((gidx & 7) + 4 * (hg & 1)) & 7) | ((gidx >> 5) << 3);
  const int c16 = lane & 15, g4 = lane >> 4;
#define STAMP(i) do { } while (0)
#define STAMP_FLUSH() do { } while (0)
  STAMP(0);
  const unsigned long long pr_t0 = __builtin_amdgcn_s_memrealtime();
  unsigned long long pr_x = pr_t0;
#define PROBE(k) do { if ((k) == PROBE_K && pr_x == pr_t0) pr_x = __builtin_amdgcn_s_memrealtime(); } while (0)
#define PROBE_END() do { if (gidx == PROBE_WG && w == PROBE_WAVE) { const unsigned long long lim = 7000ull + (unsigned long long)PROBE_MUL * (pr_x - pr_t0); while (__builtin_amdgcn_s_memrealtime() - pr_t0 < lim) __builtin_amdgcn_s_sleep(4); } } while (0)
  const auto wsr = __builtin_amdgcn_make_buffer_rsrc(p.ws, 0, WS_END, 0x00020000);

  unsigned voffK[4], voffV[4], oK[4], oV[4];
#pragma unroll
  for (int i = 0; i < 4; ++i) {
    const int t = 4 * w + i, u = 4 * i + g4;
    voffK[i] = g4 * 256 + ((c16 ^ t) << 4) + t * 4096 - i * 1024 + 4096;
    voffV[i] = g4 * 256 + ((c16 ^ (2 * (t & 7))) << 4) + t * 4096 - i * 1024 + 4096;
    oK[i] = u * 4096 + ((c16 ^ u) << 4) - i * 1024 + 4096;
    oV[i] = u * 4096 + ((c16 ^ (2 * (u & 7))) << 4) - i * 1024 + 4096;
  }
  const int lenraw = p.lens[c16];
  const unsigned gvw = __builtin_amdgcn_raw_buffer_load_b32(wsr, WS_G + ((hg * 4 + w) * 16 + c16) * 4, 0, 16);
  const unsigned gvh = __builtin_amdgcn_raw_buffer_load_b32(wsr, WS_G + ((hg * 4 + (jj & 3)) * 16 + c16) * 4, 0, 16);
  const unsigned gp = __builtin_amdgcn_raw_buffer_load_b32(wsr, WS_G, 0, 16);
  int pm, len_l, minlen, vpages, incp, sump; bool zq_l;
  unsigned ptag;
  {
    const i32x4* bt4 = (const i32x4*)p.bt;
    const i32x4 r0 = bt4[lane];
    i32x4 rr[4];
#pragma unroll
    for (int i = 0; i < 4; ++i) rr[i] = bt4[(4 * w + i) * 64 + lane];
    bool n0 = false, n1 = false, n2 = false, n3 = false;
#pragma unroll
    for (int i = 0; i < 4; ++i) {
      n0 |= rr[i][0] != r0[0]; n1 |= rr[i][1] != r0[1]; n2 |= rr[i][2] != r0[2]; n3 |= rr[i][3] != r0[3];
      *(LAS i32x4*)(lds + LDS_BT + ((4 * w + i) * 256 + 4 * lane) * 4) = rr[i];
    }
    zq_l = lenraw <= 0;
    len_l = zq_l ? LK : min(lenraw, LK);
    int mn = len_l;
    mn = min(mn, dppi_keep<0x128>(mn, mn)); mn = min(mn, dppi_keep<0x124>(mn, mn));
    mn = min(mn, dppi_keep<0x122>(mn, mn)); mn = min(mn, dppi_keep<0x121>(mn, mn));
    minlen = rfl(mn);
    vpages = (len_l + BS - 1) >> 4;
    incp = vpages;
    incp += dppi<0x111>(incp); incp += dppi<0x112>(incp); incp += dppi<0x114>(incp); incp += dppi<0x118>(incp);
    sump = rl(incp, 15);
    ptag = (unsigned)rfl((int)gp) + 1u;
    asm volatile("" :: "v"(gvw), "v"(gvh));
    const int fc = n0 ? 0 : n1 ? 1 : n2 ? 2 : n3 ? 3 : 4;
    const unsigned long long mm = __ballot(fc < 4);
    int col = NBLK;
    if (mm) { const int fl = __builtin_ctzll(mm); col = 4 * fl + rl(fc, fl); }
    if (lane == 0) *(LAS int*)(lds + LDS_MISC + 4 * w) = col;
    asm volatile("" :: "v"(voffK[0]), "v"(voffK[1]), "v"(voffK[2]), "v"(voffK[3]), "v"(voffV[0]), "v"(voffV[1]), "v"(voffV[2]), "v"(voffV[3]),
                 "v"(oK[0]), "v"(oK[1]), "v"(oK[2]), "v"(oK[3]), "v"(oV[0]), "v"(oV[1]), "v"(oV[2]), "v"(oV[3]));
    asm volatile("s_waitcnt lgkmcnt(0)\n\ts_barrier" ::: "memory");
    PROBE(1);
    const i32x4 mc = *(LAS i32x4*)(lds + LDS_MISC);
    pm = rfl(min(min(mc[0], mc[1]), min(mc[2], mc[3])));
  }
  const int P = min(pm, minlen >> 4);
  const int tsh = P * BS;
  const int vL = vpages - P;
  const int vS = incp - vpages - c16 * P;
  const int N = sump - 16 * P;
  int kH = 0;
  if (P > 0) kH = min(min(15, P), max(1, rfl((int)__builtin_ceilf((float)P * (1024.f / HRHO) * __builtin_amdgcn_rcpf((float)(P + N))))));
  const int nW = 64 - 4 * kH;
  const float inv_nw = __builtin_amdgcn_rcpf((float)nW);
  const unsigned zmask = (unsigned)(__ballot(zq_l) & 0xffffull);
  const bool isH = jj < 4 * kH;
  const float t_all = (float)(P + N) * (16.f * 819.2f / (float)PACE);
  const unsigned long long t_ref = __builtin_amdgcn_s_memrealtime();
  constexpr int A_S0 = 3, A_S1 = 11;
  const int m_avg = rfl((int)(((float)(N + nW - 1) + 0.5f) * inv_nw));
  const bool adapt_on = nW >= 24 && m_avg >= A_S1 + 8;
  auto read_progress = [&](unsigned ldsoff, float& t1, float& d) -> bool {
    const u32x4 e = *(LAS u32x4*)(lds + ldsoff + c16 * 16);
    const bool okv = e[1] == ptag && e[3] == ptag;
    const int cnt = __builtin_popcount((unsigned)__ballot(okv) & 0xffffu);
    float a = okv ? (float)(int)(e[0] - (unsigned)t_ref) : 0.f, c = okv ? (float)e[2] : 0.f;
    a += __builtin_bit_cast(float, dppi_keep<0x128>(0, __builtin_bit_cast(int, a))); c += __builtin_bit_cast(float, dppi_keep<0x128>(0, __builtin_bit_cast(int, c)));
    a += __builtin_bit_cast(float, dppi_keep<0x124>(0, __builtin_bit_cast(int, a))); c += __builtin_bit_cast(float, dppi_keep<0x124>(0, __builtin_bit_cast(int, c)));
    a += __builtin_bit_cast(float, dppi_keep<0x122>(0, __builtin_bit_cast(int, a))); c += __builtin_bit_cast(float, dppi_keep<0x122>(0, __builtin_bit_cast(int, c)));
    a += __builtin_bit_cast(float, dppi_keep<0x121>(0, __builtin_bit_cast(int, a))); c += __builtin_bit_cast(float, dppi_keep<0x121>(0, __builtin_bit_cast(int, c)));
    if (cnt < 8) return false;
    const float ic = 1.f / (float)cnt;
    t1 = rlf(a, 0) * ic; d = rlf(c, 0) * ic * (1.f / (A_S1 - A_S0));
    return true;
  };
  const int r_lo = (int)(((float)(lane * N) + 0.5f) * inv_nw), r_hi = (int)(((float)((lane + 1) * N) + 0.5f) * inv_nw);

  if (isH) {
    const int h = hg * 4 + (jj & 3), part = jj >> 2;
    const int p0 = (P * part) / kH, p1 = (P * (part + 1)) / kH;
#pragma unroll
    for (int i = 0; i < 2; ++i)
      dma16(p.q + (2 * w + i) * 16384 + h * 512, (lane >> 5) * 8192 + (lane & 31) * 16, LDS_QB + (2 * w + i) * 1024);
    const char* kb = p.k + h * 256 - 4096;
    const char* vb = p.v + h * 256 - 4096;
    const int nt = max(0, (p1 - p0 - w + 3) >> 2);
    const unsigned ring = w * (RH * 8192);
    int pg0 = 0, vpg = *(LAS int*)(lds + LDS_BT + min(p0 + w + 4 * lane, NBLK - 1) * 4);
    float pace_h = t_all * (PACE_H * 0.01f) * __builtin_amdgcn_rcpf((float)max(nt, 1)), pace_hb = -3.f * pace_h;
    float t_pub = t_all * (HPUB_AT * 0.01f);
    auto issue = [&](int t) {
      { const int due = rfl((int)(pace_hb + (float)t * pace_h)); if (due > 0) while ((int)(__builtin_amdgcn_s_memrealtime() - t_ref) < due) __builtin_amdgcn_s_sleep(1); }
      if (t - pg0 == 64) { pg0 = t; vpg = *(LAS int*)(lds + LDS_BT + min(p0 + w + 4 * (t + lane), NBLK - 1) * 4); }
      const size_t pb = (size_t)(unsigned)rl(vpg, t - pg0) * 65536;
      const unsigned dst = ring + (t & (RH - 1)) * 8192;
      dma_stage(kb + pb, vb + pb, dst, dst + 4096, oK[0], oK[1], oK[2], oK[3], oV[0], oV[1], oV[2], oV[3]);
    };
    const int npre = min(nt, RH - 1);
#pragma unroll 1
    for (int t = 0; t < npre; ++t) issue(t);
    if (npre == 3) asm volatile("s_waitcnt vmcnt(24)\n\ts_barrier" ::: "memory");
    else asm volatile("s_waitcnt vmcnt(0)\n\ts_barrier" ::: "memory");
    bf16x8 qf[2][4];
#pragma unroll
    for (int qb = 0; qb < 2; ++qb) {
      const bool z = (zmask >> (8 * qb + (c16 >> 1))) & 1;
#pragma unroll
      for (int s = 0; s < 4; ++s) {
        u32x4 x = *(LAS u32x4*)(lds + LDS_QB + (16 * qb + c16) * 256 + (4 * s + g4) * 16);
        if (z) x = u32x4{0u, 0u, 0u, 0u};
        qf[qb][s] = __builtin_bit_cast(bf16x8, x);
      }
    }
    f32x4 acc[2][8];
    float m[2] = {-INFINITY, -INFINITY}, l[2] = {0.f, 0.f};
#pragma unroll
    for (int qb = 0; qb < 2; ++qb)
#pragma unroll
      for (int k = 0; k < 8; ++k) acc[qb][k] = f32x4{0.f, 0.f, 0.f, 0.f};
    const unsigned ka0 = ring + c16 * 256 + ((g4 ^ c16) << 4);
    unsigned va0;
    { const int tq = 4 * g4 + (c16 >> 2), pp = lane & 3; va0 = ring + 4096 + tq * 256 + ((tq & 7) << 5) + ((pp >> 1) << 4) + ((pp & 1) << 3); }
#pragma unroll 1
    for (int t = 0; t < nt; ++t) {
      const int young = min(nt - 1 - t, RH - 2);
      if (young == 2) asm volatile("s_waitcnt vmcnt(16) lgkmcnt(0)" ::: "memory");
      else if (young == 1) asm volatile("s_waitcnt vmcnt(8) lgkmcnt(0)" ::: "memory");
      else asm volatile("s_waitcnt vmcnt(0) lgkmcnt(0)" ::: "memory");
      if (adapt_on && nt >= 12) {
        const int tp = (nt * 9) >> 4;
        if (t == tp + 3) {
          float t1, d;
          if (read_progress(LDS_PROG + w * 256, t1, d)) {
            const float t_end = t1 + (float)(m_avg - A_S1) * d * (A_F * 0.001f);
            const float now = (float)(int)(__builtin_amdgcn_s_memrealtime() - t_ref);
            const int t_i = t + RH - 1;
            pace_h = fmaxf((t_end - A_HMARGIN - now) / (float)max(nt - t_i, 1), 0.f); pace_hb = now - (float)t_i * pace_h;
            t_pub = t_end - A_HPUB;
          }
        }
      }
      if (t + RH - 1 < nt) issue(t + RH - 1);
      if (adapt_on && nt >= 12 && t == ((nt * 9) >> 4) && lane < 16) dma16_sc1(p.ws + WS_PROG, lane * 16, LDS_PROG + w * 256);
      const unsigned sb = (t & (RH - 1)) * 8192;
      tile_compute<2>(lds, ka0 + sb, va0 + sb, qf, acc, m, l, 0, 16, g4);
    }
    STAMP(2);
    constexpr unsigned ROWB = 528, WAVEB = 32 * ROWB, MLB = 4 * WAVEB;
    asm volatile("s_waitcnt lgkmcnt(0)\n\ts_barrier" ::: "memory");
#pragma unroll
    for (int qb = 0; qb < 2; ++qb) {
      const float lt = xg_sum(l[qb]);
#pragma unroll
      for (int k = 0; k < 8; ++k) *(LAS f32x4*)(lds + w * WAVEB + (16 * qb + c16) * ROWB + (16 * k + 4 * g4) * 4) = acc[qb][k];
      if (g4 == 0) *(LAS f32x2*)(lds + MLB + (w * 32 + 16 * qb + c16) * 8) = f32x2{m[qb] * C2, lt};
    }
    asm volatile("s_waitcnt lgkmcnt(0)\n\ts_barrier" ::: "memory");
    while ((int)(__builtin_amdgcn_s_memrealtime() - t_ref) < (int)t_pub) __builtin_amdgcn_s_sleep(8);
    const unsigned slot = WS_HP + (part * 16 + h) * HP_W;
    float mrow = 0.f, lrow = 0.f;
#pragma unroll
    for (int rr = 0; rr < 8; ++rr) {
      const int row = 8 * w + rr;
      f32x2 ml[4], o[4];
#pragma unroll
      for (int v = 0; v < 4; ++v) { ml[v] = *(LAS f32x2*)(lds + MLB + (v * 32 + row) * 8); o[v] = *(LAS f32x2*)(lds + v * WAVEB + row * ROWB + lane * 8); }
      const float M = fmaxf(fmaxf(ml[0][0], ml[1][0]), fmaxf(ml[2][0], ml[3][0]));
      float L = 0.f, o0 = 0.f, o1 = 0.f;
#pragma unroll
      for (int v = 0; v < 4; ++v) {
        const float e = ml[v][1] > 0.f ? __builtin_amdgcn_exp2f(ml[v][0] - M) : 0.f;
        L = fmaf(ml[v][1], e, L); o0 = fmaf(o[v][0], e, o0); o1 = fmaf(o[v][1], e, o1);
      }
      const unsigned tag = (unsigned)rl((int)gvh, row >> 1) + 1u;
      const u32x4 gr = {__float_as_uint(o0), tag, __float_as_uint(o1), tag};
      __builtin_amdgcn_raw_buffer_store_b128(gr, wsr, slot + row * 1024 + lane * 16, 0, 16);
      if (lane == rr) { mrow = M; lrow = L; }
    }
    const unsigned tagr = (unsigned)__shfl((int)gvh, ((8 * w + lane) >> 1) & 15) + 1u;
    if (lane < 8) {
      const unsigned tag = tagr;
      const u32x4 gm = {__float_as_uint(mrow), tag, __float_as_uint(lrow), tag};
      __builtin_amdgcn_raw_buffer_store_b128(gm, wsr, slot + 32768 + (8 * w + lane) * 16, 0, 16);
    }
    STAMP(1);
    STAMP(3);
    STAMP_FLUSH();
    return;
  }

  const int j = jj - 4 * kH, h = hg * 4 + w;
  const int x0 = rl(r_lo, j), x1 = rl(r_hi, j);
  const unsigned umask = (unsigned)(__ballot(max(x0, vS) < min(x1, vS + vL)) & 0xffffull);
  const unsigned kaddr0 = w * 256 + c16 * 1024 + ((g4 ^ c16) << 4);
  unsigned vaddr0;
  { const int tq = 4 * g4 + (c16 >> 2), pp = lane & 3; vaddr0 = 16384 + tq * 1024 + w * 256 + ((tq & 7) << 5) + ((pp >> 1) << 4) + ((pp & 1) << 3); }
  const char* kbase = p.k + hg * 1024 - 4096;
  const char* vbase = p.v + hg * 1024 - 4096;

  unsigned i_mask = umask; int i_row = 0, i_t0 = 0, i_t1 = 0, i_page; bool i_more = false;
  int n_iss = 0, i_slot = 0;
  auto unit_range = [&](int b, int& t0, int& t1) {
    const int s = rl(vS, b), L = rl(vL, b);
    t0 = (P + max(x0, s) - s) * BS; t1 = min((P + min(x1, s + L) - s) * BS, rl(len_l, b));
  };
  auto q_dma = [&](int b, int slot) {
    if (lane < 32) dma16(p.q + b * 8192 + h * 512, lane * 16, LDS_QB + (w * R + slot) * 512);
  };
  if (i_mask) {
    i_row = __builtin_ctz(i_mask); i_mask &= i_mask - 1; unit_range(i_row, i_t0, i_t1);
    q_dma(i_row, 0);
    i_more = true;
  }
  i_page = i_t0 >> 4;
  int i_pg0 = i_page, vpg = *(LAS int*)(lds + LDS_BT + (i_row * NBLK + min(i_page + lane, NBLK - 1)) * 4);
  int pace_tok = 0;
  float pace_d = t_all * __builtin_amdgcn_rcpf((float)max(x1 - x0, 1)), pace_b = -(float)PACE_LEAD * pace_d;
  const int rep = (adapt_on && hg < 2 && j >= (nW >> 1) && j < (nW >> 1) + 8) ? hg * 8 + j - (nW >> 1) : -1;
  unsigned t_s0 = 0;
  auto issue_stage = [&]() {
    const int phys = rl(vpg, i_page - i_pg0);
    const int hi = i_t1 - i_page * 16;
    {
      if (adapt_on && pace_tok == A_S1 + 3 + R - 1) {
        float t1, d;
        if (read_progress(LDS_PROG, t1, d)) { pace_d = d * (A_F * 0.001f); pace_b = t1 - (float)A_S1 * pace_d - A_LEAD; }
      }
      const int due = rfl((int)(pace_b + (float)pace_tok * pace_d));
      pace_tok += 1;
      if (due > 0) while ((int)(__builtin_amdgcn_s_memrealtime() - t_ref) < due) __builtin_amdgcn_s_sleep(1);
    }
    const size_t pb = (size_t)(unsigned)phys * 65536;
    const unsigned dst = i_slot * STAGE + 4 * w * 1024;
    if (hi >= 16) {
      dma_stage(kbase + pb, vbase + pb, dst, dst + 16384, voffK[0], voffK[1], voffK[2], voffK[3], voffV[0], voffV[1], voffV[2], voffV[3]);
    } else {
      unsigned ok[4], ov[4];
#pragma unroll
      for (int i = 0; i < 4; ++i) {
        const int d = (min(4 * w + i, hi - 1) - (4 * w + i)) * 4096;
        ok[i] = voffK[i] + d; ov[i] = voffV[i] + d;
      }
      dma_stage(kbase + pb, vbase + pb, dst, dst + 16384, ok[0], ok[1], ok[2], ok[3], ov[0], ov[1], ov[2], ov[3]);
    }
    if (adapt_on) {
      const int k = pace_tok - 1;
      if (rep >= 0 && w == 3) {
        if (k == A_S0) t_s0 = (unsigned)__builtin_amdgcn_s_memrealtime();
        if (k == A_S1 && lane == 0) {
          const unsigned now = (unsigned)__builtin_amdgcn_s_memrealtime();
          const u32x4 e = {now, ptag, now - t_s0, ptag};
          __builtin_amdgcn_raw_buffer_store_b128(e, wsr, WS_PROG + rep * 16, 0, 16);
        }
      }
      if (k == A_S1 + 3 && w == 0 && lane < 16) dma16_sc1(p.ws + WS_PROG, lane * 16, LDS_PROG);
    }
    ++n_iss; i_slot = (i_slot + 1 == R) ? 0 : i_slot + 1;
    ++i_page;
    if (i_page * 16 >= i_t1) {
      if (i_mask) {
        i_row = __builtin_ctz(i_mask); i_mask &= i_mask - 1; unit_range(i_row, i_t0, i_t1); i_page = i_t0 >> 4;
        q_dma(i_row, i_slot);
        i_pg0 = i_page; vpg = *(LAS int*)(lds + LDS_BT + (i_row * NBLK + min(i_page + lane, NBLK - 1)) * 4);
      } else i_more = false;
    } else if (i_page - i_pg0 == 64) {
      i_pg0 = i_page; vpg = *(LAS int*)(lds + LDS_BT + (i_row * NBLK + min(i_page + lane, NBLK - 1)) * 4);
    }
  };
  PROBE(2);
#pragma unroll 1
  for (int s = 0; s < R - 1; ++s) if (i_more) issue_stage();

  unsigned own = 0;
  {
    unsigned mrem = umask;
    while (mrem) {
      const int b = __builtin_ctz(mrem); mrem &= mrem - 1;
      const int s = rl(vS, b), L = rl(vL, b);
      unsigned long long pmk = __ballot(lane < nW && max(r_lo, s) < min(r_hi, s + L));
      int idx = w % __builtin_popcountll(pmk);
      while (idx--) pmk &= pmk - 1;
      if (__builtin_ctzll(pmk) == j) own |= 1u << b;
    }
    unsigned zl = (unsigned)(__ballot(vL == 0) & 0xffffull);
    while (zl) { const int b = __builtin_ctz(zl); zl &= zl - 1; if ((b * 4) % nW == j) own |= 1u << b; }
  }

  int fb = -1, fcnt = 0; unsigned vrow = 0, vmlo = 0;
  if (own && (own & (own - 1)) == 0) {
    const int b = __builtin_ctz(own);
    const unsigned above = umask >> b;
    if ((umask >> b & 1) && (above == 1 || __builtin_popcount(above) == 2)) {
      const int s = rl(vS, b), L = rl(vL, b);
      const unsigned long long pmk = __ballot(lane < nW && max(r_lo, s) < min(r_hi, s + L)) & ~(1ull << j);
      const int np = __builtin_popcountll(pmk);
      if (np + kH <= 8 && np + kH > 0) {
        fb = b; fcnt = np + kH;
        const int rank = __builtin_amdgcn_mbcnt_hi((unsigned)(pmk >> 32), __builtin_amdgcn_mbcnt_lo((unsigned)pmk, 0));
        const unsigned prow = WS_PP + ((hg * 80 + lane + b) * 4 + w) * PP_W;
        const unsigned pushed = (unsigned)__builtin_amdgcn_ds_permute((((pmk >> lane) & 1) ? rank : 63) << 2, (int)prow);
        const unsigned hb = WS_HP + (min(max(lane - np, 0), 15) * 16 + h) * HP_W;
        vrow = lane < np ? pushed : hb + b * 2048;
        const unsigned mlb = lane < np ? pushed + 2048 : hb + 32768 + b * 32;
        vmlo = (unsigned)__shfl((int)mlb, min(lane >> 1, fcnt - 1)) + (lane & 1) * 16;
      }
    }
  }

  int n_comp = 0, c_slot = 0;
  unsigned c_mask = umask;
  float ro[2][2] = {{0.f, 0.f}, {0.f, 0.f}}, rm[2] = {-INFINITY, -INFINITY}, rs[2] = {0.f, 0.f};
  float ho[2][2] = {{0.f, 0.f}, {0.f, 0.f}}, hm[2] = {-INFINITY, -INFINITY}, hs[2] = {0.f, 0.f};
  int last_b = -1, held_b = -1;
  auto publish = [&](int b, const float (&o)[2][2], const float (&mm)[2], const float (&ll)[2]) {
    const unsigned tag = (unsigned)rl((int)gvw, b) + 1u;
    const unsigned slot = WS_PP + ((hg * 80 + j + b) * 4 + w) * PP_W;
    const u32x4 g0 = {__float_as_uint(o[0][0]), tag, __float_as_uint(o[0][1]), tag};
    const u32x4 g1 = {__float_as_uint(o[1][0]), tag, __float_as_uint(o[1][1]), tag};
    __builtin_amdgcn_raw_buffer_store_b128(g0, wsr, slot + lane * 16, 0, 16);
    __builtin_amdgcn_raw_buffer_store_b128(g1, wsr, slot + 1024 + lane * 16, 0, 16);
    if (lane < 2) {
      const u32x4 gm = {__float_as_uint(lane ? mm[1] : mm[0]), tag, __float_as_uint(lane ? ll[1] : ll[0]), tag};
      __builtin_amdgcn_raw_buffer_store_b128(gm, wsr, slot + 2048 + lane * 16, 0, 16);
    }
  };
  while (c_mask) {
    const int b = __builtin_ctz(c_mask); c_mask &= c_mask - 1;
    int t0, t1; unit_range(b, t0, t1);
    bf16x8 qf[1][4];
    f32x4 acc[1][8];
    float m[1] = {-INFINITY}, l[1] = {0.f};
#pragma unroll
    for (int k = 0; k < 8; ++k) acc[0][k] = f32x4{0.f, 0.f, 0.f, 0.f};
    bool first = true;
    for (int page = t0 >> 4; page * 16 < t1; ++page) {
      {
        const int young = n_iss - n_comp - 1;
        if (R == 4 && young >= 2) asm volatile("s_waitcnt vmcnt(16) lgkmcnt(0)\n\ts_barrier" ::: "memory");
        else if (young >= 1) asm volatile("s_waitcnt vmcnt(8) lgkmcnt(0)\n\ts_barrier" ::: "memory");
        else asm volatile("s_waitcnt vmcnt(0) lgkmcnt(0)\n\ts_barrier" ::: "memory");
      }
      PROBE(3);
      if (first) {
        const bool z = (c16 >= 2) || ((zmask >> b) & 1);
#pragma unroll
        for (int s = 0; s < 4; ++s) {
          u32x4 x = *(LAS u32x4*)(lds + LDS_QB + (w * R + c_slot) * 512 + (c16 & 1) * 256 + (4 * s + g4) * 16);
          if (z) x = u32x4{0u, 0u, 0u, 0u};
          qf[0][s] = __builtin_bit_cast(bf16x8, x);
        }
        asm volatile("s_waitcnt lgkmcnt(0)" ::: "memory");
        first = false;
      }
      if (i_more) issue_stage();
      const unsigned sb = c_slot * STAGE;
      tile_compute<1>(lds, kaddr0 + sb, vaddr0 + sb, qf, acc, m, l, 0, t1 - page * 16, g4);
      ++n_comp; c_slot = (c_slot + 1 == R) ? 0 : c_slot + 1;
    }
    const float lt = xg_sum(l[0]);
    LAS char* sc = lds + LDS_SC + w * 1024;
    if (c16 < 2) {
#pragma unroll
      for (int k = 0; k < 8; ++k) *(LAS f32x4*)(sc + c16 * 512 + (16 * k + 4 * g4) * 4) = acc[0][k];
    }
    asm volatile("s_waitcnt lgkmcnt(0)" ::: "memory");
    const f32x2 a0 = *(LAS f32x2*)(sc + lane * 8), a1 = *(LAS f32x2*)(sc + 512 + lane * 8);
    if (last_b >= 0) {
      if (held_b >= 0) publish(held_b, ho, hm, hs);
      held_b = last_b;
#pragma unroll
      for (int r = 0; r < 2; ++r) { ho[r][0] = ro[r][0]; ho[r][1] = ro[r][1]; hm[r] = rm[r]; hs[r] = rs[r]; }
    }
    ro[0][0] = a0[0]; ro[0][1] = a0[1]; ro[1][0] = a1[0]; ro[1][1] = a1[1];
    rm[0] = rlf(m[0], 0) * C2; rm[1] = rlf(m[0], 1) * C2;
    rs[0] = rlf(lt, 0); rs[1] = rlf(lt, 1);
    last_b = b;
  }
  STAMP(2);
  PROBE(5);
  if (last_b >= 0 && !((own >> last_b) & 1)) { publish(last_b, ro, rm, rs); last_b = -1; }
  if (held_b >= 0 && !((own >> held_b) & 1)) { publish(held_b, ho, hm, hs); held_b = -1; }
  STAMP(1);

  if (own) { const unsigned long long c0 = __builtin_amdgcn_s_memrealtime(); while (__builtin_amdgcn_s_memrealtime() - c0 < MDELAY) __builtin_amdgcn_s_sleep(1); }
  if (fb >= 0) {
    const int b = fb;
    const unsigned tag = (unsigned)rl((int)gvw, b) + 1u;
    const bool mine_last = b == last_b;
    const float om0 = mine_last ? rm[0] : hm[0], om1 = mine_last ? rm[1] : hm[1], ol0 = mine_last ? rs[0] : hs[0], ol1 = mine_last ? rs[1] : hs[1];
    const float oo00 = mine_last ? ro[0][0] : ho[0][0], oo01 = mine_last ? ro[0][1] : ho[0][1];
    const float oo10 = mine_last ? ro[1][0] : ho[1][0], oo11 = mine_last ? ro[1][1] : ho[1][1];
    auto attempt = [&](auto nb_tag) -> bool {
      constexpr int NB = decltype(nb_tag)::value;
      u32x4 d0[NB], d1[NB];
      unsigned so[NB];
#pragma unroll
      for (int k = 0; k < NB; ++k) so[k] = (unsigned)rl((int)vrow, min(k, fcnt - 1));
      asm volatile("" ::: "memory");
      const u32x4 dm = __builtin_amdgcn_raw_buffer_load_b128(wsr, vmlo, 0, 16);
#pragma unroll
      for (int k = 0; k < NB; ++k) {
        d0[k] = __builtin_amdgcn_raw_buffer_load_b128(wsr, lane * 16, so[k], 16);
        d1[k] = __builtin_amdgcn_raw_buffer_load_b128(wsr, 1024 + lane * 16, so[k], 16);
      }
      __builtin_amdgcn_sched_barrier(0);
      const bool live = lane < 2 * fcnt;
      const float mk = live ? __uint_as_float(dm[0]) : -INFINITY, lk = live ? __uint_as_float(dm[2]) : 0.f;
      const float om = (lane & 1) ? om1 : om0, ol = (lane & 1) ? ol1 : ol0;
      float M = fmaxf(mk, om);
      M = fmaxf(M, __builtin_bit_cast(float, dppi_keep<0x122>(__builtin_bit_cast(int, M), __builtin_bit_cast(int, M))));
      M = fmaxf(M, __builtin_bit_cast(float, dppi_keep<0x124>(__builtin_bit_cast(int, M), __builtin_bit_cast(int, M))));
      M = fmaxf(M, __builtin_bit_cast(float, dppi_keep<0x128>(__builtin_bit_cast(int, M), __builtin_bit_cast(int, M))));
      const float e = __builtin_amdgcn_exp2f(mk - M), eo = __builtin_amdgcn_exp2f(om - M);
      float Ls = lk * e;
      Ls += __builtin_bit_cast(float, dppi_keep<0x122>(0, __builtin_bit_cast(int, Ls)));
      Ls += __builtin_bit_cast(float, dppi_keep<0x124>(0, __builtin_bit_cast(int, Ls)));
      Ls += __builtin_bit_cast(float, dppi_keep<0x128>(0, __builtin_bit_cast(int, Ls)));
      Ls = fmaf(ol, eo, Ls);
      const float inv = __builtin_amdgcn_rcpf(Ls);
      const float en = e * inv, eon = eo * inv;
      const float eo0 = rlf(eon, 0), eo1 = rlf(eon, 1);
      float o00 = oo00 * eo0, o01 = oo01 * eo0, o10 = oo10 * eo1, o11 = oo11 * eo1;
      unsigned bad = (dm[1] ^ tag) | (dm[3] ^ tag);
#pragma unroll
      for (int k = 0; k < NB; ++k) {
        const float w0 = rlf(en, 2 * k), w1 = rlf(en, 2 * k + 1);
        bad |= ((d0[k][1] ^ tag) | (d0[k][3] ^ tag)) | ((d1[k][1] ^ tag) | (d1[k][3] ^ tag));
        o00 = fmaf(__uint_as_float(d0[k][0]), w0, o00); o01 = fmaf(__uint_as_float(d0[k][2]), w0, o01);
        o10 = fmaf(__uint_as_float(d1[k][0]), w1, o10); o11 = fmaf(__uint_as_float(d1[k][2]), w1, o11);
      }
      if (__any(bad != 0)) return false;
      __hip_bfloat16* orow = p.out + (b * NH + 2 * h) * DH + 2 * lane;
      *(unsigned*)orow = pk_bf16(o00, o01);
      *(unsigned*)(orow + DH) = pk_bf16(o10, o11);
      if (lane == 0) __builtin_amdgcn_raw_buffer_store_b32(tag, wsr, WS_G + (h * 16 + b) * 4, 0, GAUX);
      return true;
    };
#pragma unroll 1
    for (int t = 0; t < 16; ++t) {
      if (t >= FASTTRY) {
        unsigned spins = 0;
        while (true) {
          asm volatile("" ::: "memory");
          const u32x4 x = __builtin_amdgcn_raw_buffer_load_b128(wsr, vmlo, 0, 16);
          if (!__any(x[1] != tag || x[3] != tag) || ++spins > (1u << 16)) break;
          __builtin_amdgcn_s_sleep(SLEEP2);
        }
      }
      const bool ok = fcnt <= 5 ? attempt(IntC<5>{}) : attempt(IntC<8>{});
      if (ok) { own = 0; break; }
    }
  }
  while (own) {
    const int b = __builtin_ctz(own); own &= own - 1;
    const unsigned tag = (unsigned)rl((int)gvw, b) + 1u;
    const int s = rl(vS, b), L = rl(vL, b);
    unsigned long long pmk = __ballot(lane < nW && max(r_lo, s) < min(r_hi, s + L));
    unsigned smk = (1u << kH) - 1u;
    float M0 = -INFINITY, M1 = -INFINITY, L0 = 0.f, L1 = 0.f, o00 = 0.f, o01 = 0.f, o10 = 0.f, o11 = 0.f;
    if (b == last_b) {
      pmk &= ~(1ull << j);
      M0 = rm[0]; M1 = rm[1]; L0 = rs[0]; L1 = rs[1]; o00 = ro[0][0]; o01 = ro[0][1]; o10 = ro[1][0]; o11 = ro[1][1];
    } else if (b == held_b) {
      pmk &= ~(1ull << j);
      M0 = hm[0]; M1 = hm[1]; L0 = hs[0]; L1 = hs[1]; o00 = ho[0][0]; o01 = ho[0][1]; o10 = ho[1][0]; o11 = ho[1][1];
    }
    const unsigned p_rows = WS_PP + ((hg * 80 + lane + b) * 4 + w) * PP_W;
    const unsigned s_base = WS_HP + ((lane & 15) * 16 + h) * HP_W;
    unsigned spins = 0;
    bool light = false;
    while (pmk | smk) {
      asm volatile("" ::: "memory");
      unsigned long long tp = pmk; unsigned ts = smk;
      const bool single = (__builtin_popcountll(pmk) + __builtin_popcount(smk)) == 1;
      if (light && !single) {
        const int jp = pmk ? (((pmk >> lane) & 1) ? lane : __builtin_ctzll(pmk)) : 0;
        const int js = smk ? (((smk >> (lane & 15)) & 1) ? (lane & 15) : __builtin_ctz(smk)) : 0;
        u32x4 a = {0u, tag, 0u, tag}, c = {0u, tag, 0u, tag};
        if (pmk) a = __builtin_amdgcn_raw_buffer_load_b128(wsr, WS_PP + ((hg * 80 + jp + b) * 4 + w) * PP_W + 2048 + 16, 0, 16);
        if (smk) c = __builtin_amdgcn_raw_buffer_load_b128(wsr, WS_HP + (js * 16 + h) * HP_W + 32768 + (2 * b + 1) * 16, 0, 16);
        tp &= __ballot(a[1] == tag && a[3] == tag);
        ts &= (unsigned)__ballot(c[1] == tag && c[3] == tag);
      }
      bool progress = false;
      if (tp | ts) {
        auto batch = [&](auto nb_tag) {
          constexpr int NB = decltype(nb_tag)::value;
          unsigned rows[NB], mls[NB]; unsigned long long bitp[NB]; unsigned bits[NB];
#pragma unroll
          for (int k = 0; k < NB; ++k) {
            if (tp) { const int q = __builtin_ctzll(tp); bitp[k] = tp & (~tp + 1); bits[k] = 0; tp &= tp - 1; rows[k] = (unsigned)rl((int)p_rows, q); mls[k] = rows[k] + 2048; }
            else if (ts) { const int q = __builtin_ctz(ts); bits[k] = ts & (~ts + 1); bitp[k] = 0; ts &= ts - 1;
                           const unsigned sl = (unsigned)rl((int)s_base, q); rows[k] = sl + b * 2048; mls[k] = sl + 32768 + b * 32; }
            else { bitp[k] = 0; bits[k] = 0; rows[k] = rows[0]; mls[k] = mls[0]; }
          }
          u32x4 d0[NB], d1[NB], dm[NB];
          if (R0AUX != 16 && spins == 0) {
#pragma unroll
            for (int k = 0; k < NB; ++k) {
              d0[k] = __builtin_amdgcn_raw_buffer_load_b128(wsr, lane * 16, rows[k], R0AUX);
              d1[k] = __builtin_amdgcn_raw_buffer_load_b128(wsr, 1024 + lane * 16, rows[k], R0AUX);
              dm[k] = __builtin_amdgcn_raw_buffer_load_b128(wsr, (lane & 1) * 16, mls[k], R0AUX);
            }
          } else {
#pragma unroll
            for (int k = 0; k < NB; ++k) {
              d0[k] = __builtin_amdgcn_raw_buffer_load_b128(wsr, lane * 16, rows[k], 16);
              d1[k] = __builtin_amdgcn_raw_buffer_load_b128(wsr, 1024 + lane * 16, rows[k], 16);
              dm[k] = __builtin_amdgcn_raw_buffer_load_b128(wsr, (lane & 1) * 16, mls[k], 16);
            }
          }
#pragma unroll
          for (int k = 0; k < NB; ++k) {
            const bool ok = d0[k][1] == tag && d0[k][3] == tag && d1[k][1] == tag && d1[k][3] == tag && dm[k][1] == tag && dm[k][3] == tag;
            if ((bitp[k] | bits[k]) && __all(ok)) {
              const float mv = __uint_as_float(dm[k][0]), lv = __uint_as_float(dm[k][2]);
              const float m0 = rlf(mv, 0), m1 = rlf(mv, 1), l0 = rlf(lv, 0), l1 = rlf(lv, 1);
              const float n0 = fmaxf(M0, m0), n1 = fmaxf(M1, m1);
              const float a0 = __builtin_amdgcn_exp2f(M0 - n0), b0 = __builtin_amdgcn_exp2f(m0 - n0);
              const float a1 = __builtin_amdgcn_exp2f(M1 - n1), b1 = __builtin_amdgcn_exp2f(m1 - n1);
              L0 = L0 * a0 + l0 * b0; L1 = L1 * a1 + l1 * b1;
              o00 = o00 * a0 + __uint_as_float(d0[k][0]) * b0; o01 = o01 * a0 + __uint_as_float(d0[k][2]) * b0;
              o10 = o10 * a1 + __uint_as_float(d1[k][0]) * b1; o11 = o11 * a1 + __uint_as_float(d1[k][2]) * b1;
              M0 = n0; M1 = n1;
              pmk &= ~bitp[k]; smk &= ~bits[k];
              progress = true;
            }
          }
        };
        const int cnt = __builtin_popcountll(tp) + __builtin_popcount(ts);
        if (cnt == 1) batch(IntC<1>{}); else if (cnt <= 3) batch(IntC<3>{}); else batch(IntC<6>{});

      }
      if (!progress) {
        if (++spins > (1u << 16)) break;
        if (spins > FULLR) { light = true; __builtin_amdgcn_s_sleep(SLEEP); }
      }
    }
    const float i0 = 1.f / L0, i1 = 1.f / L1;
    __hip_bfloat16* orow = p.out + (b * NH + 2 * h) * DH + 2 * lane;
    *(unsigned*)orow = pk_bf16(o00 * i0, o01 * i0);
    *(unsigned*)(orow + DH) = pk_bf16(o10 * i1, o11 * i1);
    if (lane == 0) __builtin_amdgcn_raw_buffer_store_b32(tag, wsr, WS_G + (h * 16 + b) * 4, 0, GAUX);
  }
  STAMP(3);
  STAMP_FLUSH();
  PROBE(6);
  PROBE_END();
}

}

extern "C" void kernel_launch(void* const* d_in, const int* in_sizes, int n_in, void* d_out, int out_size, void* d_ws, size_t ws_size,
                              hipStream_t stream) {
  using namespace rattn;
  Params p;
  p.q = (const char*)d_in[0]; p.k = (const char*)d_in[1]; p.v = (const char*)d_in[2];
  p.bt = (const int*)d_in[3]; p.lens = (const int*)d_in[4];
  p.out = (__hip_bfloat16*)d_out; p.ws = (char*)d_ws;
  p.kvbytes = (unsigned)in_sizes[1] * 2u; p.unused = 0;
  (void)hipFuncSetAttribute((const void*)radix_attn_kernel, hipFuncAttributeMaxDynamicSharedMemorySize, LDS_TOTAL);
  hipLaunchKernelGGL(radix_attn_kernel, dim3(256), dim3(256), LDS_TOTAL, stream, p);
}
```

```cpp
#include <hip/hip_runtime.h>
#include <hip/hip_bf16.h>
#include <stdint.h>

namespace rattn {

constexpr int NSEQ = 16, NH = 32, NKV = 16, DH = 128, NBLK = 256, BS = 16;
constexpr int LK = NBLK * BS;
#define GAUX 0
#define KVW_LANES 3
#define KVW_SHIFT 21
#define HRHO 50
#define MDELAY 30
#define PACE 7300
#define ADAPT 1
#define A_F 1008
#define FASTTRY 2

#define PACE_LEAD 2
#define PACE_H 88
#define FULLR 0
#define R0AUX 16
#define HPUB_AT 85
#define A_LEAD 0.f
#define A_HMARGIN 350.f
#define A_HPUB 150.f
#define SLEEP2 2
#define MAPMODE 1
#define SLEEP 6
#define KVPOL "nt"
#define RING 3
constexpr int R = RING;
constexpr int STAGE = 32768;
constexpr int RH = 4;
constexpr int RING_BYTES = 4 * RH * 8192;
constexpr int LDS_BT = RING_BYTES;
constexpr int LDS_QB = LDS_BT + NSEQ * NBLK * 4;
constexpr int LDS_SC = LDS_QB + 4 * R * 512;
constexpr int LDS_MISC = LDS_SC + 4096;
constexpr int LDS_PROG = LDS_MISC + 64;
constexpr int LDS_JUNK = LDS_PROG;
constexpr int LDS_TOTAL_JUNK = LDS_PROG + 1024;
constexpr int LDS_TOTAL = LDS_TOTAL_JUNK + 1024;
static_assert(LDS_TOTAL <= 160 * 1024 && LDS_MISC - LDS_QB >= 8192 && RING_BYTES >= R * STAGE, "LDS");

constexpr unsigned WS_G = 0;
constexpr unsigned WS_PROG = 2048;
constexpr unsigned WS_PP = 4096;
constexpr unsigned PP_W = 2176;
constexpr unsigned WS_HP = WS_PP + 320u * 4u * PP_W + 4096u;
constexpr unsigned HP_W = 32768 + 512;
constexpr unsigned WS_END = WS_HP + 16u * 16u * HP_W;
constexpr unsigned WS_STAMP = 64u << 20;

typedef unsigned u32x4 __attribute__((ext_vector_type(4)));
typedef unsigned u32x2 __attribute__((ext_vector_type(2)));
typedef float f32x4 __attribute__((ext_vector_type(4)));
typedef float f32x2 __attribute__((ext_vector_type(2)));
typedef __bf16 bf16x8 __attribute__((ext_vector_type(8)));
typedef __bf16 bf16x2 __attribute__((ext_vector_type(2)));
typedef short s16x4 __attribute__((ext_vector_type(4)));
typedef int i32x4 __attribute__((ext_vector_type(4)));
#define LAS __attribute__((address_space(3)))

constexpr float C2 = 0.088388347648318440f * 1.4426950408889634f;

__device__ __forceinline__ int rfl(int x) { return __builtin_amdgcn_readfirstlane(x); }
__device__ __forceinline__ int rl(int x, int l) { return __builtin_amdgcn_readlane(x, l); }
__device__ __forceinline__ float rlf(float x, int l) { return __builtin_bit_cast(float, __builtin_amdgcn_readlane(__builtin_bit_cast(int, x), l)); }

__device__ __forceinline__ void dma16(const void* sbase, unsigned voff, unsigned dst) {
  unsigned keep;
  asm volatile("s_mov_b32 %0, m0\n\ts_mov_b32 m0, %3\n\ts_nop 0\n\tglobal_load_lds_dwordx4 %1, %2\n\ts_mov_b32 m0, %0"
               : "=&s"(keep) : "v"(voff), "s"(sbase), "s"(dst) : "memory");
}
__device__ __forceinline__ void dma16_sc1(const void* sbase, unsigned voff, unsigned dst) {
  unsigned keep;
  asm volatile("s_mov_b32 %0, m0\n\ts_mov_b32 m0, %3\n\ts_nop 0\n\tglobal_load_lds_dwordx4 %1, %2 sc1\n\ts_mov_b32 m0, %0"
               : "=&s"(keep) : "v"(voff), "s"(sbase), "s"(dst) : "memory");
}
__device__ __forceinline__ void dma_stage(const void* sK, const void* sV, unsigned dstK, unsigned dstV,
                                          unsigned k0, unsigned k1, unsigned k2, unsigned k3,
                                          unsigned v0, unsigned v1, unsigned v2, unsigned v3) {
  unsigned keep;
  asm volatile("s_mov_b32 %0, m0\n\ts_mov_b32 m0, %3\n\ts_nop 0\n\t"
               "global_load_lds_dwordx4 %5, %1 " KVPOL "\n\t"
               "global_load_lds_dwordx4 %6, %1 offset:1024 " KVPOL "\n\t"
               "global_load_lds_dwordx4 %7, %1 offset:2048 " KVPOL "\n\t"
               "global_load_lds_dwordx4 %8, %1 offset:3072 " KVPOL "\n\t"
               "s_mov_b32 m0, %4\n\ts_nop 0\n\t"
               "global_load_lds_dwordx4 %9, %2 " KVPOL "\n\t"
               "global_load_lds_dwordx4 %10, %2 offset:1024 " KVPOL "\n\t"
               "global_load_lds_dwordx4 %11, %2 offset:2048 " KVPOL "\n\t"
               "global_load_lds_dwordx4 %12, %2 offset:3072 " KVPOL "\n\t"
               "s_mov_b32 m0, %0"
               : "=&s"(keep)
               : "s"(sK), "s"(sV), "s"(dstK), "s"(dstV), "v"(k0), "v"(k1), "v"(k2), "v"(k3), "v"(v0), "v"(v1), "v"(v2), "v"(v3)
               : "memory");
}
template <int CTRL> __device__ __forceinline__ int dppi(int x) { return __builtin_amdgcn_update_dpp(0, x, CTRL, 0xf, 0xf, true); }
template <int CTRL> __device__ __forceinline__ int dppi_keep(int old, int x) { return __builtin_amdgcn_update_dpp(old, x, CTRL, 0xf, 0xf, false); }

__device__ __forceinline__ float xg_max(float x) {
  auto s = __builtin_amdgcn_permlane16_swap(__float_as_uint(x), __float_as_uint(x), false, false);
  x = fmaxf(__uint_as_float(s[0]), __uint_as_float(s[1]));
  auto t = __builtin_amdgcn_permlane32_swap(__float_as_uint(x), __float_as_uint(x), false, false);
  return fmaxf(__uint_as_float(t[0]), __uint_as_float(t[1]));
}
__device__ __forceinline__ float xg_sum(float x) {
  auto s = __builtin_amdgcn_permlane16_swap(__float_as_uint(x), __float_as_uint(x), false, false);
  x = __uint_as_float(s[0]) + __uint_as_float(s[1]);
  auto t = __builtin_amdgcn_permlane32_swap(__float_as_uint(x), __float_as_uint(x), false, false);
  return __uint_as_float(t[0]) + __uint_as_float(t[1]);
}
__device__ __forceinline__ unsigned pk_bf16(float a, float b) {
  bf16x2 v = {(__bf16)a, (__bf16)b};
  return __builtin_bit_cast(unsigned, v);
}

template <int NQ>
__device__ __forceinline__ void tile_compute(LAS char* lds, unsigned ka, unsigned va, const bf16x8 (&qf)[NQ][4], f32x4 (&acc)[NQ][8],
                                             float (&m)[NQ], float (&l)[NQ], int lo, int hi, int g4) {
  bf16x8 kf[4];
#pragma unroll
  for (int s = 0; s < 4; ++s) kf[s] = *(LAS bf16x8*)(lds + (ka ^ (s << 6)));
  s16x4 vf[8];
#pragma unroll
  for (int k = 0; k < 8; ++k) vf[k] = __builtin_amdgcn_ds_read_tr16_b64_v4i16((LAS s16x4*)(lds + (va ^ (k << 5))));
  f32x4 S[NQ];
#pragma unroll
  for (int qb = 0; qb < NQ; ++qb) {
    S[qb] = f32x4{0.f, 0.f, 0.f, 0.f};
#pragma unroll
    for (int s = 0; s < 4; ++s) S[qb] = __builtin_amdgcn_mfma_f32_16x16x32_bf16(kf[s], qf[qb][s], S[qb], 0, 0, 0);
  }
  if (lo > 0 || hi < 16) {
#pragma unroll
    for (int qb = 0; qb < NQ; ++qb)
#pragma unroll
      for (int r = 0; r < 4; ++r) { const int t = 4 * g4 + r; S[qb][r] = (t >= lo && t < hi) ? S[qb][r] : -INFINITY; }
  }
  s16x4 pb[NQ];
  bool grow = false;
  float alpha[NQ];
#pragma unroll
  for (int qb = 0; qb < NQ; ++qb) {
    float mx = fmaxf(fmaxf(S[qb][0], S[qb][1]), fmaxf(S[qb][2], S[qb][3]));
    mx = xg_max(mx);
    const float mn2 = fmaxf(m[qb], mx);
    grow |= mn2 > m[qb];
    alpha[qb] = __builtin_amdgcn_exp2f((m[qb] - mn2) * C2);
    m[qb] = mn2;
    const float mc = -mn2 * C2;
    const float p0 = __builtin_amdgcn_exp2f(fmaf(S[qb][0], C2, mc)), p1 = __builtin_amdgcn_exp2f(fmaf(S[qb][1], C2, mc));
    const float p2 = __builtin_amdgcn_exp2f(fmaf(S[qb][2], C2, mc)), p3 = __builtin_amdgcn_exp2f(fmaf(S[qb][3], C2, mc));
    l[qb] = fmaf(l[qb], alpha[qb], (p0 + p1) + (p2 + p3));
    pb[qb] = __builtin_bit_cast(s16x4, u32x2{pk_bf16(p0, p1), pk_bf16(p2, p3)});
  }
  if (__any(grow)) {
#pragma unroll
    for (int qb = 0; qb < NQ; ++qb)
#pragma unroll
      for (int k = 0; k < 8; ++k) acc[qb][k] *= alpha[qb];
  }
#pragma unroll
  for (int qb = 0; qb < NQ; ++qb)
#pragma unroll
    for (int k = 0; k < 8; ++k) acc[qb][k] = __builtin_amdgcn_mfma_f32_16x16x16bf16_1k(vf[k], pb[qb], acc[qb][k], 0, 0, 0);
}

template <int V> struct IntC { static constexpr int value = V; };

struct Params {
  const char* q; const char* k; const char* v; const int* bt; const int* lens; __hip_bfloat16* out; char* ws;
  unsigned kvbytes; unsigned unused;
};

__global__ void __launch_bounds__(256, 2) radix_attn_kernel(Params p) {
  extern __shared__ __attribute__((aligned(16))) char smem[];
  LAS char* lds = (LAS char*)smem;
  const int tid = threadIdx.x, lane = tid & 63, w = rfl(tid >> 6);
  const int gidx = blockIdx.x;
  const int hg = (gidx >> 3) & 3;
  const int jj = (((gidx & 7) + 4 * (hg & 1)) & 7) | ((gidx >> 5) << 3);
  const int c16 = lane & 15, g4 = lane >> 4;
#define STAMP(i) do { } while (0)
#define STAMP_FLUSH() do { } while (0)
  STAMP(0);
#define PROBE(k) do { } while (0)
#define PROBE_END() do { } while (0)
  const auto wsr = __builtin_amdgcn_make_buffer_rsrc(p.ws, 0, WS_END, 0x00020000);

  unsigned voffK[4], voffV[4], oK[4], oV[4];
  const int lenraw = p.lens[c16];
  const unsigned gvw = __builtin_amdgcn_raw_buffer_load_b32(wsr, WS_G + ((hg * 4 + w) * 16 + c16) * 4, 0, 16);
  const unsigned gvh = __builtin_amdgcn_raw_buffer_load_b32(wsr, WS_G + ((hg * 4 + (jj & 3)) * 16 + c16) * 4, 0, 16);
  const unsigned gp = __builtin_amdgcn_raw_buffer_load_b32(wsr, WS_G, 0, 16);
  int pm, len_l, minlen, vpages, incp, sump; bool zq_l;
  unsigned ptag;
  {
    const i32x4* bt4 = (const i32x4*)p.bt;
    const i32x4 r0 = bt4[lane];
    i32x4 rr[4];
#pragma unroll
    for (int i = 0; i < 4; ++i) rr[i] = bt4[(4 * w + i) * 64 + lane];
    __builtin_amdgcn_sched_barrier(0);
#pragma unroll
  for (int i = 0; i < 4; ++i) {
    const int t = 4 * w + i, u = 4 * i + g4;
    voffK[i] = g4 * 256 + ((c16 ^ t) << 4) + t * 4096 - i * 1024 + 4096;
    voffV[i] = g4 * 256 + ((c16 ^ (2 * (t & 7))) << 4) + t * 4096 - i * 1024 + 4096;
    oK[i] = u * 4096 + ((c16 ^ u) << 4) - i * 1024 + 4096;
    oV[i] = u * 4096 + ((c16 ^ (2 * (u & 7))) << 4) - i * 1024 + 4096;
  }
    asm volatile("" :: "v"(voffK[0]), "v"(voffK[1]), "v"(voffK[2]), "v"(voffK[3]), "v"(voffV[0]), "v"(voffV[1]), "v"(voffV[2]), "v"(voffV[3]),
                 "v"(oK[0]), "v"(oK[1]), "v"(oK[2]), "v"(oK[3]), "v"(oV[0]), "v"(oV[1]), "v"(oV[2]), "v"(oV[3]) : "memory");
    bool n0 = false, n1 = false, n2 = false, n3 = false;
#pragma unroll
    for (int i = 0; i < 4; ++i) {
      n0 |= rr[i][0] != r0[0]; n1 |= rr[i][1] != r0[1]; n2 |= rr[i][2] != r0[2]; n3 |= rr[i][3] != r0[3];
      *(LAS i32x4*)(lds + LDS_BT + ((4 * w + i) * 256 + 4 * lane) * 4) = rr[i];
    }
    zq_l = lenraw <= 0;
    len_l = zq_l ? LK : min(lenraw, LK);
    int mn = len_l;
    mn = min(mn, dppi_keep<0x128>(mn, mn)); mn = min(mn, dppi_keep<0x124>(mn, mn));
    mn = min(mn, dppi_keep<0x122>(mn, mn)); mn = min(mn, dppi_keep<0x121>(mn, mn));
    minlen = rfl(mn);
    vpages = (len_l + BS - 1) >> 4;
    incp = vpages;
    incp += dppi<0x111>(incp); incp += dppi<0x112>(incp); incp += dppi<0x114>(incp); incp += dppi<0x118>(incp);
    sump = rl(incp, 15);
    ptag = (unsigned)rfl((int)gp) + 1u;
    asm volatile("" :: "v"(gvw), "v"(gvh));
    const int fc = n0 ? 0 : n1 ? 1 : n2 ? 2 : n3 ? 3 : 4;
    const unsigned long long mm = __ballot(fc < 4);
    int col = NBLK;
    if (mm) { const int fl = __builtin_ctzll(mm); col = 4 * fl + rl(fc, fl); }
    if (lane == 0) *(LAS int*)(lds + LDS_MISC + 4 * w) = col;
    asm volatile("s_waitcnt lgkmcnt(0)\n\ts_barrier" ::: "memory");
    PROBE(1);
    const i32x4 mc = *(LAS i32x4*)(lds + LDS_MISC);
    pm = rfl(min(min(mc[0], mc[1]), min(mc[2], mc[3])));
  }
  const int P = min(pm, minlen >> 4);
  const int tsh = P * BS;
  const int vL = vpages - P;
  const int vS = incp - vpages - c16 * P;
  const int N = sump - 16 * P;
  int kH = 0;
  if (P > 0) kH = min(min(15, P), max(1, rfl((int)__builtin_ceilf((float)P * (1024.f / HRHO) * __builtin_amdgcn_rcpf((float)(P + N))))));
  const int nW = 64 - 4 * kH;
  const float inv_nw = __builtin_amdgcn_rcpf((float)nW);
  const unsigned zmask = (unsigned)(__ballot(zq_l) & 0xffffull);
  const bool isH = jj < 4 * kH;
  const float t_all = (float)(P + N) * (16.f * 819.2f / (float)PACE);
  const unsigned long long t_ref = __builtin_amdgcn_s_memrealtime();
  constexpr int A_S0 = 3, A_S1 = 11;
  const int m_avg = rfl((int)(((float)(N + nW - 1) + 0.5f) * inv_nw));
  const bool adapt_on = nW >= 24 && m_avg >= A_S1 + 8;
  auto read_progress = [&](unsigned ldsoff, float& t1, float& d) -> bool {
    const u32x4 e = *(LAS u32x4*)(lds + ldsoff + c16 * 16);
    const bool okv = e[1] == ptag && e[3] == ptag;
    const int cnt = __builtin_popcount((unsigned)__ballot(okv) & 0xffffu);
    float a = okv ? (float)(int)(e[0] - (unsigned)t_ref) : 0.f, c = okv ? (float)e[2] : 0.f;
    a += __builtin_bit_cast(float, dppi_keep<0x128>(0, __builtin_bit_cast(int, a))); c += __builtin_bit_cast(float, dppi_keep<0x128>(0, __builtin_bit_cast(int, c)));
    a += __builtin_bit_cast(float, dppi_keep<0x124>(0, __builtin_bit_cast(int, a))); c += __builtin_bit_cast(float, dppi_keep<0x124>(0, __builtin_bit_cast(int, c)));
    a += __builtin_bit_cast(float, dppi_keep<0x122>(0, __builtin_bit_cast(int, a))); c += __builtin_bit_cast(float, dppi_keep<0x122>(0, __builtin_bit_cast(int, c)));
    a += __builtin_bit_cast(float, dppi_keep<0x121>(0, __builtin_bit_cast(int, a))); c += __builtin_bit_cast(float, dppi_keep<0x121>(0, __builtin_bit_cast(int, c)));
    if (cnt < 8) return false;
    const float ic = 1.f / (float)cnt;
    t1 = rlf(a, 0) * ic; d = rlf(c, 0) * ic * (1.f / (A_S1 - A_S0));
    return true;
  };
  const int r_lo = (int)(((float)(lane * N) + 0.5f) * inv_nw), r_hi = (int)(((float)((lane + 1) * N) + 0.5f) * inv_nw);

  if (isH) {
    const int h = hg * 4 + (jj & 3), part = jj >> 2;
    const int p0 = (P * part) / kH, p1 = (P * (part + 1)) / kH;
#pragma unroll
    for (int i = 0; i < 2; ++i)
      dma16(p.q + (2 * w + i) * 16384 + h * 512, (lane >> 5) * 8192 + (lane & 31) * 16, LDS_QB + (2 * w + i) * 1024);
    const char* kb = p.k + h * 256 - 4096;
    const char* vb = p.v + h * 256 - 4096;
    const int nt = max(0, (p1 - p0 - w + 3) >> 2);
    const unsigned ring = w * (RH * 8192);
    int pg0 = 0, vpg = *(LAS int*)(lds + LDS_BT + min(p0 + w + 4 * lane, NBLK - 1) * 4);
    float pace_h = t_all * (PACE_H * 0.01f) * __builtin_amdgcn_rcpf((float)max(nt, 1)), pace_hb = -3.f * pace_h;
    float t_pub = t_all * (HPUB_AT * 0.01f);
    auto issue = [&](int t) {
      { const int due = rfl((int)(pace_hb + (float)t * pace_h)); if (due > 0) while ((int)(__builtin_amdgcn_s_memrealtime() - t_ref) < due) __builtin_amdgcn_s_sleep(1); }
      if (t - pg0 == 64) { pg0 = t; vpg = *(LAS int*)(lds + LDS_BT + min(p0 + w + 4 * (t + lane), NBLK - 1) * 4); }
      const size_t pb = (size_t)(unsigned)rl(vpg, t - pg0) * 65536;
      const unsigned dst = ring + (t & (RH - 1)) * 8192;
      dma_stage(kb + pb, vb + pb, dst, dst + 4096, oK[0], oK[1], oK[2], oK[3], oV[0], oV[1], oV[2], oV[3]);
    };
    const int npre = min(nt, RH - 1);
#pragma unroll 1
    for (int t = 0; t < npre; ++t) issue(t);
    if (npre == 3) asm volatile("s_waitcnt vmcnt(24)\n\ts_barrier" ::: "memory");
    else asm volatile("s_waitcnt vmcnt(0)\n\ts_barrier" ::: "memory");
    bf16x8 qf[2][4];
#pragma unroll
    for (int qb = 0; qb < 2; ++qb) {
      const bool z = (zmask >> (8 * qb + (c16 >> 1))) & 1;
#pragma unroll
      for (int s = 0; s < 4; ++s) {
        u32x4 x = *(LAS u32x4*)(lds + LDS_QB + (16 * qb + c16) * 256 + (4 * s + g4) * 16);
        if (z) x = u32x4{0u, 0u, 0u, 0u};
        qf[qb][s] = __builtin_bit_cast(bf16x8, x);
      }
    }
    f32x4 acc[2][8];
    float m[2] = {-INFINITY, -INFINITY}, l[2] = {0.f, 0.f};
#pragma unroll
    for (int qb = 0; qb < 2; ++qb)
#pragma unroll
      for (int k = 0; k < 8; ++k) acc[qb][k] = f32x4{0.f, 0.f, 0.f, 0.f};
    const unsigned ka0 = ring + c16 * 256 + ((g4 ^ c16) << 4);
    unsigned va0;
    { const int tq = 4 * g4 + (c16 >> 2), pp = lane & 3; va0 = ring + 4096 + tq * 256 + ((tq & 7) << 5) + ((pp >> 1) << 4) + ((pp & 1) << 3); }
#pragma unroll 1
    for (int t = 0; t < nt; ++t) {
      const int young = min(nt - 1 - t, RH - 2);
      if (young == 2) asm volatile("s_waitcnt vmcnt(16) lgkmcnt(0)" ::: "memory");
      else if (young == 1) asm volatile("s_waitcnt vmcnt(8) lgkmcnt(0)" ::: "memory");
      else asm volatile("s_waitcnt vmcnt(0) lgkmcnt(0)" ::: "memory");
      if (adapt_on && nt >= 12) {
        const int tp = (nt * 9) >> 4;
        if (t == tp + 3) {
          float t1, d;
          if (read_progress(LDS_PROG + w * 256, t1, d)) {
            const float t_end = t1 + (float)(m_avg - A_S1) * d * (A_F * 0.001f);
            const float now = (float)(int)(__builtin_amdgcn_s_memrealtime() - t_ref);
            const int t_i = t + RH - 1;
            pace_h = fmaxf((t_end - A_HMARGIN - now) / (float)max(nt - t_i, 1), 0.f); pace_hb = now - (float)t_i * pace_h;
            t_pub = t_end - A_HPUB;
          }
        }
      }
      if (t + RH - 1 < nt) issue(t + RH - 1);
      if (adapt_on && nt >= 12 && t == ((nt * 9) >> 4) && lane < 16) dma16_sc1(p.ws + WS_PROG, lane * 16, LDS_PROG + w * 256);
      const unsigned sb = (t & (RH - 1)) * 8192;
      tile_compute<2>(lds, ka0 + sb, va0 + sb, qf, acc, m, l, 0, 16, g4);
    }
    STAMP(2);
    constexpr unsigned ROWB = 528, WAVEB = 32 * ROWB, MLB = 4 * WAVEB;
    asm volatile("s_waitcnt lgkmcnt(0)\n\ts_barrier" ::: "memory");
#pragma unroll
    for (int qb = 0; qb < 2; ++qb) {
      const float lt = xg_sum(l[qb]);
#pragma unroll
      for (int k = 0; k < 8; ++k) *(LAS f32x4*)(lds + w * WAVEB + (16 * qb + c16) * ROWB + (16 * k + 4 * g4) * 4) = acc[qb][k];
      if (g4 == 0) *(LAS f32x2*)(lds + MLB + (w * 32 + 16 * qb + c16) * 8) = f32x2{m[qb] * C2, lt};
    }
    asm volatile("s_waitcnt lgkmcnt(0)\n\ts_barrier" ::: "memory");
    while ((int)(__builtin_amdgcn_s_memrealtime() - t_ref) < (int)t_pub) __builtin_amdgcn_s_sleep(8);
    const unsigned slot = WS_HP + (part * 16 + h) * HP_W;
    float mrow = 0.f, lrow = 0.f;
#pragma unroll
    for (int rr = 0; rr < 8; ++rr) {
      const int row = 8 * w + rr;
      f32x2 ml[4], o[4];
#pragma unroll
      for (int v = 0; v < 4; ++v) { ml[v] = *(LAS f32x2*)(lds + MLB + (v * 32 + row) * 8); o[v] = *(LAS f32x2*)(lds + v * WAVEB + row * ROWB + lane * 8); }
      const float M = fmaxf(fmaxf(ml[0][0], ml[1][0]), fmaxf(ml[2][0], ml[3][0]));
      float L = 0.f, o0 = 0.f, o1 = 0.f;
#pragma unroll
      for (int v = 0; v < 4; ++v) {
        const float e = ml[v][1] > 0.f ? __builtin_amdgcn_exp2f(ml[v][0] - M) : 0.f;
        L = fmaf(ml[v][1], e, L); o0 = fmaf(o[v][0], e, o0); o1 = fmaf(o[v][1], e, o1);
      }
      const unsigned tag = (unsigned)rl((int)gvh, row >> 1) + 1u;
      const u32x4 gr = {__float_as_uint(o0), tag, __float_as_uint(o1), tag};
      __builtin_amdgcn_raw_buffer_store_b128(gr, wsr, slot + row * 1024 + lane * 16, 0, 16);
      if (lane == rr) { mrow = M; lrow = L; }
    }
    const unsigned tagr = (unsigned)__shfl((int)gvh, ((8 * w + lane) >> 1) & 15) + 1u;
    if (lane < 8) {
      const unsigned tag = tagr;
      const u32x4 gm = {__float_as_uint(mrow), tag, __float_as_uint(lrow), tag};
      __builtin_amdgcn_raw_buffer_store_b128(gm, wsr, slot + 32768 + (8 * w + lane) * 16, 0, 16);
    }
    STAMP(1);
    STAMP(3);
    STAMP_FLUSH();
    return;
  }

  const int j = jj - 4 * kH, h = hg * 4 + w;
  const int x0 = rl(r_lo, j), x1 = rl(r_hi, j);
  const unsigned umask = (unsigned)(__ballot(max(x0, vS) < min(x1, vS + vL)) & 0xffffull);
  const unsigned kaddr0 = w * 256 + c16 * 1024 + ((g4 ^ c16) << 4);
  unsigned vaddr0;
  { const int tq = 4 * g4 + (c16 >> 2), pp = lane & 3; vaddr0 = 16384 + tq * 1024 + w * 256 + ((tq & 7) << 5) + ((pp >> 1) << 4) + ((pp & 1) << 3); }
  const char* kbase = p.k + hg * 1024 - 4096;
  const char* vbase = p.v + hg * 1024 - 4096;

  unsigned i_mask = umask; int i_row = 0, i_t0 = 0, i_t1 = 0, i_page; bool i_more = false;
  int n_iss = 0, i_slot = 0;
  auto unit_range = [&](int b, int& t0, int& t1) {
    const int s = rl(vS, b), L = rl(vL, b);
    t0 = (P + max(x0, s) - s) * BS; t1 = min((P + min(x1, s + L) - s) * BS, rl(len_l, b));
  };
  auto q_dma = [&](int b, int slot) {
    if (lane < 32) dma16(p.q + b * 8192 + h * 512, lane * 16, LDS_QB + (w * R + slot) * 512);
  };
  if (i_mask) {
    i_row = __builtin_ctz(i_mask); i_mask &= i_mask - 1; unit_range(i_row, i_t0, i_t1);
    q_dma(i_row, 0);
    i_more = true;
  }
  i_page = i_t0 >> 4;
  int i_pg0 = i_page, vpg = *(LAS int*)(lds + LDS_BT + (i_row * NBLK + min(i_page + lane, NBLK - 1)) * 4);
  int pace_tok = 0;
  float pace_d = t_all * __builtin_amdgcn_rcpf((float)max(x1 - x0, 1)), pace_b = -(float)PACE_LEAD * pace_d;
  const int rep = (adapt_on && hg < 2 && j >= (nW >> 1) && j < (nW >> 1) + 8) ? hg * 8 + j - (nW >> 1) : -1;
  unsigned t_s0 = 0;
  auto issue_stage = [&]() {
    const int phys = rl(vpg, i_page - i_pg0);
    const int hi = i_t1 - i_page * 16;
    {
      if (adapt_on && pace_tok == A_S1 + 3 + R - 1) {
        float t1, d;
        if (read_progress(LDS_PROG, t1, d)) { pace_d = d * (A_F * 0.001f); pace_b = t1 - (float)A_S1 * pace_d - A_LEAD; }
      }
      const int due = rfl((int)(pace_b + (float)pace_tok * pace_d));
      pace_tok += 1;
      if (due > 0) while ((int)(__builtin_amdgcn_s_memrealtime() - t_ref) < due) __builtin_amdgcn_s_sleep(1);
    }
    const size_t pb = (size_t)(unsigned)phys * 65536;
    const unsigned dst = i_slot * STAGE + 4 * w * 1024;
    if (hi >= 16) {
      dma_stage(kbase + pb, vbase + pb, dst, dst + 16384, voffK[0], voffK[1], voffK[2], voffK[3], voffV[0], voffV[1], voffV[2], voffV[3]);
    } else {
      unsigned ok[4], ov[4];
#pragma unroll
      for (int i = 0; i < 4; ++i) {
        const int d = (min(4 * w + i, hi - 1) - (4 * w + i)) * 4096;
        ok[i] = voffK[i] + d; ov[i] = voffV[i] + d;
      }
      dma_stage(kbase + pb, vbase + pb, dst, dst + 16384, ok[0], ok[1], ok[2], ok[3], ov[0], ov[1], ov[2], ov[3]);
    }
    if (adapt_on) {
      const int k = pace_tok - 1;
      if (rep >= 0 && w == 3) {
        if (k == A_S0) t_s0 = (unsigned)__builtin_amdgcn_s_memrealtime();
        if (k == A_S1 && lane == 0) {
          const unsigned now = (unsigned)__builtin_amdgcn_s_memrealtime();
          const u32x4 e = {now, ptag, now - t_s0, ptag};
          __builtin_amdgcn_raw_buffer_store_b128(e, wsr, WS_PROG + rep * 16, 0, 16);
        }
      }
      if (k == A_S1 + 3 && w == 0 && lane < 16) dma16_sc1(p.ws + WS_PROG, lane * 16, LDS_PROG);
    }
    ++n_iss; i_slot = (i_slot + 1 == R) ? 0 : i_slot + 1;
    ++i_page;
    if (i_page * 16 >= i_t1) {
      if (i_mask) {
        i_row = __builtin_ctz(i_mask); i_mask &= i_mask - 1; unit_range(i_row, i_t0, i_t1); i_page = i_t0 >> 4;
        q_dma(i_row, i_slot);
        i_pg0 = i_page; vpg = *(LAS int*)(lds + LDS_BT + (i_row * NBLK + min(i_page + lane, NBLK - 1)) * 4);
      } else i_more = false;
    } else if (i_page - i_pg0 == 64) {
      i_pg0 = i_page; vpg = *(LAS int*)(lds + LDS_BT + (i_row * NBLK + min(i_page + lane, NBLK - 1)) * 4);
    }
  };
  PROBE(2);
#pragma unroll 1
  for (int s = 0; s < R - 1; ++s) if (i_more) issue_stage();

  unsigned own = 0;
  {
    unsigned mrem = umask;
    while (mrem) {
      const int b = __builtin_ctz(mrem); mrem &= mrem - 1;
      const int s = rl(vS, b), L = rl(vL, b);
      unsigned long long pmk = __ballot(lane < nW && max(r_lo, s) < min(r_hi, s + L));
      int idx = w % __builtin_popcountll(pmk);
      while (idx--) pmk &= pmk - 1;
      if (__builtin_ctzll(pmk) == j) own |= 1u << b;
    }
    unsigned zl = (unsigned)(__ballot(vL == 0) & 0xffffull);
    while (zl) { const int b = __builtin_ctz(zl); zl &= zl - 1; if ((b * 4) % nW == j) own |= 1u << b; }
  }

  int fb = -1, fcnt = 0; unsigned vrow = 0, vmlo = 0;
  if (own && (own & (own - 1)) == 0) {
    const int b = __builtin_ctz(own);
    const unsigned above = umask >> b;
    if ((umask >> b & 1) && (above == 1 || __builtin_popcount(above) == 2)) {
      const int s = rl(vS, b), L = rl(vL, b);
      const unsigned long long pmk = __ballot(lane < nW && max(r_lo, s) < min(r_hi, s + L)) & ~(1ull << j);
      const int np = __builtin_popcountll(pmk);
      if (np + kH <= 8 && np + kH > 0) {
        fb = b; fcnt = np + kH;
        const int rank = __builtin_amdgcn_mbcnt_hi((unsigned)(pmk >> 32), __builtin_amdgcn_mbcnt_lo((unsigned)pmk, 0));
        const unsigned prow = WS_PP + ((hg * 80 + lane + b) * 4 + w) * PP_W;
        const unsigned pushed = (unsigned)__builtin_amdgcn_ds_permute((((pmk >> lane) & 1) ? rank : 63) << 2, (int)prow);
        const unsigned hb = WS_HP + (min(max(lane - np, 0), 15) * 16 + h) * HP_W;
        vrow = lane < np ? pushed : hb + b * 2048;
        const unsigned mlb = lane < np ? pushed + 2048 : hb + 32768 + b * 32;
        vmlo = (unsigned)__shfl((int)mlb, min(lane >> 1, fcnt - 1)) + (lane & 1) * 16;
      }
    }
  }

  int n_comp = 0, c_slot = 0;
  unsigned c_mask = umask;
  float ro[2][2] = {{0.f, 0.f}, {0.f, 0.f}}, rm[2] = {-INFINITY, -INFINITY}, rs[2] = {0.f, 0.f};
  float ho[2][2] = {{0.f, 0.f}, {0.f, 0.f}}, hm[2] = {-INFINITY, -INFINITY}, hs[2] = {0.f, 0.f};
  int last_b = -1, held_b = -1;
  auto publish = [&](int b, const float (&o)[2][2], const float (&mm)[2], const float (&ll)[2]) {
    const unsigned tag = (unsigned)rl((int)gvw, b) + 1u;
    const unsigned slot = WS_PP + ((hg * 80 + j + b) * 4 + w) * PP_W;
    const u32x4 g0 = {__float_as_uint(o[0][0]), tag, __float_as_uint(o[0][1]), tag};
    const u32x4 g1 = {__float_as_uint(o[1][0]), tag, __float_as_uint(o[1][1]), tag};
    __builtin_amdgcn_raw_buffer_store_b128(g0, wsr, slot + lane * 16, 0, 16);
    __builtin_amdgcn_raw_buffer_store_b128(g1, wsr, slot + 1024 + lane * 16, 0, 16);
    if (lane < 2) {
      const u32x4 gm = {__float_as_uint(lane ? mm[1] : mm[0]), tag, __float_as_uint(lane ? ll[1] : ll[0]), tag};
      __builtin_amdgcn_raw_buffer_store_b128(gm, wsr, slot + 2048 + lane * 16, 0, 16);
    }
  };
  while (c_mask) {
    const int b = __builtin_ctz(c_mask); c_mask &= c_mask - 1;
    int t0, t1; unit_range(b, t0, t1);
    bf16x8 qf[1][4];
    f32x4 acc[1][8];
    float m[1] = {-INFINITY}, l[1] = {0.f};
#pragma unroll
    for (int k = 0; k < 8; ++k) acc[0][k] = f32x4{0.f, 0.f, 0.f, 0.f};
    bool first = true;
    for (int page = t0 >> 4; page * 16 < t1; ++page) {
      {
        const int young = n_iss - n_comp - 1;
        if (R == 4 && young >= 2) asm volatile("s_waitcnt vmcnt(16) lgkmcnt(0)\n\ts_barrier" ::: "memory");
        else if (young >= 1) asm volatile("s_waitcnt vmcnt(8) lgkmcnt(0)\n\ts_barrier" ::: "memory");
        else asm volatile("s_waitcnt vmcnt(0) lgkmcnt(0)\n\ts_barrier" ::: "memory");
      }
      PROBE(3);
      if (first) {
        const bool z = (c16 >= 2) || ((zmask >> b) & 1);
#pragma unroll
        for (int s = 0; s < 4; ++s) {
          u32x4 x = *(LAS u32x4*)(lds + LDS_QB + (w * R + c_slot) * 512 + (c16 & 1) * 256 + (4 * s + g4) * 16);
          if (z) x = u32x4{0u, 0u, 0u, 0u};
          qf[0][s] = __builtin_bit_cast(bf16x8, x);
        }
        asm volatile("s_waitcnt lgkmcnt(0)" ::: "memory");
        first = false;
      }
      if (i_more) issue_stage();
      const unsigned sb = c_slot * STAGE;
      tile_compute<1>(lds, kaddr0 + sb, vaddr0 + sb, qf, acc, m, l, 0, t1 - page * 16, g4);
      ++n_comp; c_slot = (c_slot + 1 == R) ? 0 : c_slot + 1;
    }
    const float lt = xg_sum(l[0]);
    LAS char* sc = lds + LDS_SC + w * 1024;
    if (c16 < 2) {
#pragma unroll
      for (int k = 0; k < 8; ++k) *(LAS f32x4*)(sc + c16 * 512 + (16 * k + 4 * g4) * 4) = acc[0][k];
    }
    asm volatile("s_waitcnt lgkmcnt(0)" ::: "memory");
    const f32x2 a0 = *(LAS f32x2*)(sc + lane * 8), a1 = *(LAS f32x2*)(sc + 512 + lane * 8);
    if (last_b >= 0) {
      if (held_b >= 0) publish(held_b, ho, hm, hs);
      held_b = last_b;
#pragma unroll
      for (int r = 0; r < 2; ++r) { ho[r][0] = ro[r][0]; ho[r][1] = ro[r][1]; hm[r] = rm[r]; hs[r] = rs[r]; }
    }
    ro[0][0] = a0[0]; ro[0][1] = a0[1]; ro[1][0] = a1[0]; ro[1][1] = a1[1];
    rm[0] = rlf(m[0], 0) * C2; rm[1] = rlf(m[0], 1) * C2;
    rs[0] = rlf(lt, 0); rs[1] = rlf(lt, 1);
    last_b = b;
  }
  STAMP(2);
  PROBE(5);
  if (last_b >= 0 && !((own >> last_b) & 1)) { publish(last_b, ro, rm, rs); last_b = -1; }
  if (held_b >= 0 && !((own >> held_b) & 1)) { publish(held_b, ho, hm, hs); held_b = -1; }
  STAMP(1);

  if (own) { const unsigned long long c0 = __builtin_amdgcn_s_memrealtime(); while (__builtin_amdgcn_s_memrealtime() - c0 < MDELAY) __builtin_amdgcn_s_sleep(1); }
  if (fb >= 0) {
    const int b = fb;
    const unsigned tag = (unsigned)rl((int)gvw, b) + 1u;
    const bool mine_last = b == last_b;
    const float om0 = mine_last ? rm[0] : hm[0], om1 = mine_last ? rm[1] : hm[1], ol0 = mine_last ? rs[0] : hs[0], ol1 = mine_last ? rs[1] : hs[1];
    const float oo00 = mine_last ? ro[0][0] : ho[0][0], oo01 = mine_last ? ro[0][1] : ho[0][1];
    const float oo10 = mine_last ? ro[1][0] : ho[1][0], oo11 = mine_last ? ro[1][1] : ho[1][1];
    auto attempt = [&](auto nb_tag) -> bool {
      constexpr int NB = decltype(nb_tag)::value;
      u32x4 d0[NB], d1[NB];
      unsigned so[NB];
#pragma unroll
      for (int k = 0; k < NB; ++k) so[k] = (unsigned)rl((int)vrow, min(k, fcnt - 1));
      asm volatile("" ::: "memory");
      const u32x4 dm = __builtin_amdgcn_raw_buffer_load_b128(wsr, vmlo, 0, 16);
#pragma unroll
      for (int k = 0; k < NB; ++k) {
        d0[k] = __builtin_amdgcn_raw_buffer_load_b128(wsr, lane * 16, so[k], 16);
        d1[k] = __builtin_amdgcn_raw_buffer_load_b128(wsr, 1024 + lane * 16, so[k], 16);
      }
      __builtin_amdgcn_sched_barrier(0);
      const bool live = lane < 2 * fcnt;
      const float mk = live ? __uint_as_float(dm[0]) : -INFINITY, lk = live ? __uint_as_float(dm[2]) : 0.f;
      const float om = (lane & 1) ? om1 : om0, ol = (lane & 1) ? ol1 : ol0;
      float M = fmaxf(mk, om);
      M = fmaxf(M, __builtin_bit_cast(float, dppi_keep<0x122>(__builtin_bit_cast(int, M), __builtin_bit_cast(int, M))));
      M = fmaxf(M, __builtin_bit_cast(float, dppi_keep<0x124>(__builtin_bit_cast(int, M), __builtin_bit_cast(int, M))));
      M = fmaxf(M, __builtin_bit_cast(float, dppi_keep<0x128>(__builtin_bit_cast(int, M), __builtin_bit_cast(int, M))));
      const float e = __builtin_amdgcn_exp2f(mk - M), eo = __builtin_amdgcn_exp2f(om - M);
      float Ls = lk * e;
      Ls += __builtin_bit_cast(float, dppi_keep<0x122>(0, __builtin_bit_cast(int, Ls)));
      Ls += __builtin_bit_cast(float, dppi_keep<0x124>(0, __builtin_bit_cast(int, Ls)));
      Ls += __builtin_bit_cast(float, dppi_keep<0x128>(0, __builtin_bit_cast(int, Ls)));
      Ls = fmaf(ol, eo, Ls);
      const float inv = __builtin_amdgcn_rcpf(Ls);
      const float en = e * inv, eon = eo * inv;
      const float eo0 = rlf(eon, 0), eo1 = rlf(eon, 1);
      float o00 = oo00 * eo0, o01 = oo01 * eo0, o10 = oo10 * eo1, o11 = oo11 * eo1;
      unsigned bad = (dm[1] ^ tag) | (dm[3] ^ tag);
#pragma unroll
      for (int k = 0; k < NB; ++k) {
        const float w0 = rlf(en, 2 * k), w1 = rlf(en, 2 * k + 1);
        bad |= ((d0[k][1] ^ tag) | (d0[k][3] ^ tag)) | ((d1[k][1] ^ tag) | (d1[k][3] ^ tag));
        o00 = fmaf(__uint_as_float(d0[k][0]), w0, o00); o01 = fmaf(__uint_as_float(d0[k][2]), w0, o01);
        o10 = fmaf(__uint_as_float(d1[k][0]), w1, o10); o11 = fmaf(__uint_as_float(d1[k][2]), w1, o11);
      }
      if (__any(bad != 0)) return false;
      __hip_bfloat16* orow = p.out + (b * NH + 2 * h) * DH + 2 * lane;
      *(unsigned*)orow = pk_bf16(o00, o01);
      *(unsigned*)(orow + DH) = pk_bf16(o10, o11);
      if (lane == 0) __builtin_amdgcn_raw_buffer_store_b32(tag, wsr, WS_G + (h * 16 + b) * 4, 0, GAUX);
      return true;
    };
#pragma unroll 1
    for (int t = 0; t < 16; ++t) {
      if (t >= FASTTRY) {
        unsigned spins = 0;
        while (true) {
          asm volatile("" ::: "memory");
          const u32x4 x = __builtin_amdgcn_raw_buffer_load_b128(wsr, vmlo, 0, 16);
          if (!__any(x[1] != tag || x[3] != tag) || ++spins > (1u << 16)) break;
          __builtin_amdgcn_s_sleep(SLEEP2);
        }
      }
      const bool ok = fcnt <= 5 ? attempt(IntC<5>{}) : attempt(IntC<8>{});
      if (ok) { own = 0; break; }
    }
  }
  while (own) {
    const int b = __builtin_ctz(own); own &= own - 1;
    const unsigned tag = (unsigned)rl((int)gvw, b) + 1u;
    const int s = rl(vS, b), L = rl(vL, b);
    unsigned long long pmk = __ballot(lane < nW && max(r_lo, s) < min(r_hi, s + L));
    unsigned smk = (1u << kH) - 1u;
    float M0 = -INFINITY, M1 = -INFINITY, L0 = 0.f, L1 = 0.f, o00 = 0.f, o01 = 0.f, o10 = 0.f, o11 = 0.f;
    if (b == last_b) {
      pmk &= ~(1ull << j);
      M0 = rm[0]; M1 = rm[1]; L0 = rs[0]; L1 = rs[1]; o00 = ro[0][0]; o01 = ro[0][1]; o10 = ro[1][0]; o11 = ro[1][1];
    } else if (b == held_b) {
      pmk &= ~(1ull << j);
      M0 = hm[0]; M1 = hm[1]; L0 = hs[0]; L1 = hs[1]; o00 = ho[0][0]; o01 = ho[0][1]; o10 = ho[1][0]; o11 = ho[1][1];
    }
    const unsigned p_rows = WS_PP + ((hg * 80 + lane + b) * 4 + w) * PP_W;
    const unsigned s_base = WS_HP + ((lane & 15) * 16 + h) * HP_W;
    unsigned spins = 0;
    bool light = false;
    while (pmk | smk) {
      asm volatile("" ::: "memory");
      unsigned long long tp = pmk; unsigned ts = smk;
      const bool single = (__builtin_popcountll(pmk) + __builtin_popcount(smk)) == 1;
      if (light && !single) {
        const int jp = pmk ? (((pmk >> lane) & 1) ? lane : __builtin_ctzll(pmk)) : 0;
        const int js = smk ? (((smk >> (lane & 15)) & 1) ? (lane & 15) : __builtin_ctz(smk)) : 0;
        u32x4 a = {0u, tag, 0u, tag}, c = {0u, tag, 0u, tag};
        if (pmk) a = __builtin_amdgcn_raw_buffer_load_b128(wsr, WS_PP + ((hg * 80 + jp + b) * 4 + w) * PP_W + 2048 + 16, 0, 16);
        if (smk) c = __builtin_amdgcn_raw_buffer_load_b128(wsr, WS_HP + (js * 16 + h) * HP_W + 32768 + (2 * b + 1) * 16, 0, 16);
        tp &= __ballot(a[1] == tag && a[3] == tag);
        ts &= (unsigned)__ballot(c[1] == tag && c[3] == tag);
      }
      bool progress = false;
      if (tp | ts) {
        auto batch = [&](auto nb_tag) {
          constexpr int NB = decltype(nb_tag)::value;
          unsigned rows[NB], mls[NB]; unsigned long long bitp[NB]; unsigned bits[NB];
#pragma unroll
          for (int k = 0; k < NB; ++k) {
            if (tp) { const int q = __builtin_ctzll(tp); bitp[k] = tp & (~tp + 1); bits[k] = 0; tp &= tp - 1; rows[k] = (unsigned)rl((int)p_rows, q); mls[k] = rows[k] + 2048; }
            else if (ts) { const int q = __builtin_ctz(ts); bits[k] = ts & (~ts + 1); bitp[k] = 0; ts &= ts - 1;
                           const unsigned sl = (unsigned)rl((int)s_base, q); rows[k] = sl + b * 2048; mls[k] = sl + 32768 + b * 32; }
            else { bitp[k] = 0; bits[k] = 0; rows[k] = rows[0]; mls[k] = mls[0]; }
          }
          u32x4 d0[NB], d1[NB], dm[NB];
          if (R0AUX != 16 && spins == 0) {
#pragma unroll
            for (int k = 0; k < NB; ++k) {
              d0[k] = __builtin_amdgcn_raw_buffer_load_b128(wsr, lane * 16, rows[k], R0AUX);
              d1[k] = __builtin_amdgcn_raw_buffer_load_b128(wsr, 1024 + lane * 16, rows[k], R0AUX);
              dm[k] = __builtin_amdgcn_raw_buffer_load_b128(wsr, (lane & 1) * 16, mls[k], R0AUX);
            }
          } else {
#pragma unroll
            for (int k = 0; k < NB; ++k) {
              d0[k] = __builtin_amdgcn_raw_buffer_load_b128(wsr, lane * 16, rows[k], 16);
              d1[k] = __builtin_amdgcn_raw_buffer_load_b128(wsr, 1024 + lane * 16, rows[k], 16);
              dm[k] = __builtin_amdgcn_raw_buffer_load_b128(wsr, (lane & 1) * 16, mls[k], 16);
            }
          }
#pragma unroll
          for (int k = 0; k < NB; ++k) {
            const bool ok = d0[k][1] == tag && d0[k][3] == tag && d1[k][1] == tag && d1[k][3] == tag && dm[k][1] == tag && dm[k][3] == tag;
            if ((bitp[k] | bits[k]) && __all(ok)) {
              const float mv = __uint_as_float(dm[k][0]), lv = __uint_as_float(dm[k][2]);
              const float m0 = rlf(mv, 0), m1 = rlf(mv, 1), l0 = rlf(lv, 0), l1 = rlf(lv, 1);
              const float n0 = fmaxf(M0, m0), n1 = fmaxf(M1, m1);
              const float a0 = __builtin_amdgcn_exp2f(M0 - n0), b0 = __builtin_amdgcn_exp2f(m0 - n0);
              const float a1 = __builtin_amdgcn_exp2f(M1 - n1), b1 = __builtin_amdgcn_exp2f(m1 - n1);
              L0 = L0 * a0 + l0 * b0; L1 = L1 * a1 + l1 * b1;
              o00 = o00 * a0 + __uint_as_float(d0[k][0]) * b0; o01 = o01 * a0 + __uint_as_float(d0[k][2]) * b0;
              o10 = o10 * a1 + __uint_as_float(d1[k][0]) * b1; o11 = o11 * a1 + __uint_as_float(d1[k][2]) * b1;
              M0 = n0; M1 = n1;
              pmk &= ~bitp[k]; smk &= ~bits[k];
              progress = true;
            }
          }
        };
        const int cnt = __builtin_popcountll(tp) + __builtin_popcount(ts);
        if (cnt == 1) batch(IntC<1>{}); else if (cnt <= 3) batch(IntC<3>{}); else batch(IntC<6>{});

      }
      if (!progress) {
        if (++spins > (1u << 16)) break;
        if (spins > FULLR) { light = true; __builtin_amdgcn_s_sleep(SLEEP); }
      }
    }
    const float i0 = 1.f / L0, i1 = 1.f / L1;
    __hip_bfloat16* orow = p.out + (b * NH + 2 * h) * DH + 2 * lane;
    *(unsigned*)orow = pk_bf16(o00 * i0, o01 * i0);
    *(unsigned*)(orow + DH) = pk_bf16(o10 * i1, o11 * i1);
    if (lane == 0) __builtin_amdgcn_raw_buffer_store_b32(tag, wsr, WS_G + (h * 16 + b) * 4, 0, GAUX);
  }
  STAMP(3);
  STAMP_FLUSH();
  PROBE(6);
  PROBE_END();
}

}

extern "C" void kernel_launch(void* const* d_in, const int* in_sizes, int n_in, void* d_out, int out_size, void* d_ws, size_t ws_size,
                              hipStream_t stream) {
  using namespace rattn;
  Params p;
  p.q = (const char*)d_in[0]; p.k = (const char*)d_in[1]; p.v = (const char*)d_in[2];
  p.bt = (const int*)d_in[3]; p.lens = (const int*)d_in[4];
  p.out = (__hip_bfloat16*)d_out; p.ws = (char*)d_ws;
  p.kvbytes = (unsigned)in_sizes[1] * 2u; p.unused = 0;
  (void)hipFuncSetAttribute((const void*)radix_attn_kernel, hipFuncAttributeMaxDynamicSharedMemorySize, LDS_TOTAL);
  hipLaunchKernelGGL(radix_attn_kernel, dim3(256), dim3(256), LDS_TOTAL, stream, p);
}
```

```cpp
#include <hip/hip_runtime.h>
#include <hip/hip_bf16.h>
#include <stdint.h>

namespace rattn {

constexpr int NSEQ = 16, NH = 32, NKV = 16, DH = 128, NBLK = 256, BS = 16;
constexpr int LK = NBLK * BS;
#define GAUX 0
#define KVW_LANES 3
#define KVW_SHIFT 21
#define HRHO 50
#define MDELAY 30
#define PACE 7300
#define ADAPT 1
#define A_F 1008
#define FASTTRY 2

#define PACE_LEAD 2
#define PACE_H 88
#define FULLR 0
#define R0AUX 16
#define HPUB_AT 85
#define A_LEAD 0.f
#define A_HMARGIN 350.f
#define A_HPUB 150.f
#define SLEEP2 2
#define MAPMODE 1
#define SLEEP 6
#define KVPOL "nt"
#define RING 3
constexpr int R = RING;
constexpr int STAGE = 32768;
constexpr int RH = 4;
constexpr int RING_BYTES = 4 * RH * 8192;
constexpr int LDS_BT = RING_BYTES;
constexpr int LDS_QB = LDS_BT + NSEQ * NBLK * 4;
constexpr int LDS_SC = LDS_QB + 4 * R * 512;
constexpr int LDS_MISC = LDS_SC + 4096;
constexpr int LDS_PROG = LDS_MISC + 64;
constexpr int LDS_GEN = LDS_PROG + 1024;
constexpr int LDS_JUNK = LDS_PROG;
constexpr int LDS_TOTAL_JUNK = LDS_GEN + 512;
constexpr int LDS_TOTAL = LDS_TOTAL_JUNK + 1024;
static_assert(LDS_TOTAL <= 160 * 1024 && LDS_MISC - LDS_QB >= 8192 && RING_BYTES >= R * STAGE, "LDS");

constexpr unsigned WS_G = 0;
constexpr unsigned WS_PROG = 2048;
constexpr unsigned WS_PP = 4096;
constexpr unsigned PP_W = 2176;
constexpr unsigned WS_HP = WS_PP + 320u * 4u * PP_W + 4096u;
constexpr unsigned HP_W = 32768 + 512;
constexpr unsigned WS_END = WS_HP + 16u * 16u * HP_W;
constexpr unsigned WS_STAMP = 64u << 20;

typedef unsigned u32x4 __attribute__((ext_vector_type(4)));
typedef unsigned u32x2 __attribute__((ext_vector_type(2)));
typedef float f32x4 __attribute__((ext_vector_type(4)));
typedef float f32x2 __attribute__((ext_vector_type(2)));
typedef __bf16 bf16x8 __attribute__((ext_vector_type(8)));
typedef __bf16 bf16x2 __attribute__((ext_vector_type(2)));
typedef short s16x4 __attribute__((ext_vector_type(4)));
typedef int i32x4 __attribute__((ext_vector_type(4)));
#define LAS __attribute__((address_space(3)))

constexpr float C2 = 0.088388347648318440f * 1.4426950408889634f;

__device__ __forceinline__ int rfl(int x) { return __builtin_amdgcn_readfirstlane(x); }
__device__ __forceinline__ int rl(int x, int l) { return __builtin_amdgcn_readlane(x, l); }
__device__ __forceinline__ float rlf(float x, int l) { return __builtin_bit_cast(float, __builtin_amdgcn_readlane(__builtin_bit_cast(int, x), l)); }

__device__ __forceinline__ void dma16(const void* sbase, unsigned voff, unsigned dst) {
  unsigned keep;
  asm volatile("s_mov_b32 %0, m0\n\ts_mov_b32 m0, %3\n\ts_nop 0\n\tglobal_load_lds_dwordx4 %1, %2\n\ts_mov_b32 m0, %0"
               : "=&s"(keep) : "v"(voff), "s"(sbase), "s"(dst) : "memory");
}
__device__ __forceinline__ void dma16_sc1(const void* sbase, unsigned voff, unsigned dst) {
  unsigned keep;
  asm volatile("s_mov_b32 %0, m0\n\ts_mov_b32 m0, %3\n\ts_nop 0\n\tglobal_load_lds_dwordx4 %1, %2 sc1\n\ts_mov_b32 m0, %0"
               : "=&s"(keep) : "v"(voff), "s"(sbase), "s"(dst) : "memory");
}
__device__ __forceinline__ void dma_stage(const void* sK, const void* sV, unsigned dstK, unsigned dstV,
                                          unsigned k0, unsigned k1, unsigned k2, unsigned k3,
                                          unsigned v0, unsigned v1, unsigned v2, unsigned v3) {
  unsigned keep;
  asm volatile("s_mov_b32 %0, m0\n\ts_mov_b32 m0, %3\n\ts_nop 0\n\t"
               "global_load_lds_dwordx4 %5, %1 " KVPOL "\n\t"
               "global_load_lds_dwordx4 %6, %1 offset:1024 " KVPOL "\n\t"
               "global_load_lds_dwordx4 %7, %1 offset:2048 " KVPOL "\n\t"
               "global_load_lds_dwordx4 %8, %1 offset:3072 " KVPOL "\n\t"
               "s_mov_b32 m0, %4\n\ts_nop 0\n\t"
               "global_load_lds_dwordx4 %9, %2 " KVPOL "\n\t"
               "global_load_lds_dwordx4 %10, %2 offset:1024 " KVPOL "\n\t"
               "global_load_lds_dwordx4 %11, %2 offset:2048 " KVPOL "\n\t"
               "global_load_lds_dwordx4 %12, %2 offset:3072 " KVPOL "\n\t"
               "s_mov_b32 m0, %0"
               : "=&s"(keep)
               : "s"(sK), "s"(sV), "s"(dstK), "s"(dstV), "v"(k0), "v"(k1), "v"(k2), "v"(k3), "v"(v0), "v"(v1), "v"(v2), "v"(v3)
               : "memory");
}
template <int CTRL> __device__ __forceinline__ int dppi(int x) { return __builtin_amdgcn_update_dpp(0, x, CTRL, 0xf, 0xf, true); }
template <int CTRL> __device__ __forceinline__ int dppi_keep(int old, int x) { return __builtin_amdgcn_update_dpp(old, x, CTRL, 0xf, 0xf, false); }

__device__ __forceinline__ float xg_max(float x) {
  auto s = __builtin_amdgcn_permlane16_swap(__float_as_uint(x), __float_as_uint(x), false, false);
  x = fmaxf(__uint_as_float(s[0]), __uint_as_float(s[1]));
  auto t = __builtin_amdgcn_permlane32_swap(__float_as_uint(x), __float_as_uint(x), false, false);
  return fmaxf(__uint_as_float(t[0]), __uint_as_float(t[1]));
}
__device__ __forceinline__ float xg_sum(float x) {
  auto s = __builtin_amdgcn_permlane16_swap(__float_as_uint(x), __float_as_uint(x), false, false);
  x = __uint_as_float(s[0]) + __uint_as_float(s[1]);
  auto t = __builtin_amdgcn_permlane32_swap(__float_as_uint(x), __float_as_uint(x), false, false);
  return __uint_as_float(t[0]) + __uint_as_float(t[1]);
}
__device__ __forceinline__ unsigned pk_bf16(float a, float b) {
  bf16x2 v = {(__bf16)a, (__bf16)b};
  return __builtin_bit_cast(unsigned, v);
}

template <int NQ>
__device__ __forceinline__ void tile_compute(LAS char* lds, unsigned ka, unsigned va, const bf16x8 (&qf)[NQ][4], f32x4 (&acc)[NQ][8],
                                             float (&m)[NQ], float (&l)[NQ], int lo, int hi, int g4) {
  bf16x8 kf[4];
#pragma unroll
  for (int s = 0; s < 4; ++s) kf[s] = *(LAS bf16x8*)(lds + (ka ^ (s << 6)));
  s16x4 vf[8];
#pragma unroll
  for (int k = 0; k < 8; ++k) vf[k] = __builtin_amdgcn_ds_read_tr16_b64_v4i16((LAS s16x4*)(lds + (va ^ (k << 5))));
  f32x4 S[NQ];
#pragma unroll
  for (int qb = 0; qb < NQ; ++qb) {
    S[qb] = f32x4{0.f, 0.f, 0.f, 0.f};
#pragma unroll
    for (int s = 0; s < 4; ++s) S[qb] = __builtin_amdgcn_mfma_f32_16x16x32_bf16(kf[s], qf[qb][s], S[qb], 0, 0, 0);
  }
  if (lo > 0 || hi < 16) {
#pragma unroll
    for (int qb = 0; qb < NQ; ++qb)
#pragma unroll
      for (int r = 0; r < 4; ++r) { const int t = 4 * g4 + r; S[qb][r] = (t >= lo && t < hi) ? S[qb][r] : -INFINITY; }
  }
  s16x4 pb[NQ];
  bool grow = false;
  float alpha[NQ];
#pragma unroll
  for (int qb = 0; qb < NQ; ++qb) {
    float mx = fmaxf(fmaxf(S[qb][0], S[qb][1]), fmaxf(S[qb][2], S[qb][3]));
    mx = xg_max(mx);
    const float mn2 = fmaxf(m[qb], mx);
    grow |= mn2 > m[qb];
    alpha[qb] = __builtin_amdgcn_exp2f((m[qb] - mn2) * C2);
    m[qb] = mn2;
    const float mc = -mn2 * C2;
    const float p0 = __builtin_amdgcn_exp2f(fmaf(S[qb][0], C2, mc)), p1 = __builtin_amdgcn_exp2f(fmaf(S[qb][1], C2, mc));
    const float p2 = __builtin_amdgcn_exp2f(fmaf(S[qb][2], C2, mc)), p3 = __builtin_amdgcn_exp2f(fmaf(S[qb][3], C2, mc));
    l[qb] = fmaf(l[qb], alpha[qb], (p0 + p1) + (p2 + p3));
    pb[qb] = __builtin_bit_cast(s16x4, u32x2{pk_bf16(p0, p1), pk_bf16(p2, p3)});
  }
  if (__any(grow)) {
#pragma unroll
    for (int qb = 0; qb < NQ; ++qb)
#pragma unroll
      for (int k = 0; k < 8; ++k) acc[qb][k] *= alpha[qb];
  }
#pragma unroll
  for (int qb = 0; qb < NQ; ++qb)
#pragma unroll
    for (int k = 0; k < 8; ++k) acc[qb][k] = __builtin_amdgcn_mfma_f32_16x16x16bf16_1k(vf[k], pb[qb], acc[qb][k], 0, 0, 0);
}

template <int V> struct IntC { static constexpr int value = V; };

struct Params {
  const char* q; const char* k; const char* v; const int* bt; const int* lens; __hip_bfloat16* out; char* ws;
  unsigned kvbytes; unsigned unused;
};

__global__ void __launch_bounds__(256, 2) radix_attn_kernel(Params p) {
  extern __shared__ __attribute__((aligned(16))) char smem[];
  LAS char* lds = (LAS char*)smem;
  const int tid = threadIdx.x, lane = tid & 63, w = rfl(tid >> 6);
  const int gidx = blockIdx.x;
  const int hg = (gidx >> 3) & 3;
  const int jj = (((gidx & 7) + 4 * (hg & 1)) & 7) | ((gidx >> 5) << 3);
  const int c16 = lane & 15, g4 = lane >> 4;
#define STAMP(i) do { } while (0)
#define STAMP_FLUSH() do { } while (0)
  STAMP(0);
#define PROBE(k) do { } while (0)
#define PROBE_END() do { } while (0)
  const auto wsr = __builtin_amdgcn_make_buffer_rsrc(p.ws, 0, WS_END, 0x00020000);

  unsigned voffK[4], voffV[4], oK[4], oV[4];
  int pm, len_l, minlen, vpages, incp, sump; bool zq_l;
  {
    const i32x4* bt4 = (const i32x4*)p.bt;
    const i32x4 r0 = bt4[lane];
    i32x4 rr[4];
#pragma unroll
    for (int i = 0; i < 4; ++i) rr[i] = bt4[(4 * w + i) * 64 + lane];
    const int lenraw = p.lens[c16];
    __builtin_amdgcn_sched_barrier(0);
#pragma unroll
  for (int i = 0; i < 4; ++i) {
    const int t = 4 * w + i, u = 4 * i + g4;
    voffK[i] = g4 * 256 + ((c16 ^ t) << 4) + t * 4096 - i * 1024 + 4096;
    voffV[i] = g4 * 256 + ((c16 ^ (2 * (t & 7))) << 4) + t * 4096 - i * 1024 + 4096;
    oK[i] = u * 4096 + ((c16 ^ u) << 4) - i * 1024 + 4096;
    oV[i] = u * 4096 + ((c16 ^ (2 * (u & 7))) << 4) - i * 1024 + 4096;
  }
    asm volatile("" :: "v"(voffK[0]), "v"(voffK[1]), "v"(voffK[2]), "v"(voffK[3]), "v"(voffV[0]), "v"(voffV[1]), "v"(voffV[2]), "v"(voffV[3]),
                 "v"(oK[0]), "v"(oK[1]), "v"(oK[2]), "v"(oK[3]), "v"(oV[0]), "v"(oV[1]), "v"(oV[2]), "v"(oV[3]) : "memory");
    bool n0 = false, n1 = false, n2 = false, n3 = false;
#pragma unroll
    for (int i = 0; i < 4; ++i) {
      n0 |= rr[i][0] != r0[0]; n1 |= rr[i][1] != r0[1]; n2 |= rr[i][2] != r0[2]; n3 |= rr[i][3] != r0[3];
      *(LAS i32x4*)(lds + LDS_BT + ((4 * w + i) * 256 + 4 * lane) * 4) = rr[i];
    }
    zq_l = lenraw <= 0;
    len_l = zq_l ? LK : min(lenraw, LK);
    int mn = len_l;
    mn = min(mn, dppi_keep<0x128>(mn, mn)); mn = min(mn, dppi_keep<0x124>(mn, mn));
    mn = min(mn, dppi_keep<0x122>(mn, mn)); mn = min(mn, dppi_keep<0x121>(mn, mn));
    minlen = rfl(mn);
    vpages = (len_l + BS - 1) >> 4;
    incp = vpages;
    incp += dppi<0x111>(incp); incp += dppi<0x112>(incp); incp += dppi<0x114>(incp); incp += dppi<0x118>(incp);
    sump = rl(incp, 15);
    const int fc = n0 ? 0 : n1 ? 1 : n2 ? 2 : n3 ? 3 : 4;
    const unsigned long long mm = __ballot(fc < 4);
    int col = NBLK;
    if (mm) { const int fl = __builtin_ctzll(mm); col = 4 * fl + rl(fc, fl); }
    if (lane == 0) *(LAS int*)(lds + LDS_MISC + 4 * w) = col;
    asm volatile("s_waitcnt lgkmcnt(0)\n\ts_barrier" ::: "memory");
    PROBE(1);
    const i32x4 mc = *(LAS i32x4*)(lds + LDS_MISC);
    pm = rfl(min(min(mc[0], mc[1]), min(mc[2], mc[3])));
  }
  const int P = min(pm, minlen >> 4);
  const int tsh = P * BS;
  const int vL = vpages - P;
  const int vS = incp - vpages - c16 * P;
  const int N = sump - 16 * P;
  int kH = 0;
  if (P > 0) kH = min(min(15, P), max(1, rfl((int)__builtin_ceilf((float)P * (1024.f / HRHO) * __builtin_amdgcn_rcpf((float)(P + N))))));
  const int nW = 64 - 4 * kH;
  const float inv_nw = __builtin_amdgcn_rcpf((float)nW);
  const unsigned zmask = (unsigned)(__ballot(zq_l) & 0xffffull);
  const bool isH = jj < 4 * kH;
  auto gen_fetch = [&](int head) { if (lane < 5) dma16_sc1(p.ws + WS_G, lane < 4 ? head * 64 + lane * 16 : 0, LDS_GEN + w * 128); };
  auto gen_row = [&]() { return *(LAS unsigned*)(lds + LDS_GEN + w * 128 + c16 * 4); };
  auto prog_tag = [&]() { return (unsigned)rfl(*(LAS int*)(lds + LDS_GEN + w * 128 + 64)) + 1u; };
  const float t_all = (float)(P + N) * (16.f * 819.2f / (float)PACE);
  const unsigned long long t_ref = __builtin_amdgcn_s_memrealtime();
  constexpr int A_S0 = 3, A_S1 = 11;
  const int m_avg = rfl((int)(((float)(N + nW - 1) + 0.5f) * inv_nw));
  const bool adapt_on = nW >= 24 && m_avg >= A_S1 + 8;
  auto read_progress = [&](unsigned ldsoff, float& t1, float& d) -> bool {
    const u32x4 e = *(LAS u32x4*)(lds + ldsoff + c16 * 16);
    const unsigned ptag = prog_tag();
    const bool okv = e[1] == ptag && e[3] == ptag;
    const int cnt = __builtin_popcount((unsigned)__ballot(okv) & 0xffffu);
    float a = okv ? (float)(int)(e[0] - (unsigned)t_ref) : 0.f, c = okv ? (float)e[2] : 0.f;
    a += __builtin_bit_cast(float, dppi_keep<0x128>(0, __builtin_bit_cast(int, a))); c += __builtin_bit_cast(float, dppi_keep<0x128>(0, __builtin_bit_cast(int, c)));
    a += __builtin_bit_cast(float, dppi_keep<0x124>(0, __builtin_bit_cast(int, a))); c += __builtin_bit_cast(float, dppi_keep<0x124>(0, __builtin_bit_cast(int, c)));
    a += __builtin_bit_cast(float, dppi_keep<0x122>(0, __builtin_bit_cast(int, a))); c += __builtin_bit_cast(float, dppi_keep<0x122>(0, __builtin_bit_cast(int, c)));
    a += __builtin_bit_cast(float, dppi_keep<0x121>(0, __builtin_bit_cast(int, a))); c += __builtin_bit_cast(float, dppi_keep<0x121>(0, __builtin_bit_cast(int, c)));
    if (cnt < 8) return false;
    const float ic = 1.f / (float)cnt;
    t1 = rlf(a, 0) * ic; d = rlf(c, 0) * ic * (1.f / (A_S1 - A_S0));
    return true;
  };
  const int r_lo = (int)(((float)(lane * N) + 0.5f) * inv_nw), r_hi = (int)(((float)((lane + 1) * N) + 0.5f) * inv_nw);

  if (isH) {
    const int h = hg * 4 + (jj & 3), part = jj >> 2;
    const int p0 = (P * part) / kH, p1 = (P * (part + 1)) / kH;
#pragma unroll
    for (int i = 0; i < 2; ++i)
      dma16(p.q + (2 * w + i) * 16384 + h * 512, (lane >> 5) * 8192 + (lane & 31) * 16, LDS_QB + (2 * w + i) * 1024);
    const char* kb = p.k + h * 256 - 4096;
    const char* vb = p.v + h * 256 - 4096;
    const int nt = max(0, (p1 - p0 - w + 3) >> 2);
    const unsigned ring = w * (RH * 8192);
    int pg0 = 0, vpg = *(LAS int*)(lds + LDS_BT + min(p0 + w + 4 * lane, NBLK - 1) * 4);
    float pace_h = t_all * (PACE_H * 0.01f) * __builtin_amdgcn_rcpf((float)max(nt, 1)), pace_hb = -3.f * pace_h;
    float t_pub = t_all * (HPUB_AT * 0.01f);
    auto issue = [&](int t) {
      { const int due = rfl((int)(pace_hb + (float)t * pace_h)); if (due > 0) while ((int)(__builtin_amdgcn_s_memrealtime() - t_ref) < due) __builtin_amdgcn_s_sleep(1); }
      if (t - pg0 == 64) { pg0 = t; vpg = *(LAS int*)(lds + LDS_BT + min(p0 + w + 4 * (t + lane), NBLK - 1) * 4); }
      const size_t pb = (size_t)(unsigned)rl(vpg, t - pg0) * 65536;
      const unsigned dst = ring + (t & (RH - 1)) * 8192;
      dma_stage(kb + pb, vb + pb, dst, dst + 4096, oK[0], oK[1], oK[2], oK[3], oV[0], oV[1], oV[2], oV[3]);
    };
    const int npre = min(nt, RH - 1);
#pragma unroll 1
    for (int t = 0; t < npre; ++t) issue(t);
    gen_fetch(h);
    if (npre == 3) asm volatile("s_waitcnt vmcnt(25)\n\ts_barrier" ::: "memory");
    else asm volatile("s_waitcnt vmcnt(0)\n\ts_barrier" ::: "memory");
    bf16x8 qf[2][4];
#pragma unroll
    for (int qb = 0; qb < 2; ++qb) {
      const bool z = (zmask >> (8 * qb + (c16 >> 1))) & 1;
#pragma unroll
      for (int s = 0; s < 4; ++s) {
        u32x4 x = *(LAS u32x4*)(lds + LDS_QB + (16 * qb + c16) * 256 + (4 * s + g4) * 16);
        if (z) x = u32x4{0u, 0u, 0u, 0u};
        qf[qb][s] = __builtin_bit_cast(bf16x8, x);
      }
    }
    f32x4 acc[2][8];
    float m[2] = {-INFINITY, -INFINITY}, l[2] = {0.f, 0.f};
#pragma unroll
    for (int qb = 0; qb < 2; ++qb)
#pragma unroll
      for (int k = 0; k < 8; ++k) acc[qb][k] = f32x4{0.f, 0.f, 0.f, 0.f};
    const unsigned ka0 = ring + c16 * 256 + ((g4 ^ c16) << 4);
    unsigned va0;
    { const int tq = 4 * g4 + (c16 >> 2), pp = lane & 3; va0 = ring + 4096 + tq * 256 + ((tq & 7) << 5) + ((pp >> 1) << 4) + ((pp & 1) << 3); }
#pragma unroll 1
    for (int t = 0; t < nt; ++t) {
      const int young = min(nt - 1 - t, RH - 2);
      if (young == 2) asm volatile("s_waitcnt vmcnt(16) lgkmcnt(0)" ::: "memory");
      else if (young == 1) asm volatile("s_waitcnt vmcnt(8) lgkmcnt(0)" ::: "memory");
      else asm volatile("s_waitcnt vmcnt(0) lgkmcnt(0)" ::: "memory");
      if (adapt_on && nt >= 12) {
        const int tp = (nt * 9) >> 4;
        if (t == tp + 3) {
          float t1, d;
          if (read_progress(LDS_PROG + w * 256, t1, d)) {
            const float t_end = t1 + (float)(m_avg - A_S1) * d * (A_F * 0.001f);
            const float now = (float)(int)(__builtin_amdgcn_s_memrealtime() - t_ref);
            const int t_i = t + RH - 1;
            pace_h = fmaxf((t_end - A_HMARGIN - now) / (float)max(nt - t_i, 1), 0.f); pace_hb = now - (float)t_i * pace_h;
            t_pub = t_end - A_HPUB;
          }
        }
      }
      if (t + RH - 1 < nt) issue(t + RH - 1);
      if (adapt_on && nt >= 12 && t == ((nt * 9) >> 4) && lane < 16) dma16_sc1(p.ws + WS_PROG, lane * 16, LDS_PROG + w * 256);
      const unsigned sb = (t & (RH - 1)) * 8192;
      tile_compute<2>(lds, ka0 + sb, va0 + sb, qf, acc, m, l, 0, 16, g4);
    }
    STAMP(2);
    constexpr unsigned ROWB = 528, WAVEB = 32 * ROWB, MLB = 4 * WAVEB;
    asm volatile("s_waitcnt lgkmcnt(0)\n\ts_barrier" ::: "memory");
#pragma unroll
    for (int qb = 0; qb < 2; ++qb) {
      const float lt = xg_sum(l[qb]);
#pragma unroll
      for (int k = 0; k < 8; ++k) *(LAS f32x4*)(lds + w * WAVEB + (16 * qb + c16) * ROWB + (16 * k + 4 * g4) * 4) = acc[qb][k];
      if (g4 == 0) *(LAS f32x2*)(lds + MLB + (w * 32 + 16 * qb + c16) * 8) = f32x2{m[qb] * C2, lt};
    }
    asm volatile("s_waitcnt lgkmcnt(0)\n\ts_barrier" ::: "memory");
    while ((int)(__builtin_amdgcn_s_memrealtime() - t_ref) < (int)t_pub) __builtin_amdgcn_s_sleep(8);
    asm volatile("s_waitcnt vmcnt(0)" ::: "memory");
    const unsigned gvh = gen_row();
    const unsigned slot = WS_HP + (part * 16 + h) * HP_W;
    float mrow = 0.f, lrow = 0.f;
#pragma unroll
    for (int rr = 0; rr < 8; ++rr) {
      const int row = 8 * w + rr;
      f32x2 ml[4], o[4];
#pragma unroll
      for (int v = 0; v < 4; ++v) { ml[v] = *(LAS f32x2*)(lds + MLB + (v * 32 + row) * 8); o[v] = *(LAS f32x2*)(lds + v * WAVEB + row * ROWB + lane * 8); }
      const float M = fmaxf(fmaxf(ml[0][0], ml[1][0]), fmaxf(ml[2][0], ml[3][0]));
      float L = 0.f, o0 = 0.f, o1 = 0.f;
#pragma unroll
      for (int v = 0; v < 4; ++v) {
        const float e = ml[v][1] > 0.f ? __builtin_amdgcn_exp2f(ml[v][0] - M) : 0.f;
        L = fmaf(ml[v][1], e, L); o0 = fmaf(o[v][0], e, o0); o1 = fmaf(o[v][1], e, o1);
      }
      const unsigned tag = (unsigned)rl((int)gvh, row >> 1) + 1u;
      const u32x4 gr = {__float_as_uint(o0), tag, __float_as_uint(o1), tag};
      __builtin_amdgcn_raw_buffer_store_b128(gr, wsr, slot + row * 1024 + lane * 16, 0, 16);
      if (lane == rr) { mrow = M; lrow = L; }
    }
    const unsigned tagr = (unsigned)__shfl((int)gvh, ((8 * w + lane) >> 1) & 15) + 1u;
    if (lane < 8) {
      const unsigned tag = tagr;
      const u32x4 gm = {__float_as_uint(mrow), tag, __float_as_uint(lrow), tag};
      __builtin_amdgcn_raw_buffer_store_b128(gm, wsr, slot + 32768 + (8 * w + lane) * 16, 0, 16);
    }
    STAMP(1);
    STAMP(3);
    STAMP_FLUSH();
    return;
  }

  const int j = jj - 4 * kH, h = hg * 4 + w;
  const int x0 = rl(r_lo, j), x1 = rl(r_hi, j);
  const unsigned umask = (unsigned)(__ballot(max(x0, vS) < min(x1, vS + vL)) & 0xffffull);
  const unsigned kaddr0 = w * 256 + c16 * 1024 + ((g4 ^ c16) << 4);
  unsigned vaddr0;
  { const int tq = 4 * g4 + (c16 >> 2), pp = lane & 3; vaddr0 = 16384 + tq * 1024 + w * 256 + ((tq & 7) << 5) + ((pp >> 1) << 4) + ((pp & 1) << 3); }
  const char* kbase = p.k + hg * 1024 - 4096;
  const char* vbase = p.v + hg * 1024 - 4096;

  unsigned i_mask = umask; int i_row = 0, i_t0 = 0, i_t1 = 0, i_page; bool i_more = false;
  int n_iss = 0, i_slot = 0;
  auto unit_range = [&](int b, int& t0, int& t1) {
    const int s = rl(vS, b), L = rl(vL, b);
    t0 = (P + max(x0, s) - s) * BS; t1 = min((P + min(x1, s + L) - s) * BS, rl(len_l, b));
  };
  auto q_dma = [&](int b, int slot) {
    if (lane < 32) dma16(p.q + b * 8192 + h * 512, lane * 16, LDS_QB + (w * R + slot) * 512);
  };
  if (i_mask) {
    i_row = __builtin_ctz(i_mask); i_mask &= i_mask - 1; unit_range(i_row, i_t0, i_t1);
    q_dma(i_row, 0);
    i_more = true;
  }
  i_page = i_t0 >> 4;
  int i_pg0 = i_page, vpg = *(LAS int*)(lds + LDS_BT + (i_row * NBLK + min(i_page + lane, NBLK - 1)) * 4);
  int pace_tok = 0;
  float pace_d = t_all * __builtin_amdgcn_rcpf((float)max(x1 - x0, 1)), pace_b = -(float)PACE_LEAD * pace_d;
  const int rep = (adapt_on && hg < 2 && j >= (nW >> 1) && j < (nW >> 1) + 8) ? hg * 8 + j - (nW >> 1) : -1;
  unsigned t_s0 = 0;
  auto issue_stage = [&]() {
    const int phys = rl(vpg, i_page - i_pg0);
    const int hi = i_t1 - i_page * 16;
    {
      if (adapt_on && pace_tok == A_S1 + 3 + R - 1) {
        float t1, d;
        if (read_progress(LDS_PROG, t1, d)) { pace_d = d * (A_F * 0.001f); pace_b = t1 - (float)A_S1 * pace_d - A_LEAD; }
      }
      const int due = rfl((int)(pace_b + (float)pace_tok * pace_d));
      pace_tok += 1;
      if (due > 0) while ((int)(__builtin_amdgcn_s_memrealtime() - t_ref) < due) __builtin_amdgcn_s_sleep(1);
    }
    const size_t pb = (size_t)(unsigned)phys * 65536;
    const unsigned dst = i_slot * STAGE + 4 * w * 1024;
    if (hi >= 16) {
      dma_stage(kbase + pb, vbase + pb, dst, dst + 16384, voffK[0], voffK[1], voffK[2], voffK[3], voffV[0], voffV[1], voffV[2], voffV[3]);
    } else {
      unsigned ok[4], ov[4];
#pragma unroll
      for (int i = 0; i < 4; ++i) {
        const int d = (min(4 * w + i, hi - 1) - (4 * w + i)) * 4096;
        ok[i] = voffK[i] + d; ov[i] = voffV[i] + d;
      }
      dma_stage(kbase + pb, vbase + pb, dst, dst + 16384, ok[0], ok[1], ok[2], ok[3], ov[0], ov[1], ov[2], ov[3]);
    }
    if (adapt_on) {
      const int k = pace_tok - 1;
      if (rep >= 0 && w == 3) {
        if (k == A_S0) t_s0 = (unsigned)__builtin_amdgcn_s_memrealtime();
        if (k == A_S1) {
          const unsigned ptag = prog_tag();
          const unsigned now = (unsigned)__builtin_amdgcn_s_memrealtime();
          const u32x4 e = {now, ptag, now - t_s0, ptag};
          if (lane == 0) __builtin_amdgcn_raw_buffer_store_b128(e, wsr, WS_PROG + rep * 16, 0, 16);
        }
      }
      if (k == A_S1 + 3 && w == 0 && lane < 16) dma16_sc1(p.ws + WS_PROG, lane * 16, LDS_PROG);
    }
    ++n_iss; i_slot = (i_slot + 1 == R) ? 0 : i_slot + 1;
    ++i_page;
    if (i_page * 16 >= i_t1) {
      if (i_mask) {
        i_row = __builtin_ctz(i_mask); i_mask &= i_mask - 1; unit_range(i_row, i_t0, i_t1); i_page = i_t0 >> 4;
        q_dma(i_row, i_slot);
        i_pg0 = i_page; vpg = *(LAS int*)(lds + LDS_BT + (i_row * NBLK + min(i_page + lane, NBLK - 1)) * 4);
      } else i_more = false;
    } else if (i_page - i_pg0 == 64) {
      i_pg0 = i_page; vpg = *(LAS int*)(lds + LDS_BT + (i_row * NBLK + min(i_page + lane, NBLK - 1)) * 4);
    }
  };
  PROBE(2);
#pragma unroll 1
  for (int s = 0; s < R - 1; ++s) if (i_more) issue_stage();
  gen_fetch(h);

  unsigned own = 0;
  {
    unsigned mrem = umask;
    while (mrem) {
      const int b = __builtin_ctz(mrem); mrem &= mrem - 1;
      const int s = rl(vS, b), L = rl(vL, b);
      unsigned long long pmk = __ballot(lane < nW && max(r_lo, s) < min(r_hi, s + L));
      int idx = w % __builtin_popcountll(pmk);
      while (idx--) pmk &= pmk - 1;
      if (__builtin_ctzll(pmk) == j) own |= 1u << b;
    }
    unsigned zl = (unsigned)(__ballot(vL == 0) & 0xffffull);
    while (zl) { const int b = __builtin_ctz(zl); zl &= zl - 1; if ((b * 4) % nW == j) own |= 1u << b; }
  }

  int fb = -1, fcnt = 0; unsigned vrow = 0, vmlo = 0;
  if (own && (own & (own - 1)) == 0) {
    const int b = __builtin_ctz(own);
    const unsigned above = umask >> b;
    if ((umask >> b & 1) && (above == 1 || __builtin_popcount(above) == 2)) {
      const int s = rl(vS, b), L = rl(vL, b);
      const unsigned long long pmk = __ballot(lane < nW && max(r_lo, s) < min(r_hi, s + L)) & ~(1ull << j);
      const int np = __builtin_popcountll(pmk);
      if (np + kH <= 8 && np + kH > 0) {
        fb = b; fcnt = np + kH;
        const int rank = __builtin_amdgcn_mbcnt_hi((unsigned)(pmk >> 32), __builtin_amdgcn_mbcnt_lo((unsigned)pmk, 0));
        const unsigned prow = WS_PP + ((hg * 80 + lane + b) * 4 + w) * PP_W;
        const unsigned pushed = (unsigned)__builtin_amdgcn_ds_permute((((pmk >> lane) & 1) ? rank : 63) << 2, (int)prow);
        const unsigned hb = WS_HP + (min(max(lane - np, 0), 15) * 16 + h) * HP_W;
        vrow = lane < np ? pushed : hb + b * 2048;
        const unsigned mlb = lane < np ? pushed + 2048 : hb + 32768 + b * 32;
        vmlo = (unsigned)__shfl((int)mlb, min(lane >> 1, fcnt - 1)) + (lane & 1) * 16;
      }
    }
  }

  int n_comp = 0, c_slot = 0;
  unsigned c_mask = umask;
  float ro[2][2] = {{0.f, 0.f}, {0.f, 0.f}}, rm[2] = {-INFINITY, -INFINITY}, rs[2] = {0.f, 0.f};
  float ho[2][2] = {{0.f, 0.f}, {0.f, 0.f}}, hm[2] = {-INFINITY, -INFINITY}, hs[2] = {0.f, 0.f};
  int last_b = -1, held_b = -1;
  auto publish = [&](int b, const float (&o)[2][2], const float (&mm)[2], const float (&ll)[2]) {
    const unsigned tag = (unsigned)rfl(*(LAS int*)(lds + LDS_GEN + w * 128 + b * 4)) + 1u;
    const unsigned slot = WS_PP + ((hg * 80 + j + b) * 4 + w) * PP_W;
    const u32x4 g0 = {__float_as_uint(o[0][0]), tag, __float_as_uint(o[0][1]), tag};
    const u32x4 g1 = {__float_as_uint(o[1][0]), tag, __float_as_uint(o[1][1]), tag};
    __builtin_amdgcn_raw_buffer_store_b128(g0, wsr, slot + lane * 16, 0, 16);
    __builtin_amdgcn_raw_buffer_store_b128(g1, wsr, slot + 1024 + lane * 16, 0, 16);
    if (lane < 2) {
      const u32x4 gm = {__float_as_uint(lane ? mm[1] : mm[0]), tag, __float_as_uint(lane ? ll[1] : ll[0]), tag};
      __builtin_amdgcn_raw_buffer_store_b128(gm, wsr, slot + 2048 + lane * 16, 0, 16);
    }
  };
  while (c_mask) {
    const int b = __builtin_ctz(c_mask); c_mask &= c_mask - 1;
    int t0, t1; unit_range(b, t0, t1);
    bf16x8 qf[1][4];
    f32x4 acc[1][8];
    float m[1] = {-INFINITY}, l[1] = {0.f};
#pragma unroll
    for (int k = 0; k < 8; ++k) acc[0][k] = f32x4{0.f, 0.f, 0.f, 0.f};
    bool first = true;
    for (int page = t0 >> 4; page * 16 < t1; ++page) {
      {
        const int young = n_iss - n_comp - 1;
        if (R == 4 && young >= 2) asm volatile("s_waitcnt vmcnt(16) lgkmcnt(0)\n\ts_barrier" ::: "memory");
        else if (young >= 1) asm volatile("s_waitcnt vmcnt(8) lgkmcnt(0)\n\ts_barrier" ::: "memory");
        else asm volatile("s_waitcnt vmcnt(0) lgkmcnt(0)\n\ts_barrier" ::: "memory");
      }
      PROBE(3);
      if (first) {
        const bool z = (c16 >= 2) || ((zmask >> b) & 1);
#pragma unroll
        for (int s = 0; s < 4; ++s) {
          u32x4 x = *(LAS u32x4*)(lds + LDS_QB + (w * R + c_slot) * 512 + (c16 & 1) * 256 + (4 * s + g4) * 16);
          if (z) x = u32x4{0u, 0u, 0u, 0u};
          qf[0][s] = __builtin_bit_cast(bf16x8, x);
        }
        asm volatile("s_waitcnt lgkmcnt(0)" ::: "memory");
        first = false;
      }
      if (i_more) issue_stage();
      const unsigned sb = c_slot * STAGE;
      tile_compute<1>(lds, kaddr0 + sb, vaddr0 + sb, qf, acc, m, l, 0, t1 - page * 16, g4);
      ++n_comp; c_slot = (c_slot + 1 == R) ? 0 : c_slot + 1;
    }
    const float lt = xg_sum(l[0]);
    LAS char* sc = lds + LDS_SC + w * 1024;
    if (c16 < 2) {
#pragma unroll
      for (int k = 0; k < 8; ++k) *(LAS f32x4*)(sc + c16 * 512 + (16 * k + 4 * g4) * 4) = acc[0][k];
    }
    asm volatile("s_waitcnt lgkmcnt(0)" ::: "memory");
    const f32x2 a0 = *(LAS f32x2*)(sc + lane * 8), a1 = *(LAS f32x2*)(sc + 512 + lane * 8);
    if (last_b >= 0) {
      if (held_b >= 0) publish(held_b, ho, hm, hs);
      held_b = last_b;
#pragma unroll
      for (int r = 0; r < 2; ++r) { ho[r][0] = ro[r][0]; ho[r][1] = ro[r][1]; hm[r] = rm[r]; hs[r] = rs[r]; }
    }
    ro[0][0] = a0[0]; ro[0][1] = a0[1]; ro[1][0] = a1[0]; ro[1][1] = a1[1];
    rm[0] = rlf(m[0], 0) * C2; rm[1] = rlf(m[0], 1) * C2;
    rs[0] = rlf(lt, 0); rs[1] = rlf(lt, 1);
    last_b = b;
  }
  asm volatile("s_waitcnt vmcnt(0)" ::: "memory");
  const unsigned gvw = gen_row();
  STAMP(2);
  PROBE(5);
  if (last_b >= 0 && !((own >> last_b) & 1)) { publish(last_b, ro, rm, rs); last_b = -1; }
  if (held_b >= 0 && !((own >> held_b) & 1)) { publish(held_b, ho, hm, hs); held_b = -1; }
  STAMP(1);

  if (own) { const unsigned long long c0 = __builtin_amdgcn_s_memrealtime(); while (__builtin_amdgcn_s_memrealtime() - c0 < MDELAY) __builtin_amdgcn_s_sleep(1); }
  if (fb >= 0) {
    const int b = fb;
    const unsigned tag = (unsigned)rl((int)gvw, b) + 1u;
    const bool mine_last = b == last_b;
    const float om0 = mine_last ? rm[0] : hm[0], om1 = mine_last ? rm[1] : hm[1], ol0 = mine_last ? rs[0] : hs[0], ol1 = mine_last ? rs[1] : hs[1];
    const float oo00 = mine_last ? ro[0][0] : ho[0][0], oo01 = mine_last ? ro[0][1] : ho[0][1];
    const float oo10 = mine_last ? ro[1][0] : ho[1][0], oo11 = mine_last ? ro[1][1] : ho[1][1];
    auto attempt = [&](auto nb_tag) -> bool {
      constexpr int NB = decltype(nb_tag)::value;
      u32x4 d0[NB], d1[NB];
      unsigned so[NB];
#pragma unroll
      for (int k = 0; k < NB; ++k) so[k] = (unsigned)rl((int)vrow, min(k, fcnt - 1));
      asm volatile("" ::: "memory");
      const u32x4 dm = __builtin_amdgcn_raw_buffer_load_b128(wsr, vmlo, 0, 16);
#pragma unroll
      for (int k = 0; k < NB; ++k) {
        d0[k] = __builtin_amdgcn_raw_buffer_load_b128(wsr, lane * 16, so[k], 16);
        d1[k] = __builtin_amdgcn_raw_buffer_load_b128(wsr, 1024 + lane * 16, so[k], 16);
      }
      __builtin_amdgcn_sched_barrier(0);
      const bool live = lane < 2 * fcnt;
      const float mk = live ? __uint_as_float(dm[0]) : -INFINITY, lk = live ? __uint_as_float(dm[2]) : 0.f;
      const float om = (lane & 1) ? om1 : om0, ol = (lane & 1) ? ol1 : ol0;
      float M = fmaxf(mk, om);
      M = fmaxf(M, __builtin_bit_cast(float, dppi_keep<0x122>(__builtin_bit_cast(int, M), __builtin_bit_cast(int, M))));
      M = fmaxf(M, __builtin_bit_cast(float, dppi_keep<0x124>(__builtin_bit_cast(int, M), __builtin_bit_cast(int, M))));
      M = fmaxf(M, __builtin_bit_cast(float, dppi_keep<0x128>(__builtin_bit_cast(int, M), __builtin_bit_cast(int, M))));
      const float e = __builtin_amdgcn_exp2f(mk - M), eo = __builtin_amdgcn_exp2f(om - M);
      float Ls = lk * e;
      Ls += __builtin_bit_cast(float, dppi_keep<0x122>(0, __builtin_bit_cast(int, Ls)));
      Ls += __builtin_bit_cast(float, dppi_keep<0x124>(0, __builtin_bit_cast(int, Ls)));
      Ls += __builtin_bit_cast(float, dppi_keep<0x128>(0, __builtin_bit_cast(int, Ls)));
      Ls = fmaf(ol, eo, Ls);
      const float inv = __builtin_amdgcn_rcpf(Ls);
      const float en = e * inv, eon = eo * inv;
      const float eo0 = rlf(eon, 0), eo1 = rlf(eon, 1);
      float o00 = oo00 * eo0, o01 = oo01 * eo0, o10 = oo10 * eo1, o11 = oo11 * eo1;
      unsigned bad = (dm[1] ^ tag) | (dm[3] ^ tag);
#pragma unroll
      for (int k = 0; k < NB; ++k) {
        const float w0 = rlf(en, 2 * k), w1 = rlf(en, 2 * k + 1);
        bad |= ((d0[k][1] ^ tag) | (d0[k][3] ^ tag)) | ((d1[k][1] ^ tag) | (d1[k][3] ^ tag));
        o00 = fmaf(__uint_as_float(d0[k][0]), w0, o00); o01 = fmaf(__uint_as_float(d0[k][2]), w0, o01);
        o10 = fmaf(__uint_as_float(d1[k][0]), w1, o10); o11 = fmaf(__uint_as_float(d1[k][2]), w1, o11);
      }
      if (__any(bad != 0)) return false;
      __hip_bfloat16* orow = p.out + (b * NH + 2 * h) * DH + 2 * lane;
      *(unsigned*)orow = pk_bf16(o00, o01);
      *(unsigned*)(orow + DH) = pk_bf16(o10, o11);
      if (lane == 0) __builtin_amdgcn_raw_buffer_store_b32(tag, wsr, WS_G + (h * 16 + b) * 4, 0, GAUX);
      return true;
    };
#pragma unroll 1
    for (int t = 0; t < 16; ++t) {
      if (t >= FASTTRY) {
        unsigned spins = 0;
        while (true) {
          asm volatile("" ::: "memory");
          const u32x4 x = __builtin_amdgcn_raw_buffer_load_b128(wsr, vmlo, 0, 16);
          if (!__any(x[1] != tag || x[3] != tag) || ++spins > (1u << 16)) break;
          __builtin_amdgcn_s_sleep(SLEEP2);
        }
      }
      const bool ok = fcnt <= 5 ? attempt(IntC<5>{}) : attempt(IntC<8>{});
      if (ok) { own = 0; break; }
    }
  }
  while (own) {
    const int b = __builtin_ctz(own); own &= own - 1;
    const unsigned tag = (unsigned)rl((int)gvw, b) + 1u;
    const int s = rl(vS, b), L = rl(vL, b);
    unsigned long long pmk = __ballot(lane < nW && max(r_lo, s) < min(r_hi, s + L));
    unsigned smk = (1u << kH) - 1u;
    float M0 = -INFINITY, M1 = -INFINITY, L0 = 0.f, L1 = 0.f, o00 = 0.f, o01 = 0.f, o10 = 0.f, o11 = 0.f;
    if (b == last_b) {
      pmk &= ~(1ull << j);
      M0 = rm[0]; M1 = rm[1]; L0 = rs[0]; L1 = rs[1]; o00 = ro[0][0]; o01 = ro[0][1]; o10 = ro[1][0]; o11 = ro[1][1];
    } else if (b == held_b) {
      pmk &= ~(1ull << j);
      M0 = hm[0]; M1 = hm[1]; L0 = hs[0]; L1 = hs[1]; o00 = ho[0][0]; o01 = ho[0][1]; o10 = ho[1][0]; o11 = ho[1][1];
    }
    const unsigned p_rows = WS_PP + ((hg * 80 + lane + b) * 4 + w) * PP_W;
    const unsigned s_base = WS_HP + ((lane & 15) * 16 + h) * HP_W;
    unsigned spins = 0;
    bool light = false;
    while (pmk | smk) {
      asm volatile("" ::: "memory");
      unsigned long long tp = pmk; unsigned ts = smk;
      const bool single = (__builtin_popcountll(pmk) + __builtin_popcount(smk)) == 1;
      if (light && !single) {
        const int jp = pmk ? (((pmk >> lane) & 1) ? lane : __builtin_ctzll(pmk)) : 0;
        const int js = smk ? (((smk >> (lane & 15)) & 1) ? (lane & 15) : __builtin_ctz(smk)) : 0;
        u32x4 a = {0u, tag, 0u, tag}, c = {0u, tag, 0u, tag};
        if (pmk) a = __builtin_amdgcn_raw_buffer_load_b128(wsr, WS_PP + ((hg * 80 + jp + b) * 4 + w) * PP_W + 2048 + 16, 0, 16);
        if (smk) c = __builtin_amdgcn_raw_buffer_load_b128(wsr, WS_HP + (js * 16 + h) * HP_W + 32768 + (2 * b + 1) * 16, 0, 16);
        tp &= __ballot(a[1] == tag && a[3] == tag);
        ts &= (unsigned)__ballot(c[1] == tag && c[3] == tag);
      }
      bool progress = false;
      if (tp | ts) {
        auto batch = [&](auto nb_tag) {
          constexpr int NB = decltype(nb_tag)::value;
          unsigned rows[NB], mls[NB]; unsigned long long bitp[NB]; unsigned bits[NB];
#pragma unroll
          for (int k = 0; k < NB; ++k) {
            if (tp) { const int q = __builtin_ctzll(tp); bitp[k] = tp & (~tp + 1); bits[k] = 0; tp &= tp - 1; rows[k] = (unsigned)rl((int)p_rows, q); mls[k] = rows[k] + 2048; }
            else if (ts) { const int q = __builtin_ctz(ts); bits[k] = ts & (~ts + 1); bitp[k] = 0; ts &= ts - 1;
                           const unsigned sl = (unsigned)rl((int)s_base, q); rows[k] = sl + b * 2048; mls[k] = sl + 32768 + b * 32; }
            else { bitp[k] = 0; bits[k] = 0; rows[k] = rows[0]; mls[k] = mls[0]; }
          }
          u32x4 d0[NB], d1[NB], dm[NB];
          if (R0AUX != 16 && spins == 0) {
#pragma unroll
            for (int k = 0; k < NB; ++k) {
              d0[k] = __builtin_amdgcn_raw_buffer_load_b128(wsr, lane * 16, rows[k], R0AUX);
              d1[k] = __builtin_amdgcn_raw_buffer_load_b128(wsr, 1024 + lane * 16, rows[k], R0AUX);
              dm[k] = __builtin_amdgcn_raw_buffer_load_b128(wsr, (lane & 1) * 16, mls[k], R0AUX);
            }
          } else {
#pragma unroll
            for (int k = 0; k < NB; ++k) {
              d0[k] = __builtin_amdgcn_raw_buffer_load_b128(wsr, lane * 16, rows[k], 16);
              d1[k] = __builtin_amdgcn_raw_buffer_load_b128(wsr, 1024 + lane * 16, rows[k], 16);
              dm[k] = __builtin_amdgcn_raw_buffer_load_b128(wsr, (lane & 1) * 16, mls[k], 16);
            }
          }
#pragma unroll
          for (int k = 0; k < NB; ++k) {
            const bool ok = d0[k][1] == tag && d0[k][3] == tag && d1[k][1] == tag && d1[k][3] == tag && dm[k][1] == tag && dm[k][3] == tag;
            if ((bitp[k] | bits[k]) && __all(ok)) {
              const float mv = __uint_as_float(dm[k][0]), lv = __uint_as_float(dm[k][2]);
              const float m0 = rlf(mv, 0), m1 = rlf(mv, 1), l0 = rlf(lv, 0), l1 = rlf(lv, 1);
              const float n0 = fmaxf(M0, m0), n1 = fmaxf(M1, m1);
              const float a0 = __builtin_amdgcn_exp2f(M0 - n0), b0 = __builtin_amdgcn_exp2f(m0 - n0);
              const float a1 = __builtin_amdgcn_exp2f(M1 - n1), b1 = __builtin_amdgcn_exp2f(m1 - n1);
              L0 = L0 * a0 + l0 * b0; L1 = L1 * a1 + l1 * b1;
              o00 = o00 * a0 + __uint_as_float(d0[k][0]) * b0; o01 = o01 * a0 + __uint_as_float(d0[k][2]) * b0;
              o10 = o10 * a1 + __uint_as_float(d1[k][0]) * b1; o11 = o11 * a1 + __uint_as_float(d1[k][2]) * b1;
              M0 = n0; M1 = n1;
              pmk &= ~bitp[k]; smk &= ~bits[k];
              progress = true;
            }
          }
        };
        const int cnt = __builtin_popcountll(tp) + __builtin_popcount(ts);
        if (cnt == 1) batch(IntC<1>{}); else if (cnt <= 3) batch(IntC<3>{}); else batch(IntC<6>{});

      }
      if (!progress) {
        if (++spins > (1u << 16)) break;
        if (spins > FULLR) { light = true; __builtin_amdgcn_s_sleep(SLEEP); }
      }
    }
    const float i0 = 1.f / L0, i1 = 1.f / L1;
    __hip_bfloat16* orow = p.out + (b * NH + 2 * h) * DH + 2 * lane;
    *(unsigned*)orow = pk_bf16(o00 * i0, o01 * i0);
    *(unsigned*)(orow + DH) = pk_bf16(o10 * i1, o11 * i1);
    if (lane == 0) __builtin_amdgcn_raw_buffer_store_b32(tag, wsr, WS_G + (h * 16 + b) * 4, 0, GAUX);
  }
  STAMP(3);
  STAMP_FLUSH();
  PROBE(6);
  PROBE_END();
}

}

extern "C" void kernel_launch(void* const* d_in, const int* in_sizes, int n_in, void* d_out, int out_size, void* d_ws, size_t ws_size,
                              hipStream_t stream) {
  using namespace rattn;
  Params p;
  p.q = (const char*)d_in[0]; p.k = (const char*)d_in[1]; p.v = (const char*)d_in[2];
  p.bt = (const int*)d_in[3]; p.lens = (const int*)d_in[4];
  p.out = (__hip_bfloat16*)d_out; p.ws = (char*)d_ws;
  p.kvbytes = (unsigned)in_sizes[1] * 2u; p.unused = 0;
  (void)hipFuncSetAttribute((const void*)radix_attn_kernel, hipFuncAttributeMaxDynamicSharedMemorySize, LDS_TOTAL);
  hipLaunchKernelGGL(radix_attn_kernel, dim3(256), dim3(256), LDS_TOTAL, stream, p);
}
```

```cpp
#include <hip/hip_runtime.h>
#include <hip/hip_bf16.h>
#include <stdint.h>

namespace rattn {

constexpr int NSEQ = 16, NH = 32, NKV = 16, DH = 128, NBLK = 256, BS = 16;
constexpr int LK = NBLK * BS;

constexpr int HRHO = 50;
constexpr int PACE = 6900;
constexpr int PACE_LEAD = 2;
constexpr int PACE_H = 88;
constexpr int HPUB_AT = 85;
constexpr int SKEW = 1;
constexpr int MDELAY = 30;
constexpr int FASTTRY = 2;

constexpr int R = 3;
constexpr int STAGE = 32768;
constexpr int RH = 4;
constexpr int RING_BYTES = 4 * RH * 8192;
constexpr int LDS_BT = RING_BYTES;
constexpr int LDS_QB = LDS_BT + NSEQ * NBLK * 4;
constexpr int LDS_SC = LDS_QB + 4 * 4 * 512;
constexpr int LDS_MISC = LDS_SC + 4096;
constexpr int LDS_GEN = LDS_MISC + 64;
constexpr int LDS_TOTAL = LDS_GEN + 256;
static_assert(LDS_TOTAL <= 160 * 1024 && LDS_MISC - LDS_QB >= 8192 && RING_BYTES >= R * STAGE, "LDS");

constexpr unsigned WS_G = 0;
constexpr unsigned WS_PP = 4096;
constexpr unsigned PP_W = 2176;
constexpr unsigned WS_HP = WS_PP + 320u * 4u * PP_W + 4096u;
constexpr unsigned HP_W = 32768 + 512;
constexpr unsigned WS_END = WS_HP + 16u * 16u * HP_W;

typedef unsigned u32x4 __attribute__((ext_vector_type(4)));
typedef unsigned u32x2 __attribute__((ext_vector_type(2)));
typedef float f32x4 __attribute__((ext_vector_type(4)));
typedef float f32x2 __attribute__((ext_vector_type(2)));
typedef __bf16 bf16x8 __attribute__((ext_vector_type(8)));
typedef __bf16 bf16x2 __attribute__((ext_vector_type(2)));
typedef short s16x4 __attribute__((ext_vector_type(4)));
typedef int i32x4 __attribute__((ext_vector_type(4)));
#define LAS __attribute__((address_space(3)))

constexpr float C2 = 0.088388347648318440f * 1.4426950408889634f;

__device__ __forceinline__ int rfl(int x) { return __builtin_amdgcn_readfirstlane(x); }
__device__ __forceinline__ int rl(int x, int l) { return __builtin_amdgcn_readlane(x, l); }
__device__ __forceinline__ float rlf(float x, int l) { return __builtin_bit_cast(float, __builtin_amdgcn_readlane(__builtin_bit_cast(int, x), l)); }

__device__ __forceinline__ void dma16(const void* sbase, unsigned voff, unsigned dst) {
  unsigned keep;
  asm volatile("s_mov_b32 %0, m0\n\ts_mov_b32 m0, %3\n\ts_nop 0\n\tglobal_load_lds_dwordx4 %1, %2\n\ts_mov_b32 m0, %0"
               : "=&s"(keep) : "v"(voff), "s"(sbase), "s"(dst) : "memory");
}
__device__ __forceinline__ void dma16_sc1(const void* sbase, unsigned voff, unsigned dst) {
  unsigned keep;
  asm volatile("s_mov_b32 %0, m0\n\ts_mov_b32 m0, %3\n\ts_nop 0\n\tglobal_load_lds_dwordx4 %1, %2 sc1\n\ts_mov_b32 m0, %0"
               : "=&s"(keep) : "v"(voff), "s"(sbase), "s"(dst) : "memory");
}
__device__ __forceinline__ void dma_stage(const void* sK, const void* sV, unsigned dstK, unsigned dstV,
                                          unsigned k0, unsigned k1, unsigned k2, unsigned k3,
                                          unsigned v0, unsigned v1, unsigned v2, unsigned v3) {
  unsigned keep;
  asm volatile("s_mov_b32 %0, m0\n\ts_mov_b32 m0, %3\n\ts_nop 0\n\t"
               "global_load_lds_dwordx4 %5, %1 nt\n\t"
               "global_load_lds_dwordx4 %6, %1 offset:1024 nt\n\t"
               "global_load_lds_dwordx4 %7, %1 offset:2048 nt\n\t"
               "global_load_lds_dwordx4 %8, %1 offset:3072 nt\n\t"
               "s_mov_b32 m0, %4\n\ts_nop 0\n\t"
               "global_load_lds_dwordx4 %9, %2 nt\n\t"
               "global_load_lds_dwordx4 %10, %2 offset:1024 nt\n\t"
               "global_load_lds_dwordx4 %11, %2 offset:2048 nt\n\t"
               "global_load_lds_dwordx4 %12, %2 offset:3072 nt\n\t"
               "s_mov_b32 m0, %0"
               : "=&s"(keep)
               : "s"(sK), "s"(sV), "s"(dstK), "s"(dstV), "v"(k0), "v"(k1), "v"(k2), "v"(k3), "v"(v0), "v"(v1), "v"(v2), "v"(v3)
               : "memory");
}
template <int CTRL> __device__ __forceinline__ int dppi(int x) { return __builtin_amdgcn_update_dpp(0, x, CTRL, 0xf, 0xf, true); }
template <int CTRL> __device__ __forceinline__ int dppi_keep(int old, int x) { return __builtin_amdgcn_update_dpp(old, x, CTRL, 0xf, 0xf, false); }

__device__ __forceinline__ float xg_max(float x) {
  auto s = __builtin_amdgcn_permlane16_swap(__float_as_uint(x), __float_as_uint(x), false, false);
  x = fmaxf(__uint_as_float(s[0]), __uint_as_float(s[1]));
  auto t = __builtin_amdgcn_permlane32_swap(__float_as_uint(x), __float_as_uint(x), false, false);
  return fmaxf(__uint_as_float(t[0]), __uint_as_float(t[1]));
}
__device__ __forceinline__ float xg_sum(float x) {
  auto s = __builtin_amdgcn_permlane16_swap(__float_as_uint(x), __float_as_uint(x), false, false);
  x = __uint_as_float(s[0]) + __uint_as_float(s[1]);
  auto t = __builtin_amdgcn_permlane32_swap(__float_as_uint(x), __float_as_uint(x), false, false);
  return __uint_as_float(t[0]) + __uint_as_float(t[1]);
}
__device__ __forceinline__ unsigned pk_bf16(float a, float b) {
  bf16x2 v = {(__bf16)a, (__bf16)b};
  return __builtin_bit_cast(unsigned, v);
}

template <int NQ>
__device__ __forceinline__ void tile_compute(LAS char* lds, unsigned ka, unsigned va, const bf16x8 (&qf)[NQ][4], f32x4 (&acc)[NQ][8],
                                             float (&m)[NQ], float (&l)[NQ], int lo, int hi, int g4) {
  bf16x8 kf[4];
#pragma unroll
  for (int s = 0; s < 4; ++s) kf[s] = *(LAS bf16x8*)(lds + (ka ^ (s << 6)));
  s16x4 vf[8];
#pragma unroll
  for (int k = 0; k < 8; ++k) vf[k] = __builtin_amdgcn_ds_read_tr16_b64_v4i16((LAS s16x4*)(lds + (va ^ (k << 5))));
  f32x4 S[NQ];
#pragma unroll
  for (int qb = 0; qb < NQ; ++qb) {
    S[qb] = f32x4{0.f, 0.f, 0.f, 0.f};
#pragma unroll
    for (int s = 0; s < 4; ++s) S[qb] = __builtin_amdgcn_mfma_f32_16x16x32_bf16(kf[s], qf[qb][s], S[qb], 0, 0, 0);
  }
  if (lo > 0 || hi < 16) {
#pragma unroll
    for (int qb = 0; qb < NQ; ++qb)
#pragma unroll
      for (int r = 0; r < 4; ++r) { const int t = 4 * g4 + r; S[qb][r] = (t >= lo && t < hi) ? S[qb][r] : -INFINITY; }
  }
  s16x4 pb[NQ];
  bool grow = false;
  float alpha[NQ];
#pragma unroll
  for (int qb = 0; qb < NQ; ++qb) {
    float mx = fmaxf(fmaxf(S[qb][0], S[qb][1]), fmaxf(S[qb][2], S[qb][3]));
    mx = xg_max(mx);
    const float mn2 = fmaxf(m[qb], mx);
    grow |= mn2 > m[qb];
    alpha[qb] = __builtin_amdgcn_exp2f((m[qb] - mn2) * C2);
    m[qb] = mn2;
    const float mc = -mn2 * C2;
    const float p0 = __builtin_amdgcn_exp2f(fmaf(S[qb][0], C2, mc)), p1 = __builtin_amdgcn_exp2f(fmaf(S[qb][1], C2, mc));
    const float p2 = __builtin_amdgcn_exp2f(fmaf(S[qb][2], C2, mc)), p3 = __builtin_amdgcn_exp2f(fmaf(S[qb][3], C2, mc));
    l[qb] = fmaf(l[qb], alpha[qb], (p0 + p1) + (p2 + p3));
    pb[qb] = __builtin_bit_cast(s16x4, u32x2{pk_bf16(p0, p1), pk_bf16(p2, p3)});
  }
  if (__any(grow)) {
#pragma unroll
    for (int qb = 0; qb < NQ; ++qb)
#pragma unroll
      for (int k = 0; k < 8; ++k) acc[qb][k] *= alpha[qb];
  }
#pragma unroll
  for (int qb = 0; qb < NQ; ++qb)
#pragma unroll
    for (int k = 0; k < 8; ++k) acc[qb][k] = __builtin_amdgcn_mfma_f32_16x16x16bf16_1k(vf[k], pb[qb], acc[qb][k], 0, 0, 0);
}

template <int V> struct IntC { static constexpr int value = V; };

struct Params {
  const char* q; const char* k; const char* v; const int* bt; const int* lens; __hip_bfloat16* out; char* ws;
};

__global__ void __launch_bounds__(256, 2) radix_attn_kernel(Params p) {
  extern __shared__ __attribute__((aligned(16))) char smem[];
  LAS char* lds = (LAS char*)smem;
  const int tid = threadIdx.x, lane = tid & 63, w = rfl(tid >> 6);
  const int gidx = blockIdx.x;
  const int hg = (gidx >> 3) & 3;
  const int jj = (((gidx & 7) + 4 * (hg & 1)) & 7) | ((gidx >> 5) << 3);
  const int c16 = lane & 15, g4 = lane >> 4;
  const auto wsr = __builtin_amdgcn_make_buffer_rsrc(p.ws, 0, WS_END, 0x00020000);

  unsigned voffK[4], voffV[4], oK[4], oV[4];
  int pm, len_l, minlen, vpages, incp, sump; bool zq_l;
  {
    const i32x4* bt4 = (const i32x4*)p.bt;
    const i32x4 r0 = bt4[lane];
    i32x4 rr[4];
#pragma unroll
    for (int i = 0; i < 4; ++i) rr[i] = bt4[(4 * w + i) * 64 + lane];
    const int lenraw = p.lens[c16];
    __builtin_amdgcn_sched_barrier(0);
#pragma unroll
  for (int i = 0; i < 4; ++i) {
    const int t = 4 * w + i, u = 4 * i + g4;
    voffK[i] = g4 * 256 + ((c16 ^ t) << 4) + t * 4096 - i * 1024 + 4096;
    voffV[i] = g4 * 256 + ((c16 ^ (2 * (t & 7))) << 4) + t * 4096 - i * 1024 + 4096;
    oK[i] = u * 4096 + ((c16 ^ u) << 4) - i * 1024 + 4096;
    oV[i] = u * 4096 + ((c16 ^ (2 * (u & 7))) << 4) - i * 1024 + 4096;
  }
    asm volatile("" :: "v"(voffK[0]), "v"(voffK[1]), "v"(voffK[2]), "v"(voffK[3]), "v"(voffV[0]), "v"(voffV[1]), "v"(voffV[2]), "v"(voffV[3]),
                 "v"(oK[0]), "v"(oK[1]), "v"(oK[2]), "v"(oK[3]), "v"(oV[0]), "v"(oV[1]), "v"(oV[2]), "v"(oV[3]) : "memory");
    bool n0 = false, n1 = false, n2 = false, n3 = false;
#pragma unroll
    for (int i = 0; i < 4; ++i) {
      n0 |= rr[i][0] != r0[0]; n1 |= rr[i][1] != r0[1]; n2 |= rr[i][2] != r0[2]; n3 |= rr[i][3] != r0[3];
      *(LAS i32x4*)(lds + LDS_BT + ((4 * w + i) * 256 + 4 * lane) * 4) = rr[i];
    }
    zq_l = lenraw <= 0;
    len_l = zq_l ? LK : min(lenraw, LK);
    int mn = len_l;
    mn = min(mn, dppi_keep<0x128>(mn, mn)); mn = min(mn, dppi_keep<0x124>(mn, mn));
    mn = min(mn, dppi_keep<0x122>(mn, mn)); mn = min(mn, dppi_keep<0x121>(mn, mn));
    minlen = rfl(mn);
    vpages = (len_l + BS - 1) >> 4;
    incp = vpages;
    incp += dppi<0x111>(incp); incp += dppi<0x112>(incp); incp += dppi<0x114>(incp); incp += dppi<0x118>(incp);
    sump = rl(incp, 15);
    const int fc = n0 ? 0 : n1 ? 1 : n2 ? 2 : n3 ? 3 : 4;
    const unsigned long long mm = __ballot(fc < 4);
    int col = NBLK;
    if (mm) { const int fl = __builtin_ctzll(mm); col = 4 * fl + rl(fc, fl); }
    if (lane == 0) *(LAS int*)(lds + LDS_MISC + 4 * w) = col;
    asm volatile("s_waitcnt lgkmcnt(0)\n\ts_barrier" ::: "memory");
    const i32x4 mc = *(LAS i32x4*)(lds + LDS_MISC);
    pm = rfl(min(min(mc[0], mc[1]), min(mc[2], mc[3])));
  }
  const int P = min(pm, minlen >> 4);
  const int tsh = P * BS;
  const int vL = vpages - P;
  const int vS = incp - vpages - c16 * P;
  const int N = sump - 16 * P;
  int kH = 0;
  if (P > 0) kH = min(min(15, P), max(1, rfl((int)__builtin_ceilf((float)P * (1024.f / HRHO) * __builtin_amdgcn_rcpf((float)(P + N))))));
  const int nW = 64 - 4 * kH;
  const float inv_nw = __builtin_amdgcn_rcpf((float)nW);
  const unsigned zmask = (unsigned)(__ballot(zq_l) & 0xffffull);
  const bool isH = jj < 4 * kH;
  auto gen_fetch = [&](int head) { if (lane < 4) dma16_sc1(p.ws + WS_G, head * 64 + lane * 16, LDS_GEN + w * 64); };
  auto gen_row = [&]() { return *(LAS unsigned*)(lds + LDS_GEN + w * 64 + c16 * 4); };
  const float t_all = (float)(P + N) * (16.f * 819.2f / (float)PACE);
  const unsigned long long t_ref = __builtin_amdgcn_s_memrealtime();
  auto cut = [&](int i) { return (int)(((float)(i * N + (i & 1) * ((N * SKEW) >> 6)) + 0.5f) * inv_nw); };
  const int r_lo = cut(lane), r_hi = cut(lane + 1);

  if (isH) {
    const int h = hg * 4 + (jj & 3), part = jj >> 2;
    const int p0 = (P * part) / kH, p1 = (P * (part + 1)) / kH;
#pragma unroll
    for (int i = 0; i < 2; ++i)
      dma16(p.q + (2 * w + i) * 16384 + h * 512, (lane >> 5) * 8192 + (lane & 31) * 16, LDS_QB + (2 * w + i) * 1024);
    const char* kb = p.k + h * 256 - 4096;
    const char* vb = p.v + h * 256 - 4096;
    const int nt = max(0, (p1 - p0 - w + 3) >> 2);
    const unsigned ring = w * (RH * 8192);
    int pg0 = 0, vpg = *(LAS int*)(lds + LDS_BT + min(p0 + w + 4 * lane, NBLK - 1) * 4);
    float pace_h = t_all * (PACE_H * 0.01f) * __builtin_amdgcn_rcpf((float)max(nt, 1)), pace_hb = -3.f * pace_h;
    float t_pub = t_all * (HPUB_AT * 0.01f);
    auto issue = [&](int t) {
      { const int due = rfl((int)(pace_hb + (float)t * pace_h)); if (due > 0) while ((int)(__builtin_amdgcn_s_memrealtime() - t_ref) < due) __builtin_amdgcn_s_sleep(1); }
      if (t - pg0 == 64) { pg0 = t; vpg = *(LAS int*)(lds + LDS_BT + min(p0 + w + 4 * (t + lane), NBLK - 1) * 4); }
      const size_t pb = (size_t)(unsigned)rl(vpg, t - pg0) * 65536;
      const unsigned dst = ring + (t & (RH - 1)) * 8192;
      dma_stage(kb + pb, vb + pb, dst, dst + 4096, oK[0], oK[1], oK[2], oK[3], oV[0], oV[1], oV[2], oV[3]);
    };
    const int npre = min(nt, RH - 1);
#pragma unroll 1
    for (int t = 0; t < npre; ++t) issue(t);
    gen_fetch(h);
    if (npre == 3) asm volatile("s_waitcnt vmcnt(25)\n\ts_barrier" ::: "memory");
    else asm volatile("s_waitcnt vmcnt(0)\n\ts_barrier" ::: "memory");
    bf16x8 qf[2][4];
#pragma unroll
    for (int qb = 0; qb < 2; ++qb) {
      const bool z = (zmask >> (8 * qb + (c16 >> 1))) & 1;
#pragma unroll
      for (int s = 0; s < 4; ++s) {
        u32x4 x = *(LAS u32x4*)(lds + LDS_QB + (16 * qb + c16) * 256 + (4 * s + g4) * 16);
        if (z) x = u32x4{0u, 0u, 0u, 0u};
        qf[qb][s] = __builtin_bit_cast(bf16x8, x);
      }
    }
    f32x4 acc[2][8];
    float m[2] = {-INFINITY, -INFINITY}, l[2] = {0.f, 0.f};
#pragma unroll
    for (int qb = 0; qb < 2; ++qb)
#pragma unroll
      for (int k = 0; k < 8; ++k) acc[qb][k] = f32x4{0.f, 0.f, 0.f, 0.f};
    const unsigned ka0 = ring + c16 * 256 + ((g4 ^ c16) << 4);
    unsigned va0;
    { const int tq = 4 * g4 + (c16 >> 2), pp = lane & 3; va0 = ring + 4096 + tq * 256 + ((tq & 7) << 5) + ((pp >> 1) << 4) + ((pp & 1) << 3); }
#pragma unroll 1
    for (int t = 0; t < nt; ++t) {
      const int young = min(nt - 1 - t, RH - 2);
      if (young == 2) asm volatile("s_waitcnt vmcnt(16) lgkmcnt(0)" ::: "memory");
      else if (young == 1) asm volatile("s_waitcnt vmcnt(8) lgkmcnt(0)" ::: "memory");
      else asm volatile("s_waitcnt vmcnt(0) lgkmcnt(0)" ::: "memory");
      if (t + RH - 1 < nt) issue(t + RH - 1);
      const unsigned sb = (t & (RH - 1)) * 8192;
      tile_compute<2>(lds, ka0 + sb, va0 + sb, qf, acc, m, l, 0, 16, g4);
    }
    constexpr unsigned ROWB = 528, WAVEB = 32 * ROWB, MLB = 4 * WAVEB;
    asm volatile("s_waitcnt lgkmcnt(0)\n\ts_barrier" ::: "memory");
#pragma unroll
    for (int qb = 0; qb < 2; ++qb) {
      const float lt = xg_sum(l[qb]);
#pragma unroll
      for (int k = 0; k < 8; ++k) *(LAS f32x4*)(lds + w * WAVEB + (16 * qb + c16) * ROWB + (16 * k + 4 * g4) * 4) = acc[qb][k];
      if (g4 == 0) *(LAS f32x2*)(lds + MLB + (w * 32 + 16 * qb + c16) * 8) = f32x2{m[qb] * C2, lt};
    }
    asm volatile("s_waitcnt lgkmcnt(0)\n\ts_barrier" ::: "memory");
    while ((int)(__builtin_amdgcn_s_memrealtime() - t_ref) < (int)t_pub) __builtin_amdgcn_s_sleep(8);
    asm volatile("s_waitcnt vmcnt(0)" ::: "memory");
    const unsigned gvh = gen_row();
    const unsigned slot = WS_HP + (part * 16 + h) * HP_W;
    float mrow = 0.f, lrow = 0.f;
#pragma unroll
    for (int rr = 0; rr < 8; ++rr) {
      const int row = 8 * w + rr;
      f32x2 ml[4], o[4];
#pragma unroll
      for (int v = 0; v < 4; ++v) { ml[v] = *(LAS f32x2*)(lds + MLB + (v * 32 + row) * 8); o[v] = *(LAS f32x2*)(lds + v * WAVEB + row * ROWB + lane * 8); }
      const float M = fmaxf(fmaxf(ml[0][0], ml[1][0]), fmaxf(ml[2][0], ml[3][0]));
      float L = 0.f, o0 = 0.f, o1 = 0.f;
#pragma unroll
      for (int v = 0; v < 4; ++v) {
        const float e = ml[v][1] > 0.f ? __builtin_amdgcn_exp2f(ml[v][0] - M) : 0.f;
        L = fmaf(ml[v][1], e, L); o0 = fmaf(o[v][0], e, o0); o1 = fmaf(o[v][1], e, o1);
      }
      const unsigned tag = (unsigned)rl((int)gvh, row >> 1) + 1u;
      const u32x4 gr = {__float_as_uint(o0), tag, __float_as_uint(o1), tag};
      __builtin_amdgcn_raw_buffer_store_b128(gr, wsr, slot + row * 1024 + lane * 16, 0, 16);
      if (lane == rr) { mrow = M; lrow = L; }
    }
    const unsigned tagr = (unsigned)__shfl((int)gvh, ((8 * w + lane) >> 1) & 15) + 1u;
    if (lane < 8) {
      const unsigned tag = tagr;
      const u32x4 gm = {__float_as_uint(mrow), tag, __float_as_uint(lrow), tag};
      __builtin_amdgcn_raw_buffer_store_b128(gm, wsr, slot + 32768 + (8 * w + lane) * 16, 0, 16);
    }
    return;
  }

  const int j = jj - 4 * kH, h = hg * 4 + w;
  const int x0 = rl(r_lo, j), x1 = rl(r_hi, j);
  const unsigned umask = (unsigned)(__ballot(max(x0, vS) < min(x1, vS + vL)) & 0xffffull);
  const unsigned kaddr0 = w * 256 + c16 * 1024 + ((g4 ^ c16) << 4);
  unsigned vaddr0;
  { const int tq = 4 * g4 + (c16 >> 2), pp = lane & 3; vaddr0 = 16384 + tq * 1024 + w * 256 + ((tq & 7) << 5) + ((pp >> 1) << 4) + ((pp & 1) << 3); }
  const char* kbase = p.k + hg * 1024 - 4096;
  const char* vbase = p.v + hg * 1024 - 4096;

  unsigned i_mask = umask; int i_row = 0, i_t0 = 0, i_t1 = 0, i_page; bool i_more = false;
  int n_iss = 0, i_slot = 0;
  auto unit_range = [&](int b, int& t0, int& t1) {
    const int s = rl(vS, b), L = rl(vL, b);
    t0 = (P + max(x0, s) - s) * BS; t1 = min((P + min(x1, s + L) - s) * BS, rl(len_l, b));
  };
  int i_piece = 0;
  auto q_dma = [&](int b) {
    if (lane < 32) dma16(p.q + b * 8192 + h * 512, lane * 16, LDS_QB + (w * 4 + (i_piece & 3)) * 512);
    ++i_piece;
  };
  if (i_mask) {
    i_row = __builtin_ctz(i_mask); i_mask &= i_mask - 1; unit_range(i_row, i_t0, i_t1);
    q_dma(i_row);
    i_more = true;
  }
  i_page = i_t0 >> 4;
  int i_pg0 = i_page, vpg = *(LAS int*)(lds + LDS_BT + (i_row * NBLK + min(i_page + lane, NBLK - 1)) * 4);
  int pace_tok = 0;
  float pace_d = t_all * __builtin_amdgcn_rcpf((float)max(x1 - x0, 1)), pace_b = -(float)PACE_LEAD * pace_d;
  auto issue_stage = [&]() {
    const int phys = rl(vpg, i_page - i_pg0);
    const int hi = i_t1 - i_page * 16;
    {
      const int due = rfl((int)(pace_b + (float)pace_tok * pace_d));
      pace_tok += 1;
      if (due > 0) while ((int)(__builtin_amdgcn_s_memrealtime() - t_ref) < due) __builtin_amdgcn_s_sleep(1);
    }
    const size_t pb = (size_t)(unsigned)phys * 65536;
    const unsigned dst = i_slot * STAGE + 4 * w * 1024;
    if (hi >= 16) {
      dma_stage(kbase + pb, vbase + pb, dst, dst + 16384, voffK[0], voffK[1], voffK[2], voffK[3], voffV[0], voffV[1], voffV[2], voffV[3]);
    } else {
      unsigned ok[4], ov[4];
#pragma unroll
      for (int i = 0; i < 4; ++i) {
        const int d = (min(4 * w + i, hi - 1) - (4 * w + i)) * 4096;
        ok[i] = voffK[i] + d; ov[i] = voffV[i] + d;
      }
      dma_stage(kbase + pb, vbase + pb, dst, dst + 16384, ok[0], ok[1], ok[2], ok[3], ov[0], ov[1], ov[2], ov[3]);
    }
    ++n_iss; i_slot = (i_slot + 1 == R) ? 0 : i_slot + 1;
    ++i_page;
    if (i_page * 16 >= i_t1) {
      if (i_mask) {
        i_row = __builtin_ctz(i_mask); i_mask &= i_mask - 1; unit_range(i_row, i_t0, i_t1); i_page = i_t0 >> 4;
        q_dma(i_row);
        i_pg0 = i_page; vpg = *(LAS int*)(lds + LDS_BT + (i_row * NBLK + min(i_page + lane, NBLK - 1)) * 4);
      } else i_more = false;
    } else if (i_page - i_pg0 == 64) {
      i_pg0 = i_page; vpg = *(LAS int*)(lds + LDS_BT + (i_row * NBLK + min(i_page + lane, NBLK - 1)) * 4);
    }
  };
#pragma unroll 1
  for (int s = 0; s < R - 1; ++s) if (i_more) issue_stage();
  gen_fetch(h);

  unsigned own = 0;
  {
    unsigned mrem = umask;
    while (mrem) {
      const int b = __builtin_ctz(mrem); mrem &= mrem - 1;
      const int s = rl(vS, b), L = rl(vL, b);
      unsigned long long pmk = __ballot(lane < nW && max(r_lo, s) < min(r_hi, s + L));
      int idx = w % __builtin_popcountll(pmk);
      while (idx--) pmk &= pmk - 1;
      if (__builtin_ctzll(pmk) == j) own |= 1u << b;
    }
    unsigned zl = (unsigned)(__ballot(vL == 0) & 0xffffull);
    while (zl) { const int b = __builtin_ctz(zl); zl &= zl - 1; if ((b * 4) % nW == j) own |= 1u << b; }
  }

  int fb = -1, fcnt = 0; unsigned vrow = 0, vmlo = 0;
  if (own && (own & (own - 1)) == 0) {
    const int b = __builtin_ctz(own);
    const unsigned above = umask >> b;
    if ((umask >> b & 1) && (above == 1 || __builtin_popcount(above) == 2)) {
      const int s = rl(vS, b), L = rl(vL, b);
      const unsigned long long pmk = __ballot(lane < nW && max(r_lo, s) < min(r_hi, s + L)) & ~(1ull << j);
      const int np = __builtin_popcountll(pmk);
      if (np + kH <= 8 && np + kH > 0) {
        fb = b; fcnt = np + kH;
        const int rank = __builtin_amdgcn_mbcnt_hi((unsigned)(pmk >> 32), __builtin_amdgcn_mbcnt_lo((unsigned)pmk, 0));
        const unsigned prow = WS_PP + ((hg * 80 + lane + b) * 4 + w) * PP_W;
        const unsigned pushed = (unsigned)__builtin_amdgcn_ds_permute((((pmk >> lane) & 1) ? rank : 63) << 2, (int)prow);
        const unsigned hb = WS_HP + (min(max(lane - np, 0), 15) * 16 + h) * HP_W;
        vrow = lane < np ? pushed : hb + b * 2048;
        const unsigned mlb = lane < np ? pushed + 2048 : hb + 32768 + b * 32;
        vmlo = (unsigned)__shfl((int)mlb, min(lane >> 1, fcnt - 1)) + (lane & 1) * 16;
      }
    }
  }

  int n_comp = 0, c_slot = 0;
  unsigned c_mask = umask;
  float ro[2][2] = {{0.f, 0.f}, {0.f, 0.f}}, rm[2] = {-INFINITY, -INFINITY}, rs[2] = {0.f, 0.f};
  float ho[2][2] = {{0.f, 0.f}, {0.f, 0.f}}, hm[2] = {-INFINITY, -INFINITY}, hs[2] = {0.f, 0.f};
  int last_b = -1, held_b = -1;
  auto publish = [&](int b, const float (&o)[2][2], const float (&mm)[2], const float (&ll)[2]) {
    const unsigned tag = (unsigned)rfl(*(LAS int*)(lds + LDS_GEN + w * 64 + b * 4)) + 1u;
    const unsigned slot = WS_PP + ((hg * 80 + j + b) * 4 + w) * PP_W;
    const u32x4 g0 = {__float_as_uint(o[0][0]), tag, __float_as_uint(o[0][1]), tag};
    const u32x4 g1 = {__float_as_uint(o[1][0]), tag, __float_as_uint(o[1][1]), tag};
    __builtin_amdgcn_raw_buffer_store_b128(g0, wsr, slot + lane * 16, 0, 16);
    __builtin_amdgcn_raw_buffer_store_b128(g1, wsr, slot + 1024 + lane * 16, 0, 16);
    if (lane < 2) {
      const u32x4 gm = {__float_as_uint(lane ? mm[1] : mm[0]), tag, __float_as_uint(lane ? ll[1] : ll[0]), tag};
      __builtin_amdgcn_raw_buffer_store_b128(gm, wsr, slot + 2048 + lane * 16, 0, 16);
    }
  };
  bf16x8 qf[1][4];
  f32x4 acc[1][8];
  float m[1] = {-INFINITY}, l[1] = {0.f};
#pragma unroll
  for (int k = 0; k < 8; ++k) acc[0][k] = f32x4{0.f, 0.f, 0.f, 0.f};
#pragma unroll
  for (int s = 0; s < 4; ++s) qf[0][s] = __builtin_bit_cast(bf16x8, u32x4{0u, 0u, 0u, 0u});
  auto finish_piece = [&](int b) {
    const float lt = xg_sum(l[0]);
    LAS char* sc = lds + LDS_SC + w * 1024;
    if (c16 < 2) {
#pragma unroll
      for (int k = 0; k < 8; ++k) *(LAS f32x4*)(sc + c16 * 512 + (16 * k + 4 * g4) * 4) = acc[0][k];
    }
    asm volatile("s_waitcnt lgkmcnt(0)" ::: "memory");
    const f32x2 a0 = *(LAS f32x2*)(sc + lane * 8), a1 = *(LAS f32x2*)(sc + 512 + lane * 8);
    if (last_b >= 0) {
      if (held_b >= 0) publish(held_b, ho, hm, hs);
      held_b = last_b;
#pragma unroll
      for (int r = 0; r < 2; ++r) { ho[r][0] = ro[r][0]; ho[r][1] = ro[r][1]; hm[r] = rm[r]; hs[r] = rs[r]; }
    }
    ro[0][0] = a0[0]; ro[0][1] = a0[1]; ro[1][0] = a1[0]; ro[1][1] = a1[1];
    rm[0] = rlf(m[0], 0) * C2; rm[1] = rlf(m[0], 1) * C2;
    rs[0] = rlf(lt, 0); rs[1] = rlf(lt, 1);
    last_b = b;
  };
  int c_b = -1, c_piece = 0, c_left = 0;
#pragma unroll 1
  while (c_left > 0 || c_mask) {
    if (n_iss - n_comp > 1) asm volatile("s_waitcnt vmcnt(8) lgkmcnt(0)\n\ts_barrier" ::: "memory");
    else asm volatile("s_waitcnt vmcnt(0) lgkmcnt(0)\n\ts_barrier" ::: "memory");
    if (i_more) issue_stage();
    if (c_left <= 0) {
      if (c_b >= 0) finish_piece(c_b);
      c_b = __builtin_ctz(c_mask); c_mask &= c_mask - 1;
      int t0, t1; unit_range(c_b, t0, t1);
      c_left = t1 - t0;
      const bool z = (c16 >= 2) || ((zmask >> c_b) & 1);
#pragma unroll
      for (int s = 0; s < 4; ++s) {
        u32x4 x = *(LAS u32x4*)(lds + LDS_QB + (w * 4 + (c_piece & 3)) * 512 + (c16 & 1) * 256 + (4 * s + g4) * 16);
        if (z) x = u32x4{0u, 0u, 0u, 0u};
        qf[0][s] = __builtin_bit_cast(bf16x8, x);
      }
      ++c_piece;
      m[0] = -INFINITY; l[0] = 0.f;
#pragma unroll
      for (int k = 0; k < 8; ++k) acc[0][k] = f32x4{0.f, 0.f, 0.f, 0.f};
    }
    const unsigned sb = c_slot * STAGE;
    tile_compute<1>(lds, kaddr0 + sb, vaddr0 + sb, qf, acc, m, l, 0, c_left, g4);
    c_left -= 16;
    ++n_comp; c_slot = (c_slot + 1 == R) ? 0 : c_slot + 1;
  }
  if (c_b >= 0) finish_piece(c_b);
  asm volatile("s_waitcnt vmcnt(0)" ::: "memory");
  const unsigned gvw = gen_row();
  if (last_b >= 0 && !((own >> last_b) & 1)) { publish(last_b, ro, rm, rs); last_b = -1; }
  if (held_b >= 0 && !((own >> held_b) & 1)) { publish(held_b, ho, hm, hs); held_b = -1; }

  if (own) { const unsigned long long c0 = __builtin_amdgcn_s_memrealtime(); while (__builtin_amdgcn_s_memrealtime() - c0 < MDELAY) __builtin_amdgcn_s_sleep(1); }
  if (fb >= 0) {
    const int b = fb;
    const unsigned tag = (unsigned)rl((int)gvw, b) + 1u;
    const bool mine_last = b == last_b;
    const float om0 = mine_last ? rm[0] : hm[0], om1 = mine_last ? rm[1] : hm[1], ol0 = mine_last ? rs[0] : hs[0], ol1 = mine_last ? rs[1] : hs[1];
    const float oo00 = mine_last ? ro[0][0] : ho[0][0], oo01 = mine_last ? ro[0][1] : ho[0][1];
    const float oo10 = mine_last ? ro[1][0] : ho[1][0], oo11 = mine_last ? ro[1][1] : ho[1][1];
    auto attempt = [&](auto nb_tag) -> bool {
      constexpr int NB = decltype(nb_tag)::value;
      u32x4 d0[NB], d1[NB];
      unsigned so[NB];
#pragma unroll
      for (int k = 0; k < NB; ++k) so[k] = (unsigned)rl((int)vrow, min(k, fcnt - 1));
      asm volatile("" ::: "memory");
      const u32x4 dm = __builtin_amdgcn_raw_buffer_load_b128(wsr, vmlo, 0, 16);
#pragma unroll
      for (int k = 0; k < NB; ++k) {
        d0[k] = __builtin_amdgcn_raw_buffer_load_b128(wsr, lane * 16, so[k], 16);
        d1[k] = __builtin_amdgcn_raw_buffer_load_b128(wsr, 1024 + lane * 16, so[k], 16);
      }
      __builtin_amdgcn_sched_barrier(0);
      const bool live = lane < 2 * fcnt;
      const float mk = live ? __uint_as_float(dm[0]) : -INFINITY, lk = live ? __uint_as_float(dm[2]) : 0.f;
      const float om = (lane & 1) ? om1 : om0, ol = (lane & 1) ? ol1 : ol0;
      float M = fmaxf(mk, om);
      M = fmaxf(M, __builtin_bit_cast(float, dppi_keep<0x122>(__builtin_bit_cast(int, M), __builtin_bit_cast(int, M))));
      M = fmaxf(M, __builtin_bit_cast(float, dppi_keep<0x124>(__builtin_bit_cast(int, M), __builtin_bit_cast(int, M))));
      M = fmaxf(M, __builtin_bit_cast(float, dppi_keep<0x128>(__builtin_bit_cast(int, M), __builtin_bit_cast(int, M))));
      const float e = __builtin_amdgcn_exp2f(mk - M), eo = __builtin_amdgcn_exp2f(om - M);
      float Ls = lk * e;
      Ls += __builtin_bit_cast(float, dppi_keep<0x122>(0, __builtin_bit_cast(int, Ls)));
      Ls += __builtin_bit_cast(float, dppi_keep<0x124>(0, __builtin_bit_cast(int, Ls)));
      Ls += __builtin_bit_cast(float, dppi_keep<0x128>(0, __builtin_bit_cast(int, Ls)));
      Ls = fmaf(ol, eo, Ls);
      const float inv = __builtin_amdgcn_rcpf(Ls);
      const float en = e * inv, eon = eo * inv;
      const float eo0 = rlf(eon, 0), eo1 = rlf(eon, 1);
      float o00 = oo00 * eo0, o01 = oo01 * eo0, o10 = oo10 * eo1, o11 = oo11 * eo1;
      unsigned bad = (dm[1] ^ tag) | (dm[3] ^ tag);
#pragma unroll
      for (int k = 0; k < NB; ++k) {
        const float w0 = rlf(en, 2 * k), w1 = rlf(en, 2 * k + 1);
        bad |= ((d0[k][1] ^ tag) | (d0[k][3] ^ tag)) | ((d1[k][1] ^ tag) | (d1[k][3] ^ tag));
        o00 = fmaf(__uint_as_float(d0[k][0]), w0, o00); o01 = fmaf(__uint_as_float(d0[k][2]), w0, o01);
        o10 = fmaf(__uint_as_float(d1[k][0]), w1, o10); o11 = fmaf(__uint_as_float(d1[k][2]), w1, o11);
      }
      if (__any(bad != 0)) return false;
      __hip_bfloat16* orow = p.out + (b * NH + 2 * h) * DH + 2 * lane;
      *(unsigned*)orow = pk_bf16(o00, o01);
      *(unsigned*)(orow + DH) = pk_bf16(o10, o11);
      if (lane == 0) __builtin_amdgcn_raw_buffer_store_b32(tag, wsr, WS_G + (h * 16 + b) * 4, 0, 0);
      return true;
    };
    unsigned spins = 0;
#pragma unroll 1
    for (int t = 0; t < 16; ++t) {
      if (t >= FASTTRY) {
        while (true) {
          asm volatile("" ::: "memory");
          const u32x4 x = __builtin_amdgcn_raw_buffer_load_b128(wsr, vmlo, 0, 16);
          if (!__any(x[1] != tag || x[3] != tag) || ++spins > (1u << 16)) break;
          __builtin_amdgcn_s_sleep(2);
        }
      }
      const bool ok = fcnt <= 5 ? attempt(IntC<5>{}) : attempt(IntC<8>{});
      if (ok) { own = 0; break; }
    }
  }
  while (own) {
    const int b = __builtin_ctz(own); own &= own - 1;
    const unsigned tag = (unsigned)rl((int)gvw, b) + 1u;
    const int s = rl(vS, b), L = rl(vL, b);
    unsigned long long pmk = __ballot(lane < nW && max(r_lo, s) < min(r_hi, s + L));
    unsigned smk = (1u << kH) - 1u;
    float M0 = -INFINITY, M1 = -INFINITY, L0 = 0.f, L1 = 0.f, o00 = 0.f, o01 = 0.f, o10 = 0.f, o11 = 0.f;
    if (b == last_b) {
      pmk &= ~(1ull << j);
      M0 = rm[0]; M1 = rm[1]; L0 = rs[0]; L1 = rs[1]; o00 = ro[0][0]; o01 = ro[0][1]; o10 = ro[1][0]; o11 = ro[1][1];
    } else if (b == held_b) {
      pmk &= ~(1ull << j);
      M0 = hm[0]; M1 = hm[1]; L0 = hs[0]; L1 = hs[1]; o00 = ho[0][0]; o01 = ho[0][1]; o10 = ho[1][0]; o11 = ho[1][1];
    }
    const unsigned p_rows = WS_PP + ((hg * 80 + lane + b) * 4 + w) * PP_W;
    const unsigned s_base = WS_HP + ((lane & 15) * 16 + h) * HP_W;
    unsigned spins = 0;
    bool light = false;
    while (pmk | smk) {
      asm volatile("" ::: "memory");
      unsigned long long tp = pmk; unsigned ts = smk;
      const bool single = (__builtin_popcountll(pmk) + __builtin_popcount(smk)) == 1;
      if (light && !single) {
        const int jp = pmk ? (((pmk >> lane) & 1) ? lane : __builtin_ctzll(pmk)) : 0;
        const int js = smk ? (((smk >> (lane & 15)) & 1) ? (lane & 15) : __builtin_ctz(smk)) : 0;
        u32x4 a = {0u, tag, 0u, tag}, c = {0u, tag, 0u, tag};
        if (pmk) a = __builtin_amdgcn_raw_buffer_load_b128(wsr, WS_PP + ((hg * 80 + jp + b) * 4 + w) * PP_W + 2048 + 16, 0, 16);
        if (smk) c = __builtin_amdgcn_raw_buffer_load_b128(wsr, WS_HP + (js * 16 + h) * HP_W + 32768 + (2 * b + 1) * 16, 0, 16);
        tp &= __ballot(a[1] == tag && a[3] == tag);
        ts &= (unsigned)__ballot(c[1] == tag && c[3] == tag);
      }
      bool progress = false;
      if (tp | ts) {
        auto batch = [&](auto nb_tag) {
          constexpr int NB = decltype(nb_tag)::value;
          unsigned rows[NB], mls[NB]; unsigned long long bitp[NB]; unsigned bits[NB];
#pragma unroll
          for (int k = 0; k < NB; ++k) {
            if (tp) { const int q = __builtin_ctzll(tp); bitp[k] = tp & (~tp + 1); bits[k] = 0; tp &= tp - 1; rows[k] = (unsigned)rl((int)p_rows, q); mls[k] = rows[k] + 2048; }
            else if (ts) { const int q = __builtin_ctz(ts); bits[k] = ts & (~ts + 1); bitp[k] = 0; ts &= ts - 1;
                           const unsigned sl = (unsigned)rl((int)s_base, q); rows[k] = sl + b * 2048; mls[k] = sl + 32768 + b * 32; }
            else { bitp[k] = 0; bits[k] = 0; rows[k] = rows[0]; mls[k] = mls[0]; }
          }
          u32x4 d0[NB], d1[NB], dm[NB];
#pragma unroll
          for (int k = 0; k < NB; ++k) {
            d0[k] = __builtin_amdgcn_raw_buffer_load_b128(wsr, lane * 16, rows[k], 16);
            d1[k] = __builtin_amdgcn_raw_buffer_load_b128(wsr, 1024 + lane * 16, rows[k], 16);
            dm[k] = __builtin_amdgcn_raw_buffer_load_b128(wsr, (lane & 1) * 16, mls[k], 16);
          }
          __builtin_amdgcn_sched_barrier(0);
#pragma unroll
          for (int k = 0; k < NB; ++k) {
            const bool ok = d0[k][1] == tag && d0[k][3] == tag && d1[k][1] == tag && d1[k][3] == tag && dm[k][1] == tag && dm[k][3] == tag;
            if ((bitp[k] | bits[k]) && __all(ok)) {
              const float mv = __uint_as_float(dm[k][0]), lv = __uint_as_float(dm[k][2]);
              const float m0 = rlf(mv, 0), m1 = rlf(mv, 1), l0 = rlf(lv, 0), l1 = rlf(lv, 1);
              const float n0 = fmaxf(M0, m0), n1 = fmaxf(M1, m1);
              const float a0 = __builtin_amdgcn_exp2f(M0 - n0), b0 = __builtin_amdgcn_exp2f(m0 - n0);
              const float a1 = __builtin_amdgcn_exp2f(M1 - n1), b1 = __builtin_amdgcn_exp2f(m1 - n1);
              L0 = L0 * a0 + l0 * b0; L1 = L1 * a1 + l1 * b1;
              o00 = o00 * a0 + __uint_as_float(d0[k][0]) * b0; o01 = o01 * a0 + __uint_as_float(d0[k][2]) * b0;
              o10 = o10 * a1 + __uint_as_float(d1[k][0]) * b1; o11 = o11 * a1 + __uint_as_float(d1[k][2]) * b1;
              M0 = n0; M1 = n1;
              pmk &= ~bitp[k]; smk &= ~bits[k];
              progress = true;
            }
          }
        };
        const int cnt = __builtin_popcountll(tp) + __builtin_popcount(ts);
        if (cnt == 1) batch(IntC<1>{}); else if (cnt <= 3) batch(IntC<3>{}); else batch(IntC<6>{});

      }
      if (!progress) {
        if (++spins > (1u << 16)) break;
        light = true;
        __builtin_amdgcn_s_sleep(6);
      }
    }
    const float i0 = 1.f / L0, i1 = 1.f / L1;
    __hip_bfloat16* orow = p.out + (b * NH + 2 * h) * DH + 2 * lane;
    *(unsigned*)orow = pk_bf16(o00 * i0, o01 * i0);
    *(unsigned*)(orow + DH) = pk_bf16(o10 * i1, o11 * i1);
    if (lane == 0) __builtin_amdgcn_raw_buffer_store_b32(tag, wsr, WS_G + (h * 16 + b) * 4, 0, 0);
  }
}

}

extern "C" void kernel_launch(void* const* d_in, const int* in_sizes, int n_in, void* d_out, int out_size, void* d_ws, size_t ws_size,
                              hipStream_t stream) {
  using namespace rattn;
  Params p;
  p.q = (const char*)d_in[0]; p.k = (const char*)d_in[1]; p.v = (const char*)d_in[2];
  p.bt = (const int*)d_in[3]; p.lens = (const int*)d_in[4];
  p.out = (__hip_bfloat16*)d_out; p.ws = (char*)d_ws;
  (void)hipFuncSetAttribute((const void*)radix_attn_kernel, hipFuncAttributeMaxDynamicSharedMemorySize, LDS_TOTAL);
  hipLaunchKernelGGL(radix_attn_kernel, dim3(256), dim3(256), LDS_TOTAL, stream, p);
}
```

```cpp
#include <hip/hip_runtime.h>
#include <hip/hip_bf16.h>
#include <stdint.h>

namespace rattn {

constexpr int NSEQ = 16, NH = 32, NKV = 16, DH = 128, NBLK = 256, BS = 16;
constexpr int LK = NBLK * BS;
#define GAUX 0
#define KVW_LANES 3
#define KVW_SHIFT 21
#define AS0 3
#define AS1 11
#define A_MIN 5000
#define HRHO 50
#define MDELAY 30
#define PACE 7300
#define ADAPT 1
#define FASTTRY 2
#define A_F 1000
#define SKEW 1
#define PACE_LEAD 2
#define PACE_H 88
#define FULLR 0
#define R0AUX 16
#define HPUB_AT 85
#define A_LEAD 0.f
#define A_HMARGIN 350.f
#define A_HPUB 150.f
#define SLEEP2 2
#define MAPMODE 1
#define SLEEP 6
#define KVPOL "nt"
#define RING 3
constexpr int R = RING;
constexpr int STAGE = 32768;
constexpr int RH = 4;
constexpr int RING_BYTES = 4 * RH * 8192;
constexpr int LDS_BT = RING_BYTES;
constexpr int LDS_QB = LDS_BT + NSEQ * NBLK * 4;
constexpr int LDS_SC = LDS_QB + 4 * 4 * 512;
constexpr int LDS_MISC = LDS_SC + 4096;
constexpr int LDS_PROG = LDS_MISC + 64;
constexpr int LDS_GEN = LDS_PROG + 1024;
constexpr int LDS_JUNK = LDS_PROG;
constexpr int LDS_TOTAL_JUNK = LDS_GEN + 512;
constexpr int LDS_TOTAL = LDS_TOTAL_JUNK + 1024;
static_assert(LDS_TOTAL <= 160 * 1024 && LDS_MISC - LDS_QB >= 8192 && RING_BYTES >= R * STAGE, "LDS");

constexpr unsigned WS_G = 0;
constexpr unsigned WS_PROG = 2048;
constexpr unsigned WS_PP = 4096;
constexpr unsigned PP_W = 2176;
constexpr unsigned WS_HP = WS_PP + 320u * 4u * PP_W + 4096u;
constexpr unsigned HP_W = 32768 + 512;
constexpr unsigned WS_END = WS_HP + 16u * 16u * HP_W;
constexpr unsigned WS_STAMP = 64u << 20;

typedef unsigned u32x4 __attribute__((ext_vector_type(4)));
typedef unsigned u32x2 __attribute__((ext_vector_type(2)));
typedef float f32x4 __attribute__((ext_vector_type(4)));
typedef float f32x2 __attribute__((ext_vector_type(2)));
typedef __bf16 bf16x8 __attribute__((ext_vector_type(8)));
typedef __bf16 bf16x2 __attribute__((ext_vector_type(2)));
typedef short s16x4 __attribute__((ext_vector_type(4)));
typedef int i32x4 __attribute__((ext_vector_type(4)));
#define LAS __attribute__((address_space(3)))

constexpr float C2 = 0.088388347648318440f * 1.4426950408889634f;

__device__ __forceinline__ int rfl(int x) { return __builtin_amdgcn_readfirstlane(x); }
__device__ __forceinline__ int rl(int x, int l) { return __builtin_amdgcn_readlane(x, l); }
__device__ __forceinline__ float rlf(float x, int l) { return __builtin_bit_cast(float, __builtin_amdgcn_readlane(__builtin_bit_cast(int, x), l)); }

__device__ __forceinline__ void dma16(const void* sbase, unsigned voff, unsigned dst) {
  unsigned keep;
  asm volatile("s_mov_b32 %0, m0\n\ts_mov_b32 m0, %3\n\ts_nop 0\n\tglobal_load_lds_dwordx4 %1, %2\n\ts_mov_b32 m0, %0"
               : "=&s"(keep) : "v"(voff), "s"(sbase), "s"(dst) : "memory");
}
__device__ __forceinline__ void dma16_sc1(const void* sbase, unsigned voff, unsigned dst) {
  unsigned keep;
  asm volatile("s_mov_b32 %0, m0\n\ts_mov_b32 m0, %3\n\ts_nop 0\n\tglobal_load_lds_dwordx4 %1, %2 sc1\n\ts_mov_b32 m0, %0"
               : "=&s"(keep) : "v"(voff), "s"(sbase), "s"(dst) : "memory");
}
__device__ __forceinline__ void dma_stage(const void* sK, const void* sV, unsigned dstK, unsigned dstV,
                                          unsigned k0, unsigned k1, unsigned k2, unsigned k3,
                                          unsigned v0, unsigned v1, unsigned v2, unsigned v3) {
  unsigned keep;
  asm volatile("s_mov_b32 %0, m0\n\ts_mov_b32 m0, %3\n\ts_nop 0\n\t"
               "global_load_lds_dwordx4 %5, %1 " KVPOL "\n\t"
               "global_load_lds_dwordx4 %6, %1 offset:1024 " KVPOL "\n\t"
               "global_load_lds_dwordx4 %7, %1 offset:2048 " KVPOL "\n\t"
               "global_load_lds_dwordx4 %8, %1 offset:3072 " KVPOL "\n\t"
               "s_mov_b32 m0, %4\n\ts_nop 0\n\t"
               "global_load_lds_dwordx4 %9, %2 " KVPOL "\n\t"
               "global_load_lds_dwordx4 %10, %2 offset:1024 " KVPOL "\n\t"
               "global_load_lds_dwordx4 %11, %2 offset:2048 " KVPOL "\n\t"
               "global_load_lds_dwordx4 %12, %2 offset:3072 " KVPOL "\n\t"
               "s_mov_b32 m0, %0"
               : "=&s"(keep)
               : "s"(sK), "s"(sV), "s"(dstK), "s"(dstV), "v"(k0), "v"(k1), "v"(k2), "v"(k3), "v"(v0), "v"(v1), "v"(v2), "v"(v3)
               : "memory");
}
template <int CTRL> __device__ __forceinline__ int dppi(int x) { return __builtin_amdgcn_update_dpp(0, x, CTRL, 0xf, 0xf, true); }
template <int CTRL> __device__ __forceinline__ int dppi_keep(int old, int x) { return __builtin_amdgcn_update_dpp(old, x, CTRL, 0xf, 0xf, false); }

__device__ __forceinline__ float xg_max(float x) {
  auto s = __builtin_amdgcn_permlane16_swap(__float_as_uint(x), __float_as_uint(x), false, false);
  x = fmaxf(__uint_as_float(s[0]), __uint_as_float(s[1]));
  auto t = __builtin_amdgcn_permlane32_swap(__float_as_uint(x), __float_as_uint(x), false, false);
  return fmaxf(__uint_as_float(t[0]), __uint_as_float(t[1]));
}
__device__ __forceinline__ float xg_sum(float x) {
  auto s = __builtin_amdgcn_permlane16_swap(__float_as_uint(x), __float_as_uint(x), false, false);
  x = __uint_as_float(s[0]) + __uint_as_float(s[1]);
  auto t = __builtin_amdgcn_permlane32_swap(__float_as_uint(x), __float_as_uint(x), false, false);
  return __uint_as_float(t[0]) + __uint_as_float(t[1]);
}
__device__ __forceinline__ unsigned pk_bf16(float a, float b) {
  bf16x2 v = {(__bf16)a, (__bf16)b};
  return __builtin_bit_cast(unsigned, v);
}

template <int NQ>
__device__ __forceinline__ void tile_compute(LAS char* lds, unsigned ka, unsigned va, const bf16x8 (&qf)[NQ][4], f32x4 (&acc)[NQ][8],
                                             float (&m)[NQ], float (&l)[NQ], int lo, int hi, int g4) {
  bf16x8 kf[4];
#pragma unroll
  for (int s = 0; s < 4; ++s) kf[s] = *(LAS bf16x8*)(lds + (ka ^ (s << 6)));
  s16x4 vf[8];
#pragma unroll
  for (int k = 0; k < 8; ++k) vf[k] = __builtin_amdgcn_ds_read_tr16_b64_v4i16((LAS s16x4*)(lds + (va ^ (k << 5))));
  f32x4 S[NQ];
#pragma unroll
  for (int qb = 0; qb < NQ; ++qb) {
    S[qb] = f32x4{0.f, 0.f, 0.f, 0.f};
#pragma unroll
    for (int s = 0; s < 4; ++s) S[qb] = __builtin_amdgcn_mfma_f32_16x16x32_bf16(kf[s], qf[qb][s], S[qb], 0, 0, 0);
  }
  if (lo > 0 || hi < 16) {
#pragma unroll
    for (int qb = 0; qb < NQ; ++qb)
#pragma unroll
      for (int r = 0; r < 4; ++r) { const int t = 4 * g4 + r; S[qb][r] = (t >= lo && t < hi) ? S[qb][r] : -INFINITY; }
  }
  s16x4 pb[NQ];
  bool grow = false;
  float alpha[NQ];
#pragma unroll
  for (int qb = 0; qb < NQ; ++qb) {
    float mx = fmaxf(fmaxf(S[qb][0], S[qb][1]), fmaxf(S[qb][2], S[qb][3]));
    mx = xg_max(mx);
    const float mn2 = fmaxf(m[qb], mx);
    grow |= mn2 > m[qb];
    alpha[qb] = __builtin_amdgcn_exp2f((m[qb] - mn2) * C2);
    m[qb] = mn2;
    const float mc = -mn2 * C2;
    const float p0 = __builtin_amdgcn_exp2f(fmaf(S[qb][0], C2, mc)), p1 = __builtin_amdgcn_exp2f(fmaf(S[qb][1], C2, mc));
    const float p2 = __builtin_amdgcn_exp2f(fmaf(S[qb][2], C2, mc)), p3 = __builtin_amdgcn_exp2f(fmaf(S[qb][3], C2, mc));
    l[qb] = fmaf(l[qb], alpha[qb], (p0 + p1) + (p2 + p3));
    pb[qb] = __builtin_bit_cast(s16x4, u32x2{pk_bf16(p0, p1), pk_bf16(p2, p3)});
  }
  if (__any(grow)) {
#pragma unroll
    for (int qb = 0; qb < NQ; ++qb)
#pragma unroll
      for (int k = 0; k < 8; ++k) acc[qb][k] *= alpha[qb];
  }
#pragma unroll
  for (int qb = 0; qb < NQ; ++qb)
#pragma unroll
    for (int k = 0; k < 8; ++k) acc[qb][k] = __builtin_amdgcn_mfma_f32_16x16x16bf16_1k(vf[k], pb[qb], acc[qb][k], 0, 0, 0);
}

template <int V> struct IntC { static constexpr int value = V; };

struct Params {
  const char* q; const char* k; const char* v; const int* bt; const int* lens; __hip_bfloat16* out; char* ws;
  unsigned kvbytes; unsigned unused;
};

__global__ void __launch_bounds__(256, 2) radix_attn_kernel(Params p) {
  extern __shared__ __attribute__((aligned(16))) char smem[];
  LAS char* lds = (LAS char*)smem;
  const int tid = threadIdx.x, lane = tid & 63, w = rfl(tid >> 6);
  const int gidx = blockIdx.x;
  const int hg = (gidx >> 3) & 3;
  const int jj = (((gidx & 7) + 4 * (hg & 1)) & 7) | ((gidx >> 5) << 3);
  const int c16 = lane & 15, g4 = lane >> 4;
#define STAMP(i) do { } while (0)
#define STAMP_FLUSH() do { } while (0)
  STAMP(0);
#define PROBE(k) do { } while (0)
#define PROBE_END() do { } while (0)
  const auto wsr = __builtin_amdgcn_make_buffer_rsrc(p.ws, 0, WS_END, 0x00020000);

  unsigned voffK[4], voffV[4], oK[4], oV[4];
  int pm, len_l, minlen, vpages, incp, sump; bool zq_l;
  {
    const i32x4* bt4 = (const i32x4*)p.bt;
    const i32x4 r0 = bt4[lane];
    i32x4 rr[4];
#pragma unroll
    for (int i = 0; i < 4; ++i) rr[i] = bt4[(4 * w + i) * 64 + lane];
    const int lenraw = p.lens[c16];
    __builtin_amdgcn_sched_barrier(0);
#pragma unroll
  for (int i = 0; i < 4; ++i) {
    const int t = 4 * w + i, u = 4 * i + g4;
    voffK[i] = g4 * 256 + ((c16 ^ t) << 4) + t * 4096 - i * 1024 + 4096;
    voffV[i] = g4 * 256 + ((c16 ^ (2 * (t & 7))) << 4) + t * 4096 - i * 1024 + 4096;
    oK[i] = u * 4096 + ((c16 ^ u) << 4) - i * 1024 + 4096;
    oV[i] = u * 4096 + ((c16 ^ (2 * (u & 7))) << 4) - i * 1024 + 4096;
  }
    asm volatile("" :: "v"(voffK[0]), "v"(voffK[1]), "v"(voffK[2]), "v"(voffK[3]), "v"(voffV[0]), "v"(voffV[1]), "v"(voffV[2]), "v"(voffV[3]),
                 "v"(oK[0]), "v"(oK[1]), "v"(oK[2]), "v"(oK[3]), "v"(oV[0]), "v"(oV[1]), "v"(oV[2]), "v"(oV[3]) : "memory");
    bool n0 = false, n1 = false, n2 = false, n3 = false;
#pragma unroll
    for (int i = 0; i < 4; ++i) {
      n0 |= rr[i][0] != r0[0]; n1 |= rr[i][1] != r0[1]; n2 |= rr[i][2] != r0[2]; n3 |= rr[i][3] != r0[3];
      *(LAS i32x4*)(lds + LDS_BT + ((4 * w + i) * 256 + 4 * lane) * 4) = rr[i];
    }
    zq_l = lenraw <= 0;
    len_l = zq_l ? LK : min(lenraw, LK);
    int mn = len_l;
    mn = min(mn, dppi_keep<0x128>(mn, mn)); mn = min(mn, dppi_keep<0x124>(mn, mn));
    mn = min(mn, dppi_keep<0x122>(mn, mn)); mn = min(mn, dppi_keep<0x121>(mn, mn));
    minlen = rfl(mn);
    vpages = (len_l + BS - 1) >> 4;
    incp = vpages;
    incp += dppi<0x111>(incp); incp += dppi<0x112>(incp); incp += dppi<0x114>(incp); incp += dppi<0x118>(incp);
    sump = rl(incp, 15);
    const int fc = n0 ? 0 : n1 ? 1 : n2 ? 2 : n3 ? 3 : 4;
    const unsigned long long mm = __ballot(fc < 4);
    int col = NBLK;
    if (mm) { const int fl = __builtin_ctzll(mm); col = 4 * fl + rl(fc, fl); }
    if (lane == 0) *(LAS int*)(lds + LDS_MISC + 4 * w) = col;
    asm volatile("s_waitcnt lgkmcnt(0)\n\ts_barrier" ::: "memory");
    PROBE(1);
    const i32x4 mc = *(LAS i32x4*)(lds + LDS_MISC);
    pm = rfl(min(min(mc[0], mc[1]), min(mc[2], mc[3])));
  }
  const int P = min(pm, minlen >> 4);
  const int tsh = P * BS;
  const int vL = vpages - P;
  const int vS = incp - vpages - c16 * P;
  const int N = sump - 16 * P;
  int kH = 0;
  if (P > 0) kH = min(min(15, P), max(1, rfl((int)__builtin_ceilf((float)P * (1024.f / HRHO) * __builtin_amdgcn_rcpf((float)(P + N))))));
  const int nW = 64 - 4 * kH;
  const float inv_nw = __builtin_amdgcn_rcpf((float)nW);
  const unsigned zmask = (unsigned)(__ballot(zq_l) & 0xffffull);
  const bool isH = jj < 4 * kH;
  auto gen_fetch = [&](int head) { if (lane < 5) dma16_sc1(p.ws + WS_G, lane < 4 ? head * 64 + lane * 16 : 0, LDS_GEN + w * 128); };
  auto gen_row = [&]() { return *(LAS unsigned*)(lds + LDS_GEN + w * 128 + c16 * 4); };
  auto prog_tag = [&]() { return (unsigned)rfl(*(LAS int*)(lds + LDS_GEN + w * 128 + 64)) + 1u; };
  const float t_all = (float)(P + N) * (16.f * 819.2f / (float)PACE);
  const unsigned long long t_ref = __builtin_amdgcn_s_memrealtime();
  constexpr int A_S0 = AS0, A_S1 = AS1;
  const int m_avg = rfl((int)(((float)(N + nW - 1) + 0.5f) * inv_nw));
  const bool adapt_on = nW >= 24 && m_avg >= A_S1 + 8;
  auto read_progress = [&](unsigned ldsoff, float& t1, float& d) -> bool {
    const u32x4 e = *(LAS u32x4*)(lds + ldsoff + c16 * 16);
    const unsigned ptag = prog_tag();
    const bool okv = e[1] == ptag && e[3] == ptag;
    const int cnt = __builtin_popcount((unsigned)__ballot(okv) & 0xffffu);
    float a = okv ? (float)(int)(e[0] - (unsigned)t_ref) : 0.f, c = okv ? (float)e[2] : 0.f;
    a += __builtin_bit_cast(float, dppi_keep<0x128>(0, __builtin_bit_cast(int, a))); c += __builtin_bit_cast(float, dppi_keep<0x128>(0, __builtin_bit_cast(int, c)));
    a += __builtin_bit_cast(float, dppi_keep<0x124>(0, __builtin_bit_cast(int, a))); c += __builtin_bit_cast(float, dppi_keep<0x124>(0, __builtin_bit_cast(int, c)));
    a += __builtin_bit_cast(float, dppi_keep<0x122>(0, __builtin_bit_cast(int, a))); c += __builtin_bit_cast(float, dppi_keep<0x122>(0, __builtin_bit_cast(int, c)));
    a += __builtin_bit_cast(float, dppi_keep<0x121>(0, __builtin_bit_cast(int, a))); c += __builtin_bit_cast(float, dppi_keep<0x121>(0, __builtin_bit_cast(int, c)));
    if (cnt < 8) return false;
    const float ic = 1.f / (float)cnt;
    t1 = rlf(a, 0) * ic; d = rlf(c, 0) * ic * (1.f / (A_S1 - A_S0));
    d = fminf(d, t_all * ((float)PACE / (float)A_MIN) / (float)m_avg);
    return true;
  };
  auto cut = [&](int i) { return (int)(((float)(i * N + (i & 1) * ((N * SKEW) >> 6)) + 0.5f) * inv_nw); };
  const int r_lo = cut(lane), r_hi = cut(lane + 1);

  if (isH) {
    const int h = hg * 4 + (jj & 3), part = jj >> 2;
    const int p0 = (P * part) / kH, p1 = (P * (part + 1)) / kH;
#pragma unroll
    for (int i = 0; i < 2; ++i)
      dma16(p.q + (2 * w + i) * 16384 + h * 512, (lane >> 5) * 8192 + (lane & 31) * 16, LDS_QB + (2 * w + i) * 1024);
    const char* kb = p.k + h * 256 - 4096;
    const char* vb = p.v + h * 256 - 4096;
    const int nt = max(0, (p1 - p0 - w + 3) >> 2);
    const unsigned ring = w * (RH * 8192);
    int pg0 = 0, vpg = *(LAS int*)(lds + LDS_BT + min(p0 + w + 4 * lane, NBLK - 1) * 4);
    float pace_h = t_all * (PACE_H * 0.01f) * __builtin_amdgcn_rcpf((float)max(nt, 1)), pace_hb = -3.f * pace_h;
    float t_pub = t_all * (HPUB_AT * 0.01f);
    auto issue = [&](int t) {
      { const int due = rfl((int)(pace_hb + (float)t * pace_h)); if (due > 0) while ((int)(__builtin_amdgcn_s_memrealtime() - t_ref) < due) __builtin_amdgcn_s_sleep(1); }
      if (t - pg0 == 64) { pg0 = t; vpg = *(LAS int*)(lds + LDS_BT + min(p0 + w + 4 * (t + lane), NBLK - 1) * 4); }
      const size_t pb = (size_t)(unsigned)rl(vpg, t - pg0) * 65536;
      const unsigned dst = ring + (t & (RH - 1)) * 8192;
      dma_stage(kb + pb, vb + pb, dst, dst + 4096, oK[0], oK[1], oK[2], oK[3], oV[0], oV[1], oV[2], oV[3]);
    };
    const int npre = min(nt, RH - 1);
#pragma unroll 1
    for (int t = 0; t < npre; ++t) issue(t);
    gen_fetch(h);
    if (npre == 3) asm volatile("s_waitcnt vmcnt(25)\n\ts_barrier" ::: "memory");
    else asm volatile("s_waitcnt vmcnt(0)\n\ts_barrier" ::: "memory");
    bf16x8 qf[2][4];
#pragma unroll
    for (int qb = 0; qb < 2; ++qb) {
      const bool z = (zmask >> (8 * qb + (c16 >> 1))) & 1;
#pragma unroll
      for (int s = 0; s < 4; ++s) {
        u32x4 x = *(LAS u32x4*)(lds + LDS_QB + (16 * qb + c16) * 256 + (4 * s + g4) * 16);
        if (z) x = u32x4{0u, 0u, 0u, 0u};
        qf[qb][s] = __builtin_bit_cast(bf16x8, x);
      }
    }
    f32x4 acc[2][8];
    float m[2] = {-INFINITY, -INFINITY}, l[2] = {0.f, 0.f};
#pragma unroll
    for (int qb = 0; qb < 2; ++qb)
#pragma unroll
      for (int k = 0; k < 8; ++k) acc[qb][k] = f32x4{0.f, 0.f, 0.f, 0.f};
    const unsigned ka0 = ring + c16 * 256 + ((g4 ^ c16) << 4);
    unsigned va0;
    { const int tq = 4 * g4 + (c16 >> 2), pp = lane & 3; va0 = ring + 4096 + tq * 256 + ((tq & 7) << 5) + ((pp >> 1) << 4) + ((pp & 1) << 3); }
#pragma unroll 1
    for (int t = 0; t < nt; ++t) {
      const int young = min(nt - 1 - t, RH - 2);
      if (young == 2) asm volatile("s_waitcnt vmcnt(16) lgkmcnt(0)" ::: "memory");
      else if (young == 1) asm volatile("s_waitcnt vmcnt(8) lgkmcnt(0)" ::: "memory");
      else asm volatile("s_waitcnt vmcnt(0) lgkmcnt(0)" ::: "memory");
      if (adapt_on && nt >= 12) {
        const int tp = (nt * 9) >> 4;
        if (t == tp + 3) {
          float t1, d;
          if (read_progress(LDS_PROG + w * 256, t1, d)) {
            const float t_end = t1 + (float)(m_avg - A_S1) * d * (A_F * 0.001f);
            const float now = (float)(int)(__builtin_amdgcn_s_memrealtime() - t_ref);
            const int t_i = t + RH - 1;
            pace_h = fmaxf((t_end - A_HMARGIN - now) / (float)max(nt - t_i, 1), 0.f); pace_hb = now - (float)t_i * pace_h;
            t_pub = t_end - A_HPUB;
          }
        }
      }
      if (t + RH - 1 < nt) issue(t + RH - 1);
      if (adapt_on && nt >= 12 && t == ((nt * 9) >> 4) && lane < 16) dma16_sc1(p.ws + WS_PROG, lane * 16, LDS_PROG + w * 256);
      const unsigned sb = (t & (RH - 1)) * 8192;
      tile_compute<2>(lds, ka0 + sb, va0 + sb, qf, acc, m, l, 0, 16, g4);
    }
    STAMP(2);
    constexpr unsigned ROWB = 528, WAVEB = 32 * ROWB, MLB = 4 * WAVEB;
    asm volatile("s_waitcnt lgkmcnt(0)\n\ts_barrier" ::: "memory");
#pragma unroll
    for (int qb = 0; qb < 2; ++qb) {
      const float lt = xg_sum(l[qb]);
#pragma unroll
      for (int k = 0; k < 8; ++k) *(LAS f32x4*)(lds + w * WAVEB + (16 * qb + c16) * ROWB + (16 * k + 4 * g4) * 4) = acc[qb][k];
      if (g4 == 0) *(LAS f32x2*)(lds + MLB + (w * 32 + 16 * qb + c16) * 8) = f32x2{m[qb] * C2, lt};
    }
    asm volatile("s_waitcnt lgkmcnt(0)\n\ts_barrier" ::: "memory");
    while ((int)(__builtin_amdgcn_s_memrealtime() - t_ref) < (int)t_pub) __builtin_amdgcn_s_sleep(8);
    asm volatile("s_waitcnt vmcnt(0)" ::: "memory");
    const unsigned gvh = gen_row();
    const unsigned slot = WS_HP + (part * 16 + h) * HP_W;
    float mrow = 0.f, lrow = 0.f;
#pragma unroll
    for (int rr = 0; rr < 8; ++rr) {
      const int row = 8 * w + rr;
      f32x2 ml[4], o[4];
#pragma unroll
      for (int v = 0; v < 4; ++v) { ml[v] = *(LAS f32x2*)(lds + MLB + (v * 32 + row) * 8); o[v] = *(LAS f32x2*)(lds + v * WAVEB + row * ROWB + lane * 8); }
      const float M = fmaxf(fmaxf(ml[0][0], ml[1][0]), fmaxf(ml[2][0], ml[3][0]));
      float L = 0.f, o0 = 0.f, o1 = 0.f;
#pragma unroll
      for (int v = 0; v < 4; ++v) {
        const float e = ml[v][1] > 0.f ? __builtin_amdgcn_exp2f(ml[v][0] - M) : 0.f;
        L = fmaf(ml[v][1], e, L); o0 = fmaf(o[v][0], e, o0); o1 = fmaf(o[v][1], e, o1);
      }
      const unsigned tag = (unsigned)rl((int)gvh, row >> 1) + 1u;
      const u32x4 gr = {__float_as_uint(o0), tag, __float_as_uint(o1), tag};
      __builtin_amdgcn_raw_buffer_store_b128(gr, wsr, slot + row * 1024 + lane * 16, 0, 16);
      if (lane == rr) { mrow = M; lrow = L; }
    }
    const unsigned tagr = (unsigned)__shfl((int)gvh, ((8 * w + lane) >> 1) & 15) + 1u;
    if (lane < 8) {
      const unsigned tag = tagr;
      const u32x4 gm = {__float_as_uint(mrow), tag, __float_as_uint(lrow), tag};
      __builtin_amdgcn_raw_buffer_store_b128(gm, wsr, slot + 32768 + (8 * w + lane) * 16, 0, 16);
    }
    STAMP(1);
    STAMP(3);
    STAMP_FLUSH();
    return;
  }

  const int j = jj - 4 * kH, h = hg * 4 + w;
  const int x0 = rl(r_lo, j), x1 = rl(r_hi, j);
  const unsigned umask = (unsigned)(__ballot(max(x0, vS) < min(x1, vS + vL)) & 0xffffull);
  const unsigned kaddr0 = w * 256 + c16 * 1024 + ((g4 ^ c16) << 4);
  unsigned vaddr0;
  { const int tq = 4 * g4 + (c16 >> 2), pp = lane & 3; vaddr0 = 16384 + tq * 1024 + w * 256 + ((tq & 7) << 5) + ((pp >> 1) << 4) + ((pp & 1) << 3); }
  const char* kbase = p.k + hg * 1024 - 4096;
  const char* vbase = p.v + hg * 1024 - 4096;

  unsigned i_mask = umask; int i_row = 0, i_t0 = 0, i_t1 = 0, i_page; bool i_more = false;
  int n_iss = 0, i_slot = 0;
  auto unit_range = [&](int b, int& t0, int& t1) {
    const int s = rl(vS, b), L = rl(vL, b);
    t0 = (P + max(x0, s) - s) * BS; t1 = min((P + min(x1, s + L) - s) * BS, rl(len_l, b));
  };
  int i_piece = 0;
  auto q_dma = [&](int b) {
    if (lane < 32) dma16(p.q + b * 8192 + h * 512, lane * 16, LDS_QB + (w * 4 + (i_piece & 3)) * 512);
    ++i_piece;
  };
  if (i_mask) {
    i_row = __builtin_ctz(i_mask); i_mask &= i_mask - 1; unit_range(i_row, i_t0, i_t1);
    q_dma(i_row);
    i_more = true;
  }
  i_page = i_t0 >> 4;
  int i_pg0 = i_page, vpg = *(LAS int*)(lds + LDS_BT + (i_row * NBLK + min(i_page + lane, NBLK - 1)) * 4);
  int pace_tok = 0;
  float pace_d = t_all * __builtin_amdgcn_rcpf((float)max(x1 - x0, 1)), pace_b = -(float)PACE_LEAD * pace_d;
  const int rep = (adapt_on && hg < 2 && j >= (nW >> 1) && j < (nW >> 1) + 8) ? hg * 8 + j - (nW >> 1) : -1;
  unsigned t_s0 = 0;
  auto issue_stage = [&]() {
    const int phys = rl(vpg, i_page - i_pg0);
    const int hi = i_t1 - i_page * 16;
    {
      if (adapt_on && pace_tok == A_S1 + 3 + R - 1) {
        float t1, d;
        if (read_progress(LDS_PROG, t1, d)) { pace_d = d * (A_F * 0.001f); pace_b = t1 - (float)A_S1 * pace_d - A_LEAD; }
      }
      const int due = rfl((int)(pace_b + (float)pace_tok * pace_d));
      pace_tok += 1;
      if (due > 0) while ((int)(__builtin_amdgcn_s_memrealtime() - t_ref) < due) __builtin_amdgcn_s_sleep(1);
    }
    const size_t pb = (size_t)(unsigned)phys * 65536;
    const unsigned dst = i_slot * STAGE + 4 * w * 1024;
    if (hi >= 16) {
      dma_stage(kbase + pb, vbase + pb, dst, dst + 16384, voffK[0], voffK[1], voffK[2], voffK[3], voffV[0], voffV[1], voffV[2], voffV[3]);
    } else {
      unsigned ok[4], ov[4];
#pragma unroll
      for (int i = 0; i < 4; ++i) {
        const int d = (min(4 * w + i, hi - 1) - (4 * w + i)) * 4096;
        ok[i] = voffK[i] + d; ov[i] = voffV[i] + d;
      }
      dma_stage(kbase + pb, vbase + pb, dst, dst + 16384, ok[0], ok[1], ok[2], ok[3], ov[0], ov[1], ov[2], ov[3]);
    }
    if (adapt_on) {
      const int k = pace_tok - 1;
      if (rep >= 0 && w == 3) {
        if (k == A_S0) t_s0 = (unsigned)__builtin_amdgcn_s_memrealtime();
        if (k == A_S1) {
          const unsigned ptag = prog_tag();
          const unsigned now = (unsigned)__builtin_amdgcn_s_memrealtime();
          const u32x4 e = {now, ptag, now - t_s0, ptag};
          if (lane == 0) __builtin_amdgcn_raw_buffer_store_b128(e, wsr, WS_PROG + rep * 16, 0, 16);
        }
      }
      if (k == A_S1 + 3 && w == 0 && lane < 16) dma16_sc1(p.ws + WS_PROG, lane * 16, LDS_PROG);
    }
    ++n_iss; i_slot = (i_slot + 1 == R) ? 0 : i_slot + 1;
    ++i_page;
    if (i_page * 16 >= i_t1) {
      if (i_mask) {
        i_row = __builtin_ctz(i_mask); i_mask &= i_mask - 1; unit_range(i_row, i_t0, i_t1); i_page = i_t0 >> 4;
        q_dma(i_row);
        i_pg0 = i_page; vpg = *(LAS int*)(lds + LDS_BT + (i_row * NBLK + min(i_page + lane, NBLK - 1)) * 4);
      } else i_more = false;
    } else if (i_page - i_pg0 == 64) {
      i_pg0 = i_page; vpg = *(LAS int*)(lds + LDS_BT + (i_row * NBLK + min(i_page + lane, NBLK - 1)) * 4);
    }
  };
  PROBE(2);
#pragma unroll 1
  for (int s = 0; s < R - 1; ++s) if (i_more) issue_stage();
  gen_fetch(h);

  unsigned own = 0;
  {
    unsigned mrem = umask;
    while (mrem) {
      const int b = __builtin_ctz(mrem); mrem &= mrem - 1;
      const int s = rl(vS, b), L = rl(vL, b);
      unsigned long long pmk = __ballot(lane < nW && max(r_lo, s) < min(r_hi, s + L));
      int idx = w % __builtin_popcountll(pmk);
      while (idx--) pmk &= pmk - 1;
      if (__builtin_ctzll(pmk) == j) own |= 1u << b;
    }
    unsigned zl = (unsigned)(__ballot(vL == 0) & 0xffffull);
    while (zl) { const int b = __builtin_ctz(zl); zl &= zl - 1; if ((b * 4) % nW == j) own |= 1u << b; }
  }

  int fb = -1, fcnt = 0; unsigned vrow = 0, vmlo = 0;
  if (own && (own & (own - 1)) == 0) {
    const int b = __builtin_ctz(own);
    const unsigned above = umask >> b;
    if ((umask >> b & 1) && (above == 1 || __builtin_popcount(above) == 2)) {
      const int s = rl(vS, b), L = rl(vL, b);
      const unsigned long long pmk = __ballot(lane < nW && max(r_lo, s) < min(r_hi, s + L)) & ~(1ull << j);
      const int np = __builtin_popcountll(pmk);
      if (np + kH <= 8 && np + kH > 0) {
        fb = b; fcnt = np + kH;
        const int rank = __builtin_amdgcn_mbcnt_hi((unsigned)(pmk >> 32), __builtin_amdgcn_mbcnt_lo((unsigned)pmk, 0));
        const unsigned prow = WS_PP + ((hg * 80 + lane + b) * 4 + w) * PP_W;
        const unsigned pushed = (unsigned)__builtin_amdgcn_ds_permute((((pmk >> lane) & 1) ? rank : 63) << 2, (int)prow);
        const unsigned hb = WS_HP + (min(max(lane - np, 0), 15) * 16 + h) * HP_W;
        vrow = lane < np ? pushed : hb + b * 2048;
        const unsigned mlb = lane < np ? pushed + 2048 : hb + 32768 + b * 32;
        vmlo = (unsigned)__shfl((int)mlb, min(lane >> 1, fcnt - 1)) + (lane & 1) * 16;
      }
    }
  }

  int n_comp = 0, c_slot = 0;
  unsigned c_mask = umask;
  float ro[2][2] = {{0.f, 0.f}, {0.f, 0.f}}, rm[2] = {-INFINITY, -INFINITY}, rs[2] = {0.f, 0.f};
  float ho[2][2] = {{0.f, 0.f}, {0.f, 0.f}}, hm[2] = {-INFINITY, -INFINITY}, hs[2] = {0.f, 0.f};
  int last_b = -1, held_b = -1;
  auto publish = [&](int b, const float (&o)[2][2], const float (&mm)[2], const float (&ll)[2]) {
    const unsigned tag = (unsigned)rfl(*(LAS int*)(lds + LDS_GEN + w * 128 + b * 4)) + 1u;
    const unsigned slot = WS_PP + ((hg * 80 + j + b) * 4 + w) * PP_W;
    const u32x4 g0 = {__float_as_uint(o[0][0]), tag, __float_as_uint(o[0][1]), tag};
    const u32x4 g1 = {__float_as_uint(o[1][0]), tag, __float_as_uint(o[1][1]), tag};
    __builtin_amdgcn_raw_buffer_store_b128(g0, wsr, slot + lane * 16, 0, 16);
    __builtin_amdgcn_raw_buffer_store_b128(g1, wsr, slot + 1024 + lane * 16, 0, 16);
    if (lane < 2) {
      const u32x4 gm = {__float_as_uint(lane ? mm[1] : mm[0]), tag, __float_as_uint(lane ? ll[1] : ll[0]), tag};
      __builtin_amdgcn_raw_buffer_store_b128(gm, wsr, slot + 2048 + lane * 16, 0, 16);
    }
  };
  bf16x8 qf[1][4];
  f32x4 acc[1][8];
  float m[1] = {-INFINITY}, l[1] = {0.f};
#pragma unroll
  for (int k = 0; k < 8; ++k) acc[0][k] = f32x4{0.f, 0.f, 0.f, 0.f};
#pragma unroll
  for (int s = 0; s < 4; ++s) qf[0][s] = __builtin_bit_cast(bf16x8, u32x4{0u, 0u, 0u, 0u});
  auto finish_piece = [&](int b) {
    const float lt = xg_sum(l[0]);
    LAS char* sc = lds + LDS_SC + w * 1024;
    if (c16 < 2) {
#pragma unroll
      for (int k = 0; k < 8; ++k) *(LAS f32x4*)(sc + c16 * 512 + (16 * k + 4 * g4) * 4) = acc[0][k];
    }
    asm volatile("s_waitcnt lgkmcnt(0)" ::: "memory");
    const f32x2 a0 = *(LAS f32x2*)(sc + lane * 8), a1 = *(LAS f32x2*)(sc + 512 + lane * 8);
    if (last_b >= 0) {
      if (held_b >= 0) publish(held_b, ho, hm, hs);
      held_b = last_b;
#pragma unroll
      for (int r = 0; r < 2; ++r) { ho[r][0] = ro[r][0]; ho[r][1] = ro[r][1]; hm[r] = rm[r]; hs[r] = rs[r]; }
    }
    ro[0][0] = a0[0]; ro[0][1] = a0[1]; ro[1][0] = a1[0]; ro[1][1] = a1[1];
    rm[0] = rlf(m[0], 0) * C2; rm[1] = rlf(m[0], 1) * C2;
    rs[0] = rlf(lt, 0); rs[1] = rlf(lt, 1);
    last_b = b;
  };
  int c_b = -1, c_piece = 0, c_left = 0;
#pragma unroll 1
  while (c_left > 0 || c_mask) {
    {
      const int young = n_iss - n_comp - 1;
      if (R == 4 && young >= 2) asm volatile("s_waitcnt vmcnt(16) lgkmcnt(0)\n\ts_barrier" ::: "memory");
      else if (young >= 1) asm volatile("s_waitcnt vmcnt(8) lgkmcnt(0)\n\ts_barrier" ::: "memory");
      else asm volatile("s_waitcnt vmcnt(0) lgkmcnt(0)\n\ts_barrier" ::: "memory");
    }
    PROBE(3);
    if (i_more) issue_stage();
    if (c_left <= 0) {
      if (c_b >= 0) finish_piece(c_b);
      c_b = __builtin_ctz(c_mask); c_mask &= c_mask - 1;
      int t0, t1; unit_range(c_b, t0, t1);
      c_left = t1 - t0;
      const bool z = (c16 >= 2) || ((zmask >> c_b) & 1);
#pragma unroll
      for (int s = 0; s < 4; ++s) {
        u32x4 x = *(LAS u32x4*)(lds + LDS_QB + (w * 4 + (c_piece & 3)) * 512 + (c16 & 1) * 256 + (4 * s + g4) * 16);
        if (z) x = u32x4{0u, 0u, 0u, 0u};
        qf[0][s] = __builtin_bit_cast(bf16x8, x);
      }
      ++c_piece;
      m[0] = -INFINITY; l[0] = 0.f;
#pragma unroll
      for (int k = 0; k < 8; ++k) acc[0][k] = f32x4{0.f, 0.f, 0.f, 0.f};
    }
    const unsigned sb = c_slot * STAGE;
    tile_compute<1>(lds, kaddr0 + sb, vaddr0 + sb, qf, acc, m, l, 0, c_left, g4);
    c_left -= 16;
    ++n_comp; c_slot = (c_slot + 1 == R) ? 0 : c_slot + 1;
  }
  if (c_b >= 0) finish_piece(c_b);
  asm volatile("s_waitcnt vmcnt(0)" ::: "memory");
  const unsigned gvw = gen_row();
  STAMP(2);
  PROBE(5);
  if (last_b >= 0 && !((own >> last_b) & 1)) { publish(last_b, ro, rm, rs); last_b = -1; }
  if (held_b >= 0 && !((own >> held_b) & 1)) { publish(held_b, ho, hm, hs); held_b = -1; }
  STAMP(1);

  if (own) { const unsigned long long c0 = __builtin_amdgcn_s_memrealtime(); while (__builtin_amdgcn_s_memrealtime() - c0 < MDELAY) __builtin_amdgcn_s_sleep(1); }
  if (fb >= 0) {
    const int b = fb;
    const unsigned tag = (unsigned)rl((int)gvw, b) + 1u;
    const bool mine_last = b == last_b;
    const float om0 = mine_last ? rm[0] : hm[0], om1 = mine_last ? rm[1] : hm[1], ol0 = mine_last ? rs[0] : hs[0], ol1 = mine_last ? rs[1] : hs[1];
    const float oo00 = mine_last ? ro[0][0] : ho[0][0], oo01 = mine_last ? ro[0][1] : ho[0][1];
    const float oo10 = mine_last ? ro[1][0] : ho[1][0], oo11 = mine_last ? ro[1][1] : ho[1][1];
    auto attempt = [&](auto nb_tag) -> bool {
      constexpr int NB = decltype(nb_tag)::value;
      u32x4 d0[NB], d1[NB];
      unsigned so[NB];
#pragma unroll
      for (int k = 0; k < NB; ++k) so[k] = (unsigned)rl((int)vrow, min(k, fcnt - 1));
      asm volatile("" ::: "memory");
      const u32x4 dm = __builtin_amdgcn_raw_buffer_load_b128(wsr, vmlo, 0, 16);
#pragma unroll
      for (int k = 0; k < NB; ++k) {
        d0[k] = __builtin_amdgcn_raw_buffer_load_b128(wsr, lane * 16, so[k], 16);
        d1[k] = __builtin_amdgcn_raw_buffer_load_b128(wsr, 1024 + lane * 16, so[k], 16);
      }
      __builtin_amdgcn_sched_barrier(0);
      const bool live = lane < 2 * fcnt;
      const float mk = live ? __uint_as_float(dm[0]) : -INFINITY, lk = live ? __uint_as_float(dm[2]) : 0.f;
      const float om = (lane & 1) ? om1 : om0, ol = (lane & 1) ? ol1 : ol0;
      float M = fmaxf(mk, om);
      M = fmaxf(M, __builtin_bit_cast(float, dppi_keep<0x122>(__builtin_bit_cast(int, M), __builtin_bit_cast(int, M))));
      M = fmaxf(M, __builtin_bit_cast(float, dppi_keep<0x124>(__builtin_bit_cast(int, M), __builtin_bit_cast(int, M))));
      M = fmaxf(M, __builtin_bit_cast(float, dppi_keep<0x128>(__builtin_bit_cast(int, M), __builtin_bit_cast(int, M))));
      const float e = __builtin_amdgcn_exp2f(mk - M), eo = __builtin_amdgcn_exp2f(om - M);
      float Ls = lk * e;
      Ls += __builtin_bit_cast(float, dppi_keep<0x122>(0, __builtin_bit_cast(int, Ls)));
      Ls += __builtin_bit_cast(float, dppi_keep<0x124>(0, __builtin_bit_cast(int, Ls)));
      Ls += __builtin_bit_cast(float, dppi_keep<0x128>(0, __builtin_bit_cast(int, Ls)));
      Ls = fmaf(ol, eo, Ls);
      const float inv = __builtin_amdgcn_rcpf(Ls);
      const float en = e * inv, eon = eo * inv;
      const float eo0 = rlf(eon, 0), eo1 = rlf(eon, 1);
      float o00 = oo00 * eo0, o01 = oo01 * eo0, o10 = oo10 * eo1, o11 = oo11 * eo1;
      unsigned bad = (dm[1] ^ tag) | (dm[3] ^ tag);
#pragma unroll
      for (int k = 0; k < NB; ++k) {
        const float w0 = rlf(en, 2 * k), w1 = rlf(en, 2 * k + 1);
        bad |= ((d0[k][1] ^ tag) | (d0[k][3] ^ tag)) | ((d1[k][1] ^ tag) | (d1[k][3] ^ tag));
        o00 = fmaf(__uint_as_float(d0[k][0]), w0, o00); o01 = fmaf(__uint_as_float(d0[k][2]), w0, o01);
        o10 = fmaf(__uint_as_float(d1[k][0]), w1, o10); o11 = fmaf(__uint_as_float(d1[k][2]), w1, o11);
      }
      if (__any(bad != 0)) return false;
      __hip_bfloat16* orow = p.out + (b * NH + 2 * h) * DH + 2 * lane;
      *(unsigned*)orow = pk_bf16(o00, o01);
      *(unsigned*)(orow + DH) = pk_bf16(o10, o11);
      if (lane == 0) __builtin_amdgcn_raw_buffer_store_b32(tag, wsr, WS_G + (h * 16 + b) * 4, 0, GAUX);
      return true;
    };
#pragma unroll 1
    for (int t = 0; t < 16; ++t) {
      if (t >= FASTTRY) {
        unsigned spins = 0;
        while (true) {
          asm volatile("" ::: "memory");
          const u32x4 x = __builtin_amdgcn_raw_buffer_load_b128(wsr, vmlo, 0, 16);
          if (!__any(x[1] != tag || x[3] != tag) || ++spins > (1u << 16)) break;
          __builtin_amdgcn_s_sleep(SLEEP2);
        }
      }
      const bool ok = fcnt <= 5 ? attempt(IntC<5>{}) : attempt(IntC<8>{});
      if (ok) { own = 0; break; }
    }
  }
  while (own) {
    const int b = __builtin_ctz(own); own &= own - 1;
    const unsigned tag = (unsigned)rl((int)gvw, b) + 1u;
    const int s = rl(vS, b), L = rl(vL, b);
    unsigned long long pmk = __ballot(lane < nW && max(r_lo, s) < min(r_hi, s + L));
    unsigned smk = (1u << kH) - 1u;
    float M0 = -INFINITY, M1 = -INFINITY, L0 = 0.f, L1 = 0.f, o00 = 0.f, o01 = 0.f, o10 = 0.f, o11 = 0.f;
    if (b == last_b) {
      pmk &= ~(1ull << j);
      M0 = rm[0]; M1 = rm[1]; L0 = rs[0]; L1 = rs[1]; o00 = ro[0][0]; o01 = ro[0][1]; o10 = ro[1][0]; o11 = ro[1][1];
    } else if (b == held_b) {
      pmk &= ~(1ull << j);
      M0 = hm[0]; M1 = hm[1]; L0 = hs[0]; L1 = hs[1]; o00 = ho[0][0]; o01 = ho[0][1]; o10 = ho[1][0]; o11 = ho[1][1];
    }
    const unsigned p_rows = WS_PP + ((hg * 80 + lane + b) * 4 + w) * PP_W;
    const unsigned s_base = WS_HP + ((lane & 15) * 16 + h) * HP_W;
    unsigned spins = 0;
    bool light = false;
    while (pmk | smk) {
      asm volatile("" ::: "memory");
      unsigned long long tp = pmk; unsigned ts = smk;
      const bool single = (__builtin_popcountll(pmk) + __builtin_popcount(smk)) == 1;
      if (light && !single) {
        const int jp = pmk ? (((pmk >> lane) & 1) ? lane : __builtin_ctzll(pmk)) : 0;
        const int js = smk ? (((smk >> (lane & 15)) & 1) ? (lane & 15) : __builtin_ctz(smk)) : 0;
        u32x4 a = {0u, tag, 0u, tag}, c = {0u, tag, 0u, tag};
        if (pmk) a = __builtin_amdgcn_raw_buffer_load_b128(wsr, WS_PP + ((hg * 80 + jp + b) * 4 + w) * PP_W + 2048 + 16, 0, 16);
        if (smk) c = __builtin_amdgcn_raw_buffer_load_b128(wsr, WS_HP + (js * 16 + h) * HP_W + 32768 + (2 * b + 1) * 16, 0, 16);
        tp &= __ballot(a[1] == tag && a[3] == tag);
        ts &= (unsigned)__ballot(c[1] == tag && c[3] == tag);
      }
      bool progress = false;
      if (tp | ts) {
        auto batch = [&](auto nb_tag) {
          constexpr int NB = decltype(nb_tag)::value;
          unsigned rows[NB], mls[NB]; unsigned long long bitp[NB]; unsigned bits[NB];
#pragma unroll
          for (int k = 0; k < NB; ++k) {
            if (tp) { const int q = __builtin_ctzll(tp); bitp[k] = tp & (~tp + 1); bits[k] = 0; tp &= tp - 1; rows[k] = (unsigned)rl((int)p_rows, q); mls[k] = rows[k] + 2048; }
            else if (ts) { const int q = __builtin_ctz(ts); bits[k] = ts & (~ts + 1); bitp[k] = 0; ts &= ts - 1;
                           const unsigned sl = (unsigned)rl((int)s_base, q); rows[k] = sl + b * 2048; mls[k] = sl + 32768 + b * 32; }
            else { bitp[k] = 0; bits[k] = 0; rows[k] = rows[0]; mls[k] = mls[0]; }
          }
          u32x4 d0[NB], d1[NB], dm[NB];
          if (R0AUX != 16 && spins == 0) {
#pragma unroll
            for (int k = 0; k < NB; ++k) {
              d0[k] = __builtin_amdgcn_raw_buffer_load_b128(wsr, lane * 16, rows[k], R0AUX);
              d1[k] = __builtin_amdgcn_raw_buffer_load_b128(wsr, 1024 + lane * 16, rows[k], R0AUX);
              dm[k] = __builtin_amdgcn_raw_buffer_load_b128(wsr, (lane & 1) * 16, mls[k], R0AUX);
            }
          } else {
#pragma unroll
            for (int k = 0; k < NB; ++k) {
              d0[k] = __builtin_amdgcn_raw_buffer_load_b128(wsr, lane * 16, rows[k], 16);
              d1[k] = __builtin_amdgcn_raw_buffer_load_b128(wsr, 1024 + lane * 16, rows[k], 16);
              dm[k] = __builtin_amdgcn_raw_buffer_load_b128(wsr, (lane & 1) * 16, mls[k], 16);
            }
          }
#pragma unroll
          for (int k = 0; k < NB; ++k) {
            const bool ok = d0[k][1] == tag && d0[k][3] == tag && d1[k][1] == tag && d1[k][3] == tag && dm[k][1] == tag && dm[k][3] == tag;
            if ((bitp[k] | bits[k]) && __all(ok)) {
              const float mv = __uint_as_float(dm[k][0]), lv = __uint_as_float(dm[k][2]);
              const float m0 = rlf(mv, 0), m1 = rlf(mv, 1), l0 = rlf(lv, 0), l1 = rlf(lv, 1);
              const float n0 = fmaxf(M0, m0), n1 = fmaxf(M1, m1);
              const float a0 = __builtin_amdgcn_exp2f(M0 - n0), b0 = __builtin_amdgcn_exp2f(m0 - n0);
              const float a1 = __builtin_amdgcn_exp2f(M1 - n1), b1 = __builtin_amdgcn_exp2f(m1 - n1);
              L0 = L0 * a0 + l0 * b0; L1 = L1 * a1 + l1 * b1;
              o00 = o00 * a0 + __uint_as_float(d0[k][0]) * b0; o01 = o01 * a0 + __uint_as_float(d0[k][2]) * b0;
              o10 = o10 * a1 + __uint_as_float(d1[k][0]) * b1; o11 = o11 * a1 + __uint_as_float(d1[k][2]) * b1;
              M0 = n0; M1 = n1;
              pmk &= ~bitp[k]; smk &= ~bits[k];
              progress = true;
            }
          }
        };
        const int cnt = __builtin_popcountll(tp) + __builtin_popcount(ts);
        if (cnt == 1) batch(IntC<1>{}); else if (cnt <= 3) batch(IntC<3>{}); else batch(IntC<6>{});

      }
      if (!progress) {
        if (++spins > (1u << 16)) break;
        if (spins > FULLR) { light = true; __builtin_amdgcn_s_sleep(SLEEP); }
      }
    }
    const float i0 = 1.f / L0, i1 = 1.f / L1;
    __hip_bfloat16* orow = p.out + (b * NH + 2 * h) * DH + 2 * lane;
    *(unsigned*)orow = pk_bf16(o00 * i0, o01 * i0);
    *(unsigned*)(orow + DH) = pk_bf16(o10 * i1, o11 * i1);
    if (lane == 0) __builtin_amdgcn_raw_buffer_store_b32(tag, wsr, WS_G + (h * 16 + b) * 4, 0, GAUX);
  }
  STAMP(3);
  STAMP_FLUSH();
  PROBE(6);
  PROBE_END();
}

}

extern "C" void kernel_launch(void* const* d_in, const int* in_sizes, int n_in, void* d_out, int out_size, void* d_ws, size_t ws_size,
                              hipStream_t stream) {
  using namespace rattn;
  Params p;
  p.q = (const char*)d_in[0]; p.k = (const char*)d_in[1]; p.v = (const char*)d_in[2];
  p.bt = (const int*)d_in[3]; p.lens = (const int*)d_in[4];
  p.out = (__hip_bfloat16*)d_out; p.ws = (char*)d_ws;
  p.kvbytes = (unsigned)in_sizes[1] * 2u; p.unused = 0;
  (void)hipFuncSetAttribute((const void*)radix_attn_kernel, hipFuncAttributeMaxDynamicSharedMemorySize, LDS_TOTAL);
  hipLaunchKernelGGL(radix_attn_kernel, dim3(256), dim3(256), LDS_TOTAL, stream, p);
}
```

```cpp
#include <hip/hip_runtime.h>
#include <hip/hip_bf16.h>
#include <stdint.h>

namespace rattn {

constexpr int NSEQ = 16, NH = 32, NKV = 16, DH = 128, NBLK = 256, BS = 16;
constexpr int LK = NBLK * BS;
#define GAUX 0
#define KVW_LANES 3
#define KVW_SHIFT 21
#define HRHO 50
#define MDELAY 30
#define PACE 7300
#define ADAPT 1
#define A_F 1008
#define FASTTRY 2
#define GAUX 16
#define PROBE_WG 200
#define PROBE_WAVE 0
#define PROBE_K 0
#define PROBE_MUL 2
#define PACE_LEAD 2
#define PACE_H 88
#define FULLR 0
#define R0AUX 16
#define HPUB_AT 85
#define A_LEAD 0.f
#define A_HMARGIN 350.f
#define A_HPUB 150.f
#define SLEEP2 2
#define MAPMODE 1
#define SLEEP 6
#define KVPOL "nt"
#define RING 3
constexpr int R = RING;
constexpr int STAGE = 32768;
constexpr int RH = 4;
constexpr int RING_BYTES = 4 * RH * 8192;
constexpr int LDS_BT = RING_BYTES;
constexpr int LDS_QB = LDS_BT + NSEQ * NBLK * 4;
constexpr int LDS_SC = LDS_QB + 4 * R * 512;
constexpr int LDS_MISC = LDS_SC + 4096;
constexpr int LDS_PROG = LDS_MISC + 64;
constexpr int LDS_JUNK = LDS_PROG;
constexpr int LDS_TOTAL_JUNK = LDS_PROG + 1024;
constexpr int LDS_TOTAL = LDS_TOTAL_JUNK + 1024;
static_assert(LDS_TOTAL <= 160 * 1024 && LDS_MISC - LDS_QB >= 8192 && RING_BYTES >= R * STAGE, "LDS");

constexpr unsigned WS_G = 0;
constexpr unsigned WS_PROG = 2048;
constexpr unsigned WS_PP = 4096;
constexpr unsigned PP_W = 2176;
constexpr unsigned WS_HP = WS_PP + 320u * 4u * PP_W + 4096u;
constexpr unsigned HP_W = 32768 + 512;
constexpr unsigned WS_END = WS_HP + 16u * 16u * HP_W;
constexpr unsigned WS_STAMP = 64u << 20;

typedef unsigned u32x4 __attribute__((ext_vector_type(4)));
typedef unsigned u32x2 __attribute__((ext_vector_type(2)));
typedef float f32x4 __attribute__((ext_vector_type(4)));
typedef float f32x2 __attribute__((ext_vector_type(2)));
typedef __bf16 bf16x8 __attribute__((ext_vector_type(8)));
typedef __bf16 bf16x2 __attribute__((ext_vector_type(2)));
typedef short s16x4 __attribute__((ext_vector_type(4)));
typedef int i32x4 __attribute__((ext_vector_type(4)));
#define LAS __attribute__((address_space(3)))

constexpr float C2 = 0.088388347648318440f * 1.4426950408889634f;

__device__ __forceinline__ int rfl(int x) { return __builtin_amdgcn_readfirstlane(x); }
__device__ __forceinline__ int rl(int x, int l) { return __builtin_amdgcn_readlane(x, l); }
__device__ __forceinline__ float rlf(float x, int l) { return __builtin_bit_cast(float, __builtin_amdgcn_readlane(__builtin_bit_cast(int, x), l)); }

__device__ __forceinline__ void dma16(const void* sbase, unsigned voff, unsigned dst) {
  unsigned keep;
  asm volatile("s_mov_b32 %0, m0\n\ts_mov_b32 m0, %3\n\ts_nop 0\n\tglobal_load_lds_dwordx4 %1, %2\n\ts_mov_b32 m0, %0"
               : "=&s"(keep) : "v"(voff), "s"(sbase), "s"(dst) : "memory");
}
__device__ __forceinline__ void dma16_sc1(const void* sbase, unsigned voff, unsigned dst) {
  unsigned keep;
  asm volatile("s_mov_b32 %0, m0\n\ts_mov_b32 m0, %3\n\ts_nop 0\n\tglobal_load_lds_dwordx4 %1, %2 sc1\n\ts_mov_b32 m0, %0"
               : "=&s"(keep) : "v"(voff), "s"(sbase), "s"(dst) : "memory");
}
__device__ __forceinline__ void dma_stage(const void* sK, const void* sV, unsigned dstK, unsigned dstV,
                                          unsigned k0, unsigned k1, unsigned k2, unsigned k3,
                                          unsigned v0, unsigned v1, unsigned v2, unsigned v3) {
  unsigned keep;
  asm volatile("s_mov_b32 %0, m0\n\ts_mov_b32 m0, %3\n\ts_nop 0\n\t"
               "global_load_lds_dwordx4 %5, %1 " KVPOL "\n\t"
               "global_load_lds_dwordx4 %6, %1 offset:1024 " KVPOL "\n\t"
               "global_load_lds_dwordx4 %7, %1 offset:2048 " KVPOL "\n\t"
               "global_load_lds_dwordx4 %8, %1 offset:3072 " KVPOL "\n\t"
               "s_mov_b32 m0, %4\n\ts_nop 0\n\t"
               "global_load_lds_dwordx4 %9, %2 " KVPOL "\n\t"
               "global_load_lds_dwordx4 %10, %2 offset:1024 " KVPOL "\n\t"
               "global_load_lds_dwordx4 %11, %2 offset:2048 " KVPOL "\n\t"
               "global_load_lds_dwordx4 %12, %2 offset:3072 " KVPOL "\n\t"
               "s_mov_b32 m0, %0"
               : "=&s"(keep)
               : "s"(sK), "s"(sV), "s"(dstK), "s"(dstV), "v"(k0), "v"(k1), "v"(k2), "v"(k3), "v"(v0), "v"(v1), "v"(v2), "v"(v3)
               : "memory");
}
template <int CTRL> __device__ __forceinline__ int dppi(int x) { return __builtin_amdgcn_update_dpp(0, x, CTRL, 0xf, 0xf, true); }
template <int CTRL> __device__ __forceinline__ int dppi_keep(int old, int x) { return __builtin_amdgcn_update_dpp(old, x, CTRL, 0xf, 0xf, false); }

__device__ __forceinline__ float xg_max(float x) {
  auto s = __builtin_amdgcn_permlane16_swap(__float_as_uint(x), __float_as_uint(x), false, false);
  x = fmaxf(__uint_as_float(s[0]), __uint_as_float(s[1]));
  auto t = __builtin_amdgcn_permlane32_swap(__float_as_uint(x), __float_as_uint(x), false, false);
  return fmaxf(__uint_as_float(t[0]), __uint_as_float(t[1]));
}
__device__ __forceinline__ float xg_sum(float x) {
  auto s = __builtin_amdgcn_permlane16_swap(__float_as_uint(x), __float_as_uint(x), false, false);
  x = __uint_as_float(s[0]) + __uint_as_float(s[1]);
  auto t = __builtin_amdgcn_permlane32_swap(__float_as_uint(x), __float_as_uint(x), false, false);
  return __uint_as_float(t[0]) + __uint_as_float(t[1]);
}
__device__ __forceinline__ unsigned pk_bf16(float a, float b) {
  bf16x2 v = {(__bf16)a, (__bf16)b};
  return __builtin_bit_cast(unsigned, v);
}

template <int NQ>
__device__ __forceinline__ void tile_compute(LAS char* lds, unsigned ka, unsigned va, const bf16x8 (&qf)[NQ][4], f32x4 (&acc)[NQ][8],
                                             float (&m)[NQ], float (&l)[NQ], int lo, int hi, int g4) {
  bf16x8 kf[4];
#pragma unroll
  for (int s = 0; s < 4; ++s) kf[s] = *(LAS bf16x8*)(lds + (ka ^ (s << 6)));
  s16x4 vf[8];
#pragma unroll
  for (int k = 0; k < 8; ++k) vf[k] = __builtin_amdgcn_ds_read_tr16_b64_v4i16((LAS s16x4*)(lds + (va ^ (k << 5))));
  f32x4 S[NQ];
#pragma unroll
  for (int qb = 0; qb < NQ; ++qb) {
    S[qb] = f32x4{0.f, 0.f, 0.f, 0.f};
#pragma unroll
    for (int s = 0; s < 4; ++s) S[qb] = __builtin_amdgcn_mfma_f32_16x16x32_bf16(kf[s], qf[qb][s], S[qb], 0, 0, 0);
  }
  if (lo > 0 || hi < 16) {
#pragma unroll
    for (int qb = 0; qb < NQ; ++qb)
#pragma unroll
      for (int r = 0; r < 4; ++r) { const int t = 4 * g4 + r; S[qb][r] = (t >= lo && t < hi) ? S[qb][r] : -INFINITY; }
  }
  s16x4 pb[NQ];
  bool grow = false;
  float alpha[NQ];
#pragma unroll
  for (int qb = 0; qb < NQ; ++qb) {
    float mx = fmaxf(fmaxf(S[qb][0], S[qb][1]), fmaxf(S[qb][2], S[qb][3]));
    mx = xg_max(mx);
    const float mn2 = fmaxf(m[qb], mx);
    grow |= mn2 > m[qb];
    alpha[qb] = __builtin_amdgcn_exp2f((m[qb] - mn2) * C2);
    m[qb] = mn2;
    const float mc = -mn2 * C2;
    const float p0 = __builtin_amdgcn_exp2f(fmaf(S[qb][0], C2, mc)), p1 = __builtin_amdgcn_exp2f(fmaf(S[qb][1], C2, mc));
    const float p2 = __builtin_amdgcn_exp2f(fmaf(S[qb][2], C2, mc)), p3 = __builtin_amdgcn_exp2f(fmaf(S[qb][3], C2, mc));
    l[qb] = fmaf(l[qb], alpha[qb], (p0 + p1) + (p2 + p3));
    pb[qb] = __builtin_bit_cast(s16x4, u32x2{pk_bf16(p0, p1), pk_bf16(p2, p3)});
  }
  if (__any(grow)) {
#pragma unroll
    for (int qb = 0; qb < NQ; ++qb)
#pragma unroll
      for (int k = 0; k < 8; ++k) acc[qb][k] *= alpha[qb];
  }
#pragma unroll
  for (int qb = 0; qb < NQ; ++qb)
#pragma unroll
    for (int k = 0; k < 8; ++k) acc[qb][k] = __builtin_amdgcn_mfma_f32_16x16x16bf16_1k(vf[k], pb[qb], acc[qb][k], 0, 0, 0);
}

template <int V> struct IntC { static constexpr int value = V; };

struct Params {
  const char* q; const char* k; const char* v; const int* bt; const int* lens; __hip_bfloat16* out; char* ws;
  unsigned kvbytes; unsigned unused;
};

__global__ void __launch_bounds__(256, 2) radix_attn_kernel(Params p) {
  extern __shared__ __attribute__((aligned(16))) char smem[];
  LAS char* lds = (LAS char*)smem;
  const int tid = threadIdx.x, lane = tid & 63, w = rfl(tid >> 6);
  const int gidx = blockIdx.x;
  const int hg = (gidx >> 3) & 3;
  const int jj = (((gidx & 7) + 4 * (hg & 1)) & 7) | ((gidx >> 5) << 3);
  const int c16 = lane & 15, g4 = lane >> 4;
#define STAMP(i) do { } while (0)
#define STAMP_FLUSH() do { } while (0)
  STAMP(0);
  const unsigned long long pr_t0 = __builtin_amdgcn_s_memrealtime();
  unsigned long long pr_x = pr_t0;
#define PROBE(k) do { if ((k) == PROBE_K && pr_x == pr_t0) pr_x = __builtin_amdgcn_s_memrealtime(); } while (0)
#define PROBE_END() do { if (gidx == PROBE_WG && w == PROBE_WAVE) { const unsigned long long lim = 7000ull + (unsigned long long)PROBE_MUL * (pr_x - pr_t0); while (__builtin_amdgcn_s_memrealtime() - pr_t0 < lim) __builtin_amdgcn_s_sleep(4); } } while (0)
  const auto wsr = __builtin_amdgcn_make_buffer_rsrc(p.ws, 0, WS_END, 0x00020000);

  unsigned voffK[4], voffV[4], oK[4], oV[4];
  const int lenraw = p.lens[c16];
  const unsigned gvw = __builtin_amdgcn_raw_buffer_load_b32(wsr, WS_G + ((hg * 4 + w) * 16 + c16) * 4, 0, 16);
  const unsigned gvh = __builtin_amdgcn_raw_buffer_load_b32(wsr, WS_G + ((hg * 4 + (jj & 3)) * 16 + c16) * 4, 0, 16);
  const unsigned gp = __builtin_amdgcn_raw_buffer_load_b32(wsr, WS_G, 0, 16);
  int pm, len_l, minlen, vpages, incp, sump; bool zq_l;
  unsigned ptag;
  {
    const i32x4* bt4 = (const i32x4*)p.bt;
    const i32x4 r0 = bt4[lane];
    i32x4 rr[4];
#pragma unroll
    for (int i = 0; i < 4; ++i) rr[i] = bt4[(4 * w + i) * 64 + lane];
    __builtin_amdgcn_sched_barrier(0);
#pragma unroll
  for (int i = 0; i < 4; ++i) {
    const int t = 4 * w + i, u = 4 * i + g4;
    voffK[i] = g4 * 256 + ((c16 ^ t) << 4) + t * 4096 - i * 1024 + 4096;
    voffV[i] = g4 * 256 + ((c16 ^ (2 * (t & 7))) << 4) + t * 4096 - i * 1024 + 4096;
    oK[i] = u * 4096 + ((c16 ^ u) << 4) - i * 1024 + 4096;
    oV[i] = u * 4096 + ((c16 ^ (2 * (u & 7))) << 4) - i * 1024 + 4096;
  }
    asm volatile("" :: "v"(voffK[0]), "v"(voffK[1]), "v"(voffK[2]), "v"(voffK[3]), "v"(voffV[0]), "v"(voffV[1]), "v"(voffV[2]), "v"(voffV[3]),
                 "v"(oK[0]), "v"(oK[1]), "v"(oK[2]), "v"(oK[3]), "v"(oV[0]), "v"(oV[1]), "v"(oV[2]), "v"(oV[3]) : "memory");
    bool n0 = false, n1 = false, n2 = false, n3 = false;
#pragma unroll
    for (int i = 0; i < 4; ++i) {
      n0 |= rr[i][0] != r0[0]; n1 |= rr[i][1] != r0[1]; n2 |= rr[i][2] != r0[2]; n3 |= rr[i][3] != r0[3];
      *(LAS i32x4*)(lds + LDS_BT + ((4 * w + i) * 256 + 4 * lane) * 4) = rr[i];
    }
    zq_l = lenraw <= 0;
    len_l = zq_l ? LK : min(lenraw, LK);
    int mn = len_l;
    mn = min(mn, dppi_keep<0x128>(mn, mn)); mn = min(mn, dppi_keep<0x124>(mn, mn));
    mn = min(mn, dppi_keep<0x122>(mn, mn)); mn = min(mn, dppi_keep<0x121>(mn, mn));
    minlen = rfl(mn);
    vpages = (len_l + BS - 1) >> 4;
    incp = vpages;
    incp += dppi<0x111>(incp); incp += dppi<0x112>(incp); incp += dppi<0x114>(incp); incp += dppi<0x118>(incp);
    sump = rl(incp, 15);
    ptag = (unsigned)rfl((int)gp) + 1u;
    asm volatile("" :: "v"(gvw), "v"(gvh));
    const int fc = n0 ? 0 : n1 ? 1 : n2 ? 2 : n3 ? 3 : 4;
    const unsigned long long mm = __ballot(fc < 4);
    int col = NBLK;
    if (mm) { const int fl = __builtin_ctzll(mm); col = 4 * fl + rl(fc, fl); }
    if (lane == 0) *(LAS int*)(lds + LDS_MISC + 4 * w) = col;
    asm volatile("s_waitcnt lgkmcnt(0)\n\ts_barrier" ::: "memory");
    PROBE(1);
    const i32x4 mc = *(LAS i32x4*)(lds + LDS_MISC);
    pm = rfl(min(min(mc[0], mc[1]), min(mc[2], mc[3])));
  }
  const int P = min(pm, minlen >> 4);
  const int tsh = P * BS;
  const int vL = vpages - P;
  const int vS = incp - vpages - c16 * P;
  const int N = sump - 16 * P;
  int kH = 0;
  if (P > 0) kH = min(min(15, P), max(1, rfl((int)__builtin_ceilf((float)P * (1024.f / HRHO) * __builtin_amdgcn_rcpf((float)(P + N))))));
  const int nW = 64 - 4 * kH;
  const float inv_nw = __builtin_amdgcn_rcpf((float)nW);
  const unsigned zmask = (unsigned)(__ballot(zq_l) & 0xffffull);
  const bool isH = jj < 4 * kH;
  const float t_all = (float)(P + N) * (16.f * 819.2f / (float)PACE);
  const unsigned long long t_ref = __builtin_amdgcn_s_memrealtime();
  constexpr int A_S0 = 3, A_S1 = 11;
  const int m_avg = rfl((int)(((float)(N + nW - 1) + 0.5f) * inv_nw));
  const bool adapt_on = nW >= 24 && m_avg >= A_S1 + 8;
  auto read_progress = [&](unsigned ldsoff, float& t1, float& d) -> bool {
    const u32x4 e = *(LAS u32x4*)(lds + ldsoff + c16 * 16);
    const bool okv = e[1] == ptag && e[3] == ptag;
    const int cnt = __builtin_popcount((unsigned)__ballot(okv) & 0xffffu);
    float a = okv ? (float)(int)(e[0] - (unsigned)t_ref) : 0.f, c = okv ? (float)e[2] : 0.f;
    a += __builtin_bit_cast(float, dppi_keep<0x128>(0, __builtin_bit_cast(int, a))); c += __builtin_bit_cast(float, dppi_keep<0x128>(0, __builtin_bit_cast(int, c)));
    a += __builtin_bit_cast(float, dppi_keep<0x124>(0, __builtin_bit_cast(int, a))); c += __builtin_bit_cast(float, dppi_keep<0x124>(0, __builtin_bit_cast(int, c)));
    a += __builtin_bit_cast(float, dppi_keep<0x122>(0, __builtin_bit_cast(int, a))); c += __builtin_bit_cast(float, dppi_keep<0x122>(0, __builtin_bit_cast(int, c)));
    a += __builtin_bit_cast(float, dppi_keep<0x121>(0, __builtin_bit_cast(int, a))); c += __builtin_bit_cast(float, dppi_keep<0x121>(0, __builtin_bit_cast(int, c)));
    if (cnt < 8) return false;
    const float ic = 1.f / (float)cnt;
    t1 = rlf(a, 0) * ic; d = rlf(c, 0) * ic * (1.f / (A_S1 - A_S0));
    return true;
  };
  const int r_lo = (int)(((float)(lane * N) + 0.5f) * inv_nw), r_hi = (int)(((float)((lane + 1) * N) + 0.5f) * inv_nw);

  if (isH) {
    const int h = hg * 4 + (jj & 3), part = jj >> 2;
    const int p0 = (P * part) / kH, p1 = (P * (part + 1)) / kH;
#pragma unroll
    for (int i = 0; i < 2; ++i)
      dma16(p.q + (2 * w + i) * 16384 + h * 512, (lane >> 5) * 8192 + (lane & 31) * 16, LDS_QB + (2 * w + i) * 1024);
    const char* kb = p.k + h * 256 - 4096;
    const char* vb = p.v + h * 256 - 4096;
    const int nt = max(0, (p1 - p0 - w + 3) >> 2);
    const unsigned ring = w * (RH * 8192);
    int pg0 = 0, vpg = *(LAS int*)(lds + LDS_BT + min(p0 + w + 4 * lane, NBLK - 1) * 4);
    float pace_h = t_all * (PACE_H * 0.01f) * __builtin_amdgcn_rcpf((float)max(nt, 1)), pace_hb = -3.f * pace_h;
    float t_pub = t_all * (HPUB_AT * 0.01f);
    auto issue = [&](int t) {
      { const int due = rfl((int)(pace_hb + (float)t * pace_h)); if (due > 0) while ((int)(__builtin_amdgcn_s_memrealtime() - t_ref) < due) __builtin_amdgcn_s_sleep(1); }
      if (t - pg0 == 64) { pg0 = t; vpg = *(LAS int*)(lds + LDS_BT + min(p0 + w + 4 * (t + lane), NBLK - 1) * 4); }
      const size_t pb = (size_t)(unsigned)rl(vpg, t - pg0) * 65536;
      const unsigned dst = ring + (t & (RH - 1)) * 8192;
      dma_stage(kb + pb, vb + pb, dst, dst + 4096, oK[0], oK[1], oK[2], oK[3], oV[0], oV[1], oV[2], oV[3]);
    };
    const int npre = min(nt, RH - 1);
#pragma unroll 1
    for (int t = 0; t < npre; ++t) issue(t);
    if (npre == 3) asm volatile("s_waitcnt vmcnt(24)\n\ts_barrier" ::: "memory");
    else asm volatile("s_waitcnt vmcnt(0)\n\ts_barrier" ::: "memory");
    bf16x8 qf[2][4];
#pragma unroll
    for (int qb = 0; qb < 2; ++qb) {
      const bool z = (zmask >> (8 * qb + (c16 >> 1))) & 1;
#pragma unroll
      for (int s = 0; s < 4; ++s) {
        u32x4 x = *(LAS u32x4*)(lds + LDS_QB + (16 * qb + c16) * 256 + (4 * s + g4) * 16);
        if (z) x = u32x4{0u, 0u, 0u, 0u};
        qf[qb][s] = __builtin_bit_cast(bf16x8, x);
      }
    }
    f32x4 acc[2][8];
    float m[2] = {-INFINITY, -INFINITY}, l[2] = {0.f, 0.f};
#pragma unroll
    for (int qb = 0; qb < 2; ++qb)
#pragma unroll
      for (int k = 0; k < 8; ++k) acc[qb][k] = f32x4{0.f, 0.f, 0.f, 0.f};
    const unsigned ka0 = ring + c16 * 256 + ((g4 ^ c16) << 4);
    unsigned va0;
    { const int tq = 4 * g4 + (c16 >> 2), pp = lane & 3; va0 = ring + 4096 + tq * 256 + ((tq & 7) << 5) + ((pp >> 1) << 4) + ((pp & 1) << 3); }
#pragma unroll 1
    for (int t = 0; t < nt; ++t) {
      const int young = min(nt - 1 - t, RH - 2);
      if (young == 2) asm volatile("s_waitcnt vmcnt(16) lgkmcnt(0)" ::: "memory");
      else if (young == 1) asm volatile("s_waitcnt vmcnt(8) lgkmcnt(0)" ::: "memory");
      else asm volatile("s_waitcnt vmcnt(0) lgkmcnt(0)" ::: "memory");
      if (adapt_on && nt >= 12) {
        const int tp = (nt * 9) >> 4;
        if (t == tp + 3) {
          float t1, d;
          if (read_progress(LDS_PROG + w * 256, t1, d)) {
            const float t_end = t1 + (float)(m_avg - A_S1) * d * (A_F * 0.001f);
            const float now = (float)(int)(__builtin_amdgcn_s_memrealtime() - t_ref);
            const int t_i = t + RH - 1;
            pace_h = fmaxf((t_end - A_HMARGIN - now) / (float)max(nt - t_i, 1), 0.f); pace_hb = now - (float)t_i * pace_h;
            t_pub = t_end - A_HPUB;
          }
        }
      }
      if (t + RH - 1 < nt) issue(t + RH - 1);
      if (adapt_on && nt >= 12 && t == ((nt * 9) >> 4) && lane < 16) dma16_sc1(p.ws + WS_PROG, lane * 16, LDS_PROG + w * 256);
      const unsigned sb = (t & (RH - 1)) * 8192;
      tile_compute<2>(lds, ka0 + sb, va0 + sb, qf, acc, m, l, 0, 16, g4);
    }
    STAMP(2);
    constexpr unsigned ROWB = 528, WAVEB = 32 * ROWB, MLB = 4 * WAVEB;
    asm volatile("s_waitcnt lgkmcnt(0)\n\ts_barrier" ::: "memory");
#pragma unroll
    for (int qb = 0; qb < 2; ++qb) {
      const float lt = xg_sum(l[qb]);
#pragma unroll
      for (int k = 0; k < 8; ++k) *(LAS f32x4*)(lds + w * WAVEB + (16 * qb + c16) * ROWB + (16 * k + 4 * g4) * 4) = acc[qb][k];
      if (g4 == 0) *(LAS f32x2*)(lds + MLB + (w * 32 + 16 * qb + c16) * 8) = f32x2{m[qb] * C2, lt};
    }
    asm volatile("s_waitcnt lgkmcnt(0)\n\ts_barrier" ::: "memory");
    while ((int)(__builtin_amdgcn_s_memrealtime() - t_ref) < (int)t_pub) __builtin_amdgcn_s_sleep(8);
    const unsigned slot = WS_HP + (part * 16 + h) * HP_W;
    float mrow = 0.f, lrow = 0.f;
#pragma unroll
    for (int rr = 0; rr < 8; ++rr) {
      const int row = 8 * w + rr;
      f32x2 ml[4], o[4];
#pragma unroll
      for (int v = 0; v < 4; ++v) { ml[v] = *(LAS f32x2*)(lds + MLB + (v * 32 + row) * 8); o[v] = *(LAS f32x2*)(lds + v * WAVEB + row * ROWB + lane * 8); }
      const float M = fmaxf(fmaxf(ml[0][0], ml[1][0]), fmaxf(ml[2][0], ml[3][0]));
      float L = 0.f, o0 = 0.f, o1 = 0.f;
#pragma unroll
      for (int v = 0; v < 4; ++v) {
        const float e = ml[v][1] > 0.f ? __builtin_amdgcn_exp2f(ml[v][0] - M) : 0.f;
        L = fmaf(ml[v][1], e, L); o0 = fmaf(o[v][0], e, o0); o1 = fmaf(o[v][1], e, o1);
      }
      const unsigned tag = (unsigned)rl((int)gvh, row >> 1) + 1u;
      const u32x4 gr = {__float_as_uint(o0), tag, __float_as_uint(o1), tag};
      __builtin_amdgcn_raw_buffer_store_b128(gr, wsr, slot + row * 1024 + lane * 16, 0, 16);
      if (lane == rr) { mrow = M; lrow = L; }
    }
    const unsigned tagr = (unsigned)__shfl((int)gvh, ((8 * w + lane) >> 1) & 15) + 1u;
    if (lane < 8) {
      const unsigned tag = tagr;
      const u32x4 gm = {__float_as_uint(mrow), tag, __float_as_uint(lrow), tag};
      __builtin_amdgcn_raw_buffer_store_b128(gm, wsr, slot + 32768 + (8 * w + lane) * 16, 0, 16);
    }
    STAMP(1);
    STAMP(3);
    STAMP_FLUSH();
    return;
  }

  const int j = jj - 4 * kH, h = hg * 4 + w;
  const int x0 = rl(r_lo, j), x1 = rl(r_hi, j);
  const unsigned umask = (unsigned)(__ballot(max(x0, vS) < min(x1, vS + vL)) & 0xffffull);
  const unsigned kaddr0 = w * 256 + c16 * 1024 + ((g4 ^ c16) << 4);
  unsigned vaddr0;
  { const int tq = 4 * g4 + (c16 >> 2), pp = lane & 3; vaddr0 = 16384 + tq * 1024 + w * 256 + ((tq & 7) << 5) + ((pp >> 1) << 4) + ((pp & 1) << 3); }
  const char* kbase = p.k + hg * 1024 - 4096;
  const char* vbase = p.v + hg * 1024 - 4096;

  unsigned i_mask = umask; int i_row = 0, i_t0 = 0, i_t1 = 0, i_page; bool i_more = false;
  int n_iss = 0, i_slot = 0;
  auto unit_range = [&](int b, int& t0, int& t1) {
    const int s = rl(vS, b), L = rl(vL, b);
    t0 = (P + max(x0, s) - s) * BS; t1 = min((P + min(x1, s + L) - s) * BS, rl(len_l, b));
  };
  auto q_dma = [&](int b, int slot) {
    if (lane < 32) dma16(p.q + b * 8192 + h * 512, lane * 16, LDS_QB + (w * R + slot) * 512);
  };
  if (i_mask) {
    i_row = __builtin_ctz(i_mask); i_mask &= i_mask - 1; unit_range(i_row, i_t0, i_t1);
    q_dma(i_row, 0);
    i_more = true;
  }
  i_page = i_t0 >> 4;
  int i_pg0 = i_page, vpg = *(LAS int*)(lds + LDS_BT + (i_row * NBLK + min(i_page + lane, NBLK - 1)) * 4);
  int pace_tok = 0;
  float pace_d = t_all * __builtin_amdgcn_rcpf((float)max(x1 - x0, 1)), pace_b = -(float)PACE_LEAD * pace_d;
  const int rep = (adapt_on && hg < 2 && j >= (nW >> 1) && j < (nW >> 1) + 8) ? hg * 8 + j - (nW >> 1) : -1;
  unsigned t_s0 = 0;
  auto issue_stage = [&]() {
    const int phys = rl(vpg, i_page - i_pg0);
    const int hi = i_t1 - i_page * 16;
    {
      if (adapt_on && pace_tok == A_S1 + 3 + R - 1) {
        float t1, d;
        if (read_progress(LDS_PROG, t1, d)) { pace_d = d * (A_F * 0.001f); pace_b = t1 - (float)A_S1 * pace_d - A_LEAD; }
      }
      const int due = rfl((int)(pace_b + (float)pace_tok * pace_d));
      pace_tok += 1;
      if (due > 0) while ((int)(__builtin_amdgcn_s_memrealtime() - t_ref) < due) __builtin_amdgcn_s_sleep(1);
    }
    const size_t pb = (size_t)(unsigned)phys * 65536;
    const unsigned dst = i_slot * STAGE + 4 * w * 1024;
    if (hi >= 16) {
      dma_stage(kbase + pb, vbase + pb, dst, dst + 16384, voffK[0], voffK[1], voffK[2], voffK[3], voffV[0], voffV[1], voffV[2], voffV[3]);
    } else {
      unsigned ok[4], ov[4];
#pragma unroll
      for (int i = 0; i < 4; ++i) {
        const int d = (min(4 * w + i, hi - 1) - (4 * w + i)) * 4096;
        ok[i] = voffK[i] + d; ov[i] = voffV[i] + d;
      }
      dma_stage(kbase + pb, vbase + pb, dst, dst + 16384, ok[0], ok[1], ok[2], ok[3], ov[0], ov[1], ov[2], ov[3]);
    }
    if (adapt_on) {
      const int k = pace_tok - 1;
      if (rep >= 0 && w == 3) {
        if (k == A_S0) t_s0 = (unsigned)__builtin_amdgcn_s_memrealtime();
        if (k == A_S1 && lane == 0) {
          const unsigned now = (unsigned)__builtin_amdgcn_s_memrealtime();
          const u32x4 e = {now, ptag, now - t_s0, ptag};
          __builtin_amdgcn_raw_buffer_store_b128(e, wsr, WS_PROG + rep * 16, 0, 16);
        }
      }
      if (k == A_S1 + 3 && w == 0 && lane < 16) dma16_sc1(p.ws + WS_PROG, lane * 16, LDS_PROG);
    }
    ++n_iss; i_slot = (i_slot + 1 == R) ? 0 : i_slot + 1;
    ++i_page;
    if (i_page * 16 >= i_t1) {
      if (i_mask) {
        i_row = __builtin_ctz(i_mask); i_mask &= i_mask - 1; unit_range(i_row, i_t0, i_t1); i_page = i_t0 >> 4;
        q_dma(i_row, i_slot);
        i_pg0 = i_page; vpg = *(LAS int*)(lds + LDS_BT + (i_row * NBLK + min(i_page + lane, NBLK - 1)) * 4);
      } else i_more = false;
    } else if (i_page - i_pg0 == 64) {
      i_pg0 = i_page; vpg = *(LAS int*)(lds + LDS_BT + (i_row * NBLK + min(i_page + lane, NBLK - 1)) * 4);
    }
  };
  PROBE(2);
#pragma unroll 1
  for (int s = 0; s < R - 1; ++s) if (i_more) issue_stage();

  unsigned own = 0;
  {
    unsigned mrem = umask;
    while (mrem) {
      const int b = __builtin_ctz(mrem); mrem &= mrem - 1;
      const int s = rl(vS, b), L = rl(vL, b);
      unsigned long long pmk = __ballot(lane < nW && max(r_lo, s) < min(r_hi, s + L));
      int idx = w % __builtin_popcountll(pmk);
      while (idx--) pmk &= pmk - 1;
      if (__builtin_ctzll(pmk) == j) own |= 1u << b;
    }
    unsigned zl = (unsigned)(__ballot(vL == 0) & 0xffffull);
    while (zl) { const int b = __builtin_ctz(zl); zl &= zl - 1; if ((b * 4) % nW == j) own |= 1u << b; }
  }

  int fb = -1, fcnt = 0; unsigned vrow = 0, vmlo = 0;
  if (own && (own & (own - 1)) == 0) {
    const int b = __builtin_ctz(own);
    const unsigned above = umask >> b;
    if ((umask >> b & 1) && (above == 1 || __builtin_popcount(above) == 2)) {
      const int s = rl(vS, b), L = rl(vL, b);
      const unsigned long long pmk = __ballot(lane < nW && max(r_lo, s) < min(r_hi, s + L)) & ~(1ull << j);
      const int np = __builtin_popcountll(pmk);
      if (np + kH <= 8 && np + kH > 0) {
        fb = b; fcnt = np + kH;
        const int rank = __builtin_amdgcn_mbcnt_hi((unsigned)(pmk >> 32), __builtin_amdgcn_mbcnt_lo((unsigned)pmk, 0));
        const unsigned prow = WS_PP + ((hg * 80 + lane + b) * 4 + w) * PP_W;
        const unsigned pushed = (unsigned)__builtin_amdgcn_ds_permute((((pmk >> lane) & 1) ? rank : 63) << 2, (int)prow);
        const unsigned hb = WS_HP + (min(max(lane - np, 0), 15) * 16 + h) * HP_W;
        vrow = lane < np ? pushed : hb + b * 2048;
        const unsigned mlb = lane < np ? pushed + 2048 : hb + 32768 + b * 32;
        vmlo = (unsigned)__shfl((int)mlb, min(lane >> 1, fcnt - 1)) + (lane & 1) * 16;
      }
    }
  }

  int n_comp = 0, c_slot = 0;
  unsigned c_mask = umask;
  float ro[2][2] = {{0.f, 0.f}, {0.f, 0.f}}, rm[2] = {-INFINITY, -INFINITY}, rs[2] = {0.f, 0.f};
  float ho[2][2] = {{0.f, 0.f}, {0.f, 0.f}}, hm[2] = {-INFINITY, -INFINITY}, hs[2] = {0.f, 0.f};
  int last_b = -1, held_b = -1;
  auto publish = [&](int b, const float (&o)[2][2], const float (&mm)[2], const float (&ll)[2]) {
    const unsigned tag = (unsigned)rl((int)gvw, b) + 1u;
    const unsigned slot = WS_PP + ((hg * 80 + j + b) * 4 + w) * PP_W;
    const u32x4 g0 = {__float_as_uint(o[0][0]), tag, __float_as_uint(o[0][1]), tag};
    const u32x4 g1 = {__float_as_uint(o[1][0]), tag, __float_as_uint(o[1][1]), tag};
    __builtin_amdgcn_raw_buffer_store_b128(g0, wsr, slot + lane * 16, 0, 16);
    __builtin_amdgcn_raw_buffer_store_b128(g1, wsr, slot + 1024 + lane * 16, 0, 16);
    if (lane < 2) {
      const u32x4 gm = {__float_as_uint(lane ? mm[1] : mm[0]), tag, __float_as_uint(lane ? ll[1] : ll[0]), tag};
      __builtin_amdgcn_raw_buffer_store_b128(gm, wsr, slot + 2048 + lane * 16, 0, 16);
    }
  };
  while (c_mask) {
    const int b = __builtin_ctz(c_mask); c_mask &= c_mask - 1;
    int t0, t1; unit_range(b, t0, t1);
    bf16x8 qf[1][4];
    f32x4 acc[1][8];
    float m[1] = {-INFINITY}, l[1] = {0.f};
#pragma unroll
    for (int k = 0; k < 8; ++k) acc[0][k] = f32x4{0.f, 0.f, 0.f, 0.f};
    bool first = true;
    for (int page = t0 >> 4; page * 16 < t1; ++page) {
      {
        const int young = n_iss - n_comp - 1;
        if (R == 4 && young >= 2) asm volatile("s_waitcnt vmcnt(16) lgkmcnt(0)\n\ts_barrier" ::: "memory");
        else if (young >= 1) asm volatile("s_waitcnt vmcnt(8) lgkmcnt(0)\n\ts_barrier" ::: "memory");
        else asm volatile("s_waitcnt vmcnt(0) lgkmcnt(0)\n\ts_barrier" ::: "memory");
      }
      PROBE(3);
      if (first) {
        const bool z = (c16 >= 2) || ((zmask >> b) & 1);
#pragma unroll
        for (int s = 0; s < 4; ++s) {
          u32x4 x = *(LAS u32x4*)(lds + LDS_QB + (w * R + c_slot) * 512 + (c16 & 1) * 256 + (4 * s + g4) * 16);
          if (z) x = u32x4{0u, 0u, 0u, 0u};
          qf[0][s] = __builtin_bit_cast(bf16x8, x);
        }
        asm volatile("s_waitcnt lgkmcnt(0)" ::: "memory");
        first = false;
      }
      if (i_more) issue_stage();
      const unsigned sb = c_slot * STAGE;
      tile_compute<1>(lds, kaddr0 + sb, vaddr0 + sb, qf, acc, m, l, 0, t1 - page * 16, g4);
      ++n_comp; c_slot = (c_slot + 1 == R) ? 0 : c_slot + 1;
    }
    const float lt = xg_sum(l[0]);
    LAS char* sc = lds + LDS_SC + w * 1024;
    if (c16 < 2) {
#pragma unroll
      for (int k = 0; k < 8; ++k) *(LAS f32x4*)(sc + c16 * 512 + (16 * k + 4 * g4) * 4) = acc[0][k];
    }
    asm volatile("s_waitcnt lgkmcnt(0)" ::: "memory");
    const f32x2 a0 = *(LAS f32x2*)(sc + lane * 8), a1 = *(LAS f32x2*)(sc + 512 + lane * 8);
    if (last_b >= 0) {
      if (held_b >= 0) publish(held_b, ho, hm, hs);
      held_b = last_b;
#pragma unroll
      for (int r = 0; r < 2; ++r) { ho[r][0] = ro[r][0]; ho[r][1] = ro[r][1]; hm[r] = rm[r]; hs[r] = rs[r]; }
    }
    ro[0][0] = a0[0]; ro[0][1] = a0[1]; ro[1][0] = a1[0]; ro[1][1] = a1[1];
    rm[0] = rlf(m[0], 0) * C2; rm[1] = rlf(m[0], 1) * C2;
    rs[0] = rlf(lt, 0); rs[1] = rlf(lt, 1);
    last_b = b;
  }
  STAMP(2);
  PROBE(5);
  if (last_b >= 0 && !((own >> last_b) & 1)) { publish(last_b, ro, rm, rs); last_b = -1; }
  if (held_b >= 0 && !((own >> held_b) & 1)) { publish(held_b, ho, hm, hs); held_b = -1; }
  STAMP(1);

  if (own) { const unsigned long long c0 = __builtin_amdgcn_s_memrealtime(); while (__builtin_amdgcn_s_memrealtime() - c0 < MDELAY) __builtin_amdgcn_s_sleep(1); }
  if (fb >= 0) {
    const int b = fb;
    const unsigned tag = (unsigned)rl((int)gvw, b) + 1u;
    const bool mine_last = b == last_b;
    const float om0 = mine_last ? rm[0] : hm[0], om1 = mine_last ? rm[1] : hm[1], ol0 = mine_last ? rs[0] : hs[0], ol1 = mine_last ? rs[1] : hs[1];
    const float oo00 = mine_last ? ro[0][0] : ho[0][0], oo01 = mine_last ? ro[0][1] : ho[0][1];
    const float oo10 = mine_last ? ro[1][0] : ho[1][0], oo11 = mine_last ? ro[1][1] : ho[1][1];
    auto attempt = [&](auto nb_tag) -> bool {
      constexpr int NB = decltype(nb_tag)::value;
      u32x4 d0[NB], d1[NB];
      unsigned so[NB];
#pragma unroll
      for (int k = 0; k < NB; ++k) so[k] = (unsigned)rl((int)vrow, min(k, fcnt - 1));
      asm volatile("" ::: "memory");
      const u32x4 dm = __builtin_amdgcn_raw_buffer_load_b128(wsr, vmlo, 0, 16);
#pragma unroll
      for (int k = 0; k < NB; ++k) {
        d0[k] = __builtin_amdgcn_raw_buffer_load_b128(wsr, lane * 16, so[k], 16);
        d1[k] = __builtin_amdgcn_raw_buffer_load_b128(wsr, 1024 + lane * 16, so[k], 16);
      }
      __builtin_amdgcn_sched_barrier(0);
      const bool live = lane < 2 * fcnt;
      const float mk = live ? __uint_as_float(dm[0]) : -INFINITY, lk = live ? __uint_as_float(dm[2]) : 0.f;
      const float om = (lane & 1) ? om1 : om0, ol = (lane & 1) ? ol1 : ol0;
      float M = fmaxf(mk, om);
      M = fmaxf(M, __builtin_bit_cast(float, dppi_keep<0x122>(__builtin_bit_cast(int, M), __builtin_bit_cast(int, M))));
      M = fmaxf(M, __builtin_bit_cast(float, dppi_keep<0x124>(__builtin_bit_cast(int, M), __builtin_bit_cast(int, M))));
      M = fmaxf(M, __builtin_bit_cast(float, dppi_keep<0x128>(__builtin_bit_cast(int, M), __builtin_bit_cast(int, M))));
      const float e = __builtin_amdgcn_exp2f(mk - M), eo = __builtin_amdgcn_exp2f(om - M);
      float Ls = lk * e;
      Ls += __builtin_bit_cast(float, dppi_keep<0x122>(0, __builtin_bit_cast(int, Ls)));
      Ls += __builtin_bit_cast(float, dppi_keep<0x124>(0, __builtin_bit_cast(int, Ls)));
      Ls += __builtin_bit_cast(float, dppi_keep<0x128>(0, __builtin_bit_cast(int, Ls)));
      Ls = fmaf(ol, eo, Ls);
      const float inv = __builtin_amdgcn_rcpf(Ls);
      const float en = e * inv, eon = eo * inv;
      const float eo0 = rlf(eon, 0), eo1 = rlf(eon, 1);
      float o00 = oo00 * eo0, o01 = oo01 * eo0, o10 = oo10 * eo1, o11 = oo11 * eo1;
      unsigned bad = (dm[1] ^ tag) | (dm[3] ^ tag);
#pragma unroll
      for (int k = 0; k < NB; ++k) {
        const float w0 = rlf(en, 2 * k), w1 = rlf(en, 2 * k + 1);
        bad |= ((d0[k][1] ^ tag) | (d0[k][3] ^ tag)) | ((d1[k][1] ^ tag) | (d1[k][3] ^ tag));
        o00 = fmaf(__uint_as_float(d0[k][0]), w0, o00); o01 = fmaf(__uint_as_float(d0[k][2]), w0, o01);
        o10 = fmaf(__uint_as_float(d1[k][0]), w1, o10); o11 = fmaf(__uint_as_float(d1[k][2]), w1, o11);
      }
      if (__any(bad != 0)) return false;
      __hip_bfloat16* orow = p.out + (b * NH + 2 * h) * DH + 2 * lane;
      *(unsigned*)orow = pk_bf16(o00, o01);
      *(unsigned*)(orow + DH) = pk_bf16(o10, o11);
      if (lane == 0) __builtin_amdgcn_raw_buffer_store_b32(tag, wsr, WS_G + (h * 16 + b) * 4, 0, GAUX);
      return true;
    };
#pragma unroll 1
    for (int t = 0; t < 16; ++t) {
      if (t >= FASTTRY) {
        unsigned spins = 0;
        while (true) {
          asm volatile("" ::: "memory");
          const u32x4 x = __builtin_amdgcn_raw_buffer_load_b128(wsr, vmlo, 0, 16);
          if (!__any(x[1] != tag || x[3] != tag) || ++spins > (1u << 16)) break;
          __builtin_amdgcn_s_sleep(SLEEP2);
        }
      }
      const bool ok = fcnt <= 5 ? attempt(IntC<5>{}) : attempt(IntC<8>{});
      if (ok) { own = 0; break; }
    }
  }
  while (own) {
    const int b = __builtin_ctz(own); own &= own - 1;
    const unsigned tag = (unsigned)rl((int)gvw, b) + 1u;
    const int s = rl(vS, b), L = rl(vL, b);
    unsigned long long pmk = __ballot(lane < nW && max(r_lo, s) < min(r_hi, s + L));
    unsigned smk = (1u << kH) - 1u;
    float M0 = -INFINITY, M1 = -INFINITY, L0 = 0.f, L1 = 0.f, o00 = 0.f, o01 = 0.f, o10 = 0.f, o11 = 0.f;
    if (b == last_b) {
      pmk &= ~(1ull << j);
      M0 = rm[0]; M1 = rm[1]; L0 = rs[0]; L1 = rs[1]; o00 = ro[0][0]; o01 = ro[0][1]; o10 = ro[1][0]; o11 = ro[1][1];
    } else if (b == held_b) {
      pmk &= ~(1ull << j);
      M0 = hm[0]; M1 = hm[1]; L0 = hs[0]; L1 = hs[1]; o00 = ho[0][0]; o01 = ho[0][1]; o10 = ho[1][0]; o11 = ho[1][1];
    }
    const unsigned p_rows = WS_PP + ((hg * 80 + lane + b) * 4 + w) * PP_W;
    const unsigned s_base = WS_HP + ((lane & 15) * 16 + h) * HP_W;
    unsigned spins = 0;
    bool light = false;
    while (pmk | smk) {
      asm volatile("" ::: "memory");
      unsigned long long tp = pmk; unsigned ts = smk;
      const bool single = (__builtin_popcountll(pmk) + __builtin_popcount(smk)) == 1;
      if (light && !single) {
        const int jp = pmk ? (((pmk >> lane) & 1) ? lane : __builtin_ctzll(pmk)) : 0;
        const int js = smk ? (((smk >> (lane & 15)) & 1) ? (lane & 15) : __builtin_ctz(smk)) : 0;
        u32x4 a = {0u, tag, 0u, tag}, c = {0u, tag, 0u, tag};
        if (pmk) a = __builtin_amdgcn_raw_buffer_load_b128(wsr, WS_PP + ((hg * 80 + jp + b) * 4 + w) * PP_W + 2048 + 16, 0, 16);
        if (smk) c = __builtin_amdgcn_raw_buffer_load_b128(wsr, WS_HP + (js * 16 + h) * HP_W + 32768 + (2 * b + 1) * 16, 0, 16);
        tp &= __ballot(a[1] == tag && a[3] == tag);
        ts &= (unsigned)__ballot(c[1] == tag && c[3] == tag);
      }
      bool progress = false;
      if (tp | ts) {
        auto batch = [&](auto nb_tag) {
          constexpr int NB = decltype(nb_tag)::value;
          unsigned rows[NB], mls[NB]; unsigned long long bitp[NB]; unsigned bits[NB];
#pragma unroll
          for (int k = 0; k < NB; ++k) {
            if (tp) { const int q = __builtin_ctzll(tp); bitp[k] = tp & (~tp + 1); bits[k] = 0; tp &= tp - 1; rows[k] = (unsigned)rl((int)p_rows, q); mls[k] = rows[k] + 2048; }
            else if (ts) { const int q = __builtin_ctz(ts); bits[k] = ts & (~ts + 1); bitp[k] = 0; ts &= ts - 1;
                           const unsigned sl = (unsigned)rl((int)s_base, q); rows[k] = sl + b * 2048; mls[k] = sl + 32768 + b * 32; }
            else { bitp[k] = 0; bits[k] = 0; rows[k] = rows[0]; mls[k] = mls[0]; }
          }
          u32x4 d0[NB], d1[NB], dm[NB];
          if (R0AUX != 16 && spins == 0) {
#pragma unroll
            for (int k = 0; k < NB; ++k) {
              d0[k] = __builtin_amdgcn_raw_buffer_load_b128(wsr, lane * 16, rows[k], R0AUX);
              d1[k] = __builtin_amdgcn_raw_buffer_load_b128(wsr, 1024 + lane * 16, rows[k], R0AUX);
              dm[k] = __builtin_amdgcn_raw_buffer_load_b128(wsr, (lane & 1) * 16, mls[k], R0AUX);
            }
          } else {
#pragma unroll
            for (int k = 0; k < NB; ++k) {
              d0[k] = __builtin_amdgcn_raw_buffer_load_b128(wsr, lane * 16, rows[k], 16);
              d1[k] = __builtin_amdgcn_raw_buffer_load_b128(wsr, 1024 + lane * 16, rows[k], 16);
              dm[k] = __builtin_amdgcn_raw_buffer_load_b128(wsr, (lane & 1) * 16, mls[k], 16);
            }
          }
#pragma unroll
          for (int k = 0; k < NB; ++k) {
            const bool ok = d0[k][1] == tag && d0[k][3] == tag && d1[k][1] == tag && d1[k][3] == tag && dm[k][1] == tag && dm[k][3] == tag;
            if ((bitp[k] | bits[k]) && __all(ok)) {
              const float mv = __uint_as_float(dm[k][0]), lv = __uint_as_float(dm[k][2]);
              const float m0 = rlf(mv, 0), m1 = rlf(mv, 1), l0 = rlf(lv, 0), l1 = rlf(lv, 1);
              const float n0 = fmaxf(M0, m0), n1 = fmaxf(M1, m1);
              const float a0 = __builtin_amdgcn_exp2f(M0 - n0), b0 = __builtin_amdgcn_exp2f(m0 - n0);
              const float a1 = __builtin_amdgcn_exp2f(M1 - n1), b1 = __builtin_amdgcn_exp2f(m1 - n1);
              L0 = L0 * a0 + l0 * b0; L1 = L1 * a1 + l1 * b1;
              o00 = o00 * a0 + __uint_as_float(d0[k][0]) * b0; o01 = o01 * a0 + __uint_as_float(d0[k][2]) * b0;
              o10 = o10 * a1 + __uint_as_float(d1[k][0]) * b1; o11 = o11 * a1 + __uint_as_float(d1[k][2]) * b1;
              M0 = n0; M1 = n1;
              pmk &= ~bitp[k]; smk &= ~bits[k];
              progress = true;
            }
          }
        };
        const int cnt = __builtin_popcountll(tp) + __builtin_popcount(ts);
        if (cnt == 1) batch(IntC<1>{}); else if (cnt <= 3) batch(IntC<3>{}); else batch(IntC<6>{});

      }
      if (!progress) {
        if (++spins > (1u << 16)) break;
        if (spins > FULLR) { light = true; __builtin_amdgcn_s_sleep(SLEEP); }
      }
    }
    const float i0 = 1.f / L0, i1 = 1.f / L1;
    __hip_bfloat16* orow = p.out + (b * NH + 2 * h) * DH + 2 * lane;
    *(unsigned*)orow = pk_bf16(o00 * i0, o01 * i0);
    *(unsigned*)(orow + DH) = pk_bf16(o10 * i1, o11 * i1);
    if (lane == 0) __builtin_amdgcn_raw_buffer_store_b32(tag, wsr, WS_G + (h * 16 + b) * 4, 0, GAUX);
  }
  STAMP(3);
  STAMP_FLUSH();
  PROBE(6);
  PROBE_END();
}

}

extern "C" void kernel_launch(void* const* d_in, const int* in_sizes, int n_in, void* d_out, int out_size, void* d_ws, size_t ws_size,
                              hipStream_t stream) {
  using namespace rattn;
  Params p;
  p.q = (const char*)d_in[0]; p.k = (const char*)d_in[1]; p.v = (const char*)d_in[2];
  p.bt = (const int*)d_in[3]; p.lens = (const int*)d_in[4];
  p.out = (__hip_bfloat16*)d_out; p.ws = (char*)d_ws;
  p.kvbytes = (unsigned)in_sizes[1] * 2u; p.unused = 0;
  (void)hipFuncSetAttribute((const void*)radix_attn_kernel, hipFuncAttributeMaxDynamicSharedMemorySize, LDS_TOTAL);
  hipLaunchKernelGGL(radix_attn_kernel, dim3(256), dim3(256), LDS_TOTAL, stream, p);
}
```

```cpp
#include <hip/hip_runtime.h>
#include <hip/hip_bf16.h>
#include <stdint.h>

namespace rattn {

constexpr int NSEQ = 16, NH = 32, NKV = 16, DH = 128, NBLK = 256, BS = 16;
constexpr int LK = NBLK * BS;
#define GAUX 0
#define KVW_LANES 3
#define KVW_SHIFT 21
#define HRHO 50
#define MDELAY 30
#define PACE 7300
#define ADAPT 1
#define FASTTRY 2
#define GAUX 16
#define A_F 1008
#define RING 4
#define PACE_LEAD 2
#define PACE_H 88
#define FULLR 0
#define R0AUX 16
#define HPUB_AT 85
#define A_LEAD 0.f
#define A_HMARGIN 350.f
#define A_HPUB 150.f
#define SLEEP2 2
#define MAPMODE 1
#define SLEEP 6
#define KVPOL "nt"
constexpr int R = RING;
constexpr int STAGE = 32768;
constexpr int RH = 4;
constexpr int RING_BYTES = 4 * RH * 8192;
constexpr int LDS_BT = RING_BYTES;
constexpr int LDS_QB = LDS_BT + NSEQ * NBLK * 4;
constexpr int LDS_SC = LDS_QB + 4 * R * 512;
constexpr int LDS_MISC = LDS_SC + 4096;
constexpr int LDS_PROG = LDS_MISC + 64;
constexpr int LDS_JUNK = LDS_PROG;
constexpr int LDS_TOTAL_JUNK = LDS_PROG + 1024;
constexpr int LDS_TOTAL = LDS_TOTAL_JUNK + 1024;
static_assert(LDS_TOTAL <= 160 * 1024 && LDS_MISC - LDS_QB >= 8192 && RING_BYTES >= R * STAGE, "LDS");

constexpr unsigned WS_G = 0;
constexpr unsigned WS_PROG = 2048;
constexpr unsigned WS_PP = 4096;
constexpr unsigned PP_W = 2176;
constexpr unsigned WS_HP = WS_PP + 320u * 4u * PP_W + 4096u;
constexpr unsigned HP_W = 32768 + 512;
constexpr unsigned WS_END = WS_HP + 16u * 16u * HP_W;
constexpr unsigned WS_STAMP = 64u << 20;

typedef unsigned u32x4 __attribute__((ext_vector_type(4)));
typedef unsigned u32x2 __attribute__((ext_vector_type(2)));
typedef float f32x4 __attribute__((ext_vector_type(4)));
typedef float f32x2 __attribute__((ext_vector_type(2)));
typedef __bf16 bf16x8 __attribute__((ext_vector_type(8)));
typedef __bf16 bf16x2 __attribute__((ext_vector_type(2)));
typedef short s16x4 __attribute__((ext_vector_type(4)));
typedef int i32x4 __attribute__((ext_vector_type(4)));
#define LAS __attribute__((address_space(3)))

constexpr float C2 = 0.088388347648318440f * 1.4426950408889634f;

__device__ __forceinline__ int rfl(int x) { return __builtin_amdgcn_readfirstlane(x); }
__device__ __forceinline__ int rl(int x, int l) { return __builtin_amdgcn_readlane(x, l); }
__device__ __forceinline__ float rlf(float x, int l) { return __builtin_bit_cast(float, __builtin_amdgcn_readlane(__builtin_bit_cast(int, x), l)); }

__device__ __forceinline__ void dma16(const void* sbase, unsigned voff, unsigned dst) {
  unsigned keep;
  asm volatile("s_mov_b32 %0, m0\n\ts_mov_b32 m0, %3\n\ts_nop 0\n\tglobal_load_lds_dwordx4 %1, %2\n\ts_mov_b32 m0, %0"
               : "=&s"(keep) : "v"(voff), "s"(sbase), "s"(dst) : "memory");
}
__device__ __forceinline__ void dma16_sc1(const void* sbase, unsigned voff, unsigned dst) {
  unsigned keep;
  asm volatile("s_mov_b32 %0, m0\n\ts_mov_b32 m0, %3\n\ts_nop 0\n\tglobal_load_lds_dwordx4 %1, %2 sc1\n\ts_mov_b32 m0, %0"
               : "=&s"(keep) : "v"(voff), "s"(sbase), "s"(dst) : "memory");
}
__device__ __forceinline__ void dma_stage(const void* sK, const void* sV, unsigned dstK, unsigned dstV,
                                          unsigned k0, unsigned k1, unsigned k2, unsigned k3,
                                          unsigned v0, unsigned v1, unsigned v2, unsigned v3) {
  unsigned keep;
  asm volatile("s_mov_b32 %0, m0\n\ts_mov_b32 m0, %3\n\ts_nop 0\n\t"
               "global_load_lds_dwordx4 %5, %1 " KVPOL "\n\t"
               "global_load_lds_dwordx4 %6, %1 offset:1024 " KVPOL "\n\t"
               "global_load_lds_dwordx4 %7, %1 offset:2048 " KVPOL "\n\t"
               "global_load_lds_dwordx4 %8, %1 offset:3072 " KVPOL "\n\t"
               "s_mov_b32 m0, %4\n\ts_nop 0\n\t"
               "global_load_lds_dwordx4 %9, %2 " KVPOL "\n\t"
               "global_load_lds_dwordx4 %10, %2 offset:1024 " KVPOL "\n\t"
               "global_load_lds_dwordx4 %11, %2 offset:2048 " KVPOL "\n\t"
               "global_load_lds_dwordx4 %12, %2 offset:3072 " KVPOL "\n\t"
               "s_mov_b32 m0, %0"
               : "=&s"(keep)
               : "s"(sK), "s"(sV), "s"(dstK), "s"(dstV), "v"(k0), "v"(k1), "v"(k2), "v"(k3), "v"(v0), "v"(v1), "v"(v2), "v"(v3)
               : "memory");
}
template <int CTRL> __device__ __forceinline__ int dppi(int x) { return __builtin_amdgcn_update_dpp(0, x, CTRL, 0xf, 0xf, true); }
template <int CTRL> __device__ __forceinline__ int dppi_keep(int old, int x) { return __builtin_amdgcn_update_dpp(old, x, CTRL, 0xf, 0xf, false); }

__device__ __forceinline__ float xg_max(float x) {
  auto s = __builtin_amdgcn_permlane16_swap(__float_as_uint(x), __float_as_uint(x), false, false);
  x = fmaxf(__uint_as_float(s[0]), __uint_as_float(s[1]));
  auto t = __builtin_amdgcn_permlane32_swap(__float_as_uint(x), __float_as_uint(x), false, false);
  return fmaxf(__uint_as_float(t[0]), __uint_as_float(t[1]));
}
__device__ __forceinline__ float xg_sum(float x) {
  auto s = __builtin_amdgcn_permlane16_swap(__float_as_uint(x), __float_as_uint(x), false, false);
  x = __uint_as_float(s[0]) + __uint_as_float(s[1]);
  auto t = __builtin_amdgcn_permlane32_swap(__float_as_uint(x), __float_as_uint(x), false, false);
  return __uint_as_float(t[0]) + __uint_as_float(t[1]);
}
__device__ __forceinline__ unsigned pk_bf16(float a, float b) {
  bf16x2 v = {(__bf16)a, (__bf16)b};
  return __builtin_bit_cast(unsigned, v);
}

template <int NQ>
__device__ __forceinline__ void tile_compute(LAS char* lds, unsigned ka, unsigned va, const bf16x8 (&qf)[NQ][4], f32x4 (&acc)[NQ][8],
                                             float (&m)[NQ], float (&l)[NQ], int lo, int hi, int g4) {
  bf16x8 kf[4];
#pragma unroll
  for (int s = 0; s < 4; ++s) kf[s] = *(LAS bf16x8*)(lds + (ka ^ (s << 6)));
  s16x4 vf[8];
#pragma unroll
  for (int k = 0; k < 8; ++k) vf[k] = __builtin_amdgcn_ds_read_tr16_b64_v4i16((LAS s16x4*)(lds + (va ^ (k << 5))));
  f32x4 S[NQ];
#pragma unroll
  for (int qb = 0; qb < NQ; ++qb) {
    S[qb] = f32x4{0.f, 0.f, 0.f, 0.f};
#pragma unroll
    for (int s = 0; s < 4; ++s) S[qb] = __builtin_amdgcn_mfma_f32_16x16x32_bf16(kf[s], qf[qb][s], S[qb], 0, 0, 0);
  }
  if (lo > 0 || hi < 16) {
#pragma unroll
    for (int qb = 0; qb < NQ; ++qb)
#pragma unroll
      for (int r = 0; r < 4; ++r) { const int t = 4 * g4 + r; S[qb][r] = (t >= lo && t < hi) ? S[qb][r] : -INFINITY; }
  }
  s16x4 pb[NQ];
  bool grow = false;
  float alpha[NQ];
#pragma unroll
  for (int qb = 0; qb < NQ; ++qb) {
    float mx = fmaxf(fmaxf(S[qb][0], S[qb][1]), fmaxf(S[qb][2], S[qb][3]));
    mx = xg_max(mx);
    const float mn2 = fmaxf(m[qb], mx);
    grow |= mn2 > m[qb];
    alpha[qb] = __builtin_amdgcn_exp2f((m[qb] - mn2) * C2);
    m[qb] = mn2;
    const float mc = -mn2 * C2;
    const float p0 = __builtin_amdgcn_exp2f(fmaf(S[qb][0], C2, mc)), p1 = __builtin_amdgcn_exp2f(fmaf(S[qb][1], C2, mc));
    const float p2 = __builtin_amdgcn_exp2f(fmaf(S[qb][2], C2, mc)), p3 = __builtin_amdgcn_exp2f(fmaf(S[qb][3], C2, mc));
    l[qb] = fmaf(l[qb], alpha[qb], (p0 + p1) + (p2 + p3));
    pb[qb] = __builtin_bit_cast(s16x4, u32x2{pk_bf16(p0, p1), pk_bf16(p2, p3)});
  }
  if (__any(grow)) {
#pragma unroll
    for (int qb = 0; qb < NQ; ++qb)
#pragma unroll
      for (int k = 0; k < 8; ++k) acc[qb][k] *= alpha[qb];
  }
#pragma unroll
  for (int qb = 0; qb < NQ; ++qb)
#pragma unroll
    for (int k = 0; k < 8; ++k) acc[qb][k] = __builtin_amdgcn_mfma_f32_16x16x16bf16_1k(vf[k], pb[qb], acc[qb][k], 0, 0, 0);
}

template <int V> struct IntC { static constexpr int value = V; };

struct Params {
  const char* q; const char* k; const char* v; const int* bt; const int* lens; __hip_bfloat16* out; char* ws;
  unsigned kvbytes; unsigned unused;
};

__global__ void __launch_bounds__(256, 2) radix_attn_kernel(Params p) {
  extern __shared__ __attribute__((aligned(16))) char smem[];
  LAS char* lds = (LAS char*)smem;
  const int tid = threadIdx.x, lane = tid & 63, w = rfl(tid >> 6);
  const int gidx = blockIdx.x;
  const int hg = (gidx >> 3) & 3;
  const int jj = (((gidx & 7) + 4 * (hg & 1)) & 7) | ((gidx >> 5) << 3);
  const int c16 = lane & 15, g4 = lane >> 4;
#define STAMP(i) do { } while (0)
#define STAMP_FLUSH() do { } while (0)
  STAMP(0);
#define PROBE(k) do { } while (0)
#define PROBE_END() do { } while (0)
  const auto wsr = __builtin_amdgcn_make_buffer_rsrc(p.ws, 0, WS_END, 0x00020000);

  unsigned voffK[4], voffV[4], oK[4], oV[4];
  const int lenraw = p.lens[c16];
  const unsigned gvw = __builtin_amdgcn_raw_buffer_load_b32(wsr, WS_G + ((hg * 4 + w) * 16 + c16) * 4, 0, 16);
  const unsigned gvh = __builtin_amdgcn_raw_buffer_load_b32(wsr, WS_G + ((hg * 4 + (jj & 3)) * 16 + c16) * 4, 0, 16);
  const unsigned gp = __builtin_amdgcn_raw_buffer_load_b32(wsr, WS_G, 0, 16);
  int pm, len_l, minlen, vpages, incp, sump; bool zq_l;
  unsigned ptag;
  {
    const i32x4* bt4 = (const i32x4*)p.bt;
    const i32x4 r0 = bt4[lane];
    i32x4 rr[4];
#pragma unroll
    for (int i = 0; i < 4; ++i) rr[i] = bt4[(4 * w + i) * 64 + lane];
    __builtin_amdgcn_sched_barrier(0);
#pragma unroll
  for (int i = 0; i < 4; ++i) {
    const int t = 4 * w + i, u = 4 * i + g4;
    voffK[i] = g4 * 256 + ((c16 ^ t) << 4) + t * 4096 - i * 1024 + 4096;
    voffV[i] = g4 * 256 + ((c16 ^ (2 * (t & 7))) << 4) + t * 4096 - i * 1024 + 4096;
    oK[i] = u * 4096 + ((c16 ^ u) << 4) - i * 1024 + 4096;
    oV[i] = u * 4096 + ((c16 ^ (2 * (u & 7))) << 4) - i * 1024 + 4096;
  }
    asm volatile("" :: "v"(voffK[0]), "v"(voffK[1]), "v"(voffK[2]), "v"(voffK[3]), "v"(voffV[0]), "v"(voffV[1]), "v"(voffV[2]), "v"(voffV[3]),
                 "v"(oK[0]), "v"(oK[1]), "v"(oK[2]), "v"(oK[3]), "v"(oV[0]), "v"(oV[1]), "v"(oV[2]), "v"(oV[3]) : "memory");
    bool n0 = false, n1 = false, n2 = false, n3 = false;
#pragma unroll
    for (int i = 0; i < 4; ++i) {
      n0 |= rr[i][0] != r0[0]; n1 |= rr[i][1] != r0[1]; n2 |= rr[i][2] != r0[2]; n3 |= rr[i][3] != r0[3];
      *(LAS i32x4*)(lds + LDS_BT + ((4 * w + i) * 256 + 4 * lane) * 4) = rr[i];
    }
    zq_l = lenraw <= 0;
    len_l = zq_l ? LK : min(lenraw, LK);
    int mn = len_l;
    mn = min(mn, dppi_keep<0x128>(mn, mn)); mn = min(mn, dppi_keep<0x124>(mn, mn));
    mn = min(mn, dppi_keep<0x122>(mn, mn)); mn = min(mn, dppi_keep<0x121>(mn, mn));
    minlen = rfl(mn);
    vpages = (len_l + BS - 1) >> 4;
    incp = vpages;
    incp += dppi<0x111>(incp); incp += dppi<0x112>(incp); incp += dppi<0x114>(incp); incp += dppi<0x118>(incp);
    sump = rl(incp, 15);
    ptag = (unsigned)rfl((int)gp) + 1u;
    asm volatile("" :: "v"(gvw), "v"(gvh));
    const int fc = n0 ? 0 : n1 ? 1 : n2 ? 2 : n3 ? 3 : 4;
    const unsigned long long mm = __ballot(fc < 4);
    int col = NBLK;
    if (mm) { const int fl = __builtin_ctzll(mm); col = 4 * fl + rl(fc, fl); }
    if (lane == 0) *(LAS int*)(lds + LDS_MISC + 4 * w) = col;
    asm volatile("s_waitcnt lgkmcnt(0)\n\ts_barrier" ::: "memory");
    PROBE(1);
    const i32x4 mc = *(LAS i32x4*)(lds + LDS_MISC);
    pm = rfl(min(min(mc[0], mc[1]), min(mc[2], mc[3])));
  }
  const int P = min(pm, minlen >> 4);
  const int tsh = P * BS;
  const int vL = vpages - P;
  const int vS = incp - vpages - c16 * P;
  const int N = sump - 16 * P;
  int kH = 0;
  if (P > 0) kH = min(min(15, P), max(1, rfl((int)__builtin_ceilf((float)P * (1024.f / HRHO) * __builtin_amdgcn_rcpf((float)(P + N))))));
  const int nW = 64 - 4 * kH;
  const float inv_nw = __builtin_amdgcn_rcpf((float)nW);
  const unsigned zmask = (unsigned)(__ballot(zq_l) & 0xffffull);
  const bool isH = jj < 4 * kH;
  const float t_all = (float)(P + N) * (16.f * 819.2f / (float)PACE);
  const unsigned long long t_ref = __builtin_amdgcn_s_memrealtime();
  constexpr int A_S0 = 3, A_S1 = 11;
  const int m_avg = rfl((int)(((float)(N + nW - 1) + 0.5f) * inv_nw));
  const bool adapt_on = nW >= 24 && m_avg >= A_S1 + 8;
  auto read_progress = [&](unsigned ldsoff, float& t1, float& d) -> bool {
    const u32x4 e = *(LAS u32x4*)(lds + ldsoff + c16 * 16);
    const bool okv = e[1] == ptag && e[3] == ptag;
    const int cnt = __builtin_popcount((unsigned)__ballot(okv) & 0xffffu);
    float a = okv ? (float)(int)(e[0] - (unsigned)t_ref) : 0.f, c = okv ? (float)e[2] : 0.f;
    a += __builtin_bit_cast(float, dppi_keep<0x128>(0, __builtin_bit_cast(int, a))); c += __builtin_bit_cast(float, dppi_keep<0x128>(0, __builtin_bit_cast(int, c)));
    a += __builtin_bit_cast(float, dppi_keep<0x124>(0, __builtin_bit_cast(int, a))); c += __builtin_bit_cast(float, dppi_keep<0x124>(0, __builtin_bit_cast(int, c)));
    a += __builtin_bit_cast(float, dppi_keep<0x122>(0, __builtin_bit_cast(int, a))); c += __builtin_bit_cast(float, dppi_keep<0x122>(0, __builtin_bit_cast(int, c)));
    a += __builtin_bit_cast(float, dppi_keep<0x121>(0, __builtin_bit_cast(int, a))); c += __builtin_bit_cast(float, dppi_keep<0x121>(0, __builtin_bit_cast(int, c)));
    if (cnt < 8) return false;
    const float ic = 1.f / (float)cnt;
    t1 = rlf(a, 0) * ic; d = rlf(c, 0) * ic * (1.f / (A_S1 - A_S0));
    return true;
  };
  const int r_lo = (int)(((float)(lane * N) + 0.5f) * inv_nw), r_hi = (int)(((float)((lane + 1) * N) + 0.5f) * inv_nw);

  if (isH) {
    const int h = hg * 4 + (jj & 3), part = jj >> 2;
    const int p0 = (P * part) / kH, p1 = (P * (part + 1)) / kH;
#pragma unroll
    for (int i = 0; i < 2; ++i)
      dma16(p.q + (2 * w + i) * 16384 + h * 512, (lane >> 5) * 8192 + (lane & 31) * 16, LDS_QB + (2 * w + i) * 1024);
    const char* kb = p.k + h * 256 - 4096;
    const char* vb = p.v + h * 256 - 4096;
    const int nt = max(0, (p1 - p0 - w + 3) >> 2);
    const unsigned ring = w * (RH * 8192);
    int pg0 = 0, vpg = *(LAS int*)(lds + LDS_BT + min(p0 + w + 4 * lane, NBLK - 1) * 4);
    float pace_h = t_all * (PACE_H * 0.01f) * __builtin_amdgcn_rcpf((float)max(nt, 1)), pace_hb = -3.f * pace_h;
    float t_pub = t_all * (HPUB_AT * 0.01f);
    auto issue = [&](int t) {
      { const int due = rfl((int)(pace_hb + (float)t * pace_h)); if (due > 0) while ((int)(__builtin_amdgcn_s_memrealtime() - t_ref) < due) __builtin_amdgcn_s_sleep(1); }
      if (t - pg0 == 64) { pg0 = t; vpg = *(LAS int*)(lds + LDS_BT + min(p0 + w + 4 * (t + lane), NBLK - 1) * 4); }
      const size_t pb = (size_t)(unsigned)rl(vpg, t - pg0) * 65536;
      const unsigned dst = ring + (t & (RH - 1)) * 8192;
      dma_stage(kb + pb, vb + pb, dst, dst + 4096, oK[0], oK[1], oK[2], oK[3], oV[0], oV[1], oV[2], oV[3]);
    };
    const int npre = min(nt, RH - 1);
#pragma unroll 1
    for (int t = 0; t < npre; ++t) issue(t);
    if (npre == 3) asm volatile("s_waitcnt vmcnt(24)\n\ts_barrier" ::: "memory");
    else asm volatile("s_waitcnt vmcnt(0)\n\ts_barrier" ::: "memory");
    bf16x8 qf[2][4];
#pragma unroll
    for (int qb = 0; qb < 2; ++qb) {
      const bool z = (zmask >> (8 * qb + (c16 >> 1))) & 1;
#pragma unroll
      for (int s = 0; s < 4; ++s) {
        u32x4 x = *(LAS u32x4*)(lds + LDS_QB + (16 * qb + c16) * 256 + (4 * s + g4) * 16);
        if (z) x = u32x4{0u, 0u, 0u, 0u};
        qf[qb][s] = __builtin_bit_cast(bf16x8, x);
      }
    }
    f32x4 acc[2][8];
    float m[2] = {-INFINITY, -INFINITY}, l[2] = {0.f, 0.f};
#pragma unroll
    for (int qb = 0; qb < 2; ++qb)
#pragma unroll
      for (int k = 0; k < 8; ++k) acc[qb][k] = f32x4{0.f, 0.f, 0.f, 0.f};
    const unsigned ka0 = ring + c16 * 256 + ((g4 ^ c16) << 4);
    unsigned va0;
    { const int tq = 4 * g4 + (c16 >> 2), pp = lane & 3; va0 = ring + 4096 + tq * 256 + ((tq & 7) << 5) + ((pp >> 1) << 4) + ((pp & 1) << 3); }
#pragma unroll 1
    for (int t = 0; t < nt; ++t) {
      const int young = min(nt - 1 - t, RH - 2);
      if (young == 2) asm volatile("s_waitcnt vmcnt(16) lgkmcnt(0)" ::: "memory");
      else if (young == 1) asm volatile("s_waitcnt vmcnt(8) lgkmcnt(0)" ::: "memory");
      else asm volatile("s_waitcnt vmcnt(0) lgkmcnt(0)" ::: "memory");
      if (adapt_on && nt >= 12) {
        const int tp = (nt * 9) >> 4;
        if (t == tp + 3) {
          float t1, d;
          if (read_progress(LDS_PROG + w * 256, t1, d)) {
            const float t_end = t1 + (float)(m_avg - A_S1) * d * (A_F * 0.001f);
            const float now = (float)(int)(__builtin_amdgcn_s_memrealtime() - t_ref);
            const int t_i = t + RH - 1;
            pace_h = fmaxf((t_end - A_HMARGIN - now) / (float)max(nt - t_i, 1), 0.f); pace_hb = now - (float)t_i * pace_h;
            t_pub = t_end - A_HPUB;
          }
        }
      }
      if (t + RH - 1 < nt) issue(t + RH - 1);
      if (adapt_on && nt >= 12 && t == ((nt * 9) >> 4) && lane < 16) dma16_sc1(p.ws + WS_PROG, lane * 16, LDS_PROG + w * 256);
      const unsigned sb = (t & (RH - 1)) * 8192;
      tile_compute<2>(lds, ka0 + sb, va0 + sb, qf, acc, m, l, 0, 16, g4);
    }
    STAMP(2);
    constexpr unsigned ROWB = 528, WAVEB = 32 * ROWB, MLB = 4 * WAVEB;
    asm volatile("s_waitcnt lgkmcnt(0)\n\ts_barrier" ::: "memory");
#pragma unroll
    for (int qb = 0; qb < 2; ++qb) {
      const float lt = xg_sum(l[qb]);
#pragma unroll
      for (int k = 0; k < 8; ++k) *(LAS f32x4*)(lds + w * WAVEB + (16 * qb + c16) * ROWB + (16 * k + 4 * g4) * 4) = acc[qb][k];
      if (g4 == 0) *(LAS f32x2*)(lds + MLB + (w * 32 + 16 * qb + c16) * 8) = f32x2{m[qb] * C2, lt};
    }
    asm volatile("s_waitcnt lgkmcnt(0)\n\ts_barrier" ::: "memory");
    while ((int)(__builtin_amdgcn_s_memrealtime() - t_ref) < (int)t_pub) __builtin_amdgcn_s_sleep(8);
    const unsigned slot = WS_HP + (part * 16 + h) * HP_W;
    float mrow = 0.f, lrow = 0.f;
#pragma unroll
    for (int rr = 0; rr < 8; ++rr) {
      const int row = 8 * w + rr;
      f32x2 ml[4], o[4];
#pragma unroll
      for (int v = 0; v < 4; ++v) { ml[v] = *(LAS f32x2*)(lds + MLB + (v * 32 + row) * 8); o[v] = *(LAS f32x2*)(lds + v * WAVEB + row * ROWB + lane * 8); }
      const float M = fmaxf(fmaxf(ml[0][0], ml[1][0]), fmaxf(ml[2][0], ml[3][0]));
      float L = 0.f, o0 = 0.f, o1 = 0.f;
#pragma unroll
      for (int v = 0; v < 4; ++v) {
        const float e = ml[v][1] > 0.f ? __builtin_amdgcn_exp2f(ml[v][0] - M) : 0.f;
        L = fmaf(ml[v][1], e, L); o0 = fmaf(o[v][0], e, o0); o1 = fmaf(o[v][1], e, o1);
      }
      const unsigned tag = (unsigned)rl((int)gvh, row >> 1) + 1u;
      const u32x4 gr = {__float_as_uint(o0), tag, __float_as_uint(o1), tag};
      __builtin_amdgcn_raw_buffer_store_b128(gr, wsr, slot + row * 1024 + lane * 16, 0, 16);
      if (lane == rr) { mrow = M; lrow = L; }
    }
    const unsigned tagr = (unsigned)__shfl((int)gvh, ((8 * w + lane) >> 1) & 15) + 1u;
    if (lane < 8) {
      const unsigned tag = tagr;
      const u32x4 gm = {__float_as_uint(mrow), tag, __float_as_uint(lrow), tag};
      __builtin_amdgcn_raw_buffer_store_b128(gm, wsr, slot + 32768 + (8 * w + lane) * 16, 0, 16);
    }
    STAMP(1);
    STAMP(3);
    STAMP_FLUSH();
    return;
  }

  const int j = jj - 4 * kH, h = hg * 4 + w;
  const int x0 = rl(r_lo, j), x1 = rl(r_hi, j);
  const unsigned umask = (unsigned)(__ballot(max(x0, vS) < min(x1, vS + vL)) & 0xffffull);
  const unsigned kaddr0 = w * 256 + c16 * 1024 + ((g4 ^ c16) << 4);
  unsigned vaddr0;
  { const int tq = 4 * g4 + (c16 >> 2), pp = lane & 3; vaddr0 = 16384 + tq * 1024 + w * 256 + ((tq & 7) << 5) + ((pp >> 1) << 4) + ((pp & 1) << 3); }
  const char* kbase = p.k + hg * 1024 - 4096;
  const char* vbase = p.v + hg * 1024 - 4096;

  unsigned i_mask = umask; int i_row = 0, i_t0 = 0, i_t1 = 0, i_page; bool i_more = false;
  int n_iss = 0, i_slot = 0;
  auto unit_range = [&](int b, int& t0, int& t1) {
    const int s = rl(vS, b), L = rl(vL, b);
    t0 = (P + max(x0, s) - s) * BS; t1 = min((P + min(x1, s + L) - s) * BS, rl(len_l, b));
  };
  auto q_dma = [&](int b, int slot) {
    if (lane < 32) dma16(p.q + b * 8192 + h * 512, lane * 16, LDS_QB + (w * R + slot) * 512);
  };
  if (i_mask) {
    i_row = __builtin_ctz(i_mask); i_mask &= i_mask - 1; unit_range(i_row, i_t0, i_t1);
    q_dma(i_row, 0);
    i_more = true;
  }
  i_page = i_t0 >> 4;
  int i_pg0 = i_page, vpg = *(LAS int*)(lds + LDS_BT + (i_row * NBLK + min(i_page + lane, NBLK - 1)) * 4);
  int pace_tok = 0;
  float pace_d = t_all * __builtin_amdgcn_rcpf((float)max(x1 - x0, 1)), pace_b = -(float)PACE_LEAD * pace_d;
  const int rep = (adapt_on && hg < 2 && j >= (nW >> 1) && j < (nW >> 1) + 8) ? hg * 8 + j - (nW >> 1) : -1;
  unsigned t_s0 = 0;
  auto issue_stage = [&]() {
    const int phys = rl(vpg, i_page - i_pg0);
    const int hi = i_t1 - i_page * 16;
    {
      if (adapt_on && pace_tok == A_S1 + 3 + R - 1) {
        float t1, d;
        if (read_progress(LDS_PROG, t1, d)) { pace_d = d * (A_F * 0.001f); pace_b = t1 - (float)A_S1 * pace_d - A_LEAD; }
      }
      const int due = rfl((int)(pace_b + (float)pace_tok * pace_d));
      pace_tok += 1;
      if (due > 0) while ((int)(__builtin_amdgcn_s_memrealtime() - t_ref) < due) __builtin_amdgcn_s_sleep(1);
    }
    const size_t pb = (size_t)(unsigned)phys * 65536;
    const unsigned dst = i_slot * STAGE + 4 * w * 1024;
    if (hi >= 16) {
      dma_stage(kbase + pb, vbase + pb, dst, dst + 16384, voffK[0], voffK[1], voffK[2], voffK[3], voffV[0], voffV[1], voffV[2], voffV[3]);
    } else {
      unsigned ok[4], ov[4];
#pragma unroll
      for (int i = 0; i < 4; ++i) {
        const int d = (min(4 * w + i, hi - 1) - (4 * w + i)) * 4096;
        ok[i] = voffK[i] + d; ov[i] = voffV[i] + d;
      }
      dma_stage(kbase + pb, vbase + pb, dst, dst + 16384, ok[0], ok[1], ok[2], ok[3], ov[0], ov[1], ov[2], ov[3]);
    }
    if (adapt_on) {
      const int k = pace_tok - 1;
      if (rep >= 0 && w == 3) {
        if (k == A_S0) t_s0 = (unsigned)__builtin_amdgcn_s_memrealtime();
        if (k == A_S1 && lane == 0) {
          const unsigned now = (unsigned)__builtin_amdgcn_s_memrealtime();
          const u32x4 e = {now, ptag, now - t_s0, ptag};
          __builtin_amdgcn_raw_buffer_store_b128(e, wsr, WS_PROG + rep * 16, 0, 16);
        }
      }
      if (k == A_S1 + 3 && w == 0 && lane < 16) dma16_sc1(p.ws + WS_PROG, lane * 16, LDS_PROG);
    }
    ++n_iss; i_slot = (i_slot + 1 == R) ? 0 : i_slot + 1;
    ++i_page;
    if (i_page * 16 >= i_t1) {
      if (i_mask) {
        i_row = __builtin_ctz(i_mask); i_mask &= i_mask - 1; unit_range(i_row, i_t0, i_t1); i_page = i_t0 >> 4;
        q_dma(i_row, i_slot);
        i_pg0 = i_page; vpg = *(LAS int*)(lds + LDS_BT + (i_row * NBLK + min(i_page + lane, NBLK - 1)) * 4);
      } else i_more = false;
    } else if (i_page - i_pg0 == 64) {
      i_pg0 = i_page; vpg = *(LAS int*)(lds + LDS_BT + (i_row * NBLK + min(i_page + lane, NBLK - 1)) * 4);
    }
  };
  PROBE(2);
#pragma unroll 1
  for (int s = 0; s < R - 1; ++s) if (i_more) issue_stage();

  unsigned own = 0;
  {
    unsigned mrem = umask;
    while (mrem) {
      const int b = __builtin_ctz(mrem); mrem &= mrem - 1;
      const int s = rl(vS, b), L = rl(vL, b);
      unsigned long long pmk = __ballot(lane < nW && max(r_lo, s) < min(r_hi, s + L));
      int idx = w % __builtin_popcountll(pmk);
      while (idx--) pmk &= pmk - 1;
      if (__builtin_ctzll(pmk) == j) own |= 1u << b;
    }
    unsigned zl = (unsigned)(__ballot(vL == 0) & 0xffffull);
    while (zl) { const int b = __builtin_ctz(zl); zl &= zl - 1; if ((b * 4) % nW == j) own |= 1u << b; }
  }

  int fb = -1, fcnt = 0; unsigned vrow = 0, vmlo = 0;
  if (own && (own & (own - 1)) == 0) {
    const int b = __builtin_ctz(own);
    const unsigned above = umask >> b;
    if ((umask >> b & 1) && (above == 1 || __builtin_popcount(above) == 2)) {
      const int s = rl(vS, b), L = rl(vL, b);
      const unsigned long long pmk = __ballot(lane < nW && max(r_lo, s) < min(r_hi, s + L)) & ~(1ull << j);
      const int np = __builtin_popcountll(pmk);
      if (np + kH <= 8 && np + kH > 0) {
        fb = b; fcnt = np + kH;
        const int rank = __builtin_amdgcn_mbcnt_hi((unsigned)(pmk >> 32), __builtin_amdgcn_mbcnt_lo((unsigned)pmk, 0));
        const unsigned prow = WS_PP + ((hg * 80 + lane + b) * 4 + w) * PP_W;
        const unsigned pushed = (unsigned)__builtin_amdgcn_ds_permute((((pmk >> lane) & 1) ? rank : 63) << 2, (int)prow);
        const unsigned hb = WS_HP + (min(max(lane - np, 0), 15) * 16 + h) * HP_W;
        vrow = lane < np ? pushed : hb + b * 2048;
        const unsigned mlb = lane < np ? pushed + 2048 : hb + 32768 + b * 32;
        vmlo = (unsigned)__shfl((int)mlb, min(lane >> 1, fcnt - 1)) + (lane & 1) * 16;
      }
    }
  }

  int n_comp = 0, c_slot = 0;
  unsigned c_mask = umask;
  float ro[2][2] = {{0.f, 0.f}, {0.f, 0.f}}, rm[2] = {-INFINITY, -INFINITY}, rs[2] = {0.f, 0.f};
  float ho[2][2] = {{0.f, 0.f}, {0.f, 0.f}}, hm[2] = {-INFINITY, -INFINITY}, hs[2] = {0.f, 0.f};
  int last_b = -1, held_b = -1;
  auto publish = [&](int b, const float (&o)[2][2], const float (&mm)[2], const float (&ll)[2]) {
    const unsigned tag = (unsigned)rl((int)gvw, b) + 1u;
    const unsigned slot = WS_PP + ((hg * 80 + j + b) * 4 + w) * PP_W;
    const u32x4 g0 = {__float_as_uint(o[0][0]), tag, __float_as_uint(o[0][1]), tag};
    const u32x4 g1 = {__float_as_uint(o[1][0]), tag, __float_as_uint(o[1][1]), tag};
    __builtin_amdgcn_raw_buffer_store_b128(g0, wsr, slot + lane * 16, 0, 16);
    __builtin_amdgcn_raw_buffer_store_b128(g1, wsr, slot + 1024 + lane * 16, 0, 16);
    if (lane < 2) {
      const u32x4 gm = {__float_as_uint(lane ? mm[1] : mm[0]), tag, __float_as_uint(lane ? ll[1] : ll[0]), tag};
      __builtin_amdgcn_raw_buffer_store_b128(gm, wsr, slot + 2048 + lane * 16, 0, 16);
    }
  };
  while (c_mask) {
    const int b = __builtin_ctz(c_mask); c_mask &= c_mask - 1;
    int t0, t1; unit_range(b, t0, t1);
    bf16x8 qf[1][4];
    f32x4 acc[1][8];
    float m[1] = {-INFINITY}, l[1] = {0.f};
#pragma unroll
    for (int k = 0; k < 8; ++k) acc[0][k] = f32x4{0.f, 0.f, 0.f, 0.f};
    bool first = true;
    for (int page = t0 >> 4; page * 16 < t1; ++page) {
      {
        const int young = n_iss - n_comp - 1;
        if (R == 4 && young >= 2) asm volatile("s_waitcnt vmcnt(16) lgkmcnt(0)\n\ts_barrier" ::: "memory");
        else if (young >= 1) asm volatile("s_waitcnt vmcnt(8) lgkmcnt(0)\n\ts_barrier" ::: "memory");
        else asm volatile("s_waitcnt vmcnt(0) lgkmcnt(0)\n\ts_barrier" ::: "memory");
      }
      PROBE(3);
      if (first) {
        const bool z = (c16 >= 2) || ((zmask >> b) & 1);
#pragma unroll
        for (int s = 0; s < 4; ++s) {
          u32x4 x = *(LAS u32x4*)(lds + LDS_QB + (w * R + c_slot) * 512 + (c16 & 1) * 256 + (4 * s + g4) * 16);
          if (z) x = u32x4{0u, 0u, 0u, 0u};
          qf[0][s] = __builtin_bit_cast(bf16x8, x);
        }
        asm volatile("s_waitcnt lgkmcnt(0)" ::: "memory");
        first = false;
      }
      if (i_more) issue_stage();
      const unsigned sb = c_slot * STAGE;
      tile_compute<1>(lds, kaddr0 + sb, vaddr0 + sb, qf, acc, m, l, 0, t1 - page * 16, g4);
      ++n_comp; c_slot = (c_slot + 1 == R) ? 0 : c_slot + 1;
    }
    const float lt = xg_sum(l[0]);
    LAS char* sc = lds + LDS_SC + w * 1024;
    if (c16 < 2) {
#pragma unroll
      for (int k = 0; k < 8; ++k) *(LAS f32x4*)(sc + c16 * 512 + (16 * k + 4 * g4) * 4) = acc[0][k];
    }
    asm volatile("s_waitcnt lgkmcnt(0)" ::: "memory");
    const f32x2 a0 = *(LAS f32x2*)(sc + lane * 8), a1 = *(LAS f32x2*)(sc + 512 + lane * 8);
    if (last_b >= 0) {
      if (held_b >= 0) publish(held_b, ho, hm, hs);
      held_b = last_b;
#pragma unroll
      for (int r = 0; r < 2; ++r) { ho[r][0] = ro[r][0]; ho[r][1] = ro[r][1]; hm[r] = rm[r]; hs[r] = rs[r]; }
    }
    ro[0][0] = a0[0]; ro[0][1] = a0[1]; ro[1][0] = a1[0]; ro[1][1] = a1[1];
    rm[0] = rlf(m[0], 0) * C2; rm[1] = rlf(m[0], 1) * C2;
    rs[0] = rlf(lt, 0); rs[1] = rlf(lt, 1);
    last_b = b;
  }
  STAMP(2);
  PROBE(5);
  if (last_b >= 0 && !((own >> last_b) & 1)) { publish(last_b, ro, rm, rs); last_b = -1; }
  if (held_b >= 0 && !((own >> held_b) & 1)) { publish(held_b, ho, hm, hs); held_b = -1; }
  STAMP(1);

  if (own) { const unsigned long long c0 = __builtin_amdgcn_s_memrealtime(); while (__builtin_amdgcn_s_memrealtime() - c0 < MDELAY) __builtin_amdgcn_s_sleep(1); }
  if (fb >= 0) {
    const int b = fb;
    const unsigned tag = (unsigned)rl((int)gvw, b) + 1u;
    const bool mine_last = b == last_b;
    const float om0 = mine_last ? rm[0] : hm[0], om1 = mine_last ? rm[1] : hm[1], ol0 = mine_last ? rs[0] : hs[0], ol1 = mine_last ? rs[1] : hs[1];
    const float oo00 = mine_last ? ro[0][0] : ho[0][0], oo01 = mine_last ? ro[0][1] : ho[0][1];
    const float oo10 = mine_last ? ro[1][0] : ho[1][0], oo11 = mine_last ? ro[1][1] : ho[1][1];
    auto attempt = [&](auto nb_tag) -> bool {
      constexpr int NB = decltype(nb_tag)::value;
      u32x4 d0[NB], d1[NB];
      unsigned so[NB];
#pragma unroll
      for (int k = 0; k < NB; ++k) so[k] = (unsigned)rl((int)vrow, min(k, fcnt - 1));
      asm volatile("" ::: "memory");
      const u32x4 dm = __builtin_amdgcn_raw_buffer_load_b128(wsr, vmlo, 0, 16);
#pragma unroll
      for (int k = 0; k < NB; ++k) {
        d0[k] = __builtin_amdgcn_raw_buffer_load_b128(wsr, lane * 16, so[k], 16);
        d1[k] = __builtin_amdgcn_raw_buffer_load_b128(wsr, 1024 + lane * 16, so[k], 16);
      }
      __builtin_amdgcn_sched_barrier(0);
      const bool live = lane < 2 * fcnt;
      const float mk = live ? __uint_as_float(dm[0]) : -INFINITY, lk = live ? __uint_as_float(dm[2]) : 0.f;
      const float om = (lane & 1) ? om1 : om0, ol = (lane & 1) ? ol1 : ol0;
      float M = fmaxf(mk, om);
      M = fmaxf(M, __builtin_bit_cast(float, dppi_keep<0x122>(__builtin_bit_cast(int, M), __builtin_bit_cast(int, M))));
      M = fmaxf(M, __builtin_bit_cast(float, dppi_keep<0x124>(__builtin_bit_cast(int, M), __builtin_bit_cast(int, M))));
      M = fmaxf(M, __builtin_bit_cast(float, dppi_keep<0x128>(__builtin_bit_cast(int, M), __builtin_bit_cast(int, M))));
      const float e = __builtin_amdgcn_exp2f(mk - M), eo = __builtin_amdgcn_exp2f(om - M);
      float Ls = lk * e;
      Ls += __builtin_bit_cast(float, dppi_keep<0x122>(0, __builtin_bit_cast(int, Ls)));
      Ls += __builtin_bit_cast(float, dppi_keep<0x124>(0, __builtin_bit_cast(int, Ls)));
      Ls += __builtin_bit_cast(float, dppi_keep<0x128>(0, __builtin_bit_cast(int, Ls)));
      Ls = fmaf(ol, eo, Ls);
      const float inv = __builtin_amdgcn_rcpf(Ls);
      const float en = e * inv, eon = eo * inv;
      const float eo0 = rlf(eon, 0), eo1 = rlf(eon, 1);
      float o00 = oo00 * eo0, o01 = oo01 * eo0, o10 = oo10 * eo1, o11 = oo11 * eo1;
      unsigned bad = (dm[1] ^ tag) | (dm[3] ^ tag);
#pragma unroll
      for (int k = 0; k < NB; ++k) {
        const float w0 = rlf(en, 2 * k), w1 = rlf(en, 2 * k + 1);
        bad |= ((d0[k][1] ^ tag) | (d0[k][3] ^ tag)) | ((d1[k][1] ^ tag) | (d1[k][3] ^ tag));
        o00 = fmaf(__uint_as_float(d0[k][0]), w0, o00); o01 = fmaf(__uint_as_float(d0[k][2]), w0, o01);
        o10 = fmaf(__uint_as_float(d1[k][0]), w1, o10); o11 = fmaf(__uint_as_float(d1[k][2]), w1, o11);
      }
      if (__any(bad != 0)) return false;
      __hip_bfloat16* orow = p.out + (b * NH + 2 * h) * DH + 2 * lane;
      *(unsigned*)orow = pk_bf16(o00, o01);
      *(unsigned*)(orow + DH) = pk_bf16(o10, o11);
      if (lane == 0) __builtin_amdgcn_raw_buffer_store_b32(tag, wsr, WS_G + (h * 16 + b) * 4, 0, GAUX);
      return true;
    };
#pragma unroll 1
    for (int t = 0; t < 16; ++t) {
      if (t >= FASTTRY) {
        unsigned spins = 0;
        while (true) {
          asm volatile("" ::: "memory");
          const u32x4 x = __builtin_amdgcn_raw_buffer_load_b128(wsr, vmlo, 0, 16);
          if (!__any(x[1] != tag || x[3] != tag) || ++spins > (1u << 16)) break;
          __builtin_amdgcn_s_sleep(SLEEP2);
        }
      }
      const bool ok = fcnt <= 5 ? attempt(IntC<5>{}) : attempt(IntC<8>{});
      if (ok) { own = 0; break; }
    }
  }
  while (own) {
    const int b = __builtin_ctz(own); own &= own - 1;
    const unsigned tag = (unsigned)rl((int)gvw, b) + 1u;
    const int s = rl(vS, b), L = rl(vL, b);
    unsigned long long pmk = __ballot(lane < nW && max(r_lo, s) < min(r_hi, s + L));
    unsigned smk = (1u << kH) - 1u;
    float M0 = -INFINITY, M1 = -INFINITY, L0 = 0.f, L1 = 0.f, o00 = 0.f, o01 = 0.f, o10 = 0.f, o11 = 0.f;
    if (b == last_b) {
      pmk &= ~(1ull << j);
      M0 = rm[0]; M1 = rm[1]; L0 = rs[0]; L1 = rs[1]; o00 = ro[0][0]; o01 = ro[0][1]; o10 = ro[1][0]; o11 = ro[1][1];
    } else if (b == held_b) {
      pmk &= ~(1ull << j);
      M0 = hm[0]; M1 = hm[1]; L0 = hs[0]; L1 = hs[1]; o00 = ho[0][0]; o01 = ho[0][1]; o10 = ho[1][0]; o11 = ho[1][1];
    }
    const unsigned p_rows = WS_PP + ((hg * 80 + lane + b) * 4 + w) * PP_W;
    const unsigned s_base = WS_HP + ((lane & 15) * 16 + h) * HP_W;
    unsigned spins = 0;
    bool light = false;
    while (pmk | smk) {
      asm volatile("" ::: "memory");
      unsigned long long tp = pmk; unsigned ts = smk;
      const bool single = (__builtin_popcountll(pmk) + __builtin_popcount(smk)) == 1;
      if (light && !single) {
        const int jp = pmk ? (((pmk >> lane) & 1) ? lane : __builtin_ctzll(pmk)) : 0;
        const int js = smk ? (((smk >> (lane & 15)) & 1) ? (lane & 15) : __builtin_ctz(smk)) : 0;
        u32x4 a = {0u, tag, 0u, tag}, c = {0u, tag, 0u, tag};
        if (pmk) a = __builtin_amdgcn_raw_buffer_load_b128(wsr, WS_PP + ((hg * 80 + jp + b) * 4 + w) * PP_W + 2048 + 16, 0, 16);
        if (smk) c = __builtin_amdgcn_raw_buffer_load_b128(wsr, WS_HP + (js * 16 + h) * HP_W + 32768 + (2 * b + 1) * 16, 0, 16);
        tp &= __ballot(a[1] == tag && a[3] == tag);
        ts &= (unsigned)__ballot(c[1] == tag && c[3] == tag);
      }
      bool progress = false;
      if (tp | ts) {
        auto batch = [&](auto nb_tag) {
          constexpr int NB = decltype(nb_tag)::value;
          unsigned rows[NB], mls[NB]; unsigned long long bitp[NB]; unsigned bits[NB];
#pragma unroll
          for (int k = 0; k < NB; ++k) {
            if (tp) { const int q = __builtin_ctzll(tp); bitp[k] = tp & (~tp + 1); bits[k] = 0; tp &= tp - 1; rows[k] = (unsigned)rl((int)p_rows, q); mls[k] = rows[k] + 2048; }
            else if (ts) { const int q = __builtin_ctz(ts); bits[k] = ts & (~ts + 1); bitp[k] = 0; ts &= ts - 1;
                           const unsigned sl = (unsigned)rl((int)s_base, q); rows[k] = sl + b * 2048; mls[k] = sl + 32768 + b * 32; }
            else { bitp[k] = 0; bits[k] = 0; rows[k] = rows[0]; mls[k] = mls[0]; }
          }
          u32x4 d0[NB], d1[NB], dm[NB];
          if (R0AUX != 16 && spins == 0) {
#pragma unroll
            for (int k = 0; k < NB; ++k) {
              d0[k] = __builtin_amdgcn_raw_buffer_load_b128(wsr, lane * 16, rows[k], R0AUX);
              d1[k] = __builtin_amdgcn_raw_buffer_load_b128(wsr, 1024 + lane * 16, rows[k], R0AUX);
              dm[k] = __builtin_amdgcn_raw_buffer_load_b128(wsr, (lane & 1) * 16, mls[k], R0AUX);
            }
          } else {
#pragma unroll
            for (int k = 0; k < NB; ++k) {
              d0[k] = __builtin_amdgcn_raw_buffer_load_b128(wsr, lane * 16, rows[k], 16);
              d1[k] = __builtin_amdgcn_raw_buffer_load_b128(wsr, 1024 + lane * 16, rows[k], 16);
              dm[k] = __builtin_amdgcn_raw_buffer_load_b128(wsr, (lane & 1) * 16, mls[k], 16);
            }
          }
#pragma unroll
          for (int k = 0; k < NB; ++k) {
            const bool ok = d0[k][1] == tag && d0[k][3] == tag && d1[k][1] == tag && d1[k][3] == tag && dm[k][1] == tag && dm[k][3] == tag;
            if ((bitp[k] | bits[k]) && __all(ok)) {
              const float mv = __uint_as_float(dm[k][0]), lv = __uint_as_float(dm[k][2]);
              const float m0 = rlf(mv, 0), m1 = rlf(mv, 1), l0 = rlf(lv, 0), l1 = rlf(lv, 1);
              const float n0 = fmaxf(M0, m0), n1 = fmaxf(M1, m1);
              const float a0 = __builtin_amdgcn_exp2f(M0 - n0), b0 = __builtin_amdgcn_exp2f(m0 - n0);
              const float a1 = __builtin_amdgcn_exp2f(M1 - n1), b1 = __builtin_amdgcn_exp2f(m1 - n1);
              L0 = L0 * a0 + l0 * b0; L1 = L1 * a1 + l1 * b1;
              o00 = o00 * a0 + __uint_as_float(d0[k][0]) * b0; o01 = o01 * a0 + __uint_as_float(d0[k][2]) * b0;
              o10 = o10 * a1 + __uint_as_float(d1[k][0]) * b1; o11 = o11 * a1 + __uint_as_float(d1[k][2]) * b1;
              M0 = n0; M1 = n1;
              pmk &= ~bitp[k]; smk &= ~bits[k];
              progress = true;
            }
          }
        };
        const int cnt = __builtin_popcountll(tp) + __builtin_popcount(ts);
        if (cnt == 1) batch(IntC<1>{}); else if (cnt <= 3) batch(IntC<3>{}); else batch(IntC<6>{});

      }
      if (!progress) {
        if (++spins > (1u << 16)) break;
        if (spins > FULLR) { light = true; __builtin_amdgcn_s_sleep(SLEEP); }
      }
    }
    const float i0 = 1.f / L0, i1 = 1.f / L1;
    __hip_bfloat16* orow = p.out + (b * NH + 2 * h) * DH + 2 * lane;
    *(unsigned*)orow = pk_bf16(o00 * i0, o01 * i0);
    *(unsigned*)(orow + DH) = pk_bf16(o10 * i1, o11 * i1);
    if (lane == 0) __builtin_amdgcn_raw_buffer_store_b32(tag, wsr, WS_G + (h * 16 + b) * 4, 0, GAUX);
  }
  STAMP(3);
  STAMP_FLUSH();
  PROBE(6);
  PROBE_END();
}

}

extern "C" void kernel_launch(void* const* d_in, const int* in_sizes, int n_in, void* d_out, int out_size, void* d_ws, size_t ws_size,
                              hipStream_t stream) {
  using namespace rattn;
  Params p;
  p.q = (const char*)d_in[0]; p.k = (const char*)d_in[1]; p.v = (const char*)d_in[2];
  p.bt = (const int*)d_in[3]; p.lens = (const int*)d_in[4];
  p.out = (__hip_bfloat16*)d_out; p.ws = (char*)d_ws;
  p.kvbytes = (unsigned)in_sizes[1] * 2u; p.unused = 0;
  (void)hipFuncSetAttribute((const void*)radix_attn_kernel, hipFuncAttributeMaxDynamicSharedMemorySize, LDS_TOTAL);
  hipLaunchKernelGGL(radix_attn_kernel, dim3(256), dim3(256), LDS_TOTAL, stream, p);
}
```

```cpp
#include <hip/hip_runtime.h>
#include <hip/hip_bf16.h>
#include <stdint.h>

namespace rattn {

constexpr int NSEQ = 16, NH = 32, NKV = 16, DH = 128, NBLK = 256, BS = 16;
constexpr int LK = NBLK * BS;

constexpr int HRHO = 50;
constexpr int PACE = 7300;
constexpr int PACE_LEAD = 2;
constexpr int PACE_H = 88;
constexpr int HPUB_AT = 85;
constexpr int A_S0 = 3, A_S1 = 11;
constexpr float A_F = 1.008f;
constexpr float A_DMAX = 1.2f;
constexpr float A_HMARGIN = 350.f;
constexpr float A_HPUB = 150.f;
constexpr int MDELAY = 30;
constexpr int FASTTRY = 3;

constexpr int R = 3;
constexpr int STAGE = 32768;
constexpr int RH = 4;
constexpr int RING_BYTES = 4 * RH * 8192;
constexpr int LDS_BT = RING_BYTES;
constexpr int LDS_QB = LDS_BT + NSEQ * NBLK * 4;
constexpr int LDS_SC = LDS_QB + 4 * R * 512;
constexpr int LDS_MISC = LDS_SC + 4096;
constexpr int LDS_PROG = LDS_MISC + 64;
constexpr int LDS_TOTAL = LDS_PROG + 1024;
static_assert(LDS_TOTAL <= 160 * 1024 && LDS_MISC - LDS_QB >= 8192 && RING_BYTES >= R * STAGE, "LDS");

constexpr unsigned WS_G = 0;
constexpr unsigned WS_PROG = 2048;
constexpr unsigned WS_PP = 4096;
constexpr unsigned PP_W = 2176;
constexpr unsigned WS_HP = WS_PP + 320u * 4u * PP_W + 4096u;
constexpr unsigned HP_W = 32768 + 512;
constexpr unsigned WS_END = WS_HP + 16u * 16u * HP_W;

typedef unsigned u32x4 __attribute__((ext_vector_type(4)));
typedef unsigned u32x2 __attribute__((ext_vector_type(2)));
typedef float f32x4 __attribute__((ext_vector_type(4)));
typedef float f32x2 __attribute__((ext_vector_type(2)));
typedef __bf16 bf16x8 __attribute__((ext_vector_type(8)));
typedef __bf16 bf16x2 __attribute__((ext_vector_type(2)));
typedef short s16x4 __attribute__((ext_vector_type(4)));
typedef int i32x4 __attribute__((ext_vector_type(4)));
#define LAS __attribute__((address_space(3)))

constexpr float C2 = 0.088388347648318440f * 1.4426950408889634f;

__device__ __forceinline__ int rfl(int x) { return __builtin_amdgcn_readfirstlane(x); }
__device__ __forceinline__ int rl(int x, int l) { return __builtin_amdgcn_readlane(x, l); }
__device__ __forceinline__ float rlf(float x, int l) { return __builtin_bit_cast(float, __builtin_amdgcn_readlane(__builtin_bit_cast(int, x), l)); }

__device__ __forceinline__ void dma16(const void* sbase, unsigned voff, unsigned dst) {
  unsigned keep;
  asm volatile("s_mov_b32 %0, m0\n\ts_mov_b32 m0, %3\n\ts_nop 2\n\tglobal_load_lds_dwordx4 %1, %2\n\ts_mov_b32 m0, %0"
               : "=&s"(keep) : "v"(voff), "s"(sbase), "s"(dst) : "memory");
}
__device__ __forceinline__ void dma16_sc1(const void* sbase, unsigned voff, unsigned dst) {
  unsigned keep;
  asm volatile("s_mov_b32 %0, m0\n\ts_mov_b32 m0, %3\n\ts_nop 2\n\tglobal_load_lds_dwordx4 %1, %2 sc1\n\ts_mov_b32 m0, %0"
               : "=&s"(keep) : "v"(voff), "s"(sbase), "s"(dst) : "memory");
}
__device__ __forceinline__ void dma_stage(const void* sK, const void* sV, unsigned dstK, unsigned dstV,
                                          unsigned k0, unsigned k1, unsigned k2, unsigned k3,
                                          unsigned v0, unsigned v1, unsigned v2, unsigned v3) {
  unsigned keep;
  asm volatile("s_mov_b32 %0, m0\n\ts_mov_b32 m0, %3\n\ts_nop 2\n\t"
               "global_load_lds_dwordx4 %5, %1 nt\n\t"
               "global_load_lds_dwordx4 %6, %1 offset:1024 nt\n\t"
               "global_load_lds_dwordx4 %7, %1 offset:2048 nt\n\t"
               "global_load_lds_dwordx4 %8, %1 offset:3072 nt\n\t"
               "s_mov_b32 m0, %4\n\ts_nop 0\n\t"
               "global_load_lds_dwordx4 %9, %2 nt\n\t"
               "global_load_lds_dwordx4 %10, %2 offset:1024 nt\n\t"
               "global_load_lds_dwordx4 %11, %2 offset:2048 nt\n\t"
               "global_load_lds_dwordx4 %12, %2 offset:3072 nt\n\t"
               "s_mov_b32 m0, %0"
               : "=&s"(keep)
               : "s"(sK), "s"(sV), "s"(dstK), "s"(dstV), "v"(k0), "v"(k1), "v"(k2), "v"(k3), "v"(v0), "v"(v1), "v"(v2), "v"(v3)
               : "memory");
}
template <int CTRL> __device__ __forceinline__ int dppi(int x) { return __builtin_amdgcn_update_dpp(0, x, CTRL, 0xf, 0xf, true); }
template <int CTRL> __device__ __forceinline__ int dppi_keep(int old, int x) { return __builtin_amdgcn_update_dpp(old, x, CTRL, 0xf, 0xf, false); }

__device__ __forceinline__ float xg_max(float x) {
  auto s = __builtin_amdgcn_permlane16_swap(__float_as_uint(x), __float_as_uint(x), false, false);
  x = fmaxf(__uint_as_float(s[0]), __uint_as_float(s[1]));
  auto t = __builtin_amdgcn_permlane32_swap(__float_as_uint(x), __float_as_uint(x), false, false);
  return fmaxf(__uint_as_float(t[0]), __uint_as_float(t[1]));
}
__device__ __forceinline__ float xg_sum(float x) {
  auto s = __builtin_amdgcn_permlane16_swap(__float_as_uint(x), __float_as_uint(x), false, false);
  x = __uint_as_float(s[0]) + __uint_as_float(s[1]);
  auto t = __builtin_amdgcn_permlane32_swap(__float_as_uint(x), __float_as_uint(x), false, false);
  return __uint_as_float(t[0]) + __uint_as_float(t[1]);
}
__device__ __forceinline__ unsigned pk_bf16(float a, float b) {
  bf16x2 v = {(__bf16)a, (__bf16)b};
  return __builtin_bit_cast(unsigned, v);
}

template <int NQ>
__device__ __forceinline__ void tile_compute(LAS char* lds, unsigned ka, unsigned va, const bf16x8 (&qf)[NQ][4], f32x4 (&acc)[NQ][8],
                                             float (&m)[NQ], float (&l)[NQ], int lo, int hi, int g4) {
  bf16x8 kf[4];
#pragma unroll
  for (int s = 0; s < 4; ++s) kf[s] = *(LAS bf16x8*)(lds + (ka ^ (s << 6)));
  s16x4 vf[8];
#pragma unroll
  for (int k = 0; k < 8; ++k) vf[k] = __builtin_amdgcn_ds_read_tr16_b64_v4i16((LAS s16x4*)(lds + (va ^ (k << 5))));
  f32x4 S[NQ];
#pragma unroll
  for (int qb = 0; qb < NQ; ++qb) {
    S[qb] = f32x4{0.f, 0.f, 0.f, 0.f};
#pragma unroll
    for (int s = 0; s < 4; ++s) S[qb] = __builtin_amdgcn_mfma_f32_16x16x32_bf16(kf[s], qf[qb][s], S[qb], 0, 0, 0);
  }
  if (lo > 0 || hi < 16) {
#pragma unroll
    for (int qb = 0; qb < NQ; ++qb)
#pragma unroll
      for (int r = 0; r < 4; ++r) { const int t = 4 * g4 + r; S[qb][r] = (t >= lo && t < hi) ? S[qb][r] : -INFINITY; }
  }
  s16x4 pb[NQ];
  bool grow = false;
  float alpha[NQ];
#pragma unroll
  for (int qb = 0; qb < NQ; ++qb) {
    float mx = fmaxf(fmaxf(S[qb][0], S[qb][1]), fmaxf(S[qb][2], S[qb][3]));
    mx = xg_max(mx);
    const float mn2 = fmaxf(m[qb], mx);
    grow |= mn2 > m[qb];
    alpha[qb] = __builtin_amdgcn_exp2f((m[qb] - mn2) * C2);
    m[qb] = mn2;
    const float mc = -mn2 * C2;
    const float p0 = __builtin_amdgcn_exp2f(fmaf(S[qb][0], C2, mc)), p1 = __builtin_amdgcn_exp2f(fmaf(S[qb][1], C2, mc));
    const float p2 = __builtin_amdgcn_exp2f(fmaf(S[qb][2], C2, mc)), p3 = __builtin_amdgcn_exp2f(fmaf(S[qb][3], C2, mc));
    l[qb] = fmaf(l[qb], alpha[qb], (p0 + p1) + (p2 + p3));
    pb[qb] = __builtin_bit_cast(s16x4, u32x2{pk_bf16(p0, p1), pk_bf16(p2, p3)});
  }
  if (__any(grow)) {
#pragma unroll
    for (int qb = 0; qb < NQ; ++qb)
#pragma unroll
      for (int k = 0; k < 8; ++k) acc[qb][k] *= alpha[qb];
  }
#pragma unroll
  for (int qb = 0; qb < NQ; ++qb)
#pragma unroll
    for (int k = 0; k < 8; ++k) acc[qb][k] = __builtin_amdgcn_mfma_f32_16x16x16bf16_1k(vf[k], pb[qb], acc[qb][k], 0, 0, 0);
}

template <int V> struct IntC { static constexpr int value = V; };

struct Params {
  const char* q; const char* k; const char* v; const int* bt; const int* lens; __hip_bfloat16* out; char* ws;
};

__global__ void __launch_bounds__(256, 2) radix_attn_kernel(Params p) {
  extern __shared__ __attribute__((aligned(16))) char smem[];
  LAS char* lds = (LAS char*)smem;
  const int tid = threadIdx.x, lane = tid & 63, w = rfl(tid >> 6);
  const int gidx = blockIdx.x;
  const int hg = (gidx >> 3) & 3;
  const int jj = (((gidx & 7) + 4 * (hg & 1)) & 7) | ((gidx >> 5) << 3);
  const int c16 = lane & 15, g4 = lane >> 4;
  const auto wsr = __builtin_amdgcn_make_buffer_rsrc(p.ws, 0, WS_END, 0x00020000);

  unsigned voffK[4], voffV[4], oK[4], oV[4];
  const int lenraw = p.lens[c16];
  const unsigned gvw = __builtin_amdgcn_raw_buffer_load_b32(wsr, WS_G + ((hg * 4 + w) * 16 + c16) * 4, 0, 16);
  const unsigned gvh = __builtin_amdgcn_raw_buffer_load_b32(wsr, WS_G + ((hg * 4 + (jj & 3)) * 16 + c16) * 4, 0, 16);
  const unsigned gp = __builtin_amdgcn_raw_buffer_load_b32(wsr, WS_G, 0, 16);
  int pm, len_l, minlen, vpages, incp, sump; bool zq_l;
  unsigned ptag;
  {
    const i32x4* bt4 = (const i32x4*)p.bt;
    const i32x4 r0 = bt4[lane];
    i32x4 rr[4];
#pragma unroll
    for (int i = 0; i < 4; ++i) rr[i] = bt4[(4 * w + i) * 64 + lane];
    __builtin_amdgcn_sched_barrier(0);
#pragma unroll
  for (int i = 0; i < 4; ++i) {
    const int t = 4 * w + i, u = 4 * i + g4;
    voffK[i] = g4 * 256 + ((c16 ^ t) << 4) + t * 4096 - i * 1024 + 4096;
    voffV[i] = g4 * 256 + ((c16 ^ (2 * (t & 7))) << 4) + t * 4096 - i * 1024 + 4096;
    oK[i] = u * 4096 + ((c16 ^ u) << 4) - i * 1024 + 4096;
    oV[i] = u * 4096 + ((c16 ^ (2 * (u & 7))) << 4) - i * 1024 + 4096;
  }
    asm volatile("" :: "v"(voffK[0]), "v"(voffK[1]), "v"(voffK[2]), "v"(voffK[3]), "v"(voffV[0]), "v"(voffV[1]), "v"(voffV[2]), "v"(voffV[3]),
                 "v"(oK[0]), "v"(oK[1]), "v"(oK[2]), "v"(oK[3]), "v"(oV[0]), "v"(oV[1]), "v"(oV[2]), "v"(oV[3]) : "memory");
    bool n0 = false, n1 = false, n2 = false, n3 = false;
#pragma unroll
    for (int i = 0; i < 4; ++i) {
      n0 |= rr[i][0] != r0[0]; n1 |= rr[i][1] != r0[1]; n2 |= rr[i][2] != r0[2]; n3 |= rr[i][3] != r0[3];
      *(LAS i32x4*)(lds + LDS_BT + ((4 * w + i) * 256 + 4 * lane) * 4) = rr[i];
    }
    zq_l = lenraw <= 0;
    len_l = zq_l ? LK : min(lenraw, LK);
    int mn = len_l;
    mn = min(mn, dppi_keep<0x128>(mn, mn)); mn = min(mn, dppi_keep<0x124>(mn, mn));
    mn = min(mn, dppi_keep<0x122>(mn, mn)); mn = min(mn, dppi_keep<0x121>(mn, mn));
    minlen = rfl(mn);
    vpages = (len_l + BS - 1) >> 4;
    incp = vpages;
    incp += dppi<0x111>(incp); incp += dppi<0x112>(incp); incp += dppi<0x114>(incp); incp += dppi<0x118>(incp);
    sump = rl(incp, 15);
    ptag = (unsigned)rfl((int)gp) + 1u;
    asm volatile("" :: "v"(gvw), "v"(gvh));
    const int fc = n0 ? 0 : n1 ? 1 : n2 ? 2 : n3 ? 3 : 4;
    const unsigned long long mm = __ballot(fc < 4);
    int col = NBLK;
    if (mm) { const int fl = __builtin_ctzll(mm); col = 4 * fl + rl(fc, fl); }
    if (lane == 0) *(LAS int*)(lds + LDS_MISC + 4 * w) = col;
    asm volatile("s_waitcnt lgkmcnt(0)\n\ts_barrier" ::: "memory");
    const i32x4 mc = *(LAS i32x4*)(lds + LDS_MISC);
    pm = rfl(min(min(mc[0], mc[1]), min(mc[2], mc[3])));
  }
  const int P = min(pm, minlen >> 4);
  const int tsh = P * BS;
  const int vL = vpages - P;
  const int vS = incp - vpages - c16 * P;
  const int N = sump - 16 * P;
  int kH = 0;
  if (P > 0) kH = min(min(15, P), max(1, rfl((int)__builtin_ceilf((float)P * (1024.f / HRHO) * __builtin_amdgcn_rcpf((float)(P + N))))));
  const int nW = 64 - 4 * kH;
  const float inv_nw = __builtin_amdgcn_rcpf((float)nW);
  const unsigned zmask = (unsigned)(__ballot(zq_l) & 0xffffull);
  const bool isH = jj < 4 * kH;
  const float t_all = (float)(P + N) * (16.f * 819.2f / (float)PACE);
  const unsigned long long t_ref = __builtin_amdgcn_s_memrealtime();
  const int m_avg = rfl((int)(((float)(N + nW - 1) + 0.5f) * inv_nw));
  const bool adapt_on = nW >= 24 && m_avg >= A_S1 + 8;
  auto read_progress = [&](unsigned ldsoff, float& t1, float& d) -> bool {
    const u32x4 e = *(LAS u32x4*)(lds + ldsoff + c16 * 16);
    const bool okv = e[1] == ptag && e[3] == ptag;
    const int cnt = __builtin_popcount((unsigned)__ballot(okv) & 0xffffu);
    float a = okv ? (float)(int)(e[0] - (unsigned)t_ref) : 0.f, c = okv ? (float)e[2] : 0.f;
    a += __builtin_bit_cast(float, dppi_keep<0x128>(0, __builtin_bit_cast(int, a))); c += __builtin_bit_cast(float, dppi_keep<0x128>(0, __builtin_bit_cast(int, c)));
    a += __builtin_bit_cast(float, dppi_keep<0x124>(0, __builtin_bit_cast(int, a))); c += __builtin_bit_cast(float, dppi_keep<0x124>(0, __builtin_bit_cast(int, c)));
    a += __builtin_bit_cast(float, dppi_keep<0x122>(0, __builtin_bit_cast(int, a))); c += __builtin_bit_cast(float, dppi_keep<0x122>(0, __builtin_bit_cast(int, c)));
    a += __builtin_bit_cast(float, dppi_keep<0x121>(0, __builtin_bit_cast(int, a))); c += __builtin_bit_cast(float, dppi_keep<0x121>(0, __builtin_bit_cast(int, c)));
    if (cnt < 8) return false;
    const float ic = 1.f / (float)cnt;
    t1 = rlf(a, 0) * ic; d = rlf(c, 0) * ic * (1.f / (A_S1 - A_S0));
    const float d0 = t_all * __builtin_amdgcn_rcpf((float)m_avg);
    d = fminf(d, A_DMAX * d0);
    return d > 0.7f * d0 && t1 > -t_all && t1 < t_all;
  };
  const int r_lo = (int)(((float)(lane * N) + 0.5f) * inv_nw), r_hi = (int)(((float)((lane + 1) * N) + 0.5f) * inv_nw);

  if (isH) {
    const int h = hg * 4 + (jj & 3), part = jj >> 2;
    const int p0 = (P * part) / kH, p1 = (P * (part + 1)) / kH;
#pragma unroll
    for (int i = 0; i < 2; ++i)
      dma16(p.q + (2 * w + i) * 16384 + h * 512, (lane >> 5) * 8192 + (lane & 31) * 16, LDS_QB + (2 * w + i) * 1024);
    const char* kb = p.k + h * 256 - 4096;
    const char* vb = p.v + h * 256 - 4096;
    const int nt = max(0, (p1 - p0 - w + 3) >> 2);
    const unsigned ring = w * (RH * 8192);
    int pg0 = 0, vpg = *(LAS int*)(lds + LDS_BT + min(p0 + w + 4 * lane, NBLK - 1) * 4);
    float pace_h = t_all * (PACE_H * 0.01f) * __builtin_amdgcn_rcpf((float)max(nt, 1)), pace_hb = -3.f * pace_h;
    float t_pub = t_all * (HPUB_AT * 0.01f);
    auto issue = [&](int t) {
      { const int due = rfl((int)(pace_hb + (float)t * pace_h)); if (due > 0) while ((int)(__builtin_amdgcn_s_memrealtime() - t_ref) < due) __builtin_amdgcn_s_sleep(1); }
      if (t - pg0 == 64) { pg0 = t; vpg = *(LAS int*)(lds + LDS_BT + min(p0 + w + 4 * (t + lane), NBLK - 1) * 4); }
      const size_t pb = (size_t)(unsigned)rl(vpg, t - pg0) * 65536;
      const unsigned dst = ring + (t & (RH - 1)) * 8192;
      dma_stage(kb + pb, vb + pb, dst, dst + 4096, oK[0], oK[1], oK[2], oK[3], oV[0], oV[1], oV[2], oV[3]);
    };
    const int npre = min(nt, RH - 1);
#pragma unroll 1
    for (int t = 0; t < npre; ++t) issue(t);
    if (npre == 3) asm volatile("s_waitcnt vmcnt(24)\n\ts_barrier" ::: "memory");
    else asm volatile("s_waitcnt vmcnt(0)\n\ts_barrier" ::: "memory");
    bf16x8 qf[2][4];
#pragma unroll
    for (int qb = 0; qb < 2; ++qb) {
      const bool z = (zmask >> (8 * qb + (c16 >> 1))) & 1;
#pragma unroll
      for (int s = 0; s < 4; ++s) {
        u32x4 x = *(LAS u32x4*)(lds + LDS_QB + (16 * qb + c16) * 256 + (4 * s + g4) * 16);
        if (z) x = u32x4{0u, 0u, 0u, 0u};
        qf[qb][s] = __builtin_bit_cast(bf16x8, x);
      }
    }
    f32x4 acc[2][8];
    float m[2] = {-INFINITY, -INFINITY}, l[2] = {0.f, 0.f};
#pragma unroll
    for (int qb = 0; qb < 2; ++qb)
#pragma unroll
      for (int k = 0; k < 8; ++k) acc[qb][k] = f32x4{0.f, 0.f, 0.f, 0.f};
    const unsigned ka0 = ring + c16 * 256 + ((g4 ^ c16) << 4);
    unsigned va0;
    { const int tq = 4 * g4 + (c16 >> 2), pp = lane & 3; va0 = ring + 4096 + tq * 256 + ((tq & 7) << 5) + ((pp >> 1) << 4) + ((pp & 1) << 3); }
#pragma unroll 1
    for (int t = 0; t < nt; ++t) {
      const int young = min(nt - 1 - t, RH - 2);
      if (young == 2) asm volatile("s_waitcnt vmcnt(16) lgkmcnt(0)" ::: "memory");
      else if (young == 1) asm volatile("s_waitcnt vmcnt(8) lgkmcnt(0)" ::: "memory");
      else asm volatile("s_waitcnt vmcnt(0) lgkmcnt(0)" ::: "memory");
      if (adapt_on && nt >= 12) {
        const int tp = (nt * 9) >> 4;
        if (t == tp + 3) {
          float t1, d;
          if (read_progress(LDS_PROG + w * 256, t1, d)) {
            const float t_end = t1 + (float)(m_avg - A_S1) * d * A_F;
            const float now = (float)(int)(__builtin_amdgcn_s_memrealtime() - t_ref);
            const int t_i = t + RH - 1;
            pace_h = fmaxf((t_end - A_HMARGIN - now) / (float)max(nt - t_i, 1), 0.f); pace_hb = now - (float)t_i * pace_h;
            t_pub = t_end - A_HPUB;
          }
        }
      }
      if (t + RH - 1 < nt) issue(t + RH - 1);
      if (adapt_on && nt >= 12 && t == ((nt * 9) >> 4) && lane < 16) dma16_sc1(p.ws + WS_PROG, lane * 16, LDS_PROG + w * 256);
      const unsigned sb = (t & (RH - 1)) * 8192;
      tile_compute<2>(lds, ka0 + sb, va0 + sb, qf, acc, m, l, 0, 16, g4);
    }
    constexpr unsigned ROWB = 528, WAVEB = 32 * ROWB, MLB = 4 * WAVEB;
    asm volatile("s_waitcnt lgkmcnt(0)\n\ts_barrier" ::: "memory");
#pragma unroll
    for (int qb = 0; qb < 2; ++qb) {
      const float lt = xg_sum(l[qb]);
#pragma unroll
      for (int k = 0; k < 8; ++k) *(LAS f32x4*)(lds + w * WAVEB + (16 * qb + c16) * ROWB + (16 * k + 4 * g4) * 4) = acc[qb][k];
      if (g4 == 0) *(LAS f32x2*)(lds + MLB + (w * 32 + 16 * qb + c16) * 8) = f32x2{m[qb] * C2, lt};
    }
    asm volatile("s_waitcnt lgkmcnt(0)\n\ts_barrier" ::: "memory");
    while ((int)(__builtin_amdgcn_s_memrealtime() - t_ref) < (int)t_pub) __builtin_amdgcn_s_sleep(8);
    const unsigned slot = WS_HP + (part * 16 + h) * HP_W;
    float mrow = 0.f, lrow = 0.f;
#pragma unroll
    for (int rr = 0; rr < 8; ++rr) {
      const int row = 8 * w + rr;
      f32x2 ml[4], o[4];
#pragma unroll
      for (int v = 0; v < 4; ++v) { ml[v] = *(LAS f32x2*)(lds + MLB + (v * 32 + row) * 8); o[v] = *(LAS f32x2*)(lds + v * WAVEB + row * ROWB + lane * 8); }
      const float M = fmaxf(fmaxf(ml[0][0], ml[1][0]), fmaxf(ml[2][0], ml[3][0]));
      float L = 0.f, o0 = 0.f, o1 = 0.f;
#pragma unroll
      for (int v = 0; v < 4; ++v) {
        const float e = ml[v][1] > 0.f ? __builtin_amdgcn_exp2f(ml[v][0] - M) : 0.f;
        L = fmaf(ml[v][1], e, L); o0 = fmaf(o[v][0], e, o0); o1 = fmaf(o[v][1], e, o1);
      }
      const unsigned tag = (unsigned)rl((int)gvh, row >> 1) + 1u;
      const u32x4 gr = {__float_as_uint(o0), tag, __float_as_uint(o1), tag};
      __builtin_amdgcn_raw_buffer_store_b128(gr, wsr, slot + row * 1024 + lane * 16, 0, 16);
      if (lane == rr) { mrow = M; lrow = L; }
    }
    const unsigned tagr = (unsigned)__shfl((int)gvh, ((8 * w + lane) >> 1) & 15) + 1u;
    if (lane < 8) {
      const unsigned tag = tagr;
      const u32x4 gm = {__float_as_uint(mrow), tag, __float_as_uint(lrow), tag};
      __builtin_amdgcn_raw_buffer_store_b128(gm, wsr, slot + 32768 + (8 * w + lane) * 16, 0, 16);
    }
    return;
  }

  const int j = jj - 4 * kH, h = hg * 4 + w;
  const int x0 = rl(r_lo, j), x1 = rl(r_hi, j);
  const unsigned umask = (unsigned)(__ballot(max(x0, vS) < min(x1, vS + vL)) & 0xffffull);
  const unsigned kaddr0 = w * 256 + c16 * 1024 + ((g4 ^ c16) << 4);
  unsigned vaddr0;
  { const int tq = 4 * g4 + (c16 >> 2), pp = lane & 3; vaddr0 = 16384 + tq * 1024 + w * 256 + ((tq & 7) << 5) + ((pp >> 1) << 4) + ((pp & 1) << 3); }
  const char* kbase = p.k + hg * 1024 - 4096;
  const char* vbase = p.v + hg * 1024 - 4096;

  unsigned i_mask = umask; int i_row = 0, i_t0 = 0, i_t1 = 0, i_page; bool i_more = false;
  int n_iss = 0, i_slot = 0;
  auto unit_range = [&](int b, int& t0, int& t1) {
    const int s = rl(vS, b), L = rl(vL, b);
    t0 = (P + max(x0, s) - s) * BS; t1 = min((P + min(x1, s + L) - s) * BS, rl(len_l, b));
  };
  auto q_dma = [&](int b, int slot) {
    if (lane < 32) dma16(p.q + b * 8192 + h * 512, lane * 16, LDS_QB + (w * R + slot) * 512);
  };
  if (i_mask) {
    i_row = __builtin_ctz(i_mask); i_mask &= i_mask - 1; unit_range(i_row, i_t0, i_t1);
    q_dma(i_row, 0);
    i_more = true;
  }
  i_page = i_t0 >> 4;
  int i_pg0 = i_page, vpg = *(LAS int*)(lds + LDS_BT + (i_row * NBLK + min(i_page + lane, NBLK - 1)) * 4);
  int pace_tok = 0;
  float pace_d = t_all * __builtin_amdgcn_rcpf((float)max(x1 - x0, 1)), pace_b = -(float)PACE_LEAD * pace_d;
  const int rep = (adapt_on && hg < 2 && j >= (nW >> 1) && j < (nW >> 1) + 8) ? hg * 8 + j - (nW >> 1) : -1;
  unsigned t_s0 = 0;
  auto issue_stage = [&]() {
    const int phys = rl(vpg, i_page - i_pg0);
    const int hi = i_t1 - i_page * 16;
    {
      if (adapt_on && pace_tok == A_S1 + 3 + R - 1) {
        float t1, d;
        if (read_progress(LDS_PROG, t1, d)) { pace_d = d * A_F; pace_b = t1 - (float)A_S1 * pace_d; }
      }
      const int due = rfl((int)(pace_b + (float)pace_tok * pace_d));
      pace_tok += 1;
      if (due > 0) while ((int)(__builtin_amdgcn_s_memrealtime() - t_ref) < due) __builtin_amdgcn_s_sleep(1);
    }
    const size_t pb = (size_t)(unsigned)phys * 65536;
    const unsigned dst = i_slot * STAGE + 4 * w * 1024;
    if (hi >= 16) {
      dma_stage(kbase + pb, vbase + pb, dst, dst + 16384, voffK[0], voffK[1], voffK[2], voffK[3], voffV[0], voffV[1], voffV[2], voffV[3]);
    } else {
      unsigned ok[4], ov[4];
#pragma unroll
      for (int i = 0; i < 4; ++i) {
        const int d = (min(4 * w + i, hi - 1) - (4 * w + i)) * 4096;
        ok[i] = voffK[i] + d; ov[i] = voffV[i] + d;
      }
      dma_stage(kbase + pb, vbase + pb, dst, dst + 16384, ok[0], ok[1], ok[2], ok[3], ov[0], ov[1], ov[2], ov[3]);
    }
    if (adapt_on) {
      const int k = pace_tok - 1;
      if (rep >= 0 && w == 3) {
        if (k == A_S0) t_s0 = (unsigned)__builtin_amdgcn_s_memrealtime();
        if (k == A_S1 && lane == 0) {
          const unsigned now = (unsigned)__builtin_amdgcn_s_memrealtime();
          const u32x4 e = {now, ptag, now - t_s0, ptag};
          __builtin_amdgcn_raw_buffer_store_b128(e, wsr, WS_PROG + rep * 16, 0, 16);
        }
      }
      if (k == A_S1 + 3 && w == 0 && lane < 16) dma16_sc1(p.ws + WS_PROG, lane * 16, LDS_PROG);
    }
    ++n_iss; i_slot = (i_slot + 1 == R) ? 0 : i_slot + 1;
    ++i_page;
    if (i_page * 16 >= i_t1) {
      if (i_mask) {
        i_row = __builtin_ctz(i_mask); i_mask &= i_mask - 1; unit_range(i_row, i_t0, i_t1); i_page = i_t0 >> 4;
        q_dma(i_row, i_slot);
        i_pg0 = i_page; vpg = *(LAS int*)(lds + LDS_BT + (i_row * NBLK + min(i_page + lane, NBLK - 1)) * 4);
      } else i_more = false;
    } else if (i_page - i_pg0 == 64) {
      i_pg0 = i_page; vpg = *(LAS int*)(lds + LDS_BT + (i_row * NBLK + min(i_page + lane, NBLK - 1)) * 4);
    }
  };
#pragma unroll 1
  for (int s = 0; s < R - 1; ++s) if (i_more) issue_stage();

  unsigned own = 0;
  {
    unsigned mrem = umask;
    while (mrem) {
      const int b = __builtin_ctz(mrem); mrem &= mrem - 1;
      const int s = rl(vS, b), L = rl(vL, b);
      unsigned long long pmk = __ballot(lane < nW && max(r_lo, s) < min(r_hi, s + L));
      int idx = w % __builtin_popcountll(pmk);
      while (idx--) pmk &= pmk - 1;
      if (__builtin_ctzll(pmk) == j) own |= 1u << b;
    }
    unsigned zl = (unsigned)(__ballot(vL == 0) & 0xffffull);
    while (zl) { const int b = __builtin_ctz(zl); zl &= zl - 1; if ((b * 4) % nW == j) own |= 1u << b; }
  }

  int fb = -1, fcnt = 0; unsigned vrow = 0, vmlo = 0;
  if (own && (own & (own - 1)) == 0) {
    const int b = __builtin_ctz(own);
    const unsigned above = umask >> b;
    if ((umask >> b & 1) && (above == 1 || __builtin_popcount(above) == 2)) {
      const int s = rl(vS, b), L = rl(vL, b);
      const unsigned long long pmk = __ballot(lane < nW && max(r_lo, s) < min(r_hi, s + L)) & ~(1ull << j);
      const int np = __builtin_popcountll(pmk);
      if (np + kH <= 8 && np + kH > 0) {
        fb = b; fcnt = np + kH;
        const int rank = __builtin_amdgcn_mbcnt_hi((unsigned)(pmk >> 32), __builtin_amdgcn_mbcnt_lo((unsigned)pmk, 0));
        const unsigned prow = WS_PP + ((hg * 80 + lane + b) * 4 + w) * PP_W;
        const unsigned pushed = (unsigned)__builtin_amdgcn_ds_permute((((pmk >> lane) & 1) ? rank : 63) << 2, (int)prow);
        const unsigned hb = WS_HP + (min(max(lane - np, 0), 15) * 16 + h) * HP_W;
        vrow = lane < np ? pushed : hb + b * 2048;
        const unsigned mlb = lane < np ? pushed + 2048 : hb + 32768 + b * 32;
        vmlo = (unsigned)__shfl((int)mlb, min(lane >> 1, fcnt - 1)) + (lane & 1) * 16;
      }
    }
  }

  int n_comp = 0, c_slot = 0;
  unsigned c_mask = umask;
  float ro[2][2] = {{0.f, 0.f}, {0.f, 0.f}}, rm[2] = {-INFINITY, -INFINITY}, rs[2] = {0.f, 0.f};
  float ho[2][2] = {{0.f, 0.f}, {0.f, 0.f}}, hm[2] = {-INFINITY, -INFINITY}, hs[2] = {0.f, 0.f};
  int last_b = -1, held_b = -1;
  auto publish = [&](int b, const float (&o)[2][2], const float (&mm)[2], const float (&ll)[2]) {
    const unsigned tag = (unsigned)rl((int)gvw, b) + 1u;
    const unsigned slot = WS_PP + ((hg * 80 + j + b) * 4 + w) * PP_W;
    const u32x4 g0 = {__float_as_uint(o[0][0]), tag, __float_as_uint(o[0][1]), tag};
    const u32x4 g1 = {__float_as_uint(o[1][0]), tag, __float_as_uint(o[1][1]), tag};
    __builtin_amdgcn_raw_buffer_store_b128(g0, wsr, slot + lane * 16, 0, 16);
    __builtin_amdgcn_raw_buffer_store_b128(g1, wsr, slot + 1024 + lane * 16, 0, 16);
    if (lane < 2) {
      const u32x4 gm = {__float_as_uint(lane ? mm[1] : mm[0]), tag, __float_as_uint(lane ? ll[1] : ll[0]), tag};
      __builtin_amdgcn_raw_buffer_store_b128(gm, wsr, slot + 2048 + lane * 16, 0, 16);
    }
  };
  while (c_mask) {
    const int b = __builtin_ctz(c_mask); c_mask &= c_mask - 1;
    int t0, t1; unit_range(b, t0, t1);
    bf16x8 qf[1][4];
    f32x4 acc[1][8];
    float m[1] = {-INFINITY}, l[1] = {0.f};
#pragma unroll
    for (int k = 0; k < 8; ++k) acc[0][k] = f32x4{0.f, 0.f, 0.f, 0.f};
    bool first = true;
    for (int page = t0 >> 4; page * 16 < t1; ++page) {
      if (n_iss - n_comp > 1) asm volatile("s_waitcnt vmcnt(8) lgkmcnt(0)\n\ts_barrier" ::: "memory");
      else asm volatile("s_waitcnt vmcnt(0) lgkmcnt(0)\n\ts_barrier" ::: "memory");
      if (first) {
        const bool z = (c16 >= 2) || ((zmask >> b) & 1);
#pragma unroll
        for (int s = 0; s < 4; ++s) {
          u32x4 x = *(LAS u32x4*)(lds + LDS_QB + (w * R + c_slot) * 512 + (c16 & 1) * 256 + (4 * s + g4) * 16);
          if (z) x = u32x4{0u, 0u, 0u, 0u};
          qf[0][s] = __builtin_bit_cast(bf16x8, x);
        }
        asm volatile("s_waitcnt lgkmcnt(0)" ::: "memory");
        first = false;
      }
      if (i_more) issue_stage();
      const unsigned sb = c_slot * STAGE;
      tile_compute<1>(lds, kaddr0 + sb, vaddr0 + sb, qf, acc, m, l, 0, t1 - page * 16, g4);
      ++n_comp; c_slot = (c_slot + 1 == R) ? 0 : c_slot + 1;
    }
    const float lt = xg_sum(l[0]);
    LAS char* sc = lds + LDS_SC + w * 1024;
    if (c16 < 2) {
#pragma unroll
      for (int k = 0; k < 8; ++k) *(LAS f32x4*)(sc + c16 * 512 + (16 * k + 4 * g4) * 4) = acc[0][k];
    }
    asm volatile("s_waitcnt lgkmcnt(0)" ::: "memory");
    const f32x2 a0 = *(LAS f32x2*)(sc + lane * 8), a1 = *(LAS f32x2*)(sc + 512 + lane * 8);
    if (last_b >= 0) {
      if (held_b >= 0) publish(held_b, ho, hm, hs);
      held_b = last_b;
#pragma unroll
      for (int r = 0; r < 2; ++r) { ho[r][0] = ro[r][0]; ho[r][1] = ro[r][1]; hm[r] = rm[r]; hs[r] = rs[r]; }
    }
    ro[0][0] = a0[0]; ro[0][1] = a0[1]; ro[1][0] = a1[0]; ro[1][1] = a1[1];
    rm[0] = rlf(m[0], 0) * C2; rm[1] = rlf(m[0], 1) * C2;
    rs[0] = rlf(lt, 0); rs[1] = rlf(lt, 1);
    last_b = b;
  }
  if (last_b >= 0 && !((own >> last_b) & 1)) { publish(last_b, ro, rm, rs); last_b = -1; }
  if (held_b >= 0 && !((own >> held_b) & 1)) { publish(held_b, ho, hm, hs); held_b = -1; }

  if (own) { const unsigned long long c0 = __builtin_amdgcn_s_memrealtime(); while (__builtin_amdgcn_s_memrealtime() - c0 < MDELAY) __builtin_amdgcn_s_sleep(1); }
  if (fb >= 0) {
    const int b = fb;
    const unsigned tag = (unsigned)rl((int)gvw, b) + 1u;
    const bool mine_last = b == last_b;
    const float om0 = mine_last ? rm[0] : hm[0], om1 = mine_last ? rm[1] : hm[1], ol0 = mine_last ? rs[0] : hs[0], ol1 = mine_last ? rs[1] : hs[1];
    const float oo00 = mine_last ? ro[0][0] : ho[0][0], oo01 = mine_last ? ro[0][1] : ho[0][1];
    const float oo10 = mine_last ? ro[1][0] : ho[1][0], oo11 = mine_last ? ro[1][1] : ho[1][1];
    auto attempt = [&](auto nb_tag) -> bool {
      constexpr int NB = decltype(nb_tag)::value;
      u32x4 d0[NB], d1[NB];
      unsigned so[NB];
#pragma unroll
      for (int k = 0; k < NB; ++k) so[k] = (unsigned)rl((int)vrow, min(k, fcnt - 1));
      asm volatile("" ::: "memory");
      const u32x4 dm = __builtin_amdgcn_raw_buffer_load_b128(wsr, vmlo, 0, 16);
#pragma unroll
      for (int k = 0; k < NB; ++k) {
        d0[k] = __builtin_amdgcn_raw_buffer_load_b128(wsr, lane * 16, so[k], 16);
        d1[k] = __builtin_amdgcn_raw_buffer_load_b128(wsr, 1024 + lane * 16, so[k], 16);
      }
      __builtin_amdgcn_sched_barrier(0);
      const bool live = lane < 2 * fcnt;
      const float mk = live ? __uint_as_float(dm[0]) : -INFINITY, lk = live ? __uint_as_float(dm[2]) : 0.f;
      const float om = (lane & 1) ? om1 : om0, ol = (lane & 1) ? ol1 : ol0;
      float M = fmaxf(mk, om);
      M = fmaxf(M, __builtin_bit_cast(float, dppi_keep<0x122>(__builtin_bit_cast(int, M), __builtin_bit_cast(int, M))));
      M = fmaxf(M, __builtin_bit_cast(float, dppi_keep<0x124>(__builtin_bit_cast(int, M), __builtin_bit_cast(int, M))));
      M = fmaxf(M, __builtin_bit_cast(float, dppi_keep<0x128>(__builtin_bit_cast(int, M), __builtin_bit_cast(int, M))));
      const float e = __builtin_amdgcn_exp2f(mk - M), eo = __builtin_amdgcn_exp2f(om - M);
      float Ls = lk * e;
      Ls += __builtin_bit_cast(float, dppi_keep<0x122>(0, __builtin_bit_cast(int, Ls)));
      Ls += __builtin_bit_cast(float, dppi_keep<0x124>(0, __builtin_bit_cast(int, Ls)));
      Ls += __builtin_bit_cast(float, dppi_keep<0x128>(0, __builtin_bit_cast(int, Ls)));
      Ls = fmaf(ol, eo, Ls);
      const float inv = __builtin_amdgcn_rcpf(Ls);
      const float en = e * inv, eon = eo * inv;
      const float eo0 = rlf(eon, 0), eo1 = rlf(eon, 1);
      float o00 = oo00 * eo0, o01 = oo01 * eo0, o10 = oo10 * eo1, o11 = oo11 * eo1;
      unsigned bad = (dm[1] ^ tag) | (dm[3] ^ tag);
#pragma unroll
      for (int k = 0; k < NB; ++k) {
        const float w0 = rlf(en, 2 * k), w1 = rlf(en, 2 * k + 1);
        bad |= ((d0[k][1] ^ tag) | (d0[k][3] ^ tag)) | ((d1[k][1] ^ tag) | (d1[k][3] ^ tag));
        o00 = fmaf(__uint_as_float(d0[k][0]), w0, o00); o01 = fmaf(__uint_as_float(d0[k][2]), w0, o01);
        o10 = fmaf(__uint_as_float(d1[k][0]), w1, o10); o11 = fmaf(__uint_as_float(d1[k][2]), w1, o11);
      }
      if (__any(bad != 0)) return false;
      __hip_bfloat16* orow = p.out + (b * NH + 2 * h) * DH + 2 * lane;
      *(unsigned*)orow = pk_bf16(o00, o01);
      *(unsigned*)(orow + DH) = pk_bf16(o10, o11);
      if (lane == 0) __builtin_amdgcn_raw_buffer_store_b32(tag, wsr, WS_G + (h * 16 + b) * 4, 0, 16);
      return true;
    };
    unsigned spins = 0;
#pragma unroll 1
    for (int t = 0; t < 16; ++t) {
      if (t >= FASTTRY) {
        while (true) {
          asm volatile("" ::: "memory");
          const u32x4 x = __builtin_amdgcn_raw_buffer_load_b128(wsr, vmlo, 0, 16);
          if (!__any(x[1] != tag || x[3] != tag) || ++spins > (1u << 16)) break;
          __builtin_amdgcn_s_sleep(2);
        }
      }
      const bool ok = fcnt <= 5 ? attempt(IntC<5>{}) : attempt(IntC<8>{});
      if (ok) { own = 0; break; }
    }
  }
  while (own) {
    const int b = __builtin_ctz(own); own &= own - 1;
    const unsigned tag = (unsigned)rl((int)gvw, b) + 1u;
    const int s = rl(vS, b), L = rl(vL, b);
    unsigned long long pmk = __ballot(lane < nW && max(r_lo, s) < min(r_hi, s + L));
    unsigned smk = (1u << kH) - 1u;
    float M0 = -INFINITY, M1 = -INFINITY, L0 = 0.f, L1 = 0.f, o00 = 0.f, o01 = 0.f, o10 = 0.f, o11 = 0.f;
    if (b == last_b) {
      pmk &= ~(1ull << j);
      M0 = rm[0]; M1 = rm[1]; L0 = rs[0]; L1 = rs[1]; o00 = ro[0][0]; o01 = ro[0][1]; o10 = ro[1][0]; o11 = ro[1][1];
    } else if (b == held_b) {
      pmk &= ~(1ull << j);
      M0 = hm[0]; M1 = hm[1]; L0 = hs[0]; L1 = hs[1]; o00 = ho[0][0]; o01 = ho[0][1]; o10 = ho[1][0]; o11 = ho[1][1];
    }
    const unsigned p_rows = WS_PP + ((hg * 80 + lane + b) * 4 + w) * PP_W;
    const unsigned s_base = WS_HP + ((lane & 15) * 16 + h) * HP_W;
    unsigned spins = 0;
    bool light = false;
    while (pmk | smk) {
      asm volatile("" ::: "memory");
      unsigned long long tp = pmk; unsigned ts = smk;
      const bool single = (__builtin_popcountll(pmk) + __builtin_popcount(smk)) == 1;
      if (light && !single) {
        const int jp = pmk ? (((pmk >> lane) & 1) ? lane : __builtin_ctzll(pmk)) : 0;
        const int js = smk ? (((smk >> (lane & 15)) & 1) ? (lane & 15) : __builtin_ctz(smk)) : 0;
        u32x4 a = {0u, tag, 0u, tag}, c = {0u, tag, 0u, tag};
        if (pmk) a = __builtin_amdgcn_raw_buffer_load_b128(wsr, WS_PP + ((hg * 80 + jp + b) * 4 + w) * PP_W + 2048 + 16, 0, 16);
        if (smk) c = __builtin_amdgcn_raw_buffer_load_b128(wsr, WS_HP + (js * 16 + h) * HP_W + 32768 + (2 * b + 1) * 16, 0, 16);
        tp &= __ballot(a[1] == tag && a[3] == tag);
        ts &= (unsigned)__ballot(c[1] == tag && c[3] == tag);
      }
      bool progress = false;
      if (tp | ts) {
        auto batch = [&](auto nb_tag) {
          constexpr int NB = decltype(nb_tag)::value;
          unsigned rows[NB], mls[NB]; unsigned long long bitp[NB]; unsigned bits[NB];
#pragma unroll
          for (int k = 0; k < NB; ++k) {
            if (tp) { const int q = __builtin_ctzll(tp); bitp[k] = tp & (~tp + 1); bits[k] = 0; tp &= tp - 1; rows[k] = (unsigned)rl((int)p_rows, q); mls[k] = rows[k] + 2048; }
            else if (ts) { const int q = __builtin_ctz(ts); bits[k] = ts & (~ts + 1); bitp[k] = 0; ts &= ts - 1;
                           const unsigned sl = (unsigned)rl((int)s_base, q); rows[k] = sl + b * 2048; mls[k] = sl + 32768 + b * 32; }
            else { bitp[k] = 0; bits[k] = 0; rows[k] = rows[0]; mls[k] = mls[0]; }
          }
          u32x4 d0[NB], d1[NB], dm[NB];
#pragma unroll
          for (int k = 0; k < NB; ++k) {
            d0[k] = __builtin_amdgcn_raw_buffer_load_b128(wsr, lane * 16, rows[k], 16);
            d1[k] = __builtin_amdgcn_raw_buffer_load_b128(wsr, 1024 + lane * 16, rows[k], 16);
            dm[k] = __builtin_amdgcn_raw_buffer_load_b128(wsr, (lane & 1) * 16, mls[k], 16);
          }
          __builtin_amdgcn_sched_barrier(0);
#pragma unroll
          for (int k = 0; k < NB; ++k) {
            const bool ok = d0[k][1] == tag && d0[k][3] == tag && d1[k][1] == tag && d1[k][3] == tag && dm[k][1] == tag && dm[k][3] == tag;
            if ((bitp[k] | bits[k]) && __all(ok)) {
              const float mv = __uint_as_float(dm[k][0]), lv = __uint_as_float(dm[k][2]);
              const float m0 = rlf(mv, 0), m1 = rlf(mv, 1), l0 = rlf(lv, 0), l1 = rlf(lv, 1);
              const float n0 = fmaxf(M0, m0), n1 = fmaxf(M1, m1);
              const float a0 = __builtin_amdgcn_exp2f(M0 - n0), b0 = __builtin_amdgcn_exp2f(m0 - n0);
              const float a1 = __builtin_amdgcn_exp2f(M1 - n1), b1 = __builtin_amdgcn_exp2f(m1 - n1);
              L0 = L0 * a0 + l0 * b0; L1 = L1 * a1 + l1 * b1;
              o00 = o00 * a0 + __uint_as_float(d0[k][0]) * b0; o01 = o01 * a0 + __uint_as_float(d0[k][2]) * b0;
              o10 = o10 * a1 + __uint_as_float(d1[k][0]) * b1; o11 = o11 * a1 + __uint_as_float(d1[k][2]) * b1;
              M0 = n0; M1 = n1;
              pmk &= ~bitp[k]; smk &= ~bits[k];
              progress = true;
            }
          }
        };
        const int cnt = __builtin_popcountll(tp) + __builtin_popcount(ts);
        if (cnt == 1) batch(IntC<1>{}); else if (cnt <= 3) batch(IntC<3>{}); else batch(IntC<6>{});

      }
      if (!progress) {
        if (++spins > (1u << 16)) break;
        light = true;
        __builtin_amdgcn_s_sleep(6);
      }
    }
    const float i0 = 1.f / L0, i1 = 1.f / L1;
    __hip_bfloat16* orow = p.out + (b * NH + 2 * h) * DH + 2 * lane;
    *(unsigned*)orow = pk_bf16(o00 * i0, o01 * i0);
    *(unsigned*)(orow + DH) = pk_bf16(o10 * i1, o11 * i1);
    if (lane == 0) __builtin_amdgcn_raw_buffer_store_b32(tag, wsr, WS_G + (h * 16 + b) * 4, 0, 16);
  }
}

}

extern "C" void kernel_launch(void* const* d_in, const int* in_sizes, int n_in, void* d_out, int out_size, void* d_ws, size_t ws_size,
                              hipStream_t stream) {
  using namespace rattn;
  Params p;
  p.q = (const char*)d_in[0]; p.k = (const char*)d_in[1]; p.v = (const char*)d_in[2];
  p.bt = (const int*)d_in[3]; p.lens = (const int*)d_in[4];
  p.out = (__hip_bfloat16*)d_out; p.ws = (char*)d_ws;
  (void)hipFuncSetAttribute((const void*)radix_attn_kernel, hipFuncAttributeMaxDynamicSharedMemorySize, LDS_TOTAL);
  hipLaunchKernelGGL(radix_attn_kernel, dim3(256), dim3(256), LDS_TOTAL, stream, p);
}
```
